```python
import torch

def _softmax(x, axis=-1):
    return torch.softmax(x, dim=axis)
def _gelu(x):
    return torch.nn.functional.gelu(x, approximate="tanh")
def _silu(x):
    return torch.nn.functional.silu(x)
def _sigmoid(x):
    return torch.sigmoid(x)
def _layernorm(x, axis=-1, eps=1e-5):
    m = x.mean(dim=axis, keepdim=True)
    v = x.var(dim=axis, keepdim=True, unbiased=False)
    return (x - m) / torch.sqrt(v + eps)
def _scatter_add(tgt, idx, src, axis=0):
    return tgt.clone().index_add_(axis, idx, src)
def _segment_sum(src, seg, n):
    out = torch.zeros((n,) + src.shape[1:], dtype=src.dtype)
    return out.index_add_(0, seg, src)
def _pad(x, pw):
    flat = tuple(v for pair in reversed(pw) for v in pair)
    return torch.nn.functional.pad(x, flat)

kernel_name = 'recurrent_scan__sliding_block_cached__dense_activation_silu__prompt'

def setup_inputs():

    _w = (lambda a: (lambda t: t.to(torch.bfloat16) if t.is_floating_point() else t)(torch.as_tensor(a)))
    def _A_setup():
        B, Sq, D = 1, 2048, 1024
        Hq, Hkv, Sk, Dh = 32, 4, 2048, 128
        assert Hq % Hkv == 0, (Hq, Hkv)
        return {
            "x": _w((lambda *s: torch.randn(*s, dtype=torch.bfloat16) * 0.05)(B, Sq, D)),
            "k_cache": _w((lambda *s: torch.randn(*s, dtype=torch.bfloat16) * 3.2)(B, Hkv, Sk, Dh)),
            "v_cache": _w((lambda *s: torch.randn(*s, dtype=torch.bfloat16) * 3.2)(B, Hkv, Sk, Dh)),
            "wq": _w((lambda *s: torch.randn(*s, dtype=torch.bfloat16) * 3.2)(D, Hq * Dh) * (D ** -0.5)),
            "wo": _w((lambda *s: torch.randn(*s, dtype=torch.bfloat16) * 3.2)(Hq * Dh, D) * ((Hq * Dh) ** -0.5)),
        }
    _A_in = _A_setup()

    def _B_setup():
        B, S, D, F = 1, 2048, 1024, 3584
        return {
            "x": _w((lambda *s: torch.randn(*s, dtype=torch.bfloat16) * 0.05)(B, S, D)),
            "w1": _w((lambda *s: torch.randn(*s, dtype=torch.bfloat16) * 3.2)(D, F) * (D ** -0.5)),
            "w2": _w((lambda *s: torch.randn(*s, dtype=torch.bfloat16) * 0.4)(F, D) * (F ** -0.5)),
        }
    _B_in = _B_setup()

    return {
        "A_x": _A_in["x"],
        "A_k_cache": _A_in["k_cache"],
        "A_v_cache": _A_in["v_cache"],
        "A_wq": _A_in["wq"],
        "A_wo": _A_in["wo"],
        "B_x": _B_in["x"],
        "B_w1": _B_in["w1"],
        "B_w2": _B_in["w2"],
    }

def reference(A_x, A_k_cache, A_v_cache, A_wq, A_wo, B_x, B_w1, B_w2):
    def _A_ref(x, k_cache, v_cache, wq, wo):
        B, Sq, D = x.shape
        Hq, Dh = 32, 128
        Hkv, Sk = k_cache.shape[1], k_cache.shape[2]
        W = min(4096, max(256, Sk // 4))
        rep = Hq // Hkv
        q = (lambda x, *d: x.permute(*d))((x @ wq).reshape(B, Sq, Hq, Dh), 0, 2, 1, 3)
        k = (lambda x, n, axis: torch.repeat_interleave(x, n, dim=axis))(k_cache[:, :, Sk - W:, :], rep, axis=1)
        v = (lambda x, n, axis: torch.repeat_interleave(x, n, dim=axis))(v_cache[:, :, Sk - W:, :], rep, axis=1)
        scores = torch.einsum("bhqd,bhkd->bhqk", q, k) * (Dh ** -0.5)
        p = _softmax(scores, axis=-1)
        o = torch.einsum("bhqk,bhkd->bhqd", p, v)
        return (lambda x, *d: x.permute(*d))(o, 0, 2, 1, 3).reshape(B, Sq, Hq * Dh) @ wo

    def _B_ref(x, w1, w2):
        h = torch.einsum("bsd,df->bsf", x, w1)
        h = _silu(h)
        out = torch.einsum("bsf,fd->bsd", h, w2)
        return out

    x = A_x
    xn = _layernorm(x, axis=-1)
    s = _A_ref(xn, A_k_cache, A_v_cache, A_wq, A_wo)
    g = _silu(_B_ref(xn, B_w1, B_w2))
    return x + s * g
```

```cpp
#include <hip/hip_runtime.h>
#include <hip/hip_bf16.h>
typedef unsigned short u16;
typedef short bf16x8 __attribute__((ext_vector_type(8)));
typedef float f32x4 __attribute__((ext_vector_type(4)));
__device__ __forceinline__ float bf2f(u16 b){ return __uint_as_float(((unsigned)b)<<16); }
__device__ __forceinline__ u16 f2bf(float f){ unsigned u=__float_as_uint(f); u += 0x7fffu + ((u>>16)&1u); return (u16)(u>>16); }
__device__ __forceinline__ float rbf(float f){ return bf2f(f2bf(f)); }

__global__ void ln_kernel(const u16* x, u16* xn){
  int row = blockIdx.x; int t = threadIdx.x;
  __shared__ float red[8];
  float v[4]; float s=0;
  for(int i=0;i<4;i++){ v[i]=bf2f(x[row*1024+t*4+i]); s+=v[i]; }
  for(int o=32;o>=1;o>>=1) s+=__shfl_xor(s,o);
  if((t&63)==0) red[t>>6]=s; __syncthreads();
  float mean=(red[0]+red[1]+red[2]+red[3])*(1.f/1024.f);
  float q=0; for(int i=0;i<4;i++){ float d=v[i]-mean; q+=d*d; }
  for(int o=32;o>=1;o>>=1) q+=__shfl_xor(q,o);
  if((t&63)==0) red[4+(t>>6)]=q; __syncthreads();
  float var=(red[4]+red[5]+red[6]+red[7])*(1.f/1024.f);
  float r=rsqrtf(var+1e-5f);
  for(int i=0;i<4;i++) xn[row*1024+t*4+i]=f2bf((v[i]-mean)*r);
}
template<int EPI>
__global__ void gemm(const u16* A, const u16* B, u16* C, int M,int N,int K, const u16* aux1, const u16* aux2){
  __shared__ u16 As[64][40]; __shared__ u16 Bs[64][40];
  int t=threadIdx.x, w=t>>6, l=t&63; int bm=blockIdx.y*64, bn=blockIdx.x*64;
  int wr=w>>1, wc=w&1;
  f32x4 acc[2][2]={};
  for(int k0=0;k0<K;k0+=32){
    { int r=t>>2, c=(t&3)*8; *(bf16x8*)&As[r][c] = *(const bf16x8*)&A[(size_t)(bm+r)*K+k0+c]; }
    { int kk=t>>3, c=(t&7)*8; bf16x8 v=*(const bf16x8*)&B[(size_t)(k0+kk)*N+bn+c]; for(int j=0;j<8;j++) Bs[c+j][kk]=v[j]; }
    __syncthreads();
    bf16x8 a[2],b[2];
    for(int i=0;i<2;i++){ a[i]=*(bf16x8*)&As[wr*32+i*16+(l&15)][(l>>4)*8]; b[i]=*(bf16x8*)&Bs[wc*32+i*16+(l&15)][(l>>4)*8]; }
    for(int i=0;i<2;i++)for(int j=0;j<2;j++) acc[i][j]=__builtin_amdgcn_mfma_f32_16x16x32_bf16(a[i],b[j],acc[i][j],0,0,0);
    __syncthreads();
  }
  for(int i=0;i<2;i++)for(int j=0;j<2;j++)for(int r=0;r<4;r++){
    int row=bm+wr*32+i*16+(l>>4)*4+r, col=bn+wc*32+j*16+(l&15);
    float v=acc[i][j][r];
    if(EPI==1){ v=rbf(v); v = v/(1.f+__expf(-v)); }
    if(EPI==2){
      v=rbf(v); float g=bf2f(aux1[(size_t)row*N+col]); v = bf2f(aux2[(size_t)row*N+col]) + rbf(v*g); }
    C[(size_t)row*N+col]=f2bf(v);
  }
}
__global__ __launch_bounds__(256) void attn(const u16* q, const u16* kc, const u16* vc, u16* o){
  int h=blockIdx.y, row=blockIdx.x; int t=threadIdx.x;
  int g=h>>3;
  __shared__ float qs[128]; __shared__ float p[512]; __shared__ float red[8];
  if(t<128) qs[t]=bf2f(q[(size_t)row*4096+h*128+t]);
  __syncthreads();
  float sc[2];
  _Pragma("unroll") for(int i=0;i<2;i++){ int key=t+i*256; const u16* kr=kc+((size_t)g*2048+1536+key)*128; float s=0;
    _Pragma("unroll 2") for(int d=0;d<128;d+=8){ bf16x8 v=*(const bf16x8*)&kr[d]; for(int j=0;j<8;j++) s+=qs[d+j]*bf2f((u16)v[j]); }
    s*=0.08838834764831845f;
    sc[i]=s; }
  float m=fmaxf(sc[0],sc[1]); for(int o2=32;o2>=1;o2>>=1) m=fmaxf(m,__shfl_xor(m,o2));
  if((t&63)==0) red[t>>6]=m; __syncthreads();
  m=fmaxf(fmaxf(red[0],red[1]),fmaxf(red[2],red[3]));
  float e0=__expf(sc[0]-m), e1=__expf(sc[1]-m); float s=e0+e1; for(int o2=32;o2>=1;o2>>=1) s+=__shfl_xor(s,o2);
  if((t&63)==0) red[4+(t>>6)]=s; __syncthreads();
  s=red[4]+red[5]+red[6]+red[7];
  p[t]=rbf(e0); p[t+256]=rbf(e1);
  __syncthreads();
  if(t<128){ float a=0; for(int k=0;k<512;k++) a+=p[k]*bf2f(vc[((size_t)g*2048+1536+k)*128+t]); o[(size_t)row*4096+h*128+t]=f2bf(a/s); }
}
extern "C" void kernel_launch(void* const* d_in, const int* in_sizes, int n_in, void* d_out, int out_size, void* d_ws, size_t ws_size, hipStream_t stream){
  const u16* x=(const u16*)d_in[0]; const u16* kc=(const u16*)d_in[1]; const u16* vc=(const u16*)d_in[2];
  const u16* wq=(const u16*)d_in[3]; const u16* wo=(const u16*)d_in[4]; const u16* w1=(const u16*)d_in[6]; const u16* w2=(const u16*)d_in[7];
  u16* ws=(u16*)d_ws; u16* xn=ws; u16* q=xn+2048*1024; u16* h=q+2048*4096; u16* o=h+2048*3584; u16* g=o+2048*4096;
  ln_kernel<<<2048,256,0,stream>>>(x,xn);
  gemm<0><<<dim3(4096/64,2048/64),256,0,stream>>>(xn,wq,q,2048,4096,1024,nullptr,nullptr);
  gemm<1><<<dim3(3584/64,2048/64),256,0,stream>>>(xn,w1,h,2048,3584,1024,nullptr,nullptr);
  attn<<<dim3(2048,32),256,0,stream>>>(q,kc,vc,o);
  gemm<1><<<dim3(1024/64,2048/64),256,0,stream>>>(h,w2,g,2048,1024,3584,nullptr,nullptr);
  gemm<2><<<dim3(1024/64,2048/64),256,0,stream>>>(o,wo,(u16*)d_out,2048,1024,4096,g,x);
}
```

```cpp
#include <hip/hip_runtime.h>
#include <stdint.h>

typedef unsigned short u16;
typedef __bf16 bf16x2 __attribute__((ext_vector_type(2)));
typedef __bf16 bf16x4 __attribute__((ext_vector_type(4)));
typedef __bf16 bf16x8 __attribute__((ext_vector_type(8)));
typedef float f32x2 __attribute__((ext_vector_type(2)));
typedef float f32x4 __attribute__((ext_vector_type(4)));
typedef float f32x16 __attribute__((ext_vector_type(16)));
typedef unsigned u32x2 __attribute__((ext_vector_type(2)));
typedef unsigned u32x4 __attribute__((ext_vector_type(4)));
#define LAS __attribute__((address_space(3)))
typedef LAS char* lptr;
#define DI __device__ __forceinline__
#define SCHED() __builtin_amdgcn_sched_barrier(0)
#define BAR() asm volatile("s_barrier" ::: "memory")
#define WAIT_V(n) asm volatile("s_waitcnt vmcnt(" #n ")" ::: "memory")
#define WAIT_L(n) do { asm volatile("" ::: "memory"); __builtin_amdgcn_s_waitcnt(0xC07F | ((n) << 8)); asm volatile("" ::: "memory"); } while (0)
#define MFMA16(a, b, c) __builtin_amdgcn_mfma_f32_16x16x32_bf16(a, b, c, 0, 0, 0)
#define MFMA32(a, b, c) __builtin_amdgcn_mfma_f32_32x32x16_bf16(a, b, c, 0, 0, 0)

DI void glds(unsigned voff, const char* sbase, unsigned ldsaddr) {
  asm volatile("s_mov_b32 m0, %2\n\ts_nop 0\n\tglobal_load_lds_dwordx4 %0, %1" ::"v"(voff), "s"(sbase), "s"(ldsaddr) : "memory");
}
DI unsigned pk2(float lo, float hi) { f32x2 v = {lo, hi}; return __builtin_bit_cast(unsigned, __builtin_convertvector(v, bf16x2)); }
DI float bf2f(unsigned b) { return __uint_as_float(b << 16); }
DI bf16x8 ldb128(lptr p) { return *(const LAS bf16x8*)p; }
DI bf16x8 ldtr2(lptr p, int d) {
  bf16x4 a = __builtin_amdgcn_ds_read_tr16_b64_v4bf16((LAS bf16x4*)p);
  bf16x4 b = __builtin_amdgcn_ds_read_tr16_b64_v4bf16((LAS bf16x4*)(p + d));
  return __builtin_shufflevector(a, b, 0, 1, 2, 3, 4, 5, 6, 7);
}
DI float silu(float v) { return v * __builtin_amdgcn_rcpf(1.0f + __builtin_amdgcn_exp2f(v * -1.4426950408889634f)); }

__global__ __launch_bounds__(256) void ln_kernel(const u16* __restrict__ x, u16* __restrict__ xn, unsigned* __restrict__ flags) {
  const int row = blockIdx.x * 4 + (threadIdx.x >> 6), lane = threadIdx.x & 63;
  if (blockIdx.x == 0 && threadIdx.x < 128) flags[threadIdx.x * 32] = 0u;
  const u32x4* p = (const u32x4*)(x + (size_t)row * 1024);
  u32x4 a = p[lane], b = p[64 + lane];
  float v[16];
#pragma unroll
  for (int i = 0; i < 4; ++i) { v[2 * i] = bf2f(a[i] & 0xffffu); v[2 * i + 1] = __uint_as_float(a[i] & 0xffff0000u); v[8 + 2 * i] = bf2f(b[i] & 0xffffu); v[9 + 2 * i] = __uint_as_float(b[i] & 0xffff0000u); }
  float s = 0.f;
#pragma unroll
  for (int i = 0; i < 16; ++i) s += v[i];
#pragma unroll
  for (int o = 32; o >= 1; o >>= 1) s += __shfl_xor(s, o);
  const float mean = s * (1.0f / 1024.0f);
  float q = 0.f;
#pragma unroll
  for (int i = 0; i < 16; ++i) { v[i] -= mean; q += v[i] * v[i]; }
#pragma unroll
  for (int o = 32; o >= 1; o >>= 1) q += __shfl_xor(q, o);
  const float r = rsqrtf(q * (1.0f / 1024.0f) + 1e-5f);
  u32x4 oa, ob;
#pragma unroll
  for (int i = 0; i < 4; ++i) { oa[i] = pk2(v[2 * i] * r, v[2 * i + 1] * r); ob[i] = pk2(v[8 + 2 * i] * r, v[9 + 2 * i] * r); }
  u32x4* po = (u32x4*)(xn + (size_t)row * 1024);
  po[lane] = oa; po[64 + lane] = ob;
}

constexpr int HTB = 16384;
#define SW(b, h) (((b) * 2 + (h)) * HTB)
#define SX(b, h) ((4 + (b) * 2 + (h)) * HTB)
constexpr int SLOT_A = 0, SLOT_B = 32768, SLOT_C = 65536, SLOT_D = 98304, SLOT_E = 131072, K1_LDS = 163840;

__global__ __launch_bounds__(512, 2) void k1_kernel(const char* __restrict__ xn, const char* __restrict__ kc, const char* __restrict__ vc,
                                                    const char* __restrict__ wq, const char* __restrict__ w1, char* __restrict__ hbuf, char* __restrict__ obuf) {
  extern __shared__ __attribute__((aligned(16))) char shm[];
  const lptr lds = (lptr)shm;
  const unsigned lds0 = (unsigned)(uintptr_t)lds;
  const int tid = threadIdx.x, wid = __builtin_amdgcn_readfirstlane(tid >> 6), lane = tid & 63, wr = wid >> 2, wc = wid & 3;
  const int bid = blockIdx.x, xcd = bid & 7, idx = bid >> 3, hd = 4 * xcd + (idx & 3), rb = idx >> 2;
  const int g = lane >> 4, t = lane & 15, q4 = (lane >> 2) & 3, p4 = lane & 3;

  unsigned voffX, voffQ, voffHA, voffHB;
  {
    const int r = lane >> 3, c = lane & 7;
    const int row = 8 * wid + r;
    voffX = (unsigned)(row * 2048 + ((c ^ ((row >> 1) & 7)) << 4));
    const int pc = wid & 3, st = wid >> 2;
    const int k = 32 * st + 16 * (pc & 1) + 4 * (pc >> 1) + 8 * (r >> 2) + (r & 3);
    const int cw = c ^ (((r >> 1) & 3) << 1);
    voffQ = (unsigned)(k * 8192 + cw * 16);
    voffHA = (unsigned)(k * 7168 + cw * 16);
    voffHB = (unsigned)(k * 7168 + (cw < 5 ? cw : 5) * 16);
  }
  const char* xb = xn + (size_t)rb * 256 * 2048;
  const char* qb = wq + hd * 256;
  const char* hB = w1 + 4096 + 96 * hd;
  const char* hA = w1 + 128 * hd;
  const unsigned ldw = lds0 + wid * 1024;
#define ST_X(b, h, kt) do { glds(voffX, xb + (h) * 262144 + (kt) * 128, ldw + SX(b, h)); glds(voffX, xb + (h) * 262144 + 131072 + (kt) * 128, ldw + SX(b, h) + 8192); } while (0)
#define ST_W0(b, kt) do { glds(voffQ, qb + (size_t)(kt) * 524288, ldw + SW(b, 0)); glds(voffQ, qb + (size_t)(kt) * 524288 + 128, ldw + SW(b, 0) + 8192); } while (0)
#define ST_W1(b, kt) do { glds(voffHB, hB + (size_t)(kt) * 458752, ldw + SW(b, 1)); glds(voffHA, hA + (size_t)(kt) * 458752, ldw + SW(b, 1) + 8192); } while (0)

  lptr wb[4], xr[2];
#pragma unroll
  for (int ft = 0; ft < 4; ++ft) wb[ft] = lds + wr * 8192 + (g >> 1) * 1024 + (4 * (g & 1) + q4) * 128 + 32 * (ft ^ (2 * (g & 1) + (q4 >> 1))) + 8 * p4;
#pragma unroll
  for (int ks = 0; ks < 2; ++ks) xr[ks] = lds + 65536 + (wc * 32 + t) * 128 + 16 * ((4 * ks + g) ^ (t >> 1));
#define LDW(dst, b, h) do { _Pragma("unroll") for (int m = 0; m < 4; ++m) _Pragma("unroll") for (int k = 0; k < 2; ++k) dst[m][k] = ldtr2(wb[m] + SW(b, h) + k * 4096, 2048); } while (0)
#define LDX(dst, b, h) do { _Pragma("unroll") for (int n = 0; n < 2; ++n) _Pragma("unroll") for (int k = 0; k < 2; ++k) dst[n][k] = ldb128(xr[k] + (SX(b, h) - 65536) + n * 2048); } while (0)
#define MMA(ai, bj, At, Bt) do { __builtin_amdgcn_s_setprio(1); \
    _Pragma("unroll") for (int m = 0; m < 3; ++m) _Pragma("unroll") for (int n = 0; n < 2; ++n) _Pragma("unroll") for (int k = 0; k < 2; ++k) \
      acc[ai][bj][m][n] = MFMA16(At[m][k], Bt[n][k], acc[ai][bj][m][n]); \
    if ((ai) == 0 || wr == 1) { _Pragma("unroll") for (int n = 0; n < 2; ++n) _Pragma("unroll") for (int k = 0; k < 2; ++k) \
      acc[ai][bj][3][n] = MFMA16(At[3][k], Bt[n][k], acc[ai][bj][3][n]); } \
    __builtin_amdgcn_s_setprio(0); } while (0)

  f32x4 acc[2][2][4][2];
#pragma unroll
  for (int a = 0; a < 2; ++a)
#pragma unroll
    for (int b = 0; b < 2; ++b)
#pragma unroll
      for (int m = 0; m < 4; ++m)
#pragma unroll
        for (int n = 0; n < 2; ++n) acc[a][b][m][n] = (f32x4){0.f, 0.f, 0.f, 0.f};
  bf16x8 At[4][2], B0[2][2], B1[2][2];

  ST_X(0, 0, 0); ST_W0(0, 0); ST_X(0, 1, 0); ST_W1(0, 0);
  ST_X(1, 0, 1); ST_W0(1, 1); ST_X(1, 1, 1);
  WAIT_V(6); BAR();
  if (wr == 1) BAR();
  SCHED();
  for (int kt = 0; kt < 14; kt += 2) {
    LDX(B0, 0, 0); SCHED(); LDW(At, 0, 0); ST_W1(1, kt + 1);
    WAIT_L(12); BAR(); WAIT_L(0); MMA(0, 0, At, B0); BAR(); SCHED();
    LDX(B1, 0, 1); ST_X(0, 0, kt + 2);
    BAR(); WAIT_L(0); MMA(0, 1, At, B1); BAR(); SCHED();
    LDW(At, 0, 1); ST_W0(0, kt + 2);
    BAR(); WAIT_L(0); MMA(1, 0, At, B0); BAR(); SCHED();
    ST_X(0, 1, kt + 2);
    WAIT_V(6); BAR(); MMA(1, 1, At, B1); BAR(); SCHED();
    LDX(B0, 1, 0); SCHED(); LDW(At, 1, 0); ST_W1(0, kt + 2);
    WAIT_L(12); BAR(); WAIT_L(0); MMA(0, 0, At, B0); BAR(); SCHED();
    LDX(B1, 1, 1); ST_X(1, 0, kt + 3);
    BAR(); WAIT_L(0); MMA(0, 1, At, B1); BAR(); SCHED();
    LDW(At, 1, 1); ST_W0(1, kt + 3);
    BAR(); WAIT_L(0); MMA(1, 0, At, B0); BAR(); SCHED();
    ST_X(1, 1, kt + 3);
    WAIT_V(6); BAR(); MMA(1, 1, At, B1); BAR(); SCHED();
  }
  const int kvg = hd >> 3;
  const char* kb_g = kc + ((size_t)kvg * 2048 + 1536) * 256;
  const char* vb_g = vc + ((size_t)kvg * 2048 + 1536) * 256;
  unsigned voffK, voffV;
  {
    const int sub = lane >> 5, r = (lane >> 2) & 7, x = lane & 3;
    voffK = (unsigned)(r * 256 + (4 * sub + (x ^ ((2 * (wid & 1) + (r >> 2)) & 3))) * 16);
    voffV = (unsigned)(r * 256 + sub * 64 + x * 16);
  }
#define ST_KV8(voff, gsrc, slot) do { glds(voff, (gsrc) + wid * 2048, lds0 + (slot) + wid * 2048); glds(voff, (gsrc) + wid * 2048 + 128, lds0 + (slot) + wid * 2048 + 1024); } while (0)
  {
    LDX(B0, 0, 0); SCHED(); LDW(At, 0, 0); ST_W1(1, 15);
    WAIT_L(12); BAR(); WAIT_L(0); MMA(0, 0, At, B0); BAR(); SCHED();
    LDX(B1, 0, 1); ST_KV8(voffK, kb_g, SLOT_E);
    BAR(); WAIT_L(0); MMA(0, 1, At, B1); BAR(); SCHED();
    LDW(At, 0, 1); ST_KV8(voffK, kb_g + 16384, SLOT_E + 16384);
    BAR(); WAIT_L(0); MMA(1, 0, At, B0); BAR(); SCHED();
    ST_KV8(voffV, vb_g, SLOT_C);
    WAIT_V(6); BAR(); MMA(1, 1, At, B1); BAR(); SCHED();
    LDX(B0, 1, 0); SCHED(); LDW(At, 1, 0); ST_KV8(voffV, vb_g + 16384, SLOT_C + 16384);
    BAR(); WAIT_L(0); MMA(0, 0, At, B0); BAR(); SCHED();
    LDX(B1, 1, 1); ST_KV8(voffK, kb_g + 32768, SLOT_A);
    BAR(); WAIT_L(0); MMA(0, 1, At, B1); BAR(); SCHED();
    LDW(At, 1, 1); ST_KV8(voffK, kb_g + 49152, SLOT_A + 16384);
    BAR(); WAIT_L(0); MMA(1, 0, At, B0); BAR(); SCHED();
    WAIT_V(8); BAR(); MMA(1, 1, At, B1); BAR(); SCHED();
  }
  {
    const float qs = 0.08838834764831845f * 1.4426950408889634f;
    const lptr qw = lds + wc * 8192 + wr * 4096 + ((g >> 1) * 32 + t) * 16 + (g & 1) * 8;
#pragma unroll
    for (int bj = 0; bj < 2; ++bj)
#pragma unroll
      for (int m = 0; m < 4; ++m)
#pragma unroll
        for (int n = 0; n < 2; ++n) {
          const f32x4 v = acc[0][bj][m][n] * qs;
          u32x2 w = {pk2(v[0], v[1]), pk2(v[2], v[3])};
          *(LAS u32x2*)(qw + (bj ? SLOT_D : SLOT_B) + m * 1024 + n * 256) = w;
        }
  }
  WAIT_L(0); BAR(); SCHED();
  bf16x8 qf[8];
  const lptr qr = lds + (wr ? SLOT_D : SLOT_B) + wc * 8192 + lane * 16;
  if (wr == 1) {
#pragma unroll
    for (int s = 0; s < 8; ++s) qf[s] = ldb128(qr + s * 1024);
  }
  {
    char* hw = hbuf + lane * 16;
#pragma unroll
    for (int bj = 0; bj < 2; ++bj)
#pragma unroll
      for (int n = 0; n < 2; ++n) {
        const int tt = rb * 16 + bj * 8 + wc * 2 + n;
        u32x2 w[4];
#pragma unroll
        for (int m = 0; m < 3; ++m) { const f32x4 v = acc[1][bj][m][n]; w[m] = (u32x2){pk2(silu(v[0]), silu(v[1])), pk2(silu(v[2]), silu(v[3]))}; }
        if (wr == 1) {
          { const f32x4 v = acc[1][bj][3][n]; w[3] = (u32x2){pk2(silu(v[0]), silu(v[1])), pk2(silu(v[2]), silu(v[3]))}; }
          char* d = hw + ((size_t)(hd * 128 + tt) * 2) * 1024;
          *(u32x4*)d = (u32x4){w[0][0], w[0][1], w[1][0], w[1][1]};
          *(u32x4*)(d + 1024) = (u32x4){w[2][0], w[2][1], w[3][0], w[3][1]};
        } else {
          const int T0 = 128 + 3 * hd;
          if ((hd & 1) == 0) {
            const int pf = T0 >> 1, ph = pf + 1;
            *(u32x4*)(hw + ((size_t)((pf >> 1) * 128 + tt) * 2 + (pf & 1)) * 1024) = (u32x4){w[0][0], w[0][1], w[1][0], w[1][1]};
            *(u32x2*)(hw + ((size_t)((ph >> 1) * 128 + tt) * 2 + (ph & 1)) * 1024) = w[2];
          } else {
            const int ph = T0 >> 1, pf = ph + 1;
            *(u32x2*)(hw + ((size_t)((ph >> 1) * 128 + tt) * 2 + (ph & 1)) * 1024 + 8) = w[0];
            *(u32x4*)(hw + ((size_t)((pf >> 1) * 128 + tt) * 2 + (pf & 1)) * 1024) = (u32x4){w[1][0], w[1][1], w[2][0], w[2][1]};
          }
        }
      }
  }
  WAIT_L(0); BAR(); SCHED();
  if (wr == 0) {
#pragma unroll
    for (int s = 0; s < 8; ++s) qf[s] = ldb128(qr + s * 1024);
  }
  const int r32 = lane & 31, hh = lane >> 5;
  lptr kr[2];
#pragma unroll
  for (int e = 0; e < 2; ++e) kr[e] = lds + 2048 * (r32 >> 3) + 64 * (r32 & 7) + 16 * ((2 * e + hh) ^ ((r32 >> 2) & 3));
  const lptr vr = lds + ((lane & 3) << 3) + (((lane >> 2) & 3) << 6) + (((lane >> 4) & 1) << 5) + (hh << 8);
  f32x16 o[4], s[4];
#pragma unroll
  for (int d = 0; d < 4; ++d)
#pragma unroll
    for (int i = 0; i < 16; ++i) o[d][i] = 0.f;
  bf16x8 pf[8];
  float mrun = -INFINITY, lrun = 0.f;
#pragma unroll 1
  for (int c = 0; c <= 4; ++c) {
    const int kslot = (c == 1) ? SLOT_A : ((c == 2) ? SLOT_D : SLOT_E);
    const int vslot = (c == 2) ? SLOT_B : ((c == 3) ? SLOT_A : SLOT_C);
    __builtin_amdgcn_s_setprio(1);
    if (c > 0) {
      const lptr v0 = vr + vslot;
#pragma unroll
      for (int st = 0; st < 8; ++st)
#pragma unroll
        for (int d = 0; d < 4; ++d) o[d] = MFMA32(ldtr2(v0 + (st * 8 + d) * 512, 2048), pf[st], o[d]);
    }
    if (c == 4) { __builtin_amdgcn_s_setprio(0); break; }
    {
      const lptr k0 = kr[0] + kslot, k1 = kr[1] + kslot;
#pragma unroll
      for (int kb = 0; kb < 4; ++kb)
#pragma unroll
        for (int i = 0; i < 16; ++i) s[kb][i] = 0.f;
#pragma unroll
      for (int st = 0; st < 8; ++st)
#pragma unroll
        for (int kb = 0; kb < 4; ++kb) s[kb] = MFMA32(ldb128(((st & 1) ? k1 : k0) + kb * 8192 + (st >> 1) * 512), qf[st], s[kb]);
    }
    __builtin_amdgcn_s_setprio(0);
    WAIT_V(0); WAIT_L(0); BAR(); SCHED();
    if (c + wr < 3) {
      const bool isK = (wr == 0) && (c < 2);
      const char* src = isK ? kb_g + (c + 2) * 32768 : vb_g + (wr ? c + 1 : 3) * 32768;
      const int slot = wr ? (c == 0 ? SLOT_B : SLOT_A) : (c == 0 ? SLOT_D : (c == 1 ? SLOT_E : SLOT_C));
      const unsigned vo = isK ? voffK : voffV;
#pragma unroll
      for (int i = 0; i < 4; ++i) {
        glds(vo, src + (wc + 4 * i) * 2048, lds0 + slot + (wc + 4 * i) * 2048);
        glds(vo, src + (wc + 4 * i) * 2048 + 128, lds0 + slot + (wc + 4 * i) * 2048 + 1024);
      }
    }
    {
      float m0 = fmaxf(s[0][0], s[1][0]), m1 = fmaxf(s[2][0], s[3][0]);
#pragma unroll
      for (int i = 1; i < 16; ++i) { m0 = fmaxf(fmaxf(m0, s[0][i]), s[1][i]); m1 = fmaxf(fmaxf(m1, s[2][i]), s[3][i]); }
      float mx = fmaxf(m0, m1);
      auto rr = __builtin_amdgcn_permlane32_swap(__float_as_uint(mx), __float_as_uint(mx), false, false);
      mx = fmaxf(fmaxf(__uint_as_float(rr[0]), __uint_as_float(rr[1])), mrun);
      const float alpha = __builtin_amdgcn_exp2f(mrun - mx);
      mrun = mx;
      float l0 = 0.f, l1 = 0.f;
#pragma unroll
      for (int kb = 0; kb < 4; ++kb) {
#pragma unroll
        for (int i = 0; i < 16; ++i) s[kb][i] = __builtin_amdgcn_exp2f(s[kb][i] - mx);
#pragma unroll
        for (int i = 0; i < 16; i += 2) { l0 += s[kb][i]; l1 += s[kb][i + 1]; }
#pragma unroll
        for (int e = 0; e < 2; ++e) {
          u32x4 w = {pk2(s[kb][8 * e], s[kb][8 * e + 1]), pk2(s[kb][8 * e + 2], s[kb][8 * e + 3]), pk2(s[kb][8 * e + 4], s[kb][8 * e + 5]), pk2(s[kb][8 * e + 6], s[kb][8 * e + 7])};
          pf[2 * kb + e] = __builtin_bit_cast(bf16x8, w);
        }
      }
      lrun = lrun * alpha + (l0 + l1);
#pragma unroll
      for (int d = 0; d < 4; ++d) o[d] *= alpha;
    }
    SCHED(); BAR(); SCHED();
  }
  if (wr == 0) BAR();
  {
    auto rr = __builtin_amdgcn_permlane32_swap(__float_as_uint(lrun), __float_as_uint(lrun), false, false);
    const float inv = __builtin_amdgcn_rcpf(__uint_as_float(rr[0]) + __uint_as_float(rr[1]));
    const int tt = rb * 16 + wr * 8 + wc * 2 + (r32 >> 4);
    char* ow = obuf + ((size_t)(2 * hd * 128 + tt) * 2) * 1024 + (hh * 16 + (r32 & 15)) * 16;
#pragma unroll
    for (int d = 0; d < 4; ++d)
#pragma unroll
      for (int e = 0; e < 2; ++e) {
        u32x4 w = {pk2(o[d][8 * e] * inv, o[d][8 * e + 1] * inv), pk2(o[d][8 * e + 2] * inv, o[d][8 * e + 3] * inv),
                   pk2(o[d][8 * e + 4] * inv, o[d][8 * e + 5] * inv), pk2(o[d][8 * e + 6] * inv, o[d][8 * e + 7] * inv)};
        *(u32x4*)(ow + (d >> 1) * 262144 + (d & 1) * 1024 + e * 512) = w;
      }
  }
}

constexpr int K2_NST = 5, K2_LDS = K2_NST * 32768;
__global__ __launch_bounds__(512, 2) void k2_kernel(const char* __restrict__ obuf, const char* __restrict__ hbuf, const char* __restrict__ wo, const char* __restrict__ w2,
                                                    const char* __restrict__ x, char* gbuf, unsigned* flags, char* __restrict__ out) {
  extern __shared__ __attribute__((aligned(16))) char shm[];
  const lptr lds = (lptr)shm;
  const unsigned lds0 = (unsigned)(uintptr_t)lds;
  const int tid = threadIdx.x, wid = __builtin_amdgcn_readfirstlane(tid >> 6), lane = tid & 63, wr = wid >> 2, wm = (wid >> 1) & 1, wn = wid & 1;
  const int bid = blockIdx.x, xcd = bid & 7, idx = bid >> 3, isG = xcd >> 2, j = xcd & 3, tm = 4 * j + (idx & 3), tn = idx >> 2;
  const int g = lane >> 4, t = lane & 15, q4 = (lane >> 2) & 3, p4 = lane & 3;
  const int nt = isG ? 56 : 64, rot = (j * nt) >> 2;
  const char* ab = (isG ? hbuf : obuf) + tm * 16384 + wid * 2048;
  const char* wbp = (isG ? w2 : wo) + tn * 256;
  unsigned voffA = lane * 16, voffW;
  {
    const int r = lane >> 3, c = lane & 7, pc = wid & 3, st = wid >> 2;
    const int kp = isG ? pc : (((pc & 1) << 1) | (pc >> 1));
    voffW = (unsigned)((32 * st + 8 * kp + r) * 2048 + ((c ^ (((r >> 1) & 3) << 1)) << 4));
  }
#define K2_ISSUE(kt_, buf_) do { int k_ = (kt_) + rot; if (k_ >= nt) k_ -= nt; const unsigned d_ = lds0 + (buf_) * 32768; \
    glds(voffA, ab + (size_t)k_ * 262144, d_ + wid * 2048); glds(voffA, ab + (size_t)k_ * 262144 + 1024, d_ + wid * 2048 + 1024); \
    glds(voffW, wbp + (size_t)k_ * 131072, d_ + 16384 + wid * 1024); glds(voffW, wbp + (size_t)k_ * 131072 + 128, d_ + 16384 + 8192 + wid * 1024); } while (0)
  lptr wb[4];
#pragma unroll
  for (int ft = 0; ft < 4; ++ft) wb[ft] = lds + 16384 + wn * 8192 + wr * 4096 + (g >> 1) * 1024 + (4 * (g & 1) + q4) * 128 + 32 * (ft ^ (2 * (g & 1) + (q4 >> 1))) + 8 * p4;
  const lptr ar = lds + wm * 8192 + wr * 1024 + lane * 16;
  f32x4 acc[4][4];
#pragma unroll
  for (int m = 0; m < 4; ++m)
#pragma unroll
    for (int n = 0; n < 4; ++n) acc[m][n] = (f32x4){0.f, 0.f, 0.f, 0.f};
  bf16x8 Wf[2][4], Af[2][4];
#define K2_READ(s_, buf_) do { _Pragma("unroll") for (int i = 0; i < 4; ++i) { Af[s_][i] = ldb128(ar + (buf_) * 32768 + i * 2048); Wf[s_][i] = ldtr2(wb[i] + (buf_) * 32768, 2048); } } while (0)
#define K2_MMA(s_) do { _Pragma("unroll") for (int m = 0; m < 4; ++m) _Pragma("unroll") for (int n = 0; n < 4; ++n) acc[m][n] = MFMA16(Wf[s_][m], Af[s_][n], acc[m][n]); } while (0)
#pragma unroll
  for (int i = 0; i < K2_NST - 1; ++i) K2_ISSUE(i, i);
  WAIT_V(12); BAR();
  K2_READ(0, 0);
  int rbuf = 1, wbuf = K2_NST - 1;
  for (int kt = 0; kt < nt; kt += 2) {
#pragma unroll
    for (int hf = 0; hf < 2; ++hf) {
      WAIT_V(8); BAR(); SCHED();
      { int nx = kt + hf + K2_NST - 1; if (nx >= nt) nx = nt - 1; K2_ISSUE(nx, wbuf); }
      K2_READ(hf ^ 1, rbuf);
      K2_MMA(hf);
      SCHED();
      rbuf = (rbuf == K2_NST - 1) ? 0 : rbuf + 1; wbuf = (wbuf == K2_NST - 1) ? 0 : wbuf + 1;
    }
  }
  WAIT_V(0); WAIT_L(0); BAR(); SCHED();
  {
    const lptr cw = lds + wr * 65536 + (wm * 64 + t) * 512;
#pragma unroll
    for (int m = 0; m < 4; ++m)
#pragma unroll
      for (int n = 0; n < 4; ++n) *(LAS f32x4*)(cw + n * 8192 + (((wn * 16 + m * 4 + g) ^ (t & 7)) << 4)) = acc[m][n];
  }
  WAIT_L(0); BAR(); SCHED();
  unsigned* flag = flags + (tm * 8 + tn) * 32;
  float v[4][8];
  const int c8 = tid & 15, r0 = tid >> 4;
#pragma unroll
  for (int i = 0; i < 4; ++i) {
    const int row = r0 + 32 * i;
    const lptr p = lds + row * 512;
    const f32x4 a0 = *(const LAS f32x4*)(p + (((2 * c8) ^ (row & 7)) << 4)), a1 = *(const LAS f32x4*)(p + (((2 * c8 + 1) ^ (row & 7)) << 4));
    const f32x4 b0 = *(const LAS f32x4*)(p + 65536 + (((2 * c8) ^ (row & 7)) << 4)), b1 = *(const LAS f32x4*)(p + 65536 + (((2 * c8 + 1) ^ (row & 7)) << 4));
#pragma unroll
    for (int e = 0; e < 4; ++e) { v[i][e] = a0[e] + b0[e]; v[i][4 + e] = a1[e] + b1[e]; }
  }
  const unsigned goff = (unsigned)((tm * 128 + r0) * 2048 + tn * 256 + c8 * 16);
  const __amdgpu_buffer_rsrc_t grs = __builtin_amdgcn_make_buffer_rsrc(gbuf, 0, 2048 * 2048, 0x00020000);
  if (isG) {
#pragma unroll
    for (int i = 0; i < 4; ++i) {
      u32x4 w;
#pragma unroll
      for (int e = 0; e < 4; ++e) w[e] = pk2(silu(v[i][2 * e]), silu(v[i][2 * e + 1]));
      __builtin_amdgcn_raw_buffer_store_b128(w, grs, goff + i * 65536, 0, 16);
    }
    WAIT_V(0); BAR();
    if (tid == 0) __hip_atomic_store(flag, 1u, __ATOMIC_RELAXED, __HIP_MEMORY_SCOPE_AGENT);
  } else {
    u32x4 xv[4];
#pragma unroll
    for (int i = 0; i < 4; ++i) xv[i] = *(const u32x4*)(x + goff + i * 65536);
    if (wid == 0) {
      unsigned spins = 0;
      while (__builtin_amdgcn_readfirstlane(__hip_atomic_load(flag, __ATOMIC_RELAXED, __HIP_MEMORY_SCOPE_AGENT)) == 0u) {
        __builtin_amdgcn_s_sleep(2);
        if (++spins > 20000000u) break;
      }
    }
    BAR();
#pragma unroll
    for (int i = 0; i < 4; ++i) {
      const u32x4 gv = __builtin_amdgcn_raw_buffer_load_b128(grs, goff + i * 65536, 0, 16);
      u32x4 w;
#pragma unroll
      for (int e = 0; e < 4; ++e) {
        const float lo = bf2f(xv[i][e] & 0xffffu) + v[i][2 * e] * bf2f(gv[e] & 0xffffu);
        const float hi = __uint_as_float(xv[i][e] & 0xffff0000u) + v[i][2 * e + 1] * __uint_as_float(gv[e] & 0xffff0000u);
        w[e] = pk2(lo, hi);
      }
      *(u32x4*)(out + goff + i * 65536) = w;
    }
  }
}

extern "C" void kernel_launch(void* const* d_in, const int* in_sizes, int n_in, void* d_out, int out_size, void* d_ws, size_t ws_size, hipStream_t stream) {
  const char* x = (const char*)d_in[0]; const char* kc = (const char*)d_in[1]; const char* vc = (const char*)d_in[2];
  const char* wq = (const char*)d_in[3]; const char* wo = (const char*)d_in[4]; const char* w1 = (const char*)d_in[6]; const char* w2 = (const char*)d_in[7];
  char* ws = (char*)d_ws;
  unsigned* flags = (unsigned*)ws;
  char* xn = ws + (1 << 20);
  char* gbuf = xn + (4 << 20);
  char* obuf = gbuf + (4 << 20);
  char* hbuf = obuf + (16 << 20);
  (void)hipFuncSetAttribute((const void*)k1_kernel, hipFuncAttributeMaxDynamicSharedMemorySize, K1_LDS);
  (void)hipFuncSetAttribute((const void*)k2_kernel, hipFuncAttributeMaxDynamicSharedMemorySize, K2_LDS);
  ln_kernel<<<512, 256, 0, stream>>>((const u16*)x, (u16*)xn, flags);
  k1_kernel<<<256, 512, K1_LDS, stream>>>(xn, kc, vc, wq, w1, hbuf, obuf);
  k2_kernel<<<256, 512, K2_LDS, stream>>>(obuf, hbuf, wo, w2, x, gbuf, flags, (char*)d_out);
}
```

```cpp
#include <hip/hip_runtime.h>
#include <stdint.h>

typedef unsigned short u16;
typedef __bf16 bf16x2 __attribute__((ext_vector_type(2)));
typedef __bf16 bf16x4 __attribute__((ext_vector_type(4)));
typedef __bf16 bf16x8 __attribute__((ext_vector_type(8)));
typedef float f32x2 __attribute__((ext_vector_type(2)));
typedef float f32x4 __attribute__((ext_vector_type(4)));
typedef float f32x16 __attribute__((ext_vector_type(16)));
typedef unsigned u32x2 __attribute__((ext_vector_type(2)));
typedef unsigned u32x4 __attribute__((ext_vector_type(4)));
#define LAS __attribute__((address_space(3)))
typedef LAS char* lptr;
#if defined(__HIP_DEVICE_COMPILE__)
#define NOPK __attribute__((target("no-packed-fp32-ops")))
#else
#define NOPK
#endif
#define DI NOPK __device__ __forceinline__
#define F2U(x) __builtin_bit_cast(unsigned, (float)(x))
#define U2F(x) __builtin_bit_cast(float, (unsigned)(x))
#define TID ((int)__builtin_amdgcn_workitem_id_x())
#define BID ((int)__builtin_amdgcn_workgroup_id_x())
#define SCHED() __builtin_amdgcn_sched_barrier(0)
#define BAR() asm volatile("s_barrier" ::: "memory")
#define WAIT_V(n) asm volatile("s_waitcnt vmcnt(%0)" :: "n"(n) : "memory")
#define WAIT_L(n) do { asm volatile("" ::: "memory"); __builtin_amdgcn_s_waitcnt(0xC07F | ((n) << 8)); asm volatile("" ::: "memory"); } while (0)
#define MFMA16(a, b, c) __builtin_amdgcn_mfma_f32_16x16x32_bf16(a, b, c, 0, 0, 0)
#define MFMA32(a, b, c) __builtin_amdgcn_mfma_f32_32x32x16_bf16(a, b, c, 0, 0, 0)

DI void glds(unsigned voff, const char* sbase, unsigned ldsaddr) {
  asm volatile("s_mov_b32 m0, %2\n\ts_nop 0\n\tglobal_load_lds_dwordx4 %0, %1" ::"v"(voff), "s"(sbase), "s"(ldsaddr) : "memory");
}
DI unsigned pk2(float lo, float hi) { f32x2 v = {lo, hi}; return __builtin_bit_cast(unsigned, __builtin_convertvector(v, bf16x2)); }
DI float bf2f(unsigned b) { return U2F(b << 16); }
DI bf16x8 ldb128(lptr p) { return *(const LAS bf16x8*)p; }
DI bf16x8 ldtr2(lptr p, int d) {
  bf16x4 a = __builtin_amdgcn_ds_read_tr16_b64_v4bf16((LAS bf16x4*)p);
  bf16x4 b = __builtin_amdgcn_ds_read_tr16_b64_v4bf16((LAS bf16x4*)(p + d));
  return __builtin_shufflevector(a, b, 0, 1, 2, 3, 4, 5, 6, 7);
}
DI float silu(float v) { return v * __builtin_amdgcn_rcpf(1.0f + __builtin_amdgcn_exp2f(v * -1.4426950408889634f)); }

#define STAMP_DECL
#define STAMP(i)
#define STAMP_OUT(buf, n)
__global__ __launch_bounds__(256) void ln_kernel(const u16* __restrict__ x, u16* __restrict__ xn, unsigned* __restrict__ flags) {
  const int row = BID * 4 + (TID >> 6), lane = TID & 63;
  if (BID < 2) flags[(BID * 256 + TID) * 32] = 0u;
  const u32x4* p = (const u32x4*)(x + (size_t)row * 1024);
  u32x4 a = p[lane], b = p[64 + lane];
  float v[16];
#pragma unroll
  for (int i = 0; i < 4; ++i) { v[2 * i] = bf2f(a[i] & 0xffffu); v[2 * i + 1] = U2F(a[i] & 0xffff0000u); v[8 + 2 * i] = bf2f(b[i] & 0xffffu); v[9 + 2 * i] = U2F(b[i] & 0xffff0000u); }
  float s = 0.f;
#pragma unroll
  for (int i = 0; i < 16; ++i) s += v[i];
#pragma unroll
  for (int o = 32; o >= 1; o >>= 1) s += __shfl_xor(s, o);
  const float mean = s * (1.0f / 1024.0f);
  float q = 0.f;
#pragma unroll
  for (int i = 0; i < 16; ++i) { v[i] -= mean; q += v[i] * v[i]; }
#pragma unroll
  for (int o = 32; o >= 1; o >>= 1) q += __shfl_xor(q, o);
  const float r = rsqrtf(q * (1.0f / 1024.0f) + 1e-5f);
  u32x4 oa, ob;
#pragma unroll
  for (int i = 0; i < 4; ++i) { oa[i] = pk2(v[2 * i] * r, v[2 * i + 1] * r); ob[i] = pk2(v[8 + 2 * i] * r, v[9 + 2 * i] * r); }
  u32x4* po = (u32x4*)(xn + (size_t)row * 1024);
  po[lane] = oa; po[64 + lane] = ob;
}

constexpr int HTB = 16384;
#define SW(b, h) (((b) * 2 + (h)) * HTB)
#define SX(b, h) ((4 + (b) * 2 + (h)) * HTB)
constexpr int SLOT_A = 0, SLOT_B = 32768, SLOT_C = 65536, SLOT_D = 98304, SLOT_E = 131072, K1_LDS = 163840;

NOPK __global__ __launch_bounds__(512, 2) void k1_kernel(const char* __restrict__ xn, const char* __restrict__ kc, const char* __restrict__ vc,
                                                    const char* __restrict__ wq, const char* __restrict__ w1, char* __restrict__ hbuf, char* __restrict__ obuf,
                                                    const char* __restrict__ wo, const char* __restrict__ w2) {
  STAMP_DECL
  STAMP(0);
  extern __shared__ __attribute__((aligned(16))) char shm[];
  const lptr lds = (lptr)shm;
  const unsigned lds0 = (unsigned)(uintptr_t)lds;
  const int tid = TID, wid = __builtin_amdgcn_readfirstlane(tid >> 6), lane = tid & 63, wr = wid >> 2, wc = wid & 3;
  const int bid = BID, xcd = bid & 7, idx = bid >> 3, hd = 4 * xcd + (idx & 3), rb = idx >> 2;
  const int g = lane >> 4, t = lane & 15, q4 = (lane >> 2) & 3, p4 = lane & 3;

  unsigned voffX, voffQ, voffHA, voffHB;
  {
    const int r = lane >> 3, c = lane & 7;
    const int row = 8 * wid + r;
    voffX = (unsigned)(row * 2048 + ((c ^ ((row >> 1) & 7)) << 4));
    const int pc = wid & 3, st = wid >> 2;
    const int k = 32 * st + 16 * (pc & 1) + 4 * (pc >> 1) + 8 * (r >> 2) + (r & 3);
    const int cw = c ^ (((r >> 1) & 3) << 1);
    voffQ = (unsigned)(k * 8192 + cw * 16);
    voffHA = (unsigned)(k * 7168 + cw * 16);
    voffHB = (unsigned)(k * 7168 + (cw < 5 ? cw : 5) * 16);
  }
  const char* xb = xn + (size_t)rb * 256 * 2048;
  const char* qb = wq + hd * 256;
  const char* hB = w1 + 4096 + 96 * hd;
  const char* hA = w1 + 128 * hd;
  const unsigned ldw = lds0 + wid * 1024;
#define ST_X(b, h, kt) do { glds(voffX, xb + (h) * 262144 + (kt) * 128, ldw + SX(b, h)); glds(voffX, xb + (h) * 262144 + 131072 + (kt) * 128, ldw + SX(b, h) + 8192); } while (0)
#define ST_W0(b, kt) do { glds(voffQ, qb + (size_t)(kt) * 524288, ldw + SW(b, 0)); glds(voffQ, qb + (size_t)(kt) * 524288 + 128, ldw + SW(b, 0) + 8192); } while (0)
#define ST_W1(b, kt) do { glds(voffHB, hB + (size_t)(kt) * 458752, ldw + SW(b, 1)); glds(voffHA, hA + (size_t)(kt) * 458752, ldw + SW(b, 1) + 8192); } while (0)

  lptr wb[4], xr[2];
#pragma unroll
  for (int ft = 0; ft < 4; ++ft) wb[ft] = lds + wr * 8192 + (g >> 1) * 1024 + (4 * (g & 1) + q4) * 128 + 32 * (ft ^ (2 * (g & 1) + (q4 >> 1))) + 8 * p4;
#pragma unroll
  for (int ks = 0; ks < 2; ++ks) xr[ks] = lds + 65536 + (wc * 32 + t) * 128 + 16 * ((4 * ks + g) ^ (t >> 1));
#define LDW(dst, b, h) do { _Pragma("unroll") for (int m = 0; m < 4; ++m) _Pragma("unroll") for (int k = 0; k < 2; ++k) dst[m][k] = ldtr2(wb[m] + SW(b, h) + k * 4096, 2048); } while (0)
#define LDX(dst, b, h) do { _Pragma("unroll") for (int n = 0; n < 2; ++n) _Pragma("unroll") for (int k = 0; k < 2; ++k) dst[n][k] = ldb128(xr[k] + (SX(b, h) - 65536) + n * 2048); } while (0)
#define MMA(ai, bj, At, Bt) do { __builtin_amdgcn_s_setprio(1); \
    _Pragma("unroll") for (int m = 0; m < 3; ++m) _Pragma("unroll") for (int n = 0; n < 2; ++n) _Pragma("unroll") for (int k = 0; k < 2; ++k) \
      acc[ai][bj][m][n] = MFMA16(At[m][k], Bt[n][k], acc[ai][bj][m][n]); \
    if ((ai) == 0 || wr == 1) { _Pragma("unroll") for (int n = 0; n < 2; ++n) _Pragma("unroll") for (int k = 0; k < 2; ++k) \
      acc[ai][bj][3][n] = MFMA16(At[3][k], Bt[n][k], acc[ai][bj][3][n]); } \
    __builtin_amdgcn_s_setprio(0); } while (0)

  f32x4 acc[2][2][4][2];
#pragma unroll
  for (int a = 0; a < 2; ++a)
#pragma unroll
    for (int b = 0; b < 2; ++b)
#pragma unroll
      for (int m = 0; m < 4; ++m)
#pragma unroll
        for (int n = 0; n < 2; ++n) acc[a][b][m][n] = (f32x4){0.f, 0.f, 0.f, 0.f};
  bf16x8 At[4][2], B0[2][2], B1[2][2];

#define MMA2(ai) do { __builtin_amdgcn_s_setprio(1); \
    _Pragma("unroll") for (int k = 0; k < 2; ++k) _Pragma("unroll") for (int m = 0; m < 3; ++m) _Pragma("unroll") for (int n = 0; n < 2; ++n) { \
      acc[ai][0][m][n] = MFMA16(At[m][k], B0[n][k], acc[ai][0][m][n]); acc[ai][1][m][n] = MFMA16(At[m][k], B1[n][k], acc[ai][1][m][n]); } \
    if ((ai) == 0 || wr == 1) { _Pragma("unroll") for (int k = 0; k < 2; ++k) _Pragma("unroll") for (int n = 0; n < 2; ++n) { \
      acc[ai][0][3][n] = MFMA16(At[3][k], B0[n][k], acc[ai][0][3][n]); acc[ai][1][3][n] = MFMA16(At[3][k], B1[n][k], acc[ai][1][3][n]); } } \
    __builtin_amdgcn_s_setprio(0); } while (0)
  ST_X(0, 0, 0); ST_X(0, 1, 0); ST_W0(0, 0); ST_W1(0, 0);
  ST_X(1, 0, 1); ST_X(1, 1, 1); ST_W0(1, 1);
  WAIT_V(6); BAR();
  if (wr == 1) BAR();
  SCHED();
  STAMP(1);
#define PH_A(b, STAGE, NW) do { LDX(B0, b, 0); LDX(B1, b, 1); LDW(At, b, 0); STAGE; WAIT_V(NW); WAIT_L(0); BAR(); SCHED(); MMA2(0); BAR(); SCHED(); } while (0)
#define PH_B(b, STAGE, NW) do { LDW(At, b, 1); STAGE; WAIT_V(NW); WAIT_L(0); BAR(); SCHED(); MMA2(1); BAR(); SCHED(); } while (0)
  for (int kt = 0; kt < 14; kt += 2) {
    PH_A(0, ST_W1(1, kt + 1), 8);
    PH_B(0, ST_X(0, 0, kt + 2); ST_X(0, 1, kt + 2); ST_W0(0, kt + 2), 8);
    PH_A(1, ST_W1(0, kt + 2), 8);
    PH_B(1, ST_X(1, 0, kt + 3); ST_X(1, 1, kt + 3); ST_W0(1, kt + 3), 8);
  }
  STAMP(2);
  const int kvg = hd >> 3;
  const char* kb_g = kc + ((size_t)kvg * 2048 + 1536) * 256;
  const char* vb_g = vc + ((size_t)kvg * 2048 + 1536) * 256;
  unsigned voffK, voffV;
  {
    const int sub = lane >> 5, r = (lane >> 2) & 7, x = lane & 3;
    voffK = (unsigned)(r * 256 + (4 * sub + (x ^ ((2 * (wid & 1) + (r >> 2)) & 3))) * 16);
    voffV = (unsigned)(r * 256 + sub * 64 + x * 16);
  }
#define ST_KV8(voff, gsrc, slot) do { glds(voff, (gsrc) + wid * 2048, lds0 + (slot) + wid * 2048); glds(voff, (gsrc) + wid * 2048 + 128, lds0 + (slot) + wid * 2048 + 1024); } while (0)
  PH_A(0, ST_W1(1, 15), 8);
  PH_B(0, ST_KV8(voffK, kb_g, SLOT_E); ST_KV8(voffK, kb_g + 16384, SLOT_E + 16384); ST_KV8(voffV, vb_g, SLOT_C), 8);
  PH_A(1, ST_KV8(voffV, vb_g + 16384, SLOT_C + 16384), 8);
  PH_B(1, ST_KV8(voffK, kb_g + 32768, SLOT_A); ST_KV8(voffK, kb_g + 49152, SLOT_A + 16384), 8);
  STAMP(3);
  {
    const float qs = 0.08838834764831845f * 1.4426950408889634f;
    const lptr qw = lds + wc * 8192 + wr * 4096 + ((g >> 1) * 32 + t) * 16 + (g & 1) * 8;
#pragma unroll
    for (int bj = 0; bj < 2; ++bj) {
      lptr qwb = qw + (bj ? SLOT_D : SLOT_B);
      asm volatile("" : "+v"(qwb));
#pragma unroll
      for (int m = 0; m < 4; ++m)
#pragma unroll
        for (int n = 0; n < 2; ++n) {
          const f32x4 v = acc[0][bj][m][n] * qs;
          u32x2 w = {pk2(v[0], v[1]), pk2(v[2], v[3])};
          *(LAS u32x2*)(qwb + m * 1024 + n * 256) = w;
        }
    }
  }
  WAIT_V(4); WAIT_L(0); BAR(); SCHED();
  STAMP(4);
  bf16x8 qf[8];
  const lptr qr = lds + (wr ? SLOT_D : SLOT_B) + wc * 8192 + lane * 16;
  if (wr == 1) {
#pragma unroll
    for (int s = 0; s < 8; ++s) qf[s] = ldb128(qr + s * 1024);
  }
  {
    u32x2 hw[2][2][4];
#define SILU16(a0, a1, a2, a3, w0, w1, w2, w3) do { \
      f32x4 e0 = a0 * -1.4426950408889634f, e1 = a1 * -1.4426950408889634f, e2 = a2 * -1.4426950408889634f, e3 = a3 * -1.4426950408889634f; SCHED(); \
      _Pragma("unroll") for (int i = 0; i < 4; ++i) { e0[i] = __builtin_amdgcn_exp2f(e0[i]); e1[i] = __builtin_amdgcn_exp2f(e1[i]); e2[i] = __builtin_amdgcn_exp2f(e2[i]); e3[i] = __builtin_amdgcn_exp2f(e3[i]); } SCHED(); \
      e0 += 1.0f; e1 += 1.0f; e2 += 1.0f; e3 += 1.0f; SCHED(); \
      _Pragma("unroll") for (int i = 0; i < 4; ++i) { e0[i] = __builtin_amdgcn_rcpf(e0[i]); e1[i] = __builtin_amdgcn_rcpf(e1[i]); e2[i] = __builtin_amdgcn_rcpf(e2[i]); e3[i] = __builtin_amdgcn_rcpf(e3[i]); } SCHED(); \
      e0 *= a0; e1 *= a1; e2 *= a2; e3 *= a3; SCHED(); \
      w0 = (u32x2){pk2(e0[0], e0[1]), pk2(e0[2], e0[3])}; w1 = (u32x2){pk2(e1[0], e1[1]), pk2(e1[2], e1[3])}; \
      w2 = (u32x2){pk2(e2[0], e2[1]), pk2(e2[2], e2[3])}; w3 = (u32x2){pk2(e3[0], e3[1]), pk2(e3[2], e3[3])}; SCHED(); } while (0)
#pragma unroll
    for (int m = 0; m < 3; ++m) SILU16(acc[1][0][m][0], acc[1][0][m][1], acc[1][1][m][0], acc[1][1][m][1], hw[0][0][m], hw[0][1][m], hw[1][0][m], hw[1][1][m]);
    const unsigned hoff = (unsigned)((rb * 16 + wc * 2) * 2048 + lane * 16);
    if (wr == 1) {
      SILU16(acc[1][0][3][0], acc[1][0][3][1], acc[1][1][3][0], acc[1][1][3][1], hw[0][0][3], hw[0][1][3], hw[1][0][3], hw[1][1][3]);
      char* d = hbuf + (size_t)hd * 262144;
#pragma unroll
      for (int bj = 0; bj < 2; ++bj)
#pragma unroll
        for (int n = 0; n < 2; ++n) {
          *(u32x4*)(d + bj * 16384 + (hoff + n * 2048)) = (u32x4){hw[bj][n][0][0], hw[bj][n][0][1], hw[bj][n][1][0], hw[bj][n][1][1]};
          *(u32x4*)(d + bj * 16384 + (hoff + n * 2048 + 1024)) = (u32x4){hw[bj][n][2][0], hw[bj][n][2][1], hw[bj][n][3][0], hw[bj][n][3][1]};
        }
    } else {
      const int p0 = (128 + 3 * hd) >> 1, p1 = p0 + 1;
      char* d0 = hbuf + (size_t)(p0 >> 1) * 262144 + (p0 & 1) * 1024;
      char* d1 = hbuf + (size_t)(p1 >> 1) * 262144 + (p1 & 1) * 1024;
      if ((hd & 1) == 0) {
#pragma unroll
        for (int bj = 0; bj < 2; ++bj)
#pragma unroll
          for (int n = 0; n < 2; ++n) {
            *(u32x4*)(d0 + bj * 16384 + (hoff + n * 2048)) = (u32x4){hw[bj][n][0][0], hw[bj][n][0][1], hw[bj][n][1][0], hw[bj][n][1][1]};
            *(u32x2*)(d1 + bj * 16384 + (hoff + n * 2048)) = hw[bj][n][2];
          }
      } else {
#pragma unroll
        for (int bj = 0; bj < 2; ++bj)
#pragma unroll
          for (int n = 0; n < 2; ++n) {
            *(u32x2*)(d0 + bj * 16384 + (hoff + n * 2048 + 8)) = hw[bj][n][0];
            *(u32x4*)(d1 + bj * 16384 + (hoff + n * 2048)) = (u32x4){hw[bj][n][1][0], hw[bj][n][1][1], hw[bj][n][2][0], hw[bj][n][2][1]};
          }
      }
    }
  }
  STAMP(5);
  WAIT_L(0); BAR(); SCHED();
  STAMP(6);
  if (wr == 0) {
#pragma unroll
    for (int s = 0; s < 8; ++s) qf[s] = ldb128(qr + s * 1024);
  }
  const int r32 = lane & 31, hh = lane >> 5;
  lptr kr[2];
#pragma unroll
  for (int e = 0; e < 2; ++e) kr[e] = lds + 2048 * (r32 >> 3) + 64 * (r32 & 7) + 16 * ((2 * e + hh) ^ ((r32 >> 2) & 3));
  const lptr vr = lds + ((lane & 3) << 3) + (((lane >> 2) & 3) << 6) + (((lane >> 4) & 1) << 5) + (hh << 8);
  f32x16 o[4], s[4];
#pragma unroll
  for (int d = 0; d < 4; ++d)
#pragma unroll
    for (int i = 0; i < 16; ++i) o[d][i] = 0.f;
  bf16x8 pf[8];
  constexpr int RD = 6;
  bf16x8 fr[RD];
  float lrun = 0.f;
  f32x16 negm = {0.f, 0.f, 0.f, 0.f, 0.f, 0.f, 0.f, 0.f, 0.f, 0.f, 0.f, 0.f, 0.f, 0.f, 0.f, 0.f};
#define SM_THR 32.0f
#define FRAG(i) ((i) < 32 ? ldtr2(v0 + (((i) >> 2) * 8 + ((i) & 3)) * 512, 2048) : ldb128((((((i) - 32) >> 2) & 1) ? k1 : k0) + ((i) & 3) * 8192 + (((i) - 32) >> 3) * 512))
#define XPRIO 1
#define YPRIO 0
#define XSEG(lo, hi) do { __builtin_amdgcn_s_setprio(XPRIO); \
    _Pragma("unroll") for (int i = (lo); i < (hi); ++i) { \
      if (i < 32) o[i & 3] = MFMA32(fr[i % RD], pf[(i >> 2) & 7], o[i & 3]); \
      else s[i & 3] = MFMA32(fr[i % RD], qf[((i - 32) >> 2) & 7], i < 36 ? negm : s[i & 3]); \
      if (i + RD < (hi)) fr[i % RD] = FRAG(i + RD); \
      SCHED(); } \
    __builtin_amdgcn_s_setprio(YPRIO); } while (0)
#define PREF(lo) do { _Pragma("unroll") for (int i = (lo); i < (lo) + RD; ++i) fr[i % RD] = FRAG(i); SCHED(); } while (0)
#define SOFTMAX(first) do { \
      float m0 = __builtin_fmaxf(s[0][0], s[0][1]), m1 = __builtin_fmaxf(s[1][0], s[1][1]), m2 = __builtin_fmaxf(s[2][0], s[2][1]), m3 = __builtin_fmaxf(s[3][0], s[3][1]); \
      _Pragma("unroll") for (int i = 2; i < 16; i += 2) { m0 = __builtin_fmaxf(__builtin_fmaxf(m0, s[0][i]), s[0][i + 1]); m1 = __builtin_fmaxf(__builtin_fmaxf(m1, s[1][i]), s[1][i + 1]); \
                                                          m2 = __builtin_fmaxf(__builtin_fmaxf(m2, s[2][i]), s[2][i + 1]); m3 = __builtin_fmaxf(__builtin_fmaxf(m3, s[3][i]), s[3][i + 1]); } \
      float mx = __builtin_fmaxf(__builtin_fmaxf(m0, m1), __builtin_fmaxf(m2, m3)); \
        \
      if ((first) || __builtin_amdgcn_ballot_w64(!(mx <= SM_THR)) != 0ull) { \
        auto rr = __builtin_amdgcn_permlane32_swap(F2U(mx), F2U(mx), false, false); \
        mx = __builtin_fmaxf(U2F(rr[0]), U2F(rr[1])); \
        const float dl = (first) ? mx : __builtin_fmaxf(mx, 0.f); \
        _Pragma("unroll") for (int kb = 0; kb < 4; ++kb) s[kb] -= dl; \
        negm -= dl; \
        if (!(first)) { const float alpha = __builtin_amdgcn_exp2f(-dl); lrun *= alpha; _Pragma("unroll") for (int d = 0; d < 4; ++d) o[d] *= alpha; } \
      } \
      SCHED(); \
      float l0 = lrun, l1 = 0.f; \
      _Pragma("unroll") for (int kb = 0; kb < 4; ++kb) { \
        _Pragma("unroll") for (int e = 0; e < 2; ++e) { \
          _Pragma("unroll") for (int i = 0; i < 8; ++i) s[kb][8 * e + i] = __builtin_amdgcn_exp2f(s[kb][8 * e + i]); \
          _Pragma("unroll") for (int i = 0; i < 8; i += 2) { l0 += s[kb][8 * e + i]; l1 += s[kb][8 * e + i + 1]; } \
          u32x4 w = {pk2(s[kb][8 * e], s[kb][8 * e + 1]), pk2(s[kb][8 * e + 2], s[kb][8 * e + 3]), pk2(s[kb][8 * e + 4], s[kb][8 * e + 5]), pk2(s[kb][8 * e + 6], s[kb][8 * e + 7])}; \
          pf[2 * kb + e] = __builtin_bit_cast(bf16x8, w); } \
        asm volatile("" : "+v"(pf[2 * kb]), "+v"(pf[2 * kb + 1]), "+v"(l0), "+v"(l1)); \
        SCHED(); } \
      lrun = l0 + l1; \
      asm volatile("" : "+v"(lrun)); } while (0)
  {
    const lptr k0 = kr[0] + SLOT_E, k1 = kr[1] + SLOT_E, v0 = vr;
    PREF(32); XSEG(32, 64);
  }
  STAMP(7);
  const char* kvsrc = wr ? kb_g + 65536 : vb_g + 32768;
  const unsigned kvoff = wr ? voffK : voffV;
#pragma unroll 1
  for (int c = 0; c < 3; ++c) {
    WAIT_V(0); WAIT_L(0); BAR(); SCHED();
    if (c == 0) STAMP(8); else if (c == 1) STAMP(12);
    if (c + wr < 3) {
      const int slot = wr ? (c == 0 ? SLOT_B : SLOT_C) : (c == 0 ? SLOT_D : (c == 1 ? SLOT_E : SLOT_A));
#pragma unroll
      for (int i = 0; i < 4; ++i) {
        glds(kvoff, kvsrc + c * 32768 + (wc + 4 * i) * 2048, lds0 + slot + (wc + 4 * i) * 2048);
        glds(kvoff, kvsrc + c * 32768 + (wc + 4 * i) * 2048 + 128, lds0 + slot + (wc + 4 * i) * 2048 + 1024);
      }
    }
    SOFTMAX(c == 0);
    const int kslot = (c == 0) ? SLOT_A : ((c == 1) ? SLOT_B : SLOT_C);
    const int vslot = (c == 0) ? SLOT_C : ((c == 1) ? SLOT_D : SLOT_E);
    const lptr k0 = kr[0] + kslot, k1 = kr[1] + kslot, v0 = vr + vslot;
    PREF(0);
    if (c == 0) STAMP(9); else if (c == 1) STAMP(13);
    BAR(); SCHED();
    if (c == 0) STAMP(10); else if (c == 1) STAMP(14);
    XSEG(0, 64);
    if (c == 0) STAMP(11); else if (c == 2) STAMP(15);
  }
  WAIT_V(0); WAIT_L(0); BAR(); SCHED();
  STAMP(16);
  unsigned pft;
  { const int li = (BID * 8 + wid) * 60 + (lane < 60 ? lane : 59); pft = *(const unsigned*)(li < 65536 ? wo + (size_t)li * 128 : w2 + (size_t)(li - 65536) * 128); }
  SOFTMAX(false);
  {
    const lptr k0 = kr[0], k1 = kr[1], v0 = vr + SLOT_A;
    PREF(0);
    STAMP(17);
    BAR(); SCHED();
    STAMP(18);
    XSEG(0, 32);
  }
  STAMP(19);
  if (wr == 0) BAR();
  {
    auto rr = __builtin_amdgcn_permlane32_swap(F2U(lrun), F2U(lrun), false, false);
    const float inv = __builtin_amdgcn_rcpf(U2F(rr[0]) + U2F(rr[1]));
    const int tt = rb * 16 + wr * 8 + wc * 2 + (r32 >> 4);
    char* ow = obuf + ((size_t)(2 * hd * 128 + tt) * 2) * 1024 + (hh * 16 + (r32 & 15)) * 16;
#pragma unroll
    for (int d = 0; d < 4; ++d)
#pragma unroll
      for (int e = 0; e < 2; ++e) {
        u32x4 w = {pk2(o[d][8 * e] * inv, o[d][8 * e + 1] * inv), pk2(o[d][8 * e + 2] * inv, o[d][8 * e + 3] * inv),
                   pk2(o[d][8 * e + 4] * inv, o[d][8 * e + 5] * inv), pk2(o[d][8 * e + 6] * inv, o[d][8 * e + 7] * inv)};
        *(u32x4*)(ow + (d >> 1) * 262144 + (d & 1) * 1024 + e * 512) = w;
      }
  }
  asm volatile("" :: "v"(pft));
  STAMP(20);
  WAIT_V(0);
  STAMP(21);
  STAMP_OUT(hbuf + (32 << 20), 22);
}

constexpr int K2_STG = 32768, K2_LDS = 5 * K2_STG;
NOPK __global__ __launch_bounds__(512, 2) void k2_kernel(const char* __restrict__ obuf, const char* __restrict__ hbuf, const char* __restrict__ wo, const char* __restrict__ w2,
                                                         const char* __restrict__ x, char* gbuf, unsigned* flags, char* __restrict__ out) {
  STAMP_DECL
  STAMP(0);
  extern __shared__ __attribute__((aligned(16))) char shm[];
  const lptr lds = (lptr)shm;
  const unsigned lds0 = (unsigned)(uintptr_t)lds;
  const int tid = TID, wid = __builtin_amdgcn_readfirstlane(tid >> 6), lane = tid & 63;
  const int bid = BID, xcd = bid & 7, idx = bid >> 3, isG = xcd >> 2, j = xcd & 3, tm = 4 * j + (idx & 3), tn = idx >> 2;
  const int nt = isG ? 56 : 64;
  if (wid >= 4) {
    const int lw = wid & 3, rot = (j * nt) >> 2;
    const char* ab = (isG ? hbuf : obuf) + tm * 16384 + lw * 4096;
    const char* wbp = (isG ? w2 : wo) + tn * 256;
    const unsigned voffA = lane * 16;
    unsigned voffW;
    {
      const int r = lane >> 3, c = lane & 7;
      const int kp = isG ? lw : (((lw & 1) << 1) | (lw >> 1));
      voffW = (unsigned)((8 * kp + r) * 2048 + ((c ^ (((r >> 1) & 3) << 1)) << 4));
    }
#define K2_ISSUE(kt_, buf_) do { int k_ = (kt_) + rot; if (k_ >= nt) k_ -= nt; const unsigned d_ = lds0 + (buf_) * K2_STG; \
      _Pragma("unroll") for (int i = 0; i < 4; ++i) { \
        glds(voffA, ab + (size_t)k_ * 262144 + i * 1024, d_ + lw * 4096 + i * 1024); \
        glds(voffW, wbp + (size_t)k_ * 131072 + (i >> 1) * 65536 + (i & 1) * 128, d_ + 16384 + (i & 1) * 8192 + (i >> 1) * 4096 + lw * 1024); } } while (0)
    K2_ISSUE(0, 0); K2_ISSUE(1, 1);
    WAIT_V(8); BAR();
    STAMP(1);
    K2_ISSUE(2, 2);
    WAIT_V(8); BAR();
    K2_ISSUE(3, 3); K2_ISSUE(4, 4);
    int buf = 0;
#pragma unroll 1
    for (int kt = 1; kt < nt - 4; ++kt) {
      WAIT_V(16); BAR();
      K2_ISSUE(kt + 4, buf);
      buf = (buf == 4) ? 0 : buf + 1;
    }
    WAIT_V(16); BAR();
    WAIT_V(8); BAR();
    WAIT_V(0); BAR();
    const int c8 = lane & 15, r0 = lw * 4 + (lane >> 4);
    const unsigned goff = (unsigned)((tm * 128 + r0) * 2048 + tn * 256 + c8 * 16);
    const __amdgpu_buffer_rsrc_t grs = __builtin_amdgcn_make_buffer_rsrc(gbuf, 0, 2048 * 2048, 0x00020000);
    unsigned* const flag = flags + (tm * 8 + tn) * 32;
    u32x4 xv[8], gv[8];
    if (!isG) {
#pragma unroll
      for (int i = 0; i < 8; ++i) xv[i] = *(const u32x4*)(x + goff + i * 32768);
    }
    BAR();
    if (!isG) {
      unsigned spins = 0;
      while (__builtin_amdgcn_readfirstlane(__hip_atomic_load(flag, __ATOMIC_RELAXED, __HIP_MEMORY_SCOPE_AGENT)) == 0u) {
        __builtin_amdgcn_s_sleep(1);
        if (++spins > 20000000u) break;
      }
      asm volatile("" ::: "memory");
#pragma unroll
      for (int i = 0; i < 8; ++i) gv[i] = __builtin_amdgcn_raw_buffer_load_b128(grs, goff + i * 32768, 0, 16);
    }
    STAMP(2);
    BAR();
    BAR();
    STAMP(3);
#pragma unroll
    for (int i = 0; i < 8; ++i) {
      const int row = r0 + 16 * i;
      const lptr p = lds + row * 512;
      const f32x4 a0 = *(const LAS f32x4*)(p + (((2 * c8) ^ (row & 7)) << 4)), a1 = *(const LAS f32x4*)(p + (((2 * c8 + 1) ^ (row & 7)) << 4));
      u32x4 w;
      if (isG) {
        w = (u32x4){pk2(silu(a0[0]), silu(a0[1])), pk2(silu(a0[2]), silu(a0[3])), pk2(silu(a1[0]), silu(a1[1])), pk2(silu(a1[2]), silu(a1[3]))};
        __builtin_amdgcn_raw_buffer_store_b128(w, grs, goff + i * 32768, 0, 16);
      } else {
#pragma unroll
        for (int e = 0; e < 4; ++e) {
          const float s0 = e < 2 ? a0[2 * e] : a1[2 * e - 4], s1 = e < 2 ? a0[2 * e + 1] : a1[2 * e - 3];
          w[e] = pk2(bf2f(xv[i][e] & 0xffffu) + s0 * bf2f(gv[i][e] & 0xffffu), U2F(xv[i][e] & 0xffff0000u) + s1 * U2F(gv[i][e] & 0xffff0000u));
        }
        *(u32x4*)(out + goff + i * 32768) = w;
      }
    }
    STAMP(4);
    if (isG) {
      WAIT_V(0); BAR();
      if (tid == 256) __hip_atomic_store(flag, 1u, __ATOMIC_RELAXED, __HIP_MEMORY_SCOPE_AGENT);
    }
  } else {
    const int wm = wid >> 1, wn = wid & 1, g = lane >> 4, t = lane & 15, q4 = (lane >> 2) & 3, p4 = lane & 3;
    lptr wb[4];
#pragma unroll
    for (int ft = 0; ft < 4; ++ft) wb[ft] = lds + 16384 + wn * 8192 + (g >> 1) * 1024 + (4 * (g & 1) + q4) * 128 + 32 * (ft ^ (2 * (g & 1) + (q4 >> 1))) + 8 * p4;
    lptr ar = lds + wm * 8192 + lane * 16;
    f32x4 acc[4][4];
#pragma unroll
    for (int m = 0; m < 4; ++m)
#pragma unroll
      for (int n = 0; n < 4; ++n) acc[m][n] = (f32x4){0.f, 0.f, 0.f, 0.f};
    bf16x8 Af[2][4];
    int dlt = 0;
    bf16x4 Wh[2][4][2];
#define K2_RD(set, ks, i) do { if ((i) == 0) Af[set][0] = ldb128(ar + (ks) * 1024); else if ((i) >= 3 && (i) < 6) Af[set][(i) - 2] = ldb128(ar + ((i) - 2) * 2048 + (ks) * 1024); \
      else { const int f_ = (i) < 3 ? 0 : ((i) - 4) >> 1, h_ = (i) < 3 ? (i) - 1 : (i) & 1; \
             Wh[set][f_][h_] = __builtin_amdgcn_ds_read_tr16_b64_v4bf16((LAS bf16x4*)(wb[f_] + (ks) * 4096 + h_ * 2048)); } } while (0)
#define K2_W(set, m) __builtin_shufflevector(Wh[set][m][0], Wh[set][m][1], 0, 1, 2, 3, 4, 5, 6, 7)
    BAR();
    STAMP(1);
#pragma unroll
    for (int i = 0; i < 12; ++i) { K2_RD(0, 0, i); SCHED(); }
    int buf = 0;
#pragma unroll 1
    for (int kt = 0; kt < nt; ++kt) {
      SCHED();
#pragma unroll
      for (int i = 0; i < 16; ++i) {
        acc[i >> 2][i & 3] = MFMA16(K2_W(0, i >> 2), Af[0][i & 3], acc[i >> 2][i & 3]);
        if (i < 12) K2_RD(1, 1, i);
        if (i == 12) { dlt = (buf == 4) ? -4 * K2_STG : K2_STG; buf = (buf == 4) ? 0 : buf + 1; ar += dlt; asm volatile("" : "+v"(ar)); }
        if (i == 13) { wb[0] += dlt; wb[1] += dlt; asm volatile("" : "+v"(wb[0]), "+v"(wb[1])); }
        if (i == 14) { wb[2] += dlt; wb[3] += dlt; asm volatile("" : "+v"(wb[2]), "+v"(wb[3])); }
        if (i == 15) BAR();
        SCHED();
      }
#pragma unroll
      for (int i = 0; i < 16; ++i) {
        acc[i >> 2][i & 3] = MFMA16(K2_W(1, i >> 2), Af[1][i & 3], acc[i >> 2][i & 3]);
        if (i < 12) K2_RD(0, 0, i);
        SCHED();
      }
    }
    WAIT_L(0); BAR();
    STAMP(2);
    const lptr cw = lds + (wm * 64 + t) * 512;
#pragma unroll
    for (int m = 0; m < 4; ++m)
#pragma unroll
      for (int n = 0; n < 4; ++n) *(LAS f32x4*)(cw + n * 8192 + (((wn * 16 + m * 4 + g) ^ (t & 7)) << 4)) = acc[m][n];
    WAIT_L(0); BAR();
    STAMP(3);
    STAMP(4);
    if (isG) BAR();
  }
  STAMP(5);
  WAIT_V(0);
  STAMP(6);
  STAMP_OUT((char*)hbuf + (33 << 20), 7);
}

extern "C" void kernel_launch(void* const* d_in, const int* in_sizes, int n_in, void* d_out, int out_size, void* d_ws, size_t ws_size, hipStream_t stream) {
  const char* x = (const char*)d_in[0]; const char* kc = (const char*)d_in[1]; const char* vc = (const char*)d_in[2];
  const char* wq = (const char*)d_in[3]; const char* wo = (const char*)d_in[4]; const char* w1 = (const char*)d_in[6]; const char* w2 = (const char*)d_in[7];
  char* ws = (char*)d_ws;
  unsigned* flags = (unsigned*)ws;
  char* xn = ws + (1 << 20);
  char* gbuf = xn + (4 << 20);
  char* obuf = gbuf + (4 << 20);
  char* hbuf = obuf + (16 << 20);
  (void)hipFuncSetAttribute((const void*)k1_kernel, hipFuncAttributeMaxDynamicSharedMemorySize, K1_LDS);
  (void)hipFuncSetAttribute((const void*)k2_kernel, hipFuncAttributeMaxDynamicSharedMemorySize, K2_LDS);
  ln_kernel<<<512, 256, 0, stream>>>((const u16*)x, (u16*)xn, flags);
  k1_kernel<<<256, 512, K1_LDS, stream>>>(xn, kc, vc, wq, w1, hbuf, obuf, wo, w2);
  k2_kernel<<<256, 512, K2_LDS, stream>>>(obuf, hbuf, wo, w2, x, gbuf, flags, (char*)d_out);
}
```

```cpp
#include <hip/hip_runtime.h>
#include <stdint.h>

typedef unsigned short u16;
typedef __bf16 bf16x2 __attribute__((ext_vector_type(2)));
typedef __bf16 bf16x4 __attribute__((ext_vector_type(4)));
typedef __bf16 bf16x8 __attribute__((ext_vector_type(8)));
typedef float f32x2 __attribute__((ext_vector_type(2)));
typedef float f32x4 __attribute__((ext_vector_type(4)));
typedef float f32x16 __attribute__((ext_vector_type(16)));
typedef unsigned u32x2 __attribute__((ext_vector_type(2)));
typedef unsigned u32x4 __attribute__((ext_vector_type(4)));
#define LAS __attribute__((address_space(3)))
typedef LAS char* lptr;
#if defined(__HIP_DEVICE_COMPILE__)
#define NOPK __attribute__((target("no-packed-fp32-ops")))
#else
#define NOPK
#endif
#define DI NOPK __device__ __forceinline__
#define F2U(x) __builtin_bit_cast(unsigned, (float)(x))
#define U2F(x) __builtin_bit_cast(float, (unsigned)(x))
#define TID ((int)__builtin_amdgcn_workitem_id_x())
#define BID ((int)__builtin_amdgcn_workgroup_id_x())
#define SCHED() __builtin_amdgcn_sched_barrier(0)
#define BAR() asm volatile("s_barrier" ::: "memory")
#define WAIT_V(n) asm volatile("s_waitcnt vmcnt(%0)" :: "n"(n) : "memory")
#define WAIT_L(n) do { asm volatile("" ::: "memory"); __builtin_amdgcn_s_waitcnt(0xC07F | ((n) << 8)); asm volatile("" ::: "memory"); } while (0)
#define MFMA16(a, b, c) __builtin_amdgcn_mfma_f32_16x16x32_bf16(a, b, c, 0, 0, 0)
#define MFMA32(a, b, c) __builtin_amdgcn_mfma_f32_32x32x16_bf16(a, b, c, 0, 0, 0)

DI void glds(unsigned voff, const char* sbase, unsigned ldsaddr) {
  asm volatile("s_mov_b32 m0, %2\n\ts_nop 0\n\tglobal_load_lds_dwordx4 %0, %1" ::"v"(voff), "s"(sbase), "s"(ldsaddr) : "memory");
}
DI unsigned pk2(float lo, float hi) { f32x2 v = {lo, hi}; return __builtin_bit_cast(unsigned, __builtin_convertvector(v, bf16x2)); }
DI float bf2f(unsigned b) { return U2F(b << 16); }
DI bf16x8 ldb128(lptr p) { return *(const LAS bf16x8*)p; }
DI bf16x8 ldtr2(lptr p, int d) {
  bf16x4 a = __builtin_amdgcn_ds_read_tr16_b64_v4bf16((LAS bf16x4*)p);
  bf16x4 b = __builtin_amdgcn_ds_read_tr16_b64_v4bf16((LAS bf16x4*)(p + d));
  return __builtin_shufflevector(a, b, 0, 1, 2, 3, 4, 5, 6, 7);
}
DI float silu(float v) { return v * __builtin_amdgcn_rcpf(1.0f + __builtin_amdgcn_exp2f(v * -1.4426950408889634f)); }

#define STAMP_DECL
#define LSTAMP(i)
#define STAMP(i)
#define STAMP_OUT(buf, n)
template <int CTRL> DI float dpp_add(float v) { return v + U2F(__builtin_amdgcn_update_dpp(0u, F2U(v), CTRL, 0xf, 0xf, true)); }
DI float half_sum(float v) {
  v = dpp_add<0xB1>(v); v = dpp_add<0x4E>(v); v = dpp_add<0x141>(v); v = dpp_add<0x140>(v);
  auto rr = __builtin_amdgcn_permlane16_swap(F2U(v), F2U(v), false, false);
  return U2F(rr[0]) + U2F(rr[1]);
}
typedef __attribute__((address_space(1))) unsigned long long gu64;

#define SX(b, h) (98304 + (b) * 32768 + (h) * 16384)
constexpr int SLOT_B = 0, SLOT_E = 32768, SLOT_A = 65536, SLOT_C = 98304, SLOT_D = 131072, K1_LDS = 163840;

NOPK __global__ __launch_bounds__(512, 2) void k1_kernel(const char* __restrict__ xn, const char* __restrict__ kc, const char* __restrict__ vc,
                                                    const char* __restrict__ wq, const char* __restrict__ w1, char* __restrict__ hbuf, char* __restrict__ obuf,
                                                    const char* __restrict__ wo, const char* __restrict__ w2, unsigned* ctl) {
  STAMP_DECL
  STAMP(0);
  extern __shared__ __attribute__((aligned(16))) char shm[];
  const lptr lds = (lptr)shm;
  const unsigned lds0 = (unsigned)(uintptr_t)lds;
  const int tid = TID, wid = __builtin_amdgcn_readfirstlane(tid >> 6), lane = tid & 63, wr = wid >> 2, wc = wid & 3;
  const int bid = BID, xcd = bid & 7, idx = bid >> 3, hd = 4 * xcd + (idx & 3), rb = idx >> 2;
  const int g = lane >> 4, t = lane & 15, q4 = (lane >> 2) & 3, p4 = lane & 3;

  unsigned voffX, voffQ, voffHA, voffHB;
  {
    const int r = lane >> 3, c = lane & 7;
    const int row = 8 * wid + r;
    voffX = (unsigned)(row * 2048 + ((c ^ ((row >> 1) & 7)) << 4));
    const int pc = wid & 3, st = wid >> 2;
    const int k = 32 * st + 16 * (pc & 1) + 4 * (pc >> 1) + 8 * (r >> 2) + (r & 3);
    const int cw = c ^ (((r >> 1) & 3) << 1);
    voffQ = (unsigned)(k * 8192 + cw * 16);
    voffHA = (unsigned)(k * 7168 + cw * 16);
    voffHB = (unsigned)(k * 7168 + (cw < 5 ? cw : 5) * 16);
  }
  const char* xb = xn + (size_t)rb * 256 * 2048;
  const char* qb = wq + hd * 256;
  const char* hB = w1 + 4096 + 96 * hd;
  const char* hA = w1 + 128 * hd;
  const unsigned ldw = lds0 + wid * 1024;
#define ST_X(b, h, kt) do { glds(voffX, xb + (h) * 262144 + (kt) * 128, ldw + SX(b, h)); glds(voffX, xb + (h) * 262144 + 131072 + (kt) * 128, ldw + SX(b, h) + 8192); } while (0)
#define ST_W0(wo_, kt) do { glds(voffQ, qb + (size_t)(kt) * 524288, ldw + (wo_)); glds(voffQ, qb + (size_t)(kt) * 524288 + 128, ldw + (wo_) + 8192); } while (0)
#define ST_W1(wo_, kt) do { glds(voffHB, hB + (size_t)(kt) * 458752, ldw + (wo_) + 16384); glds(voffHA, hA + (size_t)(kt) * 458752, ldw + (wo_) + 24576); } while (0)

  lptr wb[5], xr[2];
#pragma unroll
  for (int ft = 0; ft < 5; ++ft) wb[ft] = lds + wr * 8192 + (g >> 1) * 1024 + (4 * (g & 1) + q4) * 128 + 32 * ((ft < 4 ? ft : wc) ^ (2 * (g & 1) + (q4 >> 1))) + 8 * p4;
#pragma unroll
  for (int ks = 0; ks < 2; ++ks) xr[ks] = lds + 98304 + (wc * 32 + t) * 128 + 16 * ((4 * ks + g) ^ (t >> 1));
#define LDW(dst, h) do { _Pragma("unroll") for (int m = 0; m < 4; ++m) _Pragma("unroll") for (int k = 0; k < 2; ++k) dst[m][k] = ldtr2(wb[m] + (h) * 16384 + k * 4096, 2048); \
    Cf[0] = ldtr2(wb[4] + (h) * 16384, 2048); Cf[1] = ldtr2(wb[4] + (h) * 16384 + 4096, 2048); } while (0)
#define LDX(dst, b, h) do { _Pragma("unroll") for (int n = 0; n < 2; ++n) _Pragma("unroll") for (int k = 0; k < 2; ++k) dst[n][k] = ldb128(xr[k] + (SX(b, h) - 98304) + n * 2048); } while (0)

  f32x4 acc[2][2][4][2];
#pragma unroll
  for (int a = 0; a < 2; ++a)
#pragma unroll
    for (int b = 0; b < 2; ++b)
#pragma unroll
      for (int m = 0; m < 4; ++m)
#pragma unroll
        for (int n = 0; n < 2; ++n) acc[a][b][m][n] = (f32x4){0.f, 0.f, 0.f, 0.f};
  bf16x8 At[4][2], B0[2][2], B1[2][2], Cf[2];

#define MMA2(ai) do { __builtin_amdgcn_s_setprio(1); \
    cs[ai] = MFMA16(Cf[0], ones, cs[ai]); \
    _Pragma("unroll") for (int k = 0; k < 2; ++k) _Pragma("unroll") for (int m = 0; m < 3; ++m) _Pragma("unroll") for (int n = 0; n < 2; ++n) { \
      acc[ai][0][m][n] = MFMA16(At[m][k], B0[n][k], acc[ai][0][m][n]); acc[ai][1][m][n] = MFMA16(At[m][k], B1[n][k], acc[ai][1][m][n]); } \
    cs[ai] = MFMA16(Cf[1], ones, cs[ai]); \
    if ((ai) == 0 || wr == 1) { _Pragma("unroll") for (int k = 0; k < 2; ++k) _Pragma("unroll") for (int n = 0; n < 2; ++n) { \
      acc[ai][0][3][n] = MFMA16(At[3][k], B0[n][k], acc[ai][0][3][n]); acc[ai][1][3][n] = MFMA16(At[3][k], B1[n][k], acc[ai][1][3][n]); } } \
    __builtin_amdgcn_s_setprio(0); SCHED(); } while (0)
  char* const stats = (char*)ctl + (1 << 20);
  f32x4 cs[2] = {{0.f, 0.f, 0.f, 0.f}, {0.f, 0.f, 0.f, 0.f}};
  bf16x8 ones = __builtin_bit_cast(bf16x8, (u32x4){0x3f803f80u, 0x3f803f80u, 0x3f803f80u, 0x3f803f80u});
#define PIN_SETUP() do { asm volatile("" : "+v"(ones), "+v"(cs[0]), "+v"(cs[1]), "+v"(wb[0]), "+v"(wb[1]), "+v"(wb[2]), "+v"(wb[3]), "+v"(wb[4]), "+v"(xr[0]), "+v"(xr[1])); \
    _Pragma("unroll") for (int a = 0; a < 2; ++a) _Pragma("unroll") for (int b = 0; b < 2; ++b) \
      asm volatile("" : "+v"(acc[a][b][0][0]), "+v"(acc[a][b][0][1]), "+v"(acc[a][b][1][0]), "+v"(acc[a][b][1][1]), "+v"(acc[a][b][2][0]), "+v"(acc[a][b][2][1]), "+v"(acc[a][b][3][0]), "+v"(acc[a][b][3][1])); } while (0)
  unsigned tag;
  {
    if (wr == 0) {
      unsigned ep;
      ST_X(0, 0, 0); ST_X(0, 1, 0); ST_W0(0, 0);
      asm volatile("global_load_dword %0, %1, %2 sc1" : "=&v"(ep) : "v"(0), "s"(ctl) : "memory");
      ST_W1(0, 0); ST_W0(32768, 1); ST_X(1, 0, 1); ST_X(1, 1, 1); ST_W1(32768, 1);
      PIN_SETUP();
      asm volatile("s_waitcnt vmcnt(10)" : "+v"(ep) :: "memory");
      tag = __builtin_amdgcn_readfirstlane(ep) + 1u;
      BAR();
    } else {
      const int row = rb * 256 + hd * 8 + wc * 2 + (lane >> 5);
      u32x4 a[4];
      unsigned ep;
      asm volatile("global_load_dword %0, %1, %2 sc1" : "=&v"(ep) : "v"(0), "s"(ctl) : "memory");
      asm volatile("global_load_dwordx4 %0, %4, %5\n\tglobal_load_dwordx4 %1, %4, %5 offset:512\n\tglobal_load_dwordx4 %2, %4, %5 offset:1024\n\tglobal_load_dwordx4 %3, %4, %5 offset:1536"
                   : "=&v"(a[0]), "=&v"(a[1]), "=&v"(a[2]), "=&v"(a[3]) : "v"(row * 2048 + (lane & 31) * 16), "s"(xn) : "memory");
      ST_X(0, 0, 0); ST_X(0, 1, 0); ST_W0(0, 0);
      ST_W1(0, 0); ST_W0(32768, 1); ST_X(1, 0, 1); ST_X(1, 1, 1); ST_W1(32768, 1);
      PIN_SETUP();
      asm volatile("s_waitcnt vmcnt(10)" : "+v"(ep), "+v"(a[0]), "+v"(a[1]), "+v"(a[2]), "+v"(a[3]) :: "memory");
      BAR();
      tag = __builtin_amdgcn_readfirstlane(ep) + 1u;
      float v[32];
#pragma unroll
      for (int i = 0; i < 16; ++i) { v[2 * i] = bf2f(a[i >> 2][i & 3] & 0xffffu); v[2 * i + 1] = U2F(a[i >> 2][i & 3] & 0xffff0000u); }
      float s0 = 0.f, s1 = 0.f, s2 = 0.f, s3 = 0.f;
#pragma unroll
      for (int i = 0; i < 8; ++i) { s0 += v[i]; s1 += v[8 + i]; s2 += v[16 + i]; s3 += v[24 + i]; }
      const float mean = half_sum((s0 + s1) + (s2 + s3)) * (1.0f / 1024.0f);
      s0 = s1 = s2 = s3 = 0.f;
#pragma unroll
      for (int i = 0; i < 8; ++i) { const float d0 = v[i] - mean, d1 = v[8 + i] - mean, d2 = v[16 + i] - mean, d3 = v[24 + i] - mean; s0 += d0 * d0; s1 += d1 * d1; s2 += d2 * d2; s3 += d3 * d3; }
      const float sd = __builtin_amdgcn_sqrtf(half_sum((s0 + s1) + (s2 + s3)) * (1.0f / 1024.0f) + 1e-5f);
      if ((lane & 31) < 2) __hip_atomic_store((gu64*)(stats + row * 16 + (lane & 1) * 8), ((unsigned long long)tag << 32) | F2U((lane & 1) ? sd : mean), __ATOMIC_RELAXED, __HIP_MEMORY_SCOPE_AGENT);
    }
  }
  if (wr == 1) BAR();
  SCHED();
  STAMP(1);
  const int kvg = hd >> 3;
  const char* kb_g = kc + ((size_t)kvg * 2048 + 1536) * 256;
  const char* vb_g = vc + ((size_t)kvg * 2048 + 1536) * 256;
#define VOFFK ((unsigned)(((lane >> 2) & 7) * 256 + (4 * (lane >> 5) + ((lane & 3) ^ ((2 * (wid & 1) + ((lane >> 4) & 1)) & 3))) * 16))
#define VOFFV ((unsigned)(((lane >> 2) & 7) * 256 + (lane >> 5) * 64 + (lane & 3) * 16))
#define ST_KV8(voff, gsrc, slot) do { glds(voff, (gsrc) + wid * 2048, lds0 + (slot) + wid * 2048); glds(voff, (gsrc) + wid * 2048 + 128, lds0 + (slot) + wid * 2048 + 1024); } while (0)
  int wcur = 0, wprv = 65536;
#define W_ADV() do { const int nx_ = (wcur == 65536) ? 0 : wcur + 32768, d_ = nx_ - wcur; wprv = wcur; wcur = nx_; \
    _Pragma("unroll") for (int f = 0; f < 5; ++f) wb[f] += d_; } while (0)
#define PH_A(b, STAGE) do { LDX(B0, b, 0); LDX(B1, b, 1); SCHED(); LDW(At, 0); STAGE; WAIT_V(10); WAIT_L(12); BAR(); SCHED(); MMA2(0); BAR(); SCHED(); } while (0)
#define PH_B(STAGE) do { LDW(At, 1); STAGE; W_ADV(); WAIT_V(10); BAR(); SCHED(); MMA2(1); WAIT_L(0); BAR(); SCHED(); } while (0)
#pragma unroll 1
  for (int kt = 0; kt < 14; kt += 2) {
    PH_A(0, ST_W0(wprv, kt + 2));
    PH_B(ST_X(0, 0, kt + 2); ST_X(0, 1, kt + 2); ST_W1(wprv, kt + 2));
    PH_A(1, ST_W0(wprv, kt + 3));
    PH_B(ST_X(1, 0, kt + 3); ST_X(1, 1, kt + 3); ST_W1(wprv, kt + 3));
    LSTAMP(kt);
  }
  STAMP(2);
  const unsigned voffK = VOFFK, voffV = VOFFV;
  PH_A(0, ST_KV8(voffK, kb_g, SLOT_E));
  PH_B(ST_KV8(voffK, kb_g + 16384, SLOT_E + 16384); ST_KV8(voffV, vb_g, SLOT_C); ST_KV8(voffV, vb_g + 16384, SLOT_C + 16384));
  const __amdgpu_buffer_rsrc_t srs = __builtin_amdgcn_make_buffer_rsrc(stats, 0, 2048 * 16, 0x00020000);
  const unsigned soff = (unsigned)((rb * 256 + (g >> 1) * 128 + wc * 32 + (g & 1) * 16 + t) * 16);
  u32x4 sg;
  LDX(B0, 1, 0); LDX(B1, 1, 1); SCHED(); LDW(At, 0);
  asm volatile("global_load_dwordx4 %0, %1, %2 sc1" : "=&v"(sg) : "v"(soff), "s"(stats) : "memory");
  ST_KV8(voffK, kb_g + 32768, SLOT_A);
  WAIT_V(11); BAR(); SCHED(); MMA2(0); BAR(); SCHED();
  LDW(At, 1);
  ST_KV8(voffK, kb_g + 49152, SLOT_A + 16384);
  BAR(); SCHED(); MMA2(1);
  {
    const lptr cw = lds + SLOT_D + (t & 3) * 8192 + wr * 4096 + wc * 64 + g * 16;
    *(LAS f32x4*)cw = cs[0]; *(LAS f32x4*)(cw + 256) = cs[1];
  }
  asm volatile("s_waitcnt vmcnt(4)" : "+v"(sg) :: "memory");
  WAIT_L(0); BAR(); SCHED();
  f32x4 cv[2][4];
#pragma unroll
  for (int a = 0; a < 2; ++a)
#pragma unroll
    for (int m = 0; m < 4; ++m) cv[a][m] = *(const LAS f32x4*)(lds + SLOT_D + wc * 8192 + wr * 4096 + a * 256 + m * 64 + g * 16);
  if (__builtin_expect(__builtin_amdgcn_ballot_w64(sg[1] != tag || sg[3] != tag) != 0ull, 0)) {
    for (unsigned spins = 0; spins < 4000000u; ++spins) {
      __builtin_amdgcn_s_sleep(2);
      asm volatile("" ::: "memory");
      sg = __builtin_amdgcn_raw_buffer_load_b128(srs, soff, 0, 16);
      if (__builtin_amdgcn_ballot_w64(sg[1] != tag || sg[3] != tag) == 0ull) break;
    }
  }
  float mu[2][2], rs[2][2], sd[2][2];
  {
    auto m16 = __builtin_amdgcn_permlane16_swap(sg[0], sg[0], false, false);
    auto s16 = __builtin_amdgcn_permlane16_swap(sg[2], sg[2], false, false);
#pragma unroll
    for (int n = 0; n < 2; ++n) {
      auto m32 = __builtin_amdgcn_permlane32_swap(m16[n], m16[n], false, false);
      auto s32 = __builtin_amdgcn_permlane32_swap(s16[n], s16[n], false, false);
#pragma unroll
      for (int bj = 0; bj < 2; ++bj) { mu[bj][n] = -U2F(m32[bj]); sd[bj][n] = U2F(s32[bj]); rs[bj][n] = __builtin_amdgcn_rcpf(sd[bj][n]) * 1.4426950408889634f; }
    }
  }
  WAIT_L(0);
  STAMP(3);
  {
    const lptr qw = lds + wc * 8192 + wr * 4096 + ((g >> 1) * 32 + t) * 16 + (g & 1) * 8;
#pragma unroll
    for (int bj = 0; bj < 2; ++bj) {
      lptr qwb = qw + (bj ? SLOT_D : SLOT_B);
      asm volatile("" : "+v"(qwb));
#pragma unroll
      for (int m = 0; m < 4; ++m)
#pragma unroll
        for (int n = 0; n < 2; ++n) {
          const f32x4 v = (acc[0][bj][m][n] + cv[0][m] * mu[bj][n]) * (rs[bj][n] * 0.08838834764831845f);
          u32x2 w = {pk2(v[0], v[1]), pk2(v[2], v[3])};
          *(LAS u32x2*)(qwb + m * 1024 + n * 256) = w;
        }
    }
  }
  WAIT_L(0); BAR(); SCHED();
  STAMP(4);
  bf16x8 qf[8];
  const lptr qr = lds + (wr ? SLOT_D : SLOT_B) + wc * 8192 + lane * 16;
  if (wr == 1) {
#pragma unroll
    for (int s = 0; s < 8; ++s) qf[s] = ldb128(qr + s * 1024);
  }
  {
    u32x2 hw[2][2][4];
#define SILU16(m) do { \
      const f32x4 a0 = acc[1][0][m][0] + cv[1][m] * mu[0][0], a1 = acc[1][0][m][1] + cv[1][m] * mu[0][1], a2 = acc[1][1][m][0] + cv[1][m] * mu[1][0], a3 = acc[1][1][m][1] + cv[1][m] * mu[1][1]; \
      f32x4 e0 = a0 * -rs[0][0], e1 = a1 * -rs[0][1], e2 = a2 * -rs[1][0], e3 = a3 * -rs[1][1]; SCHED(); \
      _Pragma("unroll") for (int i = 0; i < 4; ++i) { e0[i] = __builtin_amdgcn_exp2f(e0[i]); e1[i] = __builtin_amdgcn_exp2f(e1[i]); e2[i] = __builtin_amdgcn_exp2f(e2[i]); e3[i] = __builtin_amdgcn_exp2f(e3[i]); } SCHED(); \
      e0 = e0 * sd[0][0] + sd[0][0]; e1 = e1 * sd[0][1] + sd[0][1]; e2 = e2 * sd[1][0] + sd[1][0]; e3 = e3 * sd[1][1] + sd[1][1]; SCHED(); \
      _Pragma("unroll") for (int i = 0; i < 4; ++i) { e0[i] = __builtin_amdgcn_rcpf(e0[i]); e1[i] = __builtin_amdgcn_rcpf(e1[i]); e2[i] = __builtin_amdgcn_rcpf(e2[i]); e3[i] = __builtin_amdgcn_rcpf(e3[i]); } SCHED(); \
      e0 *= a0; e1 *= a1; e2 *= a2; e3 *= a3; SCHED(); \
      hw[0][0][m] = (u32x2){pk2(e0[0], e0[1]), pk2(e0[2], e0[3])}; hw[0][1][m] = (u32x2){pk2(e1[0], e1[1]), pk2(e1[2], e1[3])}; \
      hw[1][0][m] = (u32x2){pk2(e2[0], e2[1]), pk2(e2[2], e2[3])}; hw[1][1][m] = (u32x2){pk2(e3[0], e3[1]), pk2(e3[2], e3[3])}; SCHED(); } while (0)
    SILU16(0);
    if (wr == 0) {
      BAR(); SCHED();
#pragma unroll
      for (int s = 0; s < 8; ++s) qf[s] = ldb128(qr + s * 1024);
    }
    SILU16(1); SILU16(2);
    const unsigned hoff = (unsigned)((rb * 16 + wc * 2) * 2048 + lane * 16);
    if (wr == 1) {
      SILU16(3);
      char* d = hbuf + (size_t)hd * 262144;
#pragma unroll
      for (int bj = 0; bj < 2; ++bj)
#pragma unroll
        for (int n = 0; n < 2; ++n) {
          *(u32x4*)(d + bj * 16384 + (hoff + n * 2048)) = (u32x4){hw[bj][n][0][0], hw[bj][n][0][1], hw[bj][n][1][0], hw[bj][n][1][1]};
          *(u32x4*)(d + bj * 16384 + (hoff + n * 2048 + 1024)) = (u32x4){hw[bj][n][2][0], hw[bj][n][2][1], hw[bj][n][3][0], hw[bj][n][3][1]};
        }
    } else {
      const int p0 = (128 + 3 * hd) >> 1, p1 = p0 + 1;
      char* d0 = hbuf + (size_t)(p0 >> 1) * 262144 + (p0 & 1) * 1024;
      char* d1 = hbuf + (size_t)(p1 >> 1) * 262144 + (p1 & 1) * 1024;
      if ((hd & 1) == 0) {
#pragma unroll
        for (int bj = 0; bj < 2; ++bj)
#pragma unroll
          for (int n = 0; n < 2; ++n) {
            *(u32x4*)(d0 + bj * 16384 + (hoff + n * 2048)) = (u32x4){hw[bj][n][0][0], hw[bj][n][0][1], hw[bj][n][1][0], hw[bj][n][1][1]};
            *(u32x2*)(d1 + bj * 16384 + (hoff + n * 2048)) = hw[bj][n][2];
          }
      } else {
#pragma unroll
        for (int bj = 0; bj < 2; ++bj)
#pragma unroll
          for (int n = 0; n < 2; ++n) {
            *(u32x2*)(d0 + bj * 16384 + (hoff + n * 2048 + 8)) = hw[bj][n][0];
            *(u32x4*)(d1 + bj * 16384 + (hoff + n * 2048)) = (u32x4){hw[bj][n][1][0], hw[bj][n][1][1], hw[bj][n][2][0], hw[bj][n][2][1]};
          }
      }
    }
  }
  STAMP(5);
  if (wr == 1) { WAIT_L(0); BAR(); }
  SCHED();
  STAMP(6);
  const int r32 = lane & 31, hh = lane >> 5;
  lptr kr[2];
#pragma unroll
  for (int e = 0; e < 2; ++e) kr[e] = lds + 2048 * (r32 >> 3) + 64 * (r32 & 7) + 16 * ((2 * e + hh) ^ ((r32 >> 2) & 3));
  const lptr vr = lds + ((lane & 3) << 3) + (((lane >> 2) & 3) << 6) + (((lane >> 4) & 1) << 5) + (hh << 8);
  f32x16 o[4], s[4];
#pragma unroll
  for (int d = 0; d < 4; ++d)
#pragma unroll
    for (int i = 0; i < 16; ++i) o[d][i] = 0.f;
  bf16x8 pf[8];
  constexpr int RD = 6;
  bf16x8 fr[RD];
  float lrun = 0.f;
  f32x16 negm = {0.f, 0.f, 0.f, 0.f, 0.f, 0.f, 0.f, 0.f, 0.f, 0.f, 0.f, 0.f, 0.f, 0.f, 0.f, 0.f};
#define SM_THR 60.0f
#define FRAG(i) ((i) < 32 ? ldtr2(v0 + (((i) >> 2) * 8 + ((i) & 3)) * 512, 2048) : ldb128((((((i) - 32) >> 2) & 1) ? k1 : k0) + ((i) & 3) * 8192 + (((i) - 32) >> 3) * 512))
#define XPRIO 1
#define YPRIO 0
#define XSEG(lo, hi) do { __builtin_amdgcn_s_setprio(XPRIO); \
    _Pragma("unroll") for (int i = (lo); i < (hi); ++i) { \
      if (i < 32) o[i & 3] = MFMA32(fr[i % RD], pf[(i >> 2) & 7], o[i & 3]); \
      else s[i & 3] = MFMA32(fr[i % RD], qf[((i - 32) >> 2) & 7], i < 36 ? negm : s[i & 3]); \
      if (i + RD < (hi)) fr[i % RD] = FRAG(i + RD); \
      SCHED(); } \
    __builtin_amdgcn_s_setprio(YPRIO); } while (0)
#define PREF(lo) do { _Pragma("unroll") for (int i = (lo); i < (lo) + RD; ++i) fr[i % RD] = FRAG(i); SCHED(); } while (0)
#define MX3(a, b, c) __builtin_fmaxf(__builtin_fmaxf((a), (b)), (c))
#define SOFTMAX() do { \
      float m0 = MX3(s[0][0], s[0][1], s[0][2]), m1 = MX3(s[1][0], s[1][1], s[1][2]), m2 = MX3(s[2][0], s[2][1], s[2][2]), m3 = MX3(s[3][0], s[3][1], s[3][2]); \
      _Pragma("unroll") for (int i = 3; i < 15; i += 2) { m0 = MX3(m0, s[0][i], s[0][i + 1]); m1 = MX3(m1, s[1][i], s[1][i + 1]); m2 = MX3(m2, s[2][i], s[2][i + 1]); m3 = MX3(m3, s[3][i], s[3][i + 1]); } \
      m0 = MX3(m0, s[0][15], s[1][15]); m2 = MX3(m2, s[2][15], s[3][15]); \
      float mx = __builtin_fmaxf(MX3(m0, m1, m2), m3); \
        \
      if (__builtin_expect(__builtin_amdgcn_ballot_w64(!(mx <= SM_THR)) != 0ull, 0)) { \
        auto rr = __builtin_amdgcn_permlane32_swap(F2U(mx), F2U(mx), false, false); \
        const float dl = MX3(U2F(rr[0]), U2F(rr[1]), 0.f); \
        _Pragma("unroll") for (int kb = 0; kb < 4; ++kb) s[kb] -= dl; \
        negm -= dl; \
        const float alpha = __builtin_amdgcn_exp2f(-dl); lrun *= alpha; _Pragma("unroll") for (int d = 0; d < 4; ++d) o[d] *= alpha; \
      } \
      SCHED(); \
      float l0 = lrun, l1 = 0.f; \
      _Pragma("unroll") for (int kb = 0; kb < 4; ++kb) { \
        _Pragma("unroll") for (int e = 0; e < 2; ++e) { \
          _Pragma("unroll") for (int i = 0; i < 8; ++i) s[kb][8 * e + i] = __builtin_amdgcn_exp2f(s[kb][8 * e + i]); \
          _Pragma("unroll") for (int i = 0; i < 8; i += 2) { l0 += s[kb][8 * e + i]; l1 += s[kb][8 * e + i + 1]; } \
          u32x4 w = {pk2(s[kb][8 * e], s[kb][8 * e + 1]), pk2(s[kb][8 * e + 2], s[kb][8 * e + 3]), pk2(s[kb][8 * e + 4], s[kb][8 * e + 5]), pk2(s[kb][8 * e + 6], s[kb][8 * e + 7])}; \
          pf[2 * kb + e] = __builtin_bit_cast(bf16x8, w); } \
        asm volatile("" : "+v"(pf[2 * kb]), "+v"(pf[2 * kb + 1]), "+v"(l0), "+v"(l1)); \
        SCHED(); } \
      lrun = l0 + l1; \
      asm volatile("" : "+v"(lrun)); } while (0)
  {
    const lptr k0 = kr[0] + SLOT_E, k1 = kr[1] + SLOT_E, v0 = vr;
    {
      bf16x8 kf[8];
#pragma unroll
      for (int st = 0; st < 8; ++st) kf[st] = FRAG(32 + 4 * st);
      PREF(32);
#pragma unroll
      for (int st = 0; st < 8; ++st) negm = MFMA32(kf[st], qf[st], negm);
      float m0 = MX3(negm[0], negm[1], negm[2]), m1 = MX3(negm[3], negm[4], negm[5]);
      m0 = MX3(m0, negm[6], negm[7]); m1 = MX3(m1, negm[8], negm[9]); m0 = MX3(m0, negm[10], negm[11]); m1 = MX3(m1, negm[12], negm[13]);
      m0 = __builtin_fmaxf(MX3(m0, negm[14], negm[15]), m1);
      auto rr = __builtin_amdgcn_permlane32_swap(F2U(m0), F2U(m0), false, false);
      m0 = -__builtin_fmaxf(U2F(rr[0]), U2F(rr[1]));
#pragma unroll
      for (int i = 0; i < 16; ++i) negm[i] = m0;
      asm volatile("" : "+v"(negm));
      SCHED();
    }
    XSEG(32, 64);
  }
  STAMP(7);
  const char* kvsrc = (wr ? kb_g + 65536 : vb_g + 32768) + wc * 8192 - 1024;
  unsigned kvo[2][2];
#pragma unroll
  for (int par = 0; par < 2; ++par) {
    const int xx = wr ? ((lane & 3) ^ ((2 * par + ((lane >> 4) & 1)) & 3)) : (lane & 3);
    kvo[par][0] = (unsigned)(((lane >> 2) & 7) * 256 + (lane >> 5) * 64 + xx * 16 + 1024);
    kvo[par][1] = kvo[par][0] + 128 - 1024;
  }
  const unsigned pfo = (unsigned)((lane < 21 ? lane : 20) * 128);
  unsigned pft = 0;
#pragma unroll 1
  for (int c = 0; c < 3; ++c) {
    WAIT_V(1); WAIT_L(0); BAR(); SCHED();
    if (c == 0) STAMP(8); else if (c == 1) STAMP(12);
    if (c + wr < 3) {
      const int slot = wr ? (c == 0 ? SLOT_B : SLOT_C) : (c == 0 ? SLOT_D : (c == 1 ? SLOT_E : SLOT_A));
#pragma unroll
      for (int hf = 0; hf < 2; ++hf)
        asm volatile("s_mov_b32 m0, %5\n\ts_nop 0\n\t"
                     "global_load_lds_dwordx4 %0, %4\n\tglobal_load_lds_dwordx4 %1, %4 offset:1024\n\t"
                     "global_load_lds_dwordx4 %2, %4 offset:2048\n\tglobal_load_lds_dwordx4 %3, %4 offset:3072"
                     ::"v"(kvo[0][0]), "v"(kvo[0][1]), "v"(kvo[1][0]), "v"(kvo[1][1]), "s"(kvsrc + c * 32768 + hf * 4096), "s"(lds0 + slot + wc * 8192 + hf * 4096) : "memory");
    }
    {
      const int sl = (bid * 8 + wid) * 3 + c;
      const int o1 = sl * 2688, o2 = (sl - 3121) * 2688;
      const char* pp = sl < 3121 ? wo + (o1 < 8385920 ? o1 : 8385920) : w2 + (o2 < 7337344 ? o2 : 7337344);
      asm volatile("global_load_dword %0, %1, %2" : "+v"(pft) : "v"(pfo), "s"(pp) : "memory");
    }
    SOFTMAX();
    const int kslot = (c == 0) ? SLOT_A : ((c == 1) ? SLOT_B : SLOT_C);
    const int vslot = (c == 0) ? SLOT_C : ((c == 1) ? SLOT_D : SLOT_E);
    const lptr k0 = kr[0] + kslot, k1 = kr[1] + kslot, v0 = vr + vslot;
    PREF(0);
    if (c == 0) STAMP(9); else if (c == 1) STAMP(13);
    BAR(); SCHED();
    if (c == 0) STAMP(10); else if (c == 1) STAMP(14);
    XSEG(0, 64);
    if (c == 0) STAMP(11); else if (c == 2) STAMP(15);
  }
  WAIT_V(1); WAIT_L(0); BAR(); SCHED();
  STAMP(16);
  SOFTMAX();
  {
    const lptr k0 = kr[0], k1 = kr[1], v0 = vr + SLOT_A;
    PREF(0);
    STAMP(17);
    BAR(); SCHED();
    STAMP(18);
    XSEG(0, 32);
  }
  STAMP(19);
  if (wr == 0) BAR();
  {
    auto rr = __builtin_amdgcn_permlane32_swap(F2U(lrun), F2U(lrun), false, false);
    const float inv = __builtin_amdgcn_rcpf(U2F(rr[0]) + U2F(rr[1]));
    const int tt = rb * 16 + wr * 8 + wc * 2 + (r32 >> 4);
    char* ow = obuf + ((size_t)(2 * hd * 128 + tt) * 2) * 1024 + (hh * 16 + (r32 & 15)) * 16;
#pragma unroll
    for (int d = 0; d < 4; ++d)
#pragma unroll
      for (int e = 0; e < 2; ++e) {
        u32x4 w = {pk2(o[d][8 * e] * inv, o[d][8 * e + 1] * inv), pk2(o[d][8 * e + 2] * inv, o[d][8 * e + 3] * inv),
                   pk2(o[d][8 * e + 4] * inv, o[d][8 * e + 5] * inv), pk2(o[d][8 * e + 6] * inv, o[d][8 * e + 7] * inv)};
        *(u32x4*)(ow + (d >> 1) * 262144 + (d & 1) * 1024 + e * 512) = w;
      }
  }
  if (bid == 1) ctl[1024 + tid * 32] = 0u;
  if (bid == 0) {
    for (unsigned spins = 0; spins < 4000000u; ++spins) {
      bool ok = true;
#pragma unroll
      for (int i = 0; i < 4; ++i) ok = ok && __builtin_amdgcn_raw_buffer_load_b32(srs, (unsigned)((tid + i * 512) * 16 + 4), 0, 16) == tag;
      if (__builtin_amdgcn_ballot_w64(!ok) == 0ull) break;
      __builtin_amdgcn_s_sleep(1);
      asm volatile("" ::: "memory");
    }
    BAR();
    if (tid == 0) __hip_atomic_store(ctl, tag, __ATOMIC_RELAXED, __HIP_MEMORY_SCOPE_AGENT);
  }
  STAMP(20);
  asm volatile("s_waitcnt vmcnt(0)" : "+v"(pft) :: "memory");
  STAMP(21);
  STAMP_OUT(hbuf + (32 << 20), 29);
}

#define K2MAP 1
#define K2PAIR 0
constexpr int K2_STG = 32768, K2_LDS = 5 * K2_STG;
NOPK __global__ __launch_bounds__(512, 2) void k2_kernel(const char* __restrict__ obuf, const char* __restrict__ hbuf, const char* __restrict__ wo, const char* __restrict__ w2,
                                                         const char* __restrict__ x, char* gbuf, unsigned* flags, char* __restrict__ out) {
  STAMP_DECL
  STAMP(0);
  extern __shared__ __attribute__((aligned(16))) char shm[];
  const lptr lds = (lptr)shm;
  const unsigned lds0 = (unsigned)(uintptr_t)lds;
  const int tid = TID, wid = __builtin_amdgcn_readfirstlane(tid >> 6), lane = tid & 63;
  const int bid = BID, xcd = bid & 7, idx = bid >> 3, isG = xcd >> 2, j = xcd & 3;
  const int tm = 8 * (j >> 1) + (idx & 7), tn = 4 * (j & 1) + (idx >> 3);
  const int cb0 = tn * 256, cbs = 128;
  const int nt = isG ? 56 : 64;
  if (wid >= 4) {
    const int lw = wid & 3, rot = (j * nt) >> 2;
    const char* ab = (isG ? hbuf : obuf) + tm * 16384 + lw * 4096;
    const char* wbp = (isG ? w2 : wo) + cb0;
    const unsigned voffA = lane * 16;
    unsigned voffW;
    {
      const int r = lane >> 3, c = lane & 7;
      const int kp = isG ? lw : (((lw & 1) << 1) | (lw >> 1));
      voffW = (unsigned)((8 * kp + r) * 2048 + ((c ^ (((r >> 1) & 3) << 1)) << 4));
    }
#define K2_ISSUE(kt_, buf_) do { int k_ = (kt_) + rot; if (k_ >= nt) k_ -= nt; const unsigned d_ = lds0 + (buf_) * K2_STG; \
      _Pragma("unroll") for (int i = 0; i < 4; ++i) { \
        glds(voffA, ab + (size_t)k_ * 262144 + i * 1024, d_ + lw * 4096 + i * 1024); \
        glds(voffW, wbp + (size_t)k_ * 131072 + (i >> 1) * 65536 + (i & 1) * cbs, d_ + 16384 + (i & 1) * 8192 + (i >> 1) * 4096 + lw * 1024); } } while (0)
    K2_ISSUE(0, 0); K2_ISSUE(1, 1);
    WAIT_V(8); BAR();
    STAMP(1);
    K2_ISSUE(2, 2);
    WAIT_V(8); BAR();
    K2_ISSUE(3, 3); K2_ISSUE(4, 4);
    int buf = 0;
#pragma unroll 1
    for (int kt = 1; kt < nt - 4; ++kt) {
      WAIT_V(16); BAR();
      K2_ISSUE(kt + 4, buf);
      buf = (buf == 4) ? 0 : buf + 1;
    }
    WAIT_V(16); BAR();
    WAIT_V(8); BAR();
    WAIT_V(0); BAR();
    const int c8 = lane & 15, r0 = lw * 4 + (lane >> 4);
    const unsigned goff = (unsigned)((tm * 128 + r0) * 2048 + cb0 + (c8 >> 3) * cbs + (c8 & 7) * 16);
    const __amdgpu_buffer_rsrc_t grs = __builtin_amdgcn_make_buffer_rsrc(gbuf, 0, 2048 * 2048, 0x00020000);
    unsigned* const flag = flags + (tm * 8 + tn) * 32;
    u32x4 xv[8], gv[8];
    if (!isG) {
#pragma unroll
      for (int i = 0; i < 8; ++i) xv[i] = *(const u32x4*)(x + goff + i * 32768);
    }
    BAR();
    if (!isG) {
      unsigned spins = 0;
      while (__builtin_amdgcn_readfirstlane(__hip_atomic_load(flag, __ATOMIC_RELAXED, __HIP_MEMORY_SCOPE_AGENT)) == 0u) {
        __builtin_amdgcn_s_sleep(1);
        if (++spins > 20000000u) break;
      }
      asm volatile("" ::: "memory");
#pragma unroll
      for (int i = 0; i < 8; ++i) gv[i] = __builtin_amdgcn_raw_buffer_load_b128(grs, goff + i * 32768, 0, 16);
    }
    STAMP(2);
    BAR();
    BAR();
    STAMP(3);
#pragma unroll
    for (int i = 0; i < 8; ++i) {
      const int row = r0 + 16 * i;
      const lptr p = lds + row * 512;
      const f32x4 a0 = *(const LAS f32x4*)(p + (((2 * c8) ^ (row & 7)) << 4)), a1 = *(const LAS f32x4*)(p + (((2 * c8 + 1) ^ (row & 7)) << 4));
      u32x4 w;
      if (isG) {
        w = (u32x4){pk2(silu(a0[0]), silu(a0[1])), pk2(silu(a0[2]), silu(a0[3])), pk2(silu(a1[0]), silu(a1[1])), pk2(silu(a1[2]), silu(a1[3]))};
        __builtin_amdgcn_raw_buffer_store_b128(w, grs, goff + i * 32768, 0, 16);
      } else {
#pragma unroll
        for (int e = 0; e < 4; ++e) {
          const float s0 = e < 2 ? a0[2 * e] : a1[2 * e - 4], s1 = e < 2 ? a0[2 * e + 1] : a1[2 * e - 3];
          w[e] = pk2(bf2f(xv[i][e] & 0xffffu) + s0 * bf2f(gv[i][e] & 0xffffu), U2F(xv[i][e] & 0xffff0000u) + s1 * U2F(gv[i][e] & 0xffff0000u));
        }
        *(u32x4*)(out + goff + i * 32768) = w;
      }
    }
    STAMP(4);
    if (isG) {
      WAIT_V(0); BAR();
      if (tid == 256) __hip_atomic_store(flag, 1u, __ATOMIC_RELAXED, __HIP_MEMORY_SCOPE_AGENT);
    }
  } else {
    const int wm = wid >> 1, wn = wid & 1, g = lane >> 4, t = lane & 15, q4 = (lane >> 2) & 3, p4 = lane & 3;
    lptr wb[4];
#pragma unroll
    for (int ft = 0; ft < 4; ++ft) wb[ft] = lds + 16384 + wn * 8192 + (g >> 1) * 1024 + (4 * (g & 1) + q4) * 128 + 32 * (ft ^ (2 * (g & 1) + (q4 >> 1))) + 8 * p4;
    lptr ar = lds + wm * 8192 + lane * 16;
    f32x4 acc[4][4];
#pragma unroll
    for (int m = 0; m < 4; ++m)
#pragma unroll
      for (int n = 0; n < 4; ++n) acc[m][n] = (f32x4){0.f, 0.f, 0.f, 0.f};
    bf16x8 Af[2][4];
    int dlt = 0;
    bf16x4 Wh[2][4][2];
#define K2_RD(set, ks, i) do { if ((i) == 0) Af[set][0] = ldb128(ar + (ks) * 1024); else if ((i) >= 3 && (i) < 6) Af[set][(i) - 2] = ldb128(ar + ((i) - 2) * 2048 + (ks) * 1024); \
      else { const int f_ = (i) < 3 ? 0 : ((i) - 4) >> 1, h_ = (i) < 3 ? (i) - 1 : (i) & 1; \
             Wh[set][f_][h_] = __builtin_amdgcn_ds_read_tr16_b64_v4bf16((LAS bf16x4*)(wb[f_] + (ks) * 4096 + h_ * 2048)); } } while (0)
#define K2_W(set, m) __builtin_shufflevector(Wh[set][m][0], Wh[set][m][1], 0, 1, 2, 3, 4, 5, 6, 7)
    BAR();
    STAMP(1);
#pragma unroll
    for (int i = 0; i < 12; ++i) { K2_RD(0, 0, i); SCHED(); }
    int buf = 0;
#pragma unroll 1
    for (int kt = 0; kt < nt; ++kt) {
      SCHED();
#pragma unroll
      for (int i = 0; i < 16; ++i) {
        acc[i >> 2][i & 3] = MFMA16(K2_W(0, i >> 2), Af[0][i & 3], acc[i >> 2][i & 3]);
        if (i < 12) K2_RD(1, 1, i);
        if (i == 12) { dlt = (buf == 4) ? -4 * K2_STG : K2_STG; buf = (buf == 4) ? 0 : buf + 1; ar += dlt; asm volatile("" : "+v"(ar)); }
        if (i == 13) { wb[0] += dlt; wb[1] += dlt; asm volatile("" : "+v"(wb[0]), "+v"(wb[1])); }
        if (i == 14) { wb[2] += dlt; wb[3] += dlt; asm volatile("" : "+v"(wb[2]), "+v"(wb[3])); }
        if (i == 15) BAR();
        SCHED();
      }
#pragma unroll
      for (int i = 0; i < 16; ++i) {
        acc[i >> 2][i & 3] = MFMA16(K2_W(1, i >> 2), Af[1][i & 3], acc[i >> 2][i & 3]);
        if (i < 12) K2_RD(0, 0, i);
        SCHED();
      }
    }
    WAIT_L(0); BAR();
    STAMP(2);
    const lptr cw = lds + (wm * 64 + t) * 512;
#pragma unroll
    for (int m = 0; m < 4; ++m)
#pragma unroll
      for (int n = 0; n < 4; ++n) *(LAS f32x4*)(cw + n * 8192 + (((wn * 16 + m * 4 + g) ^ (t & 7)) << 4)) = acc[m][n];
    WAIT_L(0); BAR();
    STAMP(3);
    STAMP(4);
    if (isG) BAR();
  }
  STAMP(5);
  WAIT_V(0);
  STAMP(6);
  STAMP_OUT((char*)hbuf + (33 << 20), 7);
}

extern "C" void kernel_launch(void* const* d_in, const int* in_sizes, int n_in, void* d_out, int out_size, void* d_ws, size_t ws_size, hipStream_t stream) {
  const char* x = (const char*)d_in[0]; const char* kc = (const char*)d_in[1]; const char* vc = (const char*)d_in[2];
  const char* wq = (const char*)d_in[3]; const char* wo = (const char*)d_in[4]; const char* w1 = (const char*)d_in[6]; const char* w2 = (const char*)d_in[7];
  char* ws = (char*)d_ws;
  unsigned* ctl = (unsigned*)ws;
  char* gbuf = ws + (5 << 20);
  char* obuf = gbuf + (4 << 20);
  char* hbuf = obuf + (16 << 20);
  (void)hipFuncSetAttribute((const void*)k1_kernel, hipFuncAttributeMaxDynamicSharedMemorySize, K1_LDS);
  (void)hipFuncSetAttribute((const void*)k2_kernel, hipFuncAttributeMaxDynamicSharedMemorySize, K2_LDS);
  k1_kernel<<<256, 512, K1_LDS, stream>>>(x, kc, vc, wq, w1, hbuf, obuf, wo, w2, ctl);
  k2_kernel<<<256, 512, K2_LDS, stream>>>(obuf, hbuf, wo, w2, x, gbuf, ctl + 1024, (char*)d_out);
}
```

```cpp
#include <hip/hip_runtime.h>
#include <stdint.h>

typedef unsigned short u16;
typedef __bf16 bf16x2 __attribute__((ext_vector_type(2)));
typedef __bf16 bf16x4 __attribute__((ext_vector_type(4)));
typedef __bf16 bf16x8 __attribute__((ext_vector_type(8)));
typedef float f32x2 __attribute__((ext_vector_type(2)));
typedef float f32x4 __attribute__((ext_vector_type(4)));
typedef float f32x16 __attribute__((ext_vector_type(16)));
typedef unsigned u32x2 __attribute__((ext_vector_type(2)));
typedef unsigned u32x4 __attribute__((ext_vector_type(4)));
#define LAS __attribute__((address_space(3)))
typedef LAS char* lptr;
#if defined(__HIP_DEVICE_COMPILE__)
#define NOPK __attribute__((target("no-packed-fp32-ops")))
#else
#define NOPK
#endif
#define DI NOPK __device__ __forceinline__
#define F2U(x) __builtin_bit_cast(unsigned, (float)(x))
#define U2F(x) __builtin_bit_cast(float, (unsigned)(x))
#define TID ((int)__builtin_amdgcn_workitem_id_x())
#define BID ((int)__builtin_amdgcn_workgroup_id_x())
#define SCHED() __builtin_amdgcn_sched_barrier(0)
#define BAR() asm volatile("s_barrier" ::: "memory")
#define WAIT_V(n) asm volatile("s_waitcnt vmcnt(%0)" :: "n"(n) : "memory")
#define WAIT_L(n) do { asm volatile("" ::: "memory"); __builtin_amdgcn_s_waitcnt(0xC07F | ((n) << 8)); asm volatile("" ::: "memory"); } while (0)
#define MFMA16(a, b, c) __builtin_amdgcn_mfma_f32_16x16x32_bf16(a, b, c, 0, 0, 0)
#define MFMA32(a, b, c) __builtin_amdgcn_mfma_f32_32x32x16_bf16(a, b, c, 0, 0, 0)

DI void glds(unsigned voff, const char* sbase, unsigned ldsaddr) {
  asm volatile("s_mov_b32 m0, %2\n\ts_nop 0\n\tglobal_load_lds_dwordx4 %0, %1" ::"v"(voff), "s"(sbase), "s"(ldsaddr) : "memory");
}
DI void glds1(unsigned voff, const char* sbase, unsigned ldsaddr) {
  asm volatile("s_mov_b32 m0, %2\n\ts_nop 0\n\tglobal_load_lds_dwordx4 %0, %1 sc1" ::"v"(voff), "s"(sbase), "s"(ldsaddr) : "memory");
}
DI unsigned pk2(float lo, float hi) { f32x2 v = {lo, hi}; return __builtin_bit_cast(unsigned, __builtin_convertvector(v, bf16x2)); }
DI float bf2f(unsigned b) { return U2F(b << 16); }
DI bf16x8 ldb128(lptr p) { return *(const LAS bf16x8*)p; }
DI bf16x8 ldtr2(lptr p, int d) {
  bf16x4 a = __builtin_amdgcn_ds_read_tr16_b64_v4bf16((LAS bf16x4*)p);
  bf16x4 b = __builtin_amdgcn_ds_read_tr16_b64_v4bf16((LAS bf16x4*)(p + d));
  return __builtin_shufflevector(a, b, 0, 1, 2, 3, 4, 5, 6, 7);
}
DI void ST128_(__amdgpu_buffer_rsrc_t r, u32x4 v, unsigned voff, int soff) { __builtin_amdgcn_raw_buffer_store_b128(v, r, voff + (unsigned)soff, 0, 16); }
DI float silu(float v) { return v * __builtin_amdgcn_rcpf(1.0f + __builtin_amdgcn_exp2f(v * -1.4426950408889634f)); }

#define STAMP_DECL
#define LSTAMP(i)
#define STAMP(i)
#define STAMP_OUT(buf, n)
template <int CTRL> DI float dpp_add(float v) { return v + U2F(__builtin_amdgcn_update_dpp(0u, F2U(v), CTRL, 0xf, 0xf, true)); }
DI float half_sum(float v) {
  v = dpp_add<0xB1>(v); v = dpp_add<0x4E>(v); v = dpp_add<0x141>(v); v = dpp_add<0x140>(v);
  auto rr = __builtin_amdgcn_permlane16_swap(F2U(v), F2U(v), false, false);
  return U2F(rr[0]) + U2F(rr[1]);
}
typedef __attribute__((address_space(1))) unsigned long long gu64;

#define SX(b, h) (98304 + (b) * 32768 + (h) * 16384)
constexpr int SLOT_B = 0, SLOT_E = 32768, SLOT_A = 65536, SLOT_C = 98304, SLOT_D = 131072, K1_LDS = 163840;

constexpr int WS_HFLAG = 4096;
constexpr int WS_GFLAG = 65536;
constexpr int WS_OFLAG = 131072;
#define OFS 16
constexpr int WS_STATS = 1 << 20;
constexpr int WS_G = 5 << 20, WS_O = 9 << 20, WS_H = 25 << 20, WS_END = 39 << 20;
constexpr int K2_STG = 32768;

NOPK __global__ __launch_bounds__(512, 2) void mega_kernel(const char* __restrict__ xn, const char* __restrict__ kc, const char* __restrict__ vc,
                                                           const char* __restrict__ wq, const char* __restrict__ w1, const char* __restrict__ wo, const char* __restrict__ w2,
                                                           char* ws, char* __restrict__ out) {
  STAMP_DECL
  STAMP(0);
  extern __shared__ __attribute__((aligned(16))) char shm[];
  const lptr lds = (lptr)shm;
  const unsigned lds0 = (unsigned)(uintptr_t)lds;
  const int tid = TID, wid = __builtin_amdgcn_readfirstlane(tid >> 6), lane = tid & 63, wr = wid >> 2, wc = wid & 3;
  const int bid = BID, xcd = bid & 7, idx = bid >> 3, hd = 4 * xcd + (idx & 3), rb = idx >> 2;
  const int g = lane >> 4, t = lane & 15, q4 = (lane >> 2) & 3, p4 = lane & 3;

  const bool light = xcd >= 4;
  unsigned voffX[4], voffQ[2], voffH[2];
  {
    const int r = lane >> 3, c = lane & 7;
#pragma unroll
    for (int i = 0; i < 4; ++i) voffX[i] = (unsigned)((8 * i + r) * 2048 + ((c ^ (4 * (i & 1) + (r >> 1))) << 4) - i * 1024);
    const int k = 32 * (wid >> 2) + 16 * (wid & 1) + 4 * ((wid >> 1) & 1) + 8 * (r >> 2) + (r & 3);
    const int cw = c ^ (((r >> 1) & 3) << 1);
    voffQ[0] = (unsigned)(k * 8192 + cw * 16 + 1024); voffQ[1] = voffQ[0] + 128 - 1024;
    voffH[0] = (unsigned)(k * 7168 + 128 + (light ? (cw & 3) : cw) * 16 + 1024);
    voffH[1] = (unsigned)(k * 7168 + cw * 16);
  }
  const char* xb = xn + (size_t)rb * 256 * 2048 + wid * 65536;
  const char* qb = wq + hd * 256 - 1024;
  const char* hb = w1 + (light ? 4096 + 192 * (hd - 16) : 256 * hd) - 1024;
  const bool full = wr == 1 || !light;
#define GLDS2(v0_, v1_, sb_, ld_) asm volatile("s_mov_b32 m0, %3\n\ts_nop 0\n\tglobal_load_lds_dwordx4 %0, %2\n\tglobal_load_lds_dwordx4 %1, %2 offset:1024" \
    ::"v"(v0_), "v"(v1_), "s"(sb_), "s"(ld_) : "memory")
#define GLDS4(v0_, v1_, v2_, v3_, sb_, ld_) asm volatile("s_mov_b32 m0, %5\n\ts_nop 0\n\tglobal_load_lds_dwordx4 %0, %4\n\tglobal_load_lds_dwordx4 %1, %4 offset:1024\n\t" \
    "global_load_lds_dwordx4 %2, %4 offset:2048\n\tglobal_load_lds_dwordx4 %3, %4 offset:3072" ::"v"(v0_), "v"(v1_), "v"(v2_), "v"(v3_), "s"(sb_), "s"(ld_) : "memory")
#define ST_X(b, kt) GLDS4(voffX[0], voffX[1], voffX[2], voffX[3], xb + (kt) * 128, lds0 + SX(b, 0) + wid * 4096)
#define ST_W0(wo_, kt) GLDS2(voffQ[0], voffQ[1], qb + (size_t)(kt) * 524288, lds0 + (wo_) + wid * 2048)
#define ST_W1(wo_, kt) GLDS2(voffH[0], voffH[1], hb + (size_t)(kt) * 458752, lds0 + (wo_) + 16384 + wid * 2048)

  lptr wb[4], xr[2];
#pragma unroll
  for (int m = 0; m < 4; ++m) wb[m] = lds + wr * 1024 + (g >> 1) * 2048 + (4 * (g & 1) + q4) * 128 + 32 * ((m ^ wc) ^ (2 * (g & 1) + (q4 >> 1))) + 8 * p4;
#pragma unroll
  for (int ks = 0; ks < 2; ++ks) xr[ks] = lds + 98304 + (wc * 32 + t) * 128 + 16 * ((4 * ks + g) ^ (t >> 1));
#define LDW(dst, h) do { _Pragma("unroll") for (int m = 0; m < 4; ++m) _Pragma("unroll") for (int k = 0; k < 2; ++k) dst[m][k] = ldtr2(wb[m] + (h) * 16384 + k * 8192, 4096); } while (0)
#define LDX(dst, b, h) do { _Pragma("unroll") for (int n = 0; n < 2; ++n) _Pragma("unroll") for (int k = 0; k < 2; ++k) dst[n][k] = ldb128(xr[k] + (SX(b, h) - 98304) + n * 2048); } while (0)

  f32x4 acc[2][2][4][2];
#pragma unroll
  for (int a = 0; a < 2; ++a)
#pragma unroll
    for (int b = 0; b < 2; ++b)
#pragma unroll
      for (int m = 0; m < 4; ++m)
#pragma unroll
        for (int n = 0; n < 2; ++n) acc[a][b][m][n] = (f32x4){0.f, 0.f, 0.f, 0.f};
  bf16x8 At[4][2], B0[2][2], B1[2][2];

#define MMA2(ai) do { __builtin_amdgcn_s_setprio(1); \
    cs[ai] = MFMA16(At[0][0], ones, cs[ai]); \
    _Pragma("unroll") for (int k = 0; k < 2; ++k) _Pragma("unroll") for (int m = 0; m < 2; ++m) _Pragma("unroll") for (int n = 0; n < 2; ++n) { \
      acc[ai][0][m][n] = MFMA16(At[m][k], B0[n][k], acc[ai][0][m][n]); acc[ai][1][m][n] = MFMA16(At[m][k], B1[n][k], acc[ai][1][m][n]); } \
    cs[ai] = MFMA16(At[0][1], ones, cs[ai]); \
    if ((ai) == 0 || full) { _Pragma("unroll") for (int k = 0; k < 2; ++k) _Pragma("unroll") for (int m = 2; m < 4; ++m) _Pragma("unroll") for (int n = 0; n < 2; ++n) { \
      acc[ai][0][m][n] = MFMA16(At[m][k], B0[n][k], acc[ai][0][m][n]); acc[ai][1][m][n] = MFMA16(At[m][k], B1[n][k], acc[ai][1][m][n]); } } \
    __builtin_amdgcn_s_setprio(0); SCHED(); } while (0)
  char* const stats = ws + WS_STATS;
  const __amdgpu_buffer_rsrc_t wrs = __builtin_amdgcn_make_buffer_rsrc(ws, 0, WS_END, 0x00020000);
  f32x4 cs[2] = {{0.f, 0.f, 0.f, 0.f}, {0.f, 0.f, 0.f, 0.f}};
  bf16x8 ones = __builtin_bit_cast(bf16x8, (u32x4){0x3f803f80u, 0x3f803f80u, 0x3f803f80u, 0x3f803f80u});
#define PIN_SETUP() do { asm volatile("" : "+v"(ones), "+v"(cs[0]), "+v"(cs[1]), "+v"(wb[0]), "+v"(wb[1]), "+v"(wb[2]), "+v"(wb[3]), "+v"(xr[0]), "+v"(xr[1])); \
    _Pragma("unroll") for (int a = 0; a < 2; ++a) _Pragma("unroll") for (int b = 0; b < 2; ++b) \
      asm volatile("" : "+v"(acc[a][b][0][0]), "+v"(acc[a][b][0][1]), "+v"(acc[a][b][1][0]), "+v"(acc[a][b][1][1]), "+v"(acc[a][b][2][0]), "+v"(acc[a][b][2][1]), "+v"(acc[a][b][3][0]), "+v"(acc[a][b][3][1])); } while (0)
  unsigned tag;
  {
    if (wr == 0) {
      unsigned ep;
      ST_X(0, 0); ST_W0(0, 0);
      asm volatile("global_load_dword %0, %1, %2 sc1" : "=&v"(ep) : "v"(0), "s"(ws) : "memory");
      ST_W1(0, 0); ST_W0(32768, 1); ST_X(1, 1); ST_W1(32768, 1);
      PIN_SETUP();
      asm volatile("s_waitcnt vmcnt(10)" : "+v"(ep) :: "memory");
      tag = __builtin_amdgcn_readfirstlane(ep) + 1u;
      BAR();
    } else {
      const int row = rb * 256 + hd * 8 + wc * 2 + (lane >> 5);
      u32x4 a[4];
      unsigned ep;
      asm volatile("global_load_dword %0, %1, %2 sc1" : "=&v"(ep) : "v"(0), "s"(ws) : "memory");
      asm volatile("global_load_dwordx4 %0, %4, %5\n\tglobal_load_dwordx4 %1, %4, %5 offset:512\n\tglobal_load_dwordx4 %2, %4, %5 offset:1024\n\tglobal_load_dwordx4 %3, %4, %5 offset:1536"
                   : "=&v"(a[0]), "=&v"(a[1]), "=&v"(a[2]), "=&v"(a[3]) : "v"(row * 2048 + (lane & 31) * 16), "s"(xn) : "memory");
      ST_X(0, 0); ST_W0(0, 0);
      ST_W1(0, 0); ST_W0(32768, 1); ST_X(1, 1); ST_W1(32768, 1);
      PIN_SETUP();
      asm volatile("s_waitcnt vmcnt(10)" : "+v"(ep), "+v"(a[0]), "+v"(a[1]), "+v"(a[2]), "+v"(a[3]) :: "memory");
      BAR();
      tag = __builtin_amdgcn_readfirstlane(ep) + 1u;
      float v[32];
#pragma unroll
      for (int i = 0; i < 16; ++i) { v[2 * i] = bf2f(a[i >> 2][i & 3] & 0xffffu); v[2 * i + 1] = U2F(a[i >> 2][i & 3] & 0xffff0000u); }
      float s0 = 0.f, s1 = 0.f, s2 = 0.f, s3 = 0.f;
#pragma unroll
      for (int i = 0; i < 8; ++i) { s0 += v[i]; s1 += v[8 + i]; s2 += v[16 + i]; s3 += v[24 + i]; }
      const float mean = half_sum((s0 + s1) + (s2 + s3)) * (1.0f / 1024.0f);
      s0 = s1 = s2 = s3 = 0.f;
#pragma unroll
      for (int i = 0; i < 8; ++i) { const float d0 = v[i] - mean, d1 = v[8 + i] - mean, d2 = v[16 + i] - mean, d3 = v[24 + i] - mean; s0 += d0 * d0; s1 += d1 * d1; s2 += d2 * d2; s3 += d3 * d3; }
      const float sd = __builtin_amdgcn_sqrtf(half_sum((s0 + s1) + (s2 + s3)) * (1.0f / 1024.0f) + 1e-5f);
      if ((lane & 31) < 2) __hip_atomic_store((gu64*)(stats + row * 16 + (lane & 1) * 8), ((unsigned long long)tag << 32) | F2U((lane & 1) ? sd : mean), __ATOMIC_RELAXED, __HIP_MEMORY_SCOPE_AGENT);
    }
  }
  if (wr == 1) BAR();
  SCHED();
  STAMP(1);
  const int kvg = hd >> 3;
  const char* kb_g = kc + ((size_t)kvg * 2048 + 1536) * 256;
  const char* vb_g = vc + ((size_t)kvg * 2048 + 1536) * 256;
#define VOFFK ((unsigned)(((lane >> 2) & 7) * 256 + (4 * (lane >> 5) + ((lane & 3) ^ ((2 * (wid & 1) + ((lane >> 4) & 1)) & 3))) * 16))
#define VOFFV ((unsigned)(((lane >> 2) & 7) * 256 + (lane >> 5) * 64 + (lane & 3) * 16))
#define ST_KV8(voff, gsrc, slot) do { glds(voff, (gsrc) + wid * 2048, lds0 + (slot) + wid * 2048); glds(voff, (gsrc) + wid * 2048 + 128, lds0 + (slot) + wid * 2048 + 1024); } while (0)
  int wcur = 0, wprv = 65536;
#define W_ADV() do { const int nx_ = (wcur == 65536) ? 0 : wcur + 32768, d_ = nx_ - wcur; wprv = wcur; wcur = nx_; \
    _Pragma("unroll") for (int f = 0; f < 4; ++f) wb[f] += d_; } while (0)
#define PH_A(b, STAGE) do { LDX(B0, b, 0); LDX(B1, b, 1); SCHED(); LDW(At, 0); STAGE; WAIT_V(10); WAIT_L(12); BAR(); SCHED(); MMA2(0); BAR(); SCHED(); } while (0)
#define PH_B(STAGE) do { LDW(At, 1); STAGE; W_ADV(); WAIT_V(10); BAR(); SCHED(); MMA2(1); WAIT_L(0); BAR(); SCHED(); } while (0)
#pragma unroll 1
  for (int kt = 0; kt < 14; kt += 2) {
    PH_A(0, ST_W0(wprv, kt + 2));
    PH_B(ST_X(0, kt + 2); ST_W1(wprv, kt + 2));
    PH_A(1, ST_W0(wprv, kt + 3));
    PH_B(ST_X(1, kt + 3); ST_W1(wprv, kt + 3));
  }
  STAMP(2);
  const unsigned voffK = VOFFK, voffV = VOFFV;
  PH_A(0, ST_KV8(voffK, kb_g, SLOT_E));
  PH_B(ST_KV8(voffK, kb_g + 16384, SLOT_E + 16384); ST_KV8(voffV, vb_g, SLOT_C); ST_KV8(voffV, vb_g + 16384, SLOT_C + 16384));
  const unsigned soff = (unsigned)((rb * 256 + (g >> 1) * 128 + wc * 32 + (g & 1) * 16 + t) * 16);
  u32x4 sg;
  LDX(B0, 1, 0); LDX(B1, 1, 1); SCHED(); LDW(At, 0);
  asm volatile("global_load_dwordx4 %0, %1, %2 sc1" : "=&v"(sg) : "v"(soff), "s"(stats) : "memory");
  ST_KV8(voffK, kb_g + 32768, SLOT_A);
  WAIT_V(11); BAR(); SCHED(); MMA2(0); BAR(); SCHED();
  LDW(At, 1);
  ST_KV8(voffK, kb_g + 49152, SLOT_A + 16384);
  BAR(); SCHED(); MMA2(1);
  {
    const lptr cw = lds + SLOT_D + (t & 3) * 8192 + wr * 4096 + wc * 64 + g * 16;
    *(LAS f32x4*)cw = cs[0]; *(LAS f32x4*)(cw + 256) = cs[1];
  }
  asm volatile("s_waitcnt vmcnt(4)" : "+v"(sg) :: "memory");
  WAIT_L(0); BAR(); SCHED();
  f32x4 cv[2][4];
#pragma unroll
  for (int a = 0; a < 2; ++a)
#pragma unroll
    for (int m = 0; m < 4; ++m) cv[a][m] = *(const LAS f32x4*)(lds + SLOT_D + wc * 8192 + wr * 4096 + a * 256 + (m ^ wc) * 64 + g * 16);
  if (__builtin_expect(__builtin_amdgcn_ballot_w64(sg[1] != tag || sg[3] != tag) != 0ull, 0)) {
    for (unsigned spins = 0; spins < 4000000u; ++spins) {
      __builtin_amdgcn_s_sleep(2);
      asm volatile("" ::: "memory");
      sg = __builtin_amdgcn_raw_buffer_load_b128(wrs, soff, WS_STATS, 16);
      if (__builtin_amdgcn_ballot_w64(sg[1] != tag || sg[3] != tag) == 0ull) break;
    }
  }
  float mu[2][2], rs[2][2], sd[2][2];
  {
    auto m16 = __builtin_amdgcn_permlane16_swap(sg[0], sg[0], false, false);
    auto s16 = __builtin_amdgcn_permlane16_swap(sg[2], sg[2], false, false);
#pragma unroll
    for (int n = 0; n < 2; ++n) {
      auto m32 = __builtin_amdgcn_permlane32_swap(m16[n], m16[n], false, false);
      auto s32 = __builtin_amdgcn_permlane32_swap(s16[n], s16[n], false, false);
#pragma unroll
      for (int bj = 0; bj < 2; ++bj) { mu[bj][n] = -U2F(m32[bj]); sd[bj][n] = U2F(s32[bj]); rs[bj][n] = __builtin_amdgcn_rcpf(sd[bj][n]) * 1.4426950408889634f; }
    }
  }
  WAIT_L(0);
  STAMP(3);
  {
    const lptr qw = lds + wc * 8192 + wr * 4096 + ((g >> 1) * 32 + t) * 16 + (g & 1) * 8;
#pragma unroll
    for (int bj = 0; bj < 2; ++bj) {
      lptr qwb = qw + (bj ? SLOT_D : SLOT_B);
      asm volatile("" : "+v"(qwb));
#pragma unroll
      for (int m = 0; m < 4; ++m)
#pragma unroll
        for (int n = 0; n < 2; ++n) {
          const f32x4 v = (acc[0][bj][m][n] + cv[0][m] * mu[bj][n]) * (rs[bj][n] * 0.08838834764831845f);
          u32x2 w = {pk2(v[0], v[1]), pk2(v[2], v[3])};
          *(LAS u32x2*)(qwb + (m ^ wc) * 1024 + n * 256) = w;
        }
    }
  }
  WAIT_L(0); BAR(); SCHED();
  STAMP(4);
  bf16x8 qf[8];
  const lptr qr = lds + (wr ? SLOT_D : SLOT_B) + wc * 8192 + lane * 16;
  if (wr == 1) {
#pragma unroll
    for (int s = 0; s < 8; ++s) qf[s] = ldb128(qr + s * 1024);
  }
  {
#define ST128(...) ST128_(wrs, __VA_ARGS__)
    u32x2 hw[2][2][4];
#define SILU16(m) do { \
      const f32x4 a0 = acc[1][0][m][0] + cv[1][m] * mu[0][0], a1 = acc[1][0][m][1] + cv[1][m] * mu[0][1], a2 = acc[1][1][m][0] + cv[1][m] * mu[1][0], a3 = acc[1][1][m][1] + cv[1][m] * mu[1][1]; \
      f32x4 e0 = a0 * -rs[0][0], e1 = a1 * -rs[0][1], e2 = a2 * -rs[1][0], e3 = a3 * -rs[1][1]; SCHED(); \
      _Pragma("unroll") for (int i = 0; i < 4; ++i) { e0[i] = __builtin_amdgcn_exp2f(e0[i]); e1[i] = __builtin_amdgcn_exp2f(e1[i]); e2[i] = __builtin_amdgcn_exp2f(e2[i]); e3[i] = __builtin_amdgcn_exp2f(e3[i]); } SCHED(); \
      e0 = e0 * sd[0][0] + sd[0][0]; e1 = e1 * sd[0][1] + sd[0][1]; e2 = e2 * sd[1][0] + sd[1][0]; e3 = e3 * sd[1][1] + sd[1][1]; SCHED(); \
      _Pragma("unroll") for (int i = 0; i < 4; ++i) { e0[i] = __builtin_amdgcn_rcpf(e0[i]); e1[i] = __builtin_amdgcn_rcpf(e1[i]); e2[i] = __builtin_amdgcn_rcpf(e2[i]); e3[i] = __builtin_amdgcn_rcpf(e3[i]); } SCHED(); \
      e0 *= a0; e1 *= a1; e2 *= a2; e3 *= a3; SCHED(); \
      hw[0][0][m] = (u32x2){pk2(e0[0], e0[1]), pk2(e0[2], e0[3])}; hw[0][1][m] = (u32x2){pk2(e1[0], e1[1]), pk2(e1[2], e1[3])}; \
      hw[1][0][m] = (u32x2){pk2(e2[0], e2[1]), pk2(e2[2], e2[3])}; hw[1][1][m] = (u32x2){pk2(e3[0], e3[1]), pk2(e3[2], e3[3])}; SCHED(); } while (0)
    SILU16(0);
    if (wr == 0) {
      BAR(); SCHED();
#pragma unroll
      for (int s = 0; s < 8; ++s) qf[s] = ldb128(qr + s * 1024);
    }
    SILU16(1);
    const unsigned hoff = (unsigned)((rb * 16 + wc * 2) * 2048 + lane * 16);
    const int pb = (light ? 16 + 3 * hd : 4 * hd) + (wr ? 0 : 2), sw = full ? (wc >> 1) : 0, p0 = pb + sw, p1 = pb + (sw ^ 1);
#define H_OUT1(p_, a_, b_) do { _Pragma("unroll") for (int bj = 0; bj < 2; ++bj) _Pragma("unroll") for (int n = 0; n < 2; ++n) \
      ST128((u32x4){hw[bj][n][a_][0], hw[bj][n][a_][1], hw[bj][n][b_][0], hw[bj][n][b_][1]}, hoff + n * 2048, WS_H + ((p_) >> 1) * 262144 + ((p_) & 1) * 1024 + bj * 16384); } while (0)
#define H_OUT(p_, m_) do { if (wc & 1) H_OUT1(p_, (m_) + 1, m_); else H_OUT1(p_, m_, (m_) + 1); } while (0)
    H_OUT(p0, 0);
    if (full) { SILU16(2); SILU16(3); H_OUT(p1, 2); }
  }
  STAMP(5);
  if (wr == 1) { WAIT_L(0); BAR(); }
  SCHED();
  STAMP(6);
  const int r32 = lane & 31, hh = lane >> 5;
  lptr kr[2];
#pragma unroll
  for (int e = 0; e < 2; ++e) kr[e] = lds + 2048 * (r32 >> 3) + 64 * (r32 & 7) + 16 * ((2 * e + hh) ^ ((r32 >> 2) & 3));
  const lptr vr = lds + ((lane & 3) << 3) + (((lane >> 2) & 3) << 6) + (((lane >> 4) & 1) << 5) + (hh << 8);
  f32x16 o[4], s[4];
#pragma unroll
  for (int d = 0; d < 4; ++d)
#pragma unroll
    for (int i = 0; i < 16; ++i) o[d][i] = 0.f;
  bf16x8 pf[8];
  constexpr int RD = 6;
  bf16x8 fr[RD];
  float lrun = 0.f;
  f32x16 negm = {0.f, 0.f, 0.f, 0.f, 0.f, 0.f, 0.f, 0.f, 0.f, 0.f, 0.f, 0.f, 0.f, 0.f, 0.f, 0.f};
#define SM_THR 60.0f
#define FRAG(i) ((i) < 32 ? ldtr2(v0 + (((i) >> 2) * 8 + ((i) & 3)) * 512, 2048) : ldb128((((((i) - 32) >> 2) & 1) ? k1 : k0) + ((i) & 3) * 8192 + (((i) - 32) >> 3) * 512))
#define XPRIO 1
#define YPRIO 0
#define XSEG(lo, hi) do { __builtin_amdgcn_s_setprio(XPRIO); \
    _Pragma("unroll") for (int i = (lo); i < (hi); ++i) { \
      if (i < 32) o[i & 3] = MFMA32(fr[i % RD], pf[(i >> 2) & 7], o[i & 3]); \
      else s[i & 3] = MFMA32(fr[i % RD], qf[((i - 32) >> 2) & 7], i < 36 ? negm : s[i & 3]); \
      if (i + RD < (hi)) fr[i % RD] = FRAG(i + RD); \
      SCHED(); } \
    __builtin_amdgcn_s_setprio(YPRIO); } while (0)
#define PREF(lo) do { _Pragma("unroll") for (int i = (lo); i < (lo) + RD; ++i) fr[i % RD] = FRAG(i); SCHED(); } while (0)
#define MX3(a, b, c) __builtin_fmaxf(__builtin_fmaxf((a), (b)), (c))
#define SOFTMAX() do { \
      float m0 = MX3(s[0][0], s[0][1], s[0][2]), m1 = MX3(s[1][0], s[1][1], s[1][2]), m2 = MX3(s[2][0], s[2][1], s[2][2]), m3 = MX3(s[3][0], s[3][1], s[3][2]); \
      _Pragma("unroll") for (int i = 3; i < 15; i += 2) { m0 = MX3(m0, s[0][i], s[0][i + 1]); m1 = MX3(m1, s[1][i], s[1][i + 1]); m2 = MX3(m2, s[2][i], s[2][i + 1]); m3 = MX3(m3, s[3][i], s[3][i + 1]); } \
      m0 = MX3(m0, s[0][15], s[1][15]); m2 = MX3(m2, s[2][15], s[3][15]); \
      float mx = __builtin_fmaxf(MX3(m0, m1, m2), m3); \
        \
      if (__builtin_expect(__builtin_amdgcn_ballot_w64(!(mx <= SM_THR)) != 0ull, 0)) { \
        auto rr = __builtin_amdgcn_permlane32_swap(F2U(mx), F2U(mx), false, false); \
        const float dl = MX3(U2F(rr[0]), U2F(rr[1]), 0.f); \
        _Pragma("unroll") for (int kb = 0; kb < 4; ++kb) s[kb] -= dl; \
        negm -= dl; \
        const float alpha = __builtin_amdgcn_exp2f(-dl); lrun *= alpha; _Pragma("unroll") for (int d = 0; d < 4; ++d) o[d] *= alpha; \
      } \
      SCHED(); \
      float l0 = lrun, l1 = 0.f; \
      _Pragma("unroll") for (int kb = 0; kb < 4; ++kb) { \
        _Pragma("unroll") for (int e = 0; e < 2; ++e) { \
          _Pragma("unroll") for (int i = 0; i < 8; ++i) s[kb][8 * e + i] = __builtin_amdgcn_exp2f(s[kb][8 * e + i]); \
          _Pragma("unroll") for (int i = 0; i < 8; i += 2) { l0 += s[kb][8 * e + i]; l1 += s[kb][8 * e + i + 1]; } \
          u32x4 w = {pk2(s[kb][8 * e], s[kb][8 * e + 1]), pk2(s[kb][8 * e + 2], s[kb][8 * e + 3]), pk2(s[kb][8 * e + 4], s[kb][8 * e + 5]), pk2(s[kb][8 * e + 6], s[kb][8 * e + 7])}; \
          pf[2 * kb + e] = __builtin_bit_cast(bf16x8, w); } \
        asm volatile("" : "+v"(pf[2 * kb]), "+v"(pf[2 * kb + 1]), "+v"(l0), "+v"(l1)); \
        SCHED(); } \
      lrun = l0 + l1; \
      asm volatile("" : "+v"(lrun)); } while (0)
  {
    const lptr k0 = kr[0] + SLOT_E, k1 = kr[1] + SLOT_E, v0 = vr;
    {
      bf16x8 kf[8];
#pragma unroll
      for (int st = 0; st < 8; ++st) kf[st] = FRAG(32 + 4 * st);
      PREF(32);
#pragma unroll
      for (int st = 0; st < 8; ++st) negm = MFMA32(kf[st], qf[st], negm);
      float m0 = MX3(negm[0], negm[1], negm[2]), m1 = MX3(negm[3], negm[4], negm[5]);
      m0 = MX3(m0, negm[6], negm[7]); m1 = MX3(m1, negm[8], negm[9]); m0 = MX3(m0, negm[10], negm[11]); m1 = MX3(m1, negm[12], negm[13]);
      m0 = __builtin_fmaxf(MX3(m0, negm[14], negm[15]), m1);
      auto rr = __builtin_amdgcn_permlane32_swap(F2U(m0), F2U(m0), false, false);
      m0 = -__builtin_fmaxf(U2F(rr[0]), U2F(rr[1]));
#pragma unroll
      for (int i = 0; i < 16; ++i) negm[i] = m0;
      asm volatile("" : "+v"(negm));
      SCHED();
    }
    XSEG(32, 64);
  }
  STAMP(7);
  const char* kvsrc = (wr ? kb_g + 65536 : vb_g + 32768) + wc * 8192 - 1024;
  unsigned kvo[2][2];
#pragma unroll
  for (int par = 0; par < 2; ++par) {
    const int xx = wr ? ((lane & 3) ^ ((2 * par + ((lane >> 4) & 1)) & 3)) : (lane & 3);
    kvo[par][0] = (unsigned)(((lane >> 2) & 7) * 256 + (lane >> 5) * 64 + xx * 16 + 1024);
    kvo[par][1] = kvo[par][0] + 128 - 1024;
  }
  const unsigned pfo = (unsigned)((lane < 21 ? lane : 20) * 128);
  unsigned pft = 0;
#pragma unroll 1
  for (int c = 0; c < 3; ++c) {
    if (c == 0) WAIT_V(4); else if (c == 1) WAIT_V(0); else WAIT_V(1);
    WAIT_L(0); BAR(); SCHED();
    if (c == 0) STAMP(8); else if (c == 1) STAMP(12);
    if (c == 1 && wid == 4 && lane == 0) __builtin_amdgcn_raw_buffer_store_b32(tag, wrs, 0, WS_HFLAG + (rb * 32 + hd) * 128, 16);
    if (c + wr < 3) {
      const int slot = wr ? (c == 0 ? SLOT_B : SLOT_C) : (c == 0 ? SLOT_D : (c == 1 ? SLOT_E : SLOT_A));
#pragma unroll
      for (int hf = 0; hf < 2; ++hf)
        asm volatile("s_mov_b32 m0, %5\n\ts_nop 0\n\t"
                     "global_load_lds_dwordx4 %0, %4\n\tglobal_load_lds_dwordx4 %1, %4 offset:1024\n\t"
                     "global_load_lds_dwordx4 %2, %4 offset:2048\n\tglobal_load_lds_dwordx4 %3, %4 offset:3072"
                     ::"v"(kvo[0][0]), "v"(kvo[0][1]), "v"(kvo[1][0]), "v"(kvo[1][1]), "s"(kvsrc + c * 32768 + hf * 4096), "s"(lds0 + slot + wc * 8192 + hf * 4096) : "memory");
    }
    {
      const int sl = (bid * 8 + wid) * 3 + c;
      const int o1 = sl * 2688, o2 = (sl - 3121) * 2688;
      const char* pp = sl < 3121 ? wo + (o1 < 8385920 ? o1 : 8385920) : w2 + (o2 < 7337344 ? o2 : 7337344);
      asm volatile("global_load_dword %0, %1, %2" : "+v"(pft) : "v"(pfo), "s"(pp) : "memory");
    }
    SOFTMAX();
    const int kslot = (c == 0) ? SLOT_A : ((c == 1) ? SLOT_B : SLOT_C);
    const int vslot = (c == 0) ? SLOT_C : ((c == 1) ? SLOT_D : SLOT_E);
    const lptr k0 = kr[0] + kslot, k1 = kr[1] + kslot, v0 = vr + vslot;
    PREF(0);
    if (c == 0) STAMP(9); else if (c == 1) STAMP(13);
    BAR(); SCHED();
    if (c == 0) STAMP(10); else if (c == 1) STAMP(14);
    XSEG(0, 64);
    if (c == 0) STAMP(11); else if (c == 2) STAMP(15);
  }
  WAIT_V(1); WAIT_L(0); BAR(); SCHED();
  STAMP(16);
  SOFTMAX();
  {
    const lptr k0 = kr[0], k1 = kr[1], v0 = vr + SLOT_A;
    PREF(0);
    STAMP(17);
    if (wr == 0) BAR();
    SCHED();
    STAMP(18);
    XSEG(0, 32);
  }
  STAMP(19);
  int lane2 = __builtin_amdgcn_mbcnt_hi(~0u, __builtin_amdgcn_mbcnt_lo(~0u, 0u));
  asm volatile("" : "+v"(lane2));
#define lane lane2
  const int isG = xcd >> 2, j2 = xcd & 3, tm = 8 * (j2 >> 1) + (idx & 7), tn = 4 * (j2 & 1) + (idx >> 3), nt = isG ? 56 : 64;
#define O_OUT() do { \
    auto rr = __builtin_amdgcn_permlane32_swap(F2U(lrun), F2U(lrun), false, false); \
    const float inv = __builtin_amdgcn_rcpf(U2F(rr[0]) + U2F(rr[1])); \
    const unsigned oo = (unsigned)((rb * 16 + wr * 8 + wc * 2 + (r32 >> 4)) * 2048 + (hh * 16 + (r32 & 15)) * 16); \
    _Pragma("unroll") for (int d = 0; d < 4; ++d) _Pragma("unroll") for (int e = 0; e < 2; ++e) { \
        u32x4 w = {pk2(o[d][8 * e] * inv, o[d][8 * e + 1] * inv), pk2(o[d][8 * e + 2] * inv, o[d][8 * e + 3] * inv), \
                   pk2(o[d][8 * e + 4] * inv, o[d][8 * e + 5] * inv), pk2(o[d][8 * e + 6] * inv, o[d][8 * e + 7] * inv)}; \
        ST128(w, oo + (d & 1) * 1024 + e * 512, WS_O + (2 * hd + (d >> 1)) * 262144); } \
    asm volatile("s_waitcnt vmcnt(0)" :: "v"(pft) : "memory"); } while (0)
#define POLL32(off_, slp) do { for (unsigned spins = 0; spins < 2000000u; ++spins) { \
      if (__builtin_amdgcn_ballot_w64(__builtin_amdgcn_raw_buffer_load_b32(wrs, (off_), 0, 16) != tag) == 0ull) break; \
      __builtin_amdgcn_s_sleep(slp); asm volatile("" ::: "memory"); } \
    asm volatile("" ::: "memory"); } while (0)
  if (wr == 0) {
    const char* ab = ws + (isG ? WS_H : WS_O) + tm * 16384 + wc * 4096;
    const char* wbp = (isG ? w2 : wo) + tn * 256 - 1024 + (wc >> 1) * 65536 + (wc & 1) * (isG ? 32768 : 16384);
    const unsigned voffA = lane * 16;
    unsigned voffW[2][2];
    {
      const int r = lane >> 3, c = lane & 7;
      voffW[0][0] = (unsigned)(r * 2048 + ((c ^ (((r >> 1) & 3) << 1)) << 4) + 1024);
      voffW[0][1] = voffW[0][0] + 128 - 1024;
      voffW[1][0] = voffW[0][0] + (isG ? 16384 : 32768) - 2048;
      voffW[1][1] = voffW[1][0] + 128 - 1024;
    }
#define K2_ISSUE(p_, buf_) do { const int k_ = isG ? ((p_) + 14 * j2 >= 56 ? (p_) + 14 * j2 - 56 : (p_) + 14 * j2) : ((((p_) + 8 * j2) & 31) | (~(p_) & 32)); \
      const unsigned d_ = lds0 + (buf_) * K2_STG + wc * 4096; \
      asm volatile("s_mov_b32 m0, %2\n\ts_nop 0\n\tglobal_load_lds_dwordx4 %0, %1 sc1\n\tglobal_load_lds_dwordx4 %0, %1 offset:1024 sc1\n\t" \
                   "global_load_lds_dwordx4 %0, %1 offset:2048 sc1\n\tglobal_load_lds_dwordx4 %0, %1 offset:3072 sc1" \
                   ::"v"(voffA), "s"(ab + (size_t)k_ * 262144), "s"(d_) : "memory"); \
      asm volatile("s_mov_b32 m0, %5\n\ts_nop 0\n\tglobal_load_lds_dwordx4 %0, %4\n\tglobal_load_lds_dwordx4 %1, %4 offset:1024\n\t" \
                   "global_load_lds_dwordx4 %2, %4 offset:2048\n\tglobal_load_lds_dwordx4 %3, %4 offset:3072" \
                   ::"v"(voffW[0][0]), "v"(voffW[0][1]), "v"(voffW[1][0]), "v"(voffW[1][1]), "s"(wbp + (size_t)k_ * 131072), "s"(d_ + 16384) : "memory"); } while (0)
    const unsigned f1 = isG ? (unsigned)(WS_HFLAG + ((tm >> 1) * 32 + (lane & 31)) * 128) : (unsigned)(WS_OFLAG + ((tm >> 1) * 32 + 16 + (lane & 15)) * OFS);
    const unsigned f2 = (unsigned)(WS_OFLAG + ((tm >> 1) * 32 + (lane & 15)) * OFS);
    O_OUT();
    const bool early = !isG && __builtin_amdgcn_ballot_w64(__builtin_amdgcn_raw_buffer_load_b32(wrs, f1, 0, 16) != tag) == 0ull;
    asm volatile("" ::: "memory");
    if (early) { K2_ISSUE(0, 3); K2_ISSUE(1, 4); K2_ISSUE(2, 0); K2_ISSUE(3, 1); }
    STAMP(20);
    BAR();
    if (wid == 0 && lane == 0) __builtin_amdgcn_raw_buffer_store_b32(tag, wrs, 0, WS_OFLAG + (rb * 32 + hd) * OFS, 16);
    if (!early) { POLL32(f1, 4); K2_ISSUE(0, 3); K2_ISSUE(1, 4); K2_ISSUE(2, 0); K2_ISSUE(3, 1); }
    STAMP(21);
    WAIT_V(24); BAR();
    STAMP(22);
    int buf = 2;
    unsigned pol = 0;
#pragma unroll 1
    for (int b = 0; b < nt - 4; ++b) {
      WAIT_V(16); BAR();
      if (b == 24) asm volatile("buffer_load_dword %0, %1, %2, 0 offen sc1" : "+v"(pol) : "v"(f2), "s"(wrs) : "memory");
      if (b == 28 && !isG) {
        asm volatile("" : "+v"(pol));
        if (__builtin_amdgcn_ballot_w64(pol != tag) != 0ull) POLL32(f2, 4);
      }
      K2_ISSUE(b + 4, buf);
      buf = (buf == 4) ? 0 : buf + 1;
    }
    WAIT_V(16); BAR();
    WAIT_V(8); BAR();
    WAIT_V(0); BAR();
    const int c8 = lane & 15, r0 = wc * 4 + (lane >> 4);
    const unsigned goff = (unsigned)((tm * 128 + r0) * 2048 + tn * 256 + c8 * 16);
    u32x4 xv[8], gv[8];
    if (!isG) {
#pragma unroll
      for (int i = 0; i < 8; ++i) xv[i] = *(const u32x4*)(xn + goff + i * 32768);
    }
    BAR();
    if (!isG) {
      for (unsigned spins = 0; spins < 4000000u; ++spins) {
        if (__builtin_amdgcn_readfirstlane(__builtin_amdgcn_raw_buffer_load_b32(wrs, 0, WS_GFLAG + (tm * 8 + tn) * 128, 16)) == tag) break;
        __builtin_amdgcn_s_sleep(1);
        asm volatile("" ::: "memory");
      }
      asm volatile("" ::: "memory");
#pragma unroll
      for (int i = 0; i < 8; ++i) gv[i] = __builtin_amdgcn_raw_buffer_load_b128(wrs, goff + i * 32768, WS_G, 16);
    }
    STAMP(23);
    BAR();
    BAR();
#pragma unroll
    for (int i = 0; i < 8; ++i) {
      const int row = r0 + 16 * i;
      const lptr p = lds + row * 512;
      const f32x4 a0 = *(const LAS f32x4*)(p + (((2 * c8) ^ (row & 7)) << 4)), a1 = *(const LAS f32x4*)(p + (((2 * c8 + 1) ^ (row & 7)) << 4));
      u32x4 w;
      if (isG) {
        w = (u32x4){pk2(silu(a0[0]), silu(a0[1])), pk2(silu(a0[2]), silu(a0[3])), pk2(silu(a1[0]), silu(a1[1])), pk2(silu(a1[2]), silu(a1[3]))};
        ST128(w, goff + i * 32768, WS_G);
      } else {
#pragma unroll
        for (int e = 0; e < 4; ++e) {
          const float s0 = e < 2 ? a0[2 * e] : a1[2 * e - 4], s1 = e < 2 ? a0[2 * e + 1] : a1[2 * e - 3];
          w[e] = pk2(bf2f(xv[i][e] & 0xffffu) + s0 * bf2f(gv[i][e] & 0xffffu), U2F(xv[i][e] & 0xffff0000u) + s1 * U2F(gv[i][e] & 0xffff0000u));
        }
        *(u32x4*)(out + goff + i * 32768) = w;
      }
    }
    if (isG) {
      WAIT_V(0); BAR();
      if (wid == 0 && lane == 0) __builtin_amdgcn_raw_buffer_store_b32(tag, wrs, 0, WS_GFLAG + (tm * 8 + tn) * 128, 16);
      if (bid == 4 && wid == 0) {
        for (unsigned spins = 0; spins < 2000000u; ++spins) {
          bool ok = true;
#pragma unroll
          for (int i = 0; i < 4; ++i) ok = ok && __builtin_amdgcn_raw_buffer_load_b32(wrs, (unsigned)((lane + i * 64) * 128), WS_HFLAG, 16) == tag;
          if (__builtin_amdgcn_ballot_w64(!ok) == 0ull) break;
          __builtin_amdgcn_s_sleep(4);
          asm volatile("" ::: "memory");
        }
        asm volatile("" ::: "memory");
        if (lane == 0) __builtin_amdgcn_raw_buffer_store_b32(tag, wrs, 0, 0, 16);
      }
    }
  } else {
    O_OUT();
    STAMP(20);
    BAR();
    STAMP(21);
    const int wm = wc >> 1, wn = wc & 1, g = lane >> 4, t = lane & 15, q4 = (lane >> 2) & 3, p4 = lane & 3;
    lptr w2b[4];
#pragma unroll
    for (int ft = 0; ft < 4; ++ft) w2b[ft] = lds + 3 * K2_STG + 16384 + wn * 1024 + (g >> 1) * 2048 + (4 * (g & 1) + q4) * 128 + 32 * (ft ^ (2 * (g & 1) + (q4 >> 1))) + 8 * p4;
    lptr ar = lds + 3 * K2_STG + wm * 8192 + lane * 16;
    f32x4 ac2[4][4];
#pragma unroll
    for (int m = 0; m < 4; ++m)
#pragma unroll
      for (int n = 0; n < 4; ++n) ac2[m][n] = (f32x4){0.f, 0.f, 0.f, 0.f};
    bf16x8 Af[2][4];
    int dlt = 0;
    bf16x4 Wh[2][4][2];
#define K2_RD(set, ks, i) do { if ((i) == 0) Af[set][0] = ldb128(ar + (ks) * 1024); else if ((i) >= 3 && (i) < 6) Af[set][(i) - 2] = ldb128(ar + ((i) - 2) * 2048 + (ks) * 1024); \
      else { const int f_ = (i) < 3 ? 0 : ((i) - 4) >> 1, h_ = (i) < 3 ? (i) - 1 : (i) & 1; \
             Wh[set][f_][h_] = __builtin_amdgcn_ds_read_tr16_b64_v4bf16((LAS bf16x4*)(w2b[f_] + (ks) * 8192 + h_ * 4096)); } } while (0)
#define K2_W(set, m) __builtin_shufflevector(Wh[set][m][0], Wh[set][m][1], 0, 1, 2, 3, 4, 5, 6, 7)
    BAR();
    STAMP(22);
#pragma unroll
    for (int i = 0; i < 12; ++i) { K2_RD(0, 0, i); SCHED(); }
    int buf = 3;
#pragma unroll 1
    for (int kt = 0; kt < nt; ++kt) {
      SCHED();
#pragma unroll
      for (int i = 0; i < 16; ++i) {
        ac2[i >> 2][i & 3] = MFMA16(K2_W(0, i >> 2), Af[0][i & 3], ac2[i >> 2][i & 3]);
        if (i < 12) K2_RD(1, 1, i);
        if (i == 12) { dlt = (buf == 4) ? -4 * K2_STG : K2_STG; buf = (buf == 4) ? 0 : buf + 1; ar += dlt; asm volatile("" : "+v"(ar)); }
        if (i == 13) { w2b[0] += dlt; w2b[1] += dlt; asm volatile("" : "+v"(w2b[0]), "+v"(w2b[1])); }
        if (i == 14) { w2b[2] += dlt; w2b[3] += dlt; asm volatile("" : "+v"(w2b[2]), "+v"(w2b[3])); }
        if (i == 15) BAR();
        SCHED();
      }
#pragma unroll
      for (int i = 0; i < 16; ++i) {
        ac2[i >> 2][i & 3] = MFMA16(K2_W(1, i >> 2), Af[1][i & 3], ac2[i >> 2][i & 3]);
        if (i < 12) K2_RD(0, 0, i);
        SCHED();
      }
    }
    STAMP(23);
    WAIT_L(0); BAR();
    const lptr cw = lds + (wm * 64 + t) * 512;
#pragma unroll
    for (int m = 0; m < 4; ++m)
#pragma unroll
      for (int n = 0; n < 4; ++n) *(LAS f32x4*)(cw + n * 8192 + (((wn * 16 + m * 4 + g) ^ (t & 7)) << 4)) = ac2[m][n];
    WAIT_L(0); BAR();
    if (isG) BAR();
  }
  STAMP(24);
  STAMP_OUT(ws + (57 << 20), 30);
#undef lane
}

extern "C" void kernel_launch(void* const* d_in, const int* in_sizes, int n_in, void* d_out, int out_size, void* d_ws, size_t ws_size, hipStream_t stream) {
  const char* x = (const char*)d_in[0]; const char* kc = (const char*)d_in[1]; const char* vc = (const char*)d_in[2];
  const char* wq = (const char*)d_in[3]; const char* wo = (const char*)d_in[4]; const char* w1 = (const char*)d_in[6]; const char* w2 = (const char*)d_in[7];
  (void)hipFuncSetAttribute((const void*)mega_kernel, hipFuncAttributeMaxDynamicSharedMemorySize, K1_LDS);
  mega_kernel<<<256, 512, K1_LDS, stream>>>(x, kc, vc, wq, w1, wo, w2, (char*)d_ws, (char*)d_out);
}
```

```cpp
#include <hip/hip_runtime.h>
#include <stdint.h>

typedef unsigned short u16;
typedef __bf16 bf16x2 __attribute__((ext_vector_type(2)));
typedef __bf16 bf16x4 __attribute__((ext_vector_type(4)));
typedef __bf16 bf16x8 __attribute__((ext_vector_type(8)));
typedef float f32x2 __attribute__((ext_vector_type(2)));
typedef float f32x4 __attribute__((ext_vector_type(4)));
typedef float f32x16 __attribute__((ext_vector_type(16)));
typedef unsigned u32x2 __attribute__((ext_vector_type(2)));
typedef unsigned u32x4 __attribute__((ext_vector_type(4)));
#define LAS __attribute__((address_space(3)))
typedef LAS char* lptr;
#if defined(__HIP_DEVICE_COMPILE__) && !defined(USEPK)
#define NOPK __attribute__((target("no-packed-fp32-ops")))
#else
#define NOPK
#endif
#define DI NOPK __device__ __forceinline__
#define F2U(x) __builtin_bit_cast(unsigned, (float)(x))
#define U2F(x) __builtin_bit_cast(float, (unsigned)(x))
#define TID ((int)__builtin_amdgcn_workitem_id_x())
#define BID ((int)__builtin_amdgcn_workgroup_id_x())
#define SCHED() __builtin_amdgcn_sched_barrier(0)
#define BAR() asm volatile("s_barrier" ::: "memory")
#define WAIT_V(n) asm volatile("s_waitcnt vmcnt(%0)" :: "n"(n) : "memory")
#define WAIT_L(n) do { asm volatile("" ::: "memory"); __builtin_amdgcn_s_waitcnt(0xC07F | ((n) << 8)); asm volatile("" ::: "memory"); } while (0)
#define MFMA16(a, b, c) __builtin_amdgcn_mfma_f32_16x16x32_bf16(a, b, c, 0, 0, 0)
#define MFMA32(a, b, c) __builtin_amdgcn_mfma_f32_32x32x16_bf16(a, b, c, 0, 0, 0)

DI void glds(unsigned voff, const char* sbase, unsigned ldsaddr) {
  asm volatile("s_mov_b32 m0, %2\n\ts_nop 0\n\tglobal_load_lds_dwordx4 %0, %1" ::"v"(voff), "s"(sbase), "s"(ldsaddr) : "memory");
}
DI void glds1(unsigned voff, const char* sbase, unsigned ldsaddr) {
  asm volatile("s_mov_b32 m0, %2\n\ts_nop 0\n\tglobal_load_lds_dwordx4 %0, %1 sc1" ::"v"(voff), "s"(sbase), "s"(ldsaddr) : "memory");
}
DI unsigned pk2(float lo, float hi) { f32x2 v = {lo, hi}; return __builtin_bit_cast(unsigned, __builtin_convertvector(v, bf16x2)); }
DI float bf2f(unsigned b) { return U2F(b << 16); }
DI bf16x8 ldb128(lptr p) { return *(const LAS bf16x8*)p; }
DI bf16x8 ldtr2(lptr p, int d) {
  bf16x4 a = __builtin_amdgcn_ds_read_tr16_b64_v4bf16((LAS bf16x4*)p);
  bf16x4 b = __builtin_amdgcn_ds_read_tr16_b64_v4bf16((LAS bf16x4*)(p + d));
  return __builtin_shufflevector(a, b, 0, 1, 2, 3, 4, 5, 6, 7);
}
DI void ST128_(__amdgpu_buffer_rsrc_t r, u32x4 v, unsigned voff, int soff) { __builtin_amdgcn_raw_buffer_store_b128(v, r, voff + (unsigned)soff, 0, 16); }
DI float silu(float v) { return v * __builtin_amdgcn_rcpf(1.0f + __builtin_amdgcn_exp2f(v * -1.4426950408889634f)); }

#define STAMP_DECL
#define LSTAMP(i)
#define STAMP(i)
#define STAMP_OUT(buf, n)
template <int CTRL> DI float dpp_add(float v) { return v + U2F(__builtin_amdgcn_update_dpp(0u, F2U(v), CTRL, 0xf, 0xf, true)); }
DI float half_sum(float v) {
  v = dpp_add<0xB1>(v); v = dpp_add<0x4E>(v); v = dpp_add<0x141>(v); v = dpp_add<0x140>(v);
  auto rr = __builtin_amdgcn_permlane16_swap(F2U(v), F2U(v), false, false);
  return U2F(rr[0]) + U2F(rr[1]);
}
typedef __attribute__((address_space(1))) unsigned long long gu64;

#define SX(b, h) (98304 + (b) * 32768 + (h) * 16384)
constexpr int SLOT_B = 0, SLOT_E = 32768, SLOT_A = 65536, SLOT_C = 98304, SLOT_D = 131072, K1_LDS = 163840;

#define XSWAP 0
constexpr int WS_HFLAG = 4096;
constexpr int WS_GFLAG = 65536;
constexpr int WS_OFLAG = 131072;
#define OFS 16
constexpr int WS_STATS = 1 << 20;
constexpr int WS_G = 5 << 20, WS_O = 9 << 20, WS_H = 25 << 20, WS_END = 39 << 20;
constexpr int K2_STG = 32768;

NOPK __global__ __launch_bounds__(512, 2) void mega_kernel(const char* __restrict__ xn, const char* __restrict__ kc, const char* __restrict__ vc,
                                                           const char* __restrict__ wq, const char* __restrict__ w1, const char* __restrict__ wo, const char* __restrict__ w2,
                                                           char* ws, char* __restrict__ out) {
  STAMP_DECL
  STAMP(0);
  extern __shared__ __attribute__((aligned(16))) char shm[];
  const lptr lds = (lptr)shm;
  const unsigned lds0 = (unsigned)(uintptr_t)lds;
  const int tid = TID, wid = __builtin_amdgcn_readfirstlane(tid >> 6), lane = tid & 63, wr = wid >> 2, wc = wid & 3;
  const int bid = BID ^ XSWAP, xcd = bid & 7, idx = bid >> 3, hd = 4 * xcd + (idx & 3), rb = idx >> 2;
  const int g = lane >> 4, t = lane & 15, q4 = (lane >> 2) & 3, p4 = lane & 3;

  const bool light = xcd >= 4;
  unsigned voffX[4], voffQ[2], voffH[2];
  {
    const int r = lane >> 3, c = lane & 7;
#pragma unroll
    for (int i = 0; i < 4; ++i) voffX[i] = (unsigned)((8 * i + r) * 2048 + ((c ^ (4 * (i & 1) + (r >> 1))) << 4) - i * 1024);
    const int k = 32 * (wid >> 2) + 16 * (wid & 1) + 4 * ((wid >> 1) & 1) + 8 * (r >> 2) + (r & 3);
    const int cw = c ^ (((r >> 1) & 3) << 1);
    voffQ[0] = (unsigned)(k * 8192 + cw * 16 + 1024); voffQ[1] = voffQ[0] + 128 - 1024;
    voffH[0] = (unsigned)(k * 7168 + 128 + (light ? (cw & 3) : cw) * 16 + 1024);
    voffH[1] = (unsigned)(k * 7168 + cw * 16);
  }
  const char* xb = xn + (size_t)rb * 256 * 2048 + wid * 65536;
  const char* qb = wq + hd * 256 - 1024;
  const char* hb = w1 + (light ? 4096 + 192 * (hd - 16) : 256 * hd) - 1024;
  const bool full = wr == 1 || !light;
#define GLDS2(v0_, v1_, sb_, ld_) asm volatile("s_mov_b32 m0, %3\n\ts_nop 0\n\tglobal_load_lds_dwordx4 %0, %2\n\tglobal_load_lds_dwordx4 %1, %2 offset:1024" \
    ::"v"(v0_), "v"(v1_), "s"(sb_), "s"(ld_) : "memory")
#define GLDS4(v0_, v1_, v2_, v3_, sb_, ld_) asm volatile("s_mov_b32 m0, %5\n\ts_nop 0\n\tglobal_load_lds_dwordx4 %0, %4\n\tglobal_load_lds_dwordx4 %1, %4 offset:1024\n\t" \
    "global_load_lds_dwordx4 %2, %4 offset:2048\n\tglobal_load_lds_dwordx4 %3, %4 offset:3072" ::"v"(v0_), "v"(v1_), "v"(v2_), "v"(v3_), "s"(sb_), "s"(ld_) : "memory")
#define ST_X(b, kt) GLDS4(voffX[0], voffX[1], voffX[2], voffX[3], xb + (kt) * 128, lds0 + SX(b, 0) + wid * 4096)
#define ST_W0(wo_, kt) GLDS2(voffQ[0], voffQ[1], qb + (size_t)(kt) * 524288, lds0 + (wo_) + wid * 2048)
#define ST_W1(wo_, kt) GLDS2(voffH[0], voffH[1], hb + (size_t)(kt) * 458752, lds0 + (wo_) + 16384 + wid * 2048)

  lptr wb[4], xr[2];
#pragma unroll
  for (int m = 0; m < 4; ++m) wb[m] = lds + wr * 1024 + (g >> 1) * 2048 + (4 * (g & 1) + q4) * 128 + 32 * ((m ^ wc) ^ (2 * (g & 1) + (q4 >> 1))) + 8 * p4;
#pragma unroll
  for (int ks = 0; ks < 2; ++ks) xr[ks] = lds + 98304 + (wc * 32 + t) * 128 + 16 * ((4 * ks + g) ^ (t >> 1));
#define LDW(dst, h) do { _Pragma("unroll") for (int m = 0; m < 4; ++m) _Pragma("unroll") for (int k = 0; k < 2; ++k) dst[m][k] = ldtr2(wb[m] + (h) * 16384 + k * 8192, 4096); } while (0)
#define LDX(dst, b, h) do { _Pragma("unroll") for (int n = 0; n < 2; ++n) _Pragma("unroll") for (int k = 0; k < 2; ++k) dst[n][k] = ldb128(xr[k] + (SX(b, h) - 98304) + n * 2048); } while (0)

  f32x4 acc[2][2][4][2];
#pragma unroll
  for (int a = 0; a < 2; ++a)
#pragma unroll
    for (int b = 0; b < 2; ++b)
#pragma unroll
      for (int m = 0; m < 4; ++m)
#pragma unroll
        for (int n = 0; n < 2; ++n) acc[a][b][m][n] = (f32x4){0.f, 0.f, 0.f, 0.f};
  bf16x8 At[4][2], B0[2][2], B1[2][2];

#define MM4(ai, m, k) do { _Pragma("unroll") for (int n = 0; n < 2; ++n) { \
      acc[ai][0][m][n] = MFMA16(At[m][k], B0[n][k], acc[ai][0][m][n]); acc[ai][1][m][n] = MFMA16(At[m][k], B1[n][k], acc[ai][1][m][n]); } } while (0)
#define MMA2(ai, CLOSE) do { __builtin_amdgcn_s_setprio(1); \
    MM4(ai, 0, 0); cs[ai] = MFMA16(At[0][0], ones, cs[ai]); MM4(ai, 1, 0); MM4(ai, 0, 1); cs[ai] = MFMA16(At[0][1], ones, cs[ai]); MM4(ai, 1, 1); \
    asm volatile("" : "+v"(cs[ai])); \
    if ((ai) == 0 || full) { MM4(ai, 2, 0); MM4(ai, 3, 0); MM4(ai, 2, 1); MM4(ai, 3, 1); } \
    SCHED(); CLOSE; __builtin_amdgcn_s_setprio(0); SCHED(); } while (0)
  char* const stats = ws + WS_STATS;
  const __amdgpu_buffer_rsrc_t wrs = __builtin_amdgcn_make_buffer_rsrc(ws, 0, WS_END, 0x00020000);
  f32x4 cs[2] = {{0.f, 0.f, 0.f, 0.f}, {0.f, 0.f, 0.f, 0.f}};
  bf16x8 ones = __builtin_bit_cast(bf16x8, (u32x4){0x3f803f80u, 0x3f803f80u, 0x3f803f80u, 0x3f803f80u});
#define PIN_SETUP() do { asm volatile("" : "+v"(ones), "+v"(cs[0]), "+v"(cs[1]), "+v"(wb[0]), "+v"(wb[1]), "+v"(wb[2]), "+v"(wb[3]), "+v"(xr[0]), "+v"(xr[1])); \
    _Pragma("unroll") for (int a = 0; a < 2; ++a) _Pragma("unroll") for (int b = 0; b < 2; ++b) \
      asm volatile("" : "+v"(acc[a][b][0][0]), "+v"(acc[a][b][0][1]), "+v"(acc[a][b][1][0]), "+v"(acc[a][b][1][1]), "+v"(acc[a][b][2][0]), "+v"(acc[a][b][2][1]), "+v"(acc[a][b][3][0]), "+v"(acc[a][b][3][1])); } while (0)
  unsigned tag;
  {
    if (wr == 0) {
      unsigned ep;
      ST_X(0, 0); ST_W0(0, 0);
      asm volatile("global_load_dword %0, %1, %2 sc1" : "=&v"(ep) : "v"(0), "s"(ws) : "memory");
      ST_W1(0, 0); ST_W0(32768, 1); ST_X(1, 1);
      PIN_SETUP();
      asm volatile("s_waitcnt vmcnt(8)" : "+v"(ep) :: "memory");
      tag = __builtin_amdgcn_readfirstlane(ep) + 1u;
      BAR();
    } else {
      const int row = rb * 256 + hd * 8 + wc * 2 + (lane >> 5);
      u32x4 a[4];
      unsigned ep;
      asm volatile("global_load_dword %0, %1, %2 sc1" : "=&v"(ep) : "v"(0), "s"(ws) : "memory");
      asm volatile("global_load_dwordx4 %0, %4, %5\n\tglobal_load_dwordx4 %1, %4, %5 offset:512\n\tglobal_load_dwordx4 %2, %4, %5 offset:1024\n\tglobal_load_dwordx4 %3, %4, %5 offset:1536"
                   : "=&v"(a[0]), "=&v"(a[1]), "=&v"(a[2]), "=&v"(a[3]) : "v"(row * 2048 + (lane & 31) * 16), "s"(xn) : "memory");
      ST_X(0, 0); ST_W0(0, 0);
      ST_W1(0, 0); ST_W0(32768, 1); ST_X(1, 1);
      PIN_SETUP();
      asm volatile("s_waitcnt vmcnt(8)" : "+v"(ep), "+v"(a[0]), "+v"(a[1]), "+v"(a[2]), "+v"(a[3]) :: "memory");
      BAR();
      tag = __builtin_amdgcn_readfirstlane(ep) + 1u;
      float v[32];
#pragma unroll
      for (int i = 0; i < 16; ++i) { v[2 * i] = bf2f(a[i >> 2][i & 3] & 0xffffu); v[2 * i + 1] = U2F(a[i >> 2][i & 3] & 0xffff0000u); }
      float s0 = 0.f, s1 = 0.f, s2 = 0.f, s3 = 0.f;
#pragma unroll
      for (int i = 0; i < 8; ++i) { s0 += v[i]; s1 += v[8 + i]; s2 += v[16 + i]; s3 += v[24 + i]; }
      const float mean = half_sum((s0 + s1) + (s2 + s3)) * (1.0f / 1024.0f);
      s0 = s1 = s2 = s3 = 0.f;
#pragma unroll
      for (int i = 0; i < 8; ++i) { const float d0 = v[i] - mean, d1 = v[8 + i] - mean, d2 = v[16 + i] - mean, d3 = v[24 + i] - mean; s0 += d0 * d0; s1 += d1 * d1; s2 += d2 * d2; s3 += d3 * d3; }
      const float sd = __builtin_amdgcn_sqrtf(half_sum((s0 + s1) + (s2 + s3)) * (1.0f / 1024.0f) + 1e-5f);
      if ((lane & 31) < 2) __hip_atomic_store((gu64*)(stats + row * 16 + (lane & 1) * 8), ((unsigned long long)tag << 32) | F2U((lane & 1) ? sd : mean), __ATOMIC_RELAXED, __HIP_MEMORY_SCOPE_AGENT);
    }
  }
  if (wr == 1) BAR();
  SCHED();
  STAMP(1);
  const int kvg = hd >> 3;
  const char* kb_g = kc + ((size_t)kvg * 2048 + 1536) * 256;
  const char* vb_g = vc + ((size_t)kvg * 2048 + 1536) * 256;
#define VOFFK ((unsigned)(((lane >> 2) & 7) * 256 + (4 * (lane >> 5) + ((lane & 3) ^ ((2 * (wid & 1) + ((lane >> 4) & 1)) & 3))) * 16))
#define VOFFV ((unsigned)(((lane >> 2) & 7) * 256 + (lane >> 5) * 64 + (lane & 3) * 16))
#define ST_KV8(voff, gsrc, slot) do { glds(voff, (gsrc) + wid * 2048, lds0 + (slot) + wid * 2048); glds(voff, (gsrc) + wid * 2048 + 128, lds0 + (slot) + wid * 2048 + 1024); } while (0)
  int wprv = 65536, wnxt = 32768;
#define W_ADV() do { const int d_ = (wnxt == 0) ? -65536 : 32768, cur_ = wnxt - d_; wnxt = wprv; wprv = cur_; \
    _Pragma("unroll") for (int f = 0; f < 4; ++f) wb[f] += d_; \
    asm volatile("" : "+v"(wb[0]), "+v"(wb[1]), "+v"(wb[2]), "+v"(wb[3])); } while (0)
#define PH_A(b, STAGE) do { LDX(B0, b, 0); LDX(B1, b, 1); SCHED(); LDW(At, 0); STAGE; WAIT_V(10); WAIT_L(12); BAR(); SCHED(); MMA2(0, WAIT_L(0); BAR()); } while (0)
#define PH_B(STAGE) do { LDW(At, 1); STAGE; W_ADV(); WAIT_V(8); BAR(); SCHED(); MMA2(1, WAIT_L(0); BAR()); } while (0)
#pragma unroll 1
  for (int kt = 0; kt < 14; kt += 2) {
    PH_A(0, ST_W1(wnxt, kt + 1); ST_W0(wprv, kt + 2));
    PH_B(ST_X(0, kt + 2));
    PH_A(1, ST_W1(wnxt, kt + 2); ST_W0(wprv, kt + 3));
    PH_B(ST_X(1, kt + 3));
  }
  STAMP(2);
  const unsigned voffK = VOFFK, voffV = VOFFV;
  PH_A(0, ST_W1(0, 15); ST_KV8(voffK, kb_g, SLOT_E));
  PH_B(ST_KV8(voffV, vb_g, SLOT_C); ST_KV8(voffV, vb_g + 16384, SLOT_C + 16384));
  const unsigned soff = (unsigned)((rb * 256 + (g >> 1) * 128 + wc * 32 + (g & 1) * 16 + t) * 16);
  u32x4 sg;
  LDX(B0, 1, 0); LDX(B1, 1, 1); SCHED(); LDW(At, 0);
  asm volatile("global_load_dwordx4 %0, %1, %2 sc1" : "=&v"(sg) : "v"(soff), "s"(stats) : "memory");
  ST_KV8(voffK, kb_g + 16384, SLOT_E + 16384); ST_KV8(voffK, kb_g + 32768, SLOT_A);
  WAIT_V(11); BAR(); SCHED(); MMA2(0, WAIT_L(0); BAR());
  LDW(At, 1);
  ST_KV8(voffK, kb_g + 49152, SLOT_A + 16384);
  BAR(); SCHED();
  const lptr csw = lds + SLOT_D + (t & 3) * 8192 + wr * 4096 + wc * 64 + g * 16;
  MMA2(1, *(LAS f32x4*)csw = cs[0]; *(LAS f32x4*)(csw + 256) = cs[1]; asm volatile("s_waitcnt vmcnt(4)" : "+v"(sg) :: "memory"); WAIT_L(0); BAR());
  f32x4 cv[2][4];
#pragma unroll
  for (int a = 0; a < 2; ++a)
#pragma unroll
    for (int m = 0; m < 4; ++m) cv[a][m] = *(const LAS f32x4*)(lds + SLOT_D + wc * 8192 + wr * 4096 + a * 256 + (m ^ wc) * 64 + g * 16);
  if (__builtin_expect(__builtin_amdgcn_ballot_w64(sg[1] != tag || sg[3] != tag) != 0ull, 0)) {
    for (unsigned spins = 0; spins < 4000000u; ++spins) {
      __builtin_amdgcn_s_sleep(2);
      asm volatile("" ::: "memory");
      sg = __builtin_amdgcn_raw_buffer_load_b128(wrs, soff, WS_STATS, 16);
      if (__builtin_amdgcn_ballot_w64(sg[1] != tag || sg[3] != tag) == 0ull) break;
    }
  }
  float mu[2][2], rs[2][2], sd[2][2];
  {
    auto m16 = __builtin_amdgcn_permlane16_swap(sg[0], sg[0], false, false);
    auto s16 = __builtin_amdgcn_permlane16_swap(sg[2], sg[2], false, false);
#pragma unroll
    for (int n = 0; n < 2; ++n) {
      auto m32 = __builtin_amdgcn_permlane32_swap(m16[n], m16[n], false, false);
      auto s32 = __builtin_amdgcn_permlane32_swap(s16[n], s16[n], false, false);
#pragma unroll
      for (int bj = 0; bj < 2; ++bj) { mu[bj][n] = -U2F(m32[bj]); sd[bj][n] = U2F(s32[bj]); rs[bj][n] = __builtin_amdgcn_rcpf(sd[bj][n]) * 1.4426950408889634f; }
    }
  }
  WAIT_L(0);
  STAMP(3);
  {
    const lptr qw = lds + wc * 8192 + wr * 4096 + ((g >> 1) * 32 + t) * 16 + (g & 1) * 8;
#pragma unroll
    for (int bj = 0; bj < 2; ++bj) {
      lptr qwb = qw + (bj ? SLOT_D : SLOT_B);
      asm volatile("" : "+v"(qwb));
#pragma unroll
      for (int m = 0; m < 4; ++m)
#pragma unroll
        for (int n = 0; n < 2; ++n) {
          const f32x4 v = (acc[0][bj][m][n] + cv[0][m] * mu[bj][n]) * (rs[bj][n] * 0.08838834764831845f);
          u32x2 w = {pk2(v[0], v[1]), pk2(v[2], v[3])};
          *(LAS u32x2*)(qwb + (m ^ wc) * 1024 + n * 256) = w;
        }
    }
  }
  WAIT_L(0); BAR(); SCHED();
  STAMP(4);
  bf16x8 qf[8];
  const lptr qr = lds + (wr ? SLOT_D : SLOT_B) + wc * 8192 + lane * 16;
  if (wr == 1) {
#pragma unroll
    for (int s = 0; s < 8; ++s) qf[s] = ldb128(qr + s * 1024);
  }
  {
#define ST128(...) ST128_(wrs, __VA_ARGS__)
    u32x2 hw[2][2][4];
#define SILU16(m) do { \
      const f32x4 a0 = acc[1][0][m][0] + cv[1][m] * mu[0][0], a1 = acc[1][0][m][1] + cv[1][m] * mu[0][1], a2 = acc[1][1][m][0] + cv[1][m] * mu[1][0], a3 = acc[1][1][m][1] + cv[1][m] * mu[1][1]; \
      f32x4 e0 = a0 * -rs[0][0], e1 = a1 * -rs[0][1], e2 = a2 * -rs[1][0], e3 = a3 * -rs[1][1]; SCHED(); \
      _Pragma("unroll") for (int i = 0; i < 4; ++i) { e0[i] = __builtin_amdgcn_exp2f(e0[i]); e1[i] = __builtin_amdgcn_exp2f(e1[i]); e2[i] = __builtin_amdgcn_exp2f(e2[i]); e3[i] = __builtin_amdgcn_exp2f(e3[i]); } SCHED(); \
      e0 = e0 * sd[0][0] + sd[0][0]; e1 = e1 * sd[0][1] + sd[0][1]; e2 = e2 * sd[1][0] + sd[1][0]; e3 = e3 * sd[1][1] + sd[1][1]; SCHED(); \
      _Pragma("unroll") for (int i = 0; i < 4; ++i) { e0[i] = __builtin_amdgcn_rcpf(e0[i]); e1[i] = __builtin_amdgcn_rcpf(e1[i]); e2[i] = __builtin_amdgcn_rcpf(e2[i]); e3[i] = __builtin_amdgcn_rcpf(e3[i]); } SCHED(); \
      e0 *= a0; e1 *= a1; e2 *= a2; e3 *= a3; SCHED(); \
      hw[0][0][m] = (u32x2){pk2(e0[0], e0[1]), pk2(e0[2], e0[3])}; hw[0][1][m] = (u32x2){pk2(e1[0], e1[1]), pk2(e1[2], e1[3])}; \
      hw[1][0][m] = (u32x2){pk2(e2[0], e2[1]), pk2(e2[2], e2[3])}; hw[1][1][m] = (u32x2){pk2(e3[0], e3[1]), pk2(e3[2], e3[3])}; SCHED(); } while (0)
    SILU16(0);
    if (wr == 0) {
      BAR(); SCHED();
#pragma unroll
      for (int s = 0; s < 8; ++s) qf[s] = ldb128(qr + s * 1024);
    }
    SILU16(1);
    const unsigned hoff = (unsigned)((rb * 16 + wc * 2) * 2048 + lane * 16);
    const int pb = (light ? 16 + 3 * hd : 4 * hd) + (wr ? 0 : 2), sw = full ? (wc >> 1) : 0, p0 = pb + sw, p1 = pb + (sw ^ 1);
#define H_OUT1(p_, a_, b_) do { _Pragma("unroll") for (int bj = 0; bj < 2; ++bj) _Pragma("unroll") for (int n = 0; n < 2; ++n) \
      ST128((u32x4){hw[bj][n][a_][0], hw[bj][n][a_][1], hw[bj][n][b_][0], hw[bj][n][b_][1]}, hoff + n * 2048, WS_H + ((p_) >> 1) * 262144 + ((p_) & 1) * 1024 + bj * 16384); } while (0)
#define H_OUT(p_, m_) do { if (wc & 1) H_OUT1(p_, (m_) + 1, m_); else H_OUT1(p_, m_, (m_) + 1); } while (0)
    H_OUT(p0, 0);
    if (full) { SILU16(2); SILU16(3); H_OUT(p1, 2); }
  }
  STAMP(5);
  if (wr == 1) { WAIT_L(0); BAR(); }
  __builtin_amdgcn_s_setprio(1);
  SCHED();
  STAMP(6);
  const int r32 = lane & 31, hh = lane >> 5;
  lptr kr[2];
#pragma unroll
  for (int e = 0; e < 2; ++e) kr[e] = lds + 2048 * (r32 >> 3) + 64 * (r32 & 7) + 16 * ((2 * e + hh) ^ ((r32 >> 2) & 3));
  const lptr vr = lds + ((lane & 3) << 3) + (((lane >> 2) & 3) << 6) + (((lane >> 4) & 1) << 5) + (hh << 8);
  f32x16 o[4], s[4];
#pragma unroll
  for (int d = 0; d < 4; ++d)
#pragma unroll
    for (int i = 0; i < 16; ++i) o[d][i] = 0.f;
  bf16x8 pf[8];
  constexpr int RD = 6;
  bf16x8 fr[RD];
  f32x4 lacc = {0.f, 0.f, 0.f, 0.f};
  const unsigned selw = (((lane >> 4) ^ (lane >> 2)) & 1) ? 0u : 0x3f803f80u;
  bf16x8 sel = __builtin_bit_cast(bf16x8, (u32x4){selw, selw, selw, selw});
  asm volatile("" : "+v"(sel));
  f32x16 negm = {0.f, 0.f, 0.f, 0.f, 0.f, 0.f, 0.f, 0.f, 0.f, 0.f, 0.f, 0.f, 0.f, 0.f, 0.f, 0.f};
#define SM_THR 60.0f
#define FRAG(i) ((i) < 32 ? ldtr2(v0 + (((i) >> 2) * 8 + ((i) & 3)) * 512, 2048) : ldb128((((((i) - 32) >> 2) & 1) ? k1 : k0) + ((i) & 3) * 8192 + (((i) - 32) >> 3) * 512))
#define XEARLY 3
#define XPRIO 1
#define YPRIO 0
#define XSEG(lo, hi, CLOSE) do { __builtin_amdgcn_s_setprio(XPRIO); \
    _Pragma("unroll") for (int i = (lo); i < (hi); ++i) { \
      if (i < 32) o[i & 3] = MFMA32(fr[i % RD], pf[(i >> 2) & 7], o[i & 3]); \
      else s[i & 3] = MFMA32(fr[i % RD], qf[((i - 32) >> 2) & 7], i < 36 ? negm : s[i & 3]); \
      if (i < 32 && (i & 3) == 1) lacc = MFMA16(sel, pf[(i >> 2) & 7], lacc); \
      if (i + RD < (hi)) fr[i % RD] = FRAG(i + RD); \
      SCHED(); \
      if (i == 63 - XEARLY) { __builtin_amdgcn_s_setprio(2); CLOSE; WAIT_L(0); BAR(); SCHED(); } } \
    __builtin_amdgcn_s_setprio(YPRIO); } while (0)
#define PREF(lo) do { _Pragma("unroll") for (int i = (lo); i < (lo) + RD; ++i) fr[i % RD] = FRAG(i); SCHED(); } while (0)
#define MX3(a, b, c) __builtin_fmaxf(__builtin_fmaxf((a), (b)), (c))
#define SOFTMAX() do { \
      float m0 = MX3(s[0][0], s[0][1], s[0][2]), m1 = MX3(s[1][0], s[1][1], s[1][2]), m2 = MX3(s[2][0], s[2][1], s[2][2]), m3 = MX3(s[3][0], s[3][1], s[3][2]); \
      _Pragma("unroll") for (int i = 3; i < 15; i += 2) { m0 = MX3(m0, s[0][i], s[0][i + 1]); m1 = MX3(m1, s[1][i], s[1][i + 1]); m2 = MX3(m2, s[2][i], s[2][i + 1]); m3 = MX3(m3, s[3][i], s[3][i + 1]); } \
      m0 = MX3(m0, s[0][15], s[1][15]); m2 = MX3(m2, s[2][15], s[3][15]); \
      float mx = __builtin_fmaxf(MX3(m0, m1, m2), m3); \
        \
      if (__builtin_expect(__builtin_amdgcn_ballot_w64(!(mx <= SM_THR)) != 0ull, 0)) { \
        auto rr = __builtin_amdgcn_permlane32_swap(F2U(mx), F2U(mx), false, false); \
        const float dl = MX3(U2F(rr[0]), U2F(rr[1]), 0.f); \
        _Pragma("unroll") for (int kb = 0; kb < 4; ++kb) s[kb] -= dl; \
        negm -= dl; \
        const float alpha = __builtin_amdgcn_exp2f(-dl); lacc *= alpha; _Pragma("unroll") for (int d = 0; d < 4; ++d) o[d] *= alpha; \
      } \
      SCHED(); \
      _Pragma("unroll") for (int kb = 0; kb < 4; ++kb) { \
        _Pragma("unroll") for (int e = 0; e < 2; ++e) { \
          _Pragma("unroll") for (int i = 0; i < 8; ++i) s[kb][8 * e + i] = __builtin_amdgcn_exp2f(s[kb][8 * e + i]); \
          u32x4 w = {pk2(s[kb][8 * e], s[kb][8 * e + 1]), pk2(s[kb][8 * e + 2], s[kb][8 * e + 3]), pk2(s[kb][8 * e + 4], s[kb][8 * e + 5]), pk2(s[kb][8 * e + 6], s[kb][8 * e + 7])}; \
          pf[2 * kb + e] = __builtin_bit_cast(bf16x8, w); } \
        asm volatile("" : "+v"(pf[2 * kb]), "+v"(pf[2 * kb + 1])); \
        SCHED(); } } while (0)
  {
    const lptr k0 = kr[0] + SLOT_E, k1 = kr[1] + SLOT_E, v0 = vr;
    {
      bf16x8 kf[8];
#pragma unroll
      for (int st = 0; st < 8; ++st) kf[st] = FRAG(32 + 4 * st);
      PREF(32);
#pragma unroll
      for (int st = 0; st < 8; ++st) negm = MFMA32(kf[st], qf[st], negm);
      float m0 = MX3(negm[0], negm[1], negm[2]), m1 = MX3(negm[3], negm[4], negm[5]);
      m0 = MX3(m0, negm[6], negm[7]); m1 = MX3(m1, negm[8], negm[9]); m0 = MX3(m0, negm[10], negm[11]); m1 = MX3(m1, negm[12], negm[13]);
      m0 = __builtin_fmaxf(MX3(m0, negm[14], negm[15]), m1);
      auto rr = __builtin_amdgcn_permlane32_swap(F2U(m0), F2U(m0), false, false);
      m0 = -__builtin_fmaxf(U2F(rr[0]), U2F(rr[1]));
#pragma unroll
      for (int i = 0; i < 16; ++i) negm[i] = m0;
      asm volatile("" : "+v"(negm));
      SCHED();
    }
    XSEG(32, 64, WAIT_V(4));
  }
  STAMP(7);
  const char* kvsrc = (wr ? kb_g + 65536 : vb_g + 32768) + wc * 8192 - 1024;
  unsigned kvo[2][2];
#pragma unroll
  for (int par = 0; par < 2; ++par) {
    const int xx = wr ? ((lane & 3) ^ ((2 * par + ((lane >> 4) & 1)) & 3)) : (lane & 3);
    kvo[par][0] = (unsigned)(((lane >> 2) & 7) * 256 + (lane >> 5) * 64 + xx * 16 + 1024);
    kvo[par][1] = kvo[par][0] + 128 - 1024;
  }
  const unsigned pfo = (unsigned)((lane < 21 ? lane : 20) * 128);
  unsigned pft = 0;
#pragma unroll 1
  for (int c = 0; c < 3; ++c) {
    if (c == 0) STAMP(8); else if (c == 1) STAMP(12);
    if (c == 1 && wid == 4 && lane == 0) __builtin_amdgcn_raw_buffer_store_b32(tag, wrs, 0, WS_HFLAG + (rb * 32 + hd) * 128, 16);
    if (c + wr < 3) {
      const int slot = wr ? (c == 0 ? SLOT_B : SLOT_C) : (c == 0 ? SLOT_D : (c == 1 ? SLOT_E : SLOT_A));
#pragma unroll
      for (int hf = 0; hf < 2; ++hf)
        asm volatile("s_mov_b32 m0, %5\n\ts_nop 0\n\t"
                     "global_load_lds_dwordx4 %0, %4\n\tglobal_load_lds_dwordx4 %1, %4 offset:1024\n\t"
                     "global_load_lds_dwordx4 %2, %4 offset:2048\n\tglobal_load_lds_dwordx4 %3, %4 offset:3072"
                     ::"v"(kvo[0][0]), "v"(kvo[0][1]), "v"(kvo[1][0]), "v"(kvo[1][1]), "s"(kvsrc + c * 32768 + hf * 4096), "s"(lds0 + slot + wc * 8192 + hf * 4096) : "memory");
    }
    {
      const int sl = (bid * 8 + wid) * 3 + c;
      const int o1 = sl * 2688, o2 = (sl - 3121) * 2688;
      const char* pp = sl < 3121 ? wo + (o1 < 8385920 ? o1 : 8385920) : w2 + (o2 < 7337344 ? o2 : 7337344);
      asm volatile("global_load_dword %0, %1, %2" : "+v"(pft) : "v"(pfo), "s"(pp) : "memory");
    }
    SOFTMAX();
    const int kslot = (c == 0) ? SLOT_A : ((c == 1) ? SLOT_B : SLOT_C);
    const int vslot = (c == 0) ? SLOT_C : ((c == 1) ? SLOT_D : SLOT_E);
    const lptr k0 = kr[0] + kslot, k1 = kr[1] + kslot, v0 = vr + vslot;
    PREF(0);
    if (c == 0) STAMP(9); else if (c == 1) STAMP(13);
    BAR(); SCHED();
    if (c == 0) STAMP(10); else if (c == 1) STAMP(14);
    XSEG(0, 64, WAIT_V(1); if (c == 0) WAIT_V(0));
    if (c == 0) STAMP(11); else if (c == 2) STAMP(15);
  }
  STAMP(16);
  SOFTMAX();
  {
    const lptr k0 = kr[0], k1 = kr[1], v0 = vr + SLOT_A;
    PREF(0);
    STAMP(17);
    if (wr == 0) BAR();
    SCHED();
    STAMP(18);
    XSEG(0, 32, );
  }
  STAMP(19);
  int lane2 = __builtin_amdgcn_mbcnt_hi(~0u, __builtin_amdgcn_mbcnt_lo(~0u, 0u));
  asm volatile("" : "+v"(lane2));
#define lane lane2
  const int isG = xcd >> 2, j2 = xcd & 3, tm = 8 * (j2 >> 1) + (idx & 7), tn = 4 * (j2 & 1) + (idx >> 3), nt = isG ? 56 : 64;
#define O_OUT() do { \
    const float inv = __builtin_amdgcn_rcpf(lacc[0]); \
    const unsigned oo = (unsigned)((rb * 16 + wr * 8 + wc * 2 + (r32 >> 4)) * 2048 + (hh * 16 + (r32 & 15)) * 16); \
    _Pragma("unroll") for (int d = 0; d < 4; ++d) _Pragma("unroll") for (int e = 0; e < 2; ++e) { \
        u32x4 w = {pk2(o[d][8 * e] * inv, o[d][8 * e + 1] * inv), pk2(o[d][8 * e + 2] * inv, o[d][8 * e + 3] * inv), \
                   pk2(o[d][8 * e + 4] * inv, o[d][8 * e + 5] * inv), pk2(o[d][8 * e + 6] * inv, o[d][8 * e + 7] * inv)}; \
        ST128(w, oo + (d & 1) * 1024 + e * 512, WS_O + (2 * hd + (d >> 1)) * 262144); } } while (0)
#define O_FLAG() do { if (wid == 0 && lane == 0) __builtin_amdgcn_raw_buffer_store_b32(tag, wrs, 0, WS_OFLAG + (rb * 32 + hd) * OFS, 16); } while (0)
#define POLL32(off_, slp) do { for (unsigned spins = 0; spins < 2000000u; ++spins) { \
      if (__builtin_amdgcn_ballot_w64(__builtin_amdgcn_raw_buffer_load_b32(wrs, (off_), 0, 16) != tag) == 0ull) break; \
      __builtin_amdgcn_s_sleep(slp); asm volatile("" ::: "memory"); } \
    asm volatile("" ::: "memory"); } while (0)
  if (wr == 0) {
    const char* ab = ws + (isG ? WS_H : WS_O) + tm * 16384 + wc * 4096;
    const char* wbp = (isG ? w2 : wo) + tn * 256 - 1024 + (wc >> 1) * 65536 + (wc & 1) * (isG ? 32768 : 16384);
    const unsigned voffA = lane * 16;
    unsigned voffW[2][2];
    {
      const int r = lane >> 3, c = lane & 7;
      voffW[0][0] = (unsigned)(r * 2048 + ((c ^ (((r >> 1) & 3) << 1)) << 4) + 1024);
      voffW[0][1] = voffW[0][0] + 128 - 1024;
      voffW[1][0] = voffW[0][0] + (isG ? 16384 : 32768) - 2048;
      voffW[1][1] = voffW[1][0] + 128 - 1024;
    }
#define K2_ISSUE(p_, buf_) do { const int k_ = isG ? ((p_) + 14 * j2 >= 56 ? (p_) + 14 * j2 - 56 : (p_) + 14 * j2) : ((((p_) + 8 * j2) & 31) | (~(p_) & 32)); \
      const unsigned d_ = lds0 + (buf_) * K2_STG + wc * 4096; \
      asm volatile("s_mov_b32 m0, %2\n\ts_nop 0\n\tglobal_load_lds_dwordx4 %0, %1 sc1\n\tglobal_load_lds_dwordx4 %0, %1 offset:1024 sc1\n\t" \
                   "global_load_lds_dwordx4 %0, %1 offset:2048 sc1\n\tglobal_load_lds_dwordx4 %0, %1 offset:3072 sc1" \
                   ::"v"(voffA), "s"(ab + (size_t)k_ * 262144), "s"(d_) : "memory"); \
      asm volatile("s_mov_b32 m0, %5\n\ts_nop 0\n\tglobal_load_lds_dwordx4 %0, %4\n\tglobal_load_lds_dwordx4 %1, %4 offset:1024\n\t" \
                   "global_load_lds_dwordx4 %2, %4 offset:2048\n\tglobal_load_lds_dwordx4 %3, %4 offset:3072" \
                   ::"v"(voffW[0][0]), "v"(voffW[0][1]), "v"(voffW[1][0]), "v"(voffW[1][1]), "s"(wbp + (size_t)k_ * 131072), "s"(d_ + 16384) : "memory"); } while (0)
    const unsigned f1 = isG ? (unsigned)(WS_HFLAG + ((tm >> 1) * 32 + (lane & 31)) * 128) : (unsigned)(WS_OFLAG + ((tm >> 1) * 32 + 16 + (lane & 15)) * OFS);
    const unsigned f2 = (unsigned)(WS_OFLAG + ((tm >> 1) * 32 + (lane & 15)) * OFS);
    O_OUT();
    asm volatile("s_waitcnt vmcnt(0)" :: "v"(pft) : "memory");
    STAMP(20);
    POLL32(f1, 4);
    if (isG) { BAR(); O_FLAG(); }
    K2_ISSUE(0, 3); K2_ISSUE(1, 4);
    STAMP(21);
    WAIT_V(8); BAR();
    K2_ISSUE(2, 0); K2_ISSUE(3, 1);
    STAMP(22);
    int buf = 2;
    unsigned pol = 0;
#pragma unroll 1
    for (int b = 0; b < nt - 4; ++b) {
      WAIT_V(16); BAR();
      if (b == 6 && !isG) O_FLAG();
      if (b == 24) asm volatile("buffer_load_dword %0, %1, %2, 0 offen sc1" : "+v"(pol) : "v"(f2), "s"(wrs) : "memory");
      if (b == 28 && !isG) {
        asm volatile("" : "+v"(pol));
        if (__builtin_amdgcn_ballot_w64(pol != tag) != 0ull) POLL32(f2, 4);
      }
      K2_ISSUE(b + 4, buf);
      buf = (buf == 4) ? 0 : buf + 1;
    }
    const int c8 = lane & 15, r0 = wc * 4 + (lane >> 4);
    const unsigned goff = (unsigned)((tm * 128 + r0) * 2048 + tn * 256 + c8 * 16);
    const lptr cb = lds + (isG ? 0 : 65536) + r0 * 512;
    WAIT_V(16); BAR();
    WAIT_V(8); BAR();
    WAIT_V(0); BAR();
    if (isG) {
      BAR();
      STAMP(23);
      BAR();
#pragma unroll
      for (int i = 0; i < 8; ++i) {
        const f32x4 a0 = *(const LAS f32x4*)(cb + i * 8192 + (((2 * c8) ^ (r0 & 7)) << 4)), a1 = *(const LAS f32x4*)(cb + i * 8192 + (((2 * c8 + 1) ^ (r0 & 7)) << 4));
        ST128((u32x4){pk2(silu(a0[0]), silu(a0[1])), pk2(silu(a0[2]), silu(a0[3])), pk2(silu(a1[0]), silu(a1[1])), pk2(silu(a1[2]), silu(a1[3]))}, goff + i * 32768, WS_G);
      }
    } else {
      u32x4 xv[8], gv[8];
      const unsigned gf = __builtin_amdgcn_raw_buffer_load_b32(wrs, 0, WS_GFLAG + (tm * 8 + tn) * 128, 16);
#pragma unroll
      for (int i = 0; i < 8; ++i) xv[i] = *(const u32x4*)(xn + goff + i * 32768);
      BAR();
      if (__builtin_amdgcn_readfirstlane(gf) != tag) POLL32((unsigned)(WS_GFLAG + (tm * 8 + tn) * 128), 1);
      asm volatile("" ::: "memory");
#pragma unroll
      for (int i = 0; i < 8; ++i) gv[i] = __builtin_amdgcn_raw_buffer_load_b128(wrs, goff + i * 32768, WS_G, 16);
      float xf[8][8], gf2[8][8];
#pragma unroll
      for (int i = 0; i < 8; ++i) {
#pragma unroll
        for (int e = 0; e < 4; ++e) { xf[i][2 * e] = bf2f(xv[i][e] & 0xffffu); xf[i][2 * e + 1] = U2F(xv[i][e] & 0xffff0000u); }
        asm volatile("" : "+v"(xf[i][0]), "+v"(xf[i][1]), "+v"(xf[i][2]), "+v"(xf[i][3]), "+v"(xf[i][4]), "+v"(xf[i][5]), "+v"(xf[i][6]), "+v"(xf[i][7]));
      }
#pragma unroll
      for (int i = 0; i < 8; ++i) {
#pragma unroll
        for (int e = 0; e < 4; ++e) { gf2[i][2 * e] = bf2f(gv[i][e] & 0xffffu); gf2[i][2 * e + 1] = U2F(gv[i][e] & 0xffff0000u); }
        asm volatile("" : "+v"(gf2[i][0]), "+v"(gf2[i][1]), "+v"(gf2[i][2]), "+v"(gf2[i][3]), "+v"(gf2[i][4]), "+v"(gf2[i][5]), "+v"(gf2[i][6]), "+v"(gf2[i][7]));
      }
      STAMP(23);
      BAR();
      f32x4 a0[8], a1[8];
#pragma unroll
      for (int i = 0; i < 8; ++i) { a0[i] = *(const LAS f32x4*)(cb + i * 8192 + (((2 * c8) ^ (r0 & 7)) << 4)); a1[i] = *(const LAS f32x4*)(cb + i * 8192 + (((2 * c8 + 1) ^ (r0 & 7)) << 4)); }
      SCHED();
#pragma unroll
      for (int i = 0; i < 8; ++i) {
        const u32x4 w = {pk2(xf[i][0] + a0[i][0] * gf2[i][0], xf[i][1] + a0[i][1] * gf2[i][1]), pk2(xf[i][2] + a0[i][2] * gf2[i][2], xf[i][3] + a0[i][3] * gf2[i][3]),
                         pk2(xf[i][4] + a1[i][0] * gf2[i][4], xf[i][5] + a1[i][1] * gf2[i][5]), pk2(xf[i][6] + a1[i][2] * gf2[i][6], xf[i][7] + a1[i][3] * gf2[i][7])};
        *(u32x4*)(out + goff + i * 32768) = w;
      }
    }
    if (isG) {
      WAIT_V(0); BAR();
      if (wid == 0 && lane == 0) __builtin_amdgcn_raw_buffer_store_b32(tag, wrs, 0, WS_GFLAG + (tm * 8 + tn) * 128, 16);
      if (bid == 4 && wid == 0) {
        for (unsigned spins = 0; spins < 2000000u; ++spins) {
          bool ok = true;
#pragma unroll
          for (int i = 0; i < 4; ++i) ok = ok && __builtin_amdgcn_raw_buffer_load_b32(wrs, (unsigned)((lane + i * 64) * 128), WS_HFLAG, 16) == tag;
          if (__builtin_amdgcn_ballot_w64(!ok) == 0ull) break;
          __builtin_amdgcn_s_sleep(4);
          asm volatile("" ::: "memory");
        }
        asm volatile("" ::: "memory");
        if (lane == 0) __builtin_amdgcn_raw_buffer_store_b32(tag, wrs, 0, 0, 16);
      }
    }
  } else {
    asm volatile("s_waitcnt vmcnt(0)" :: "v"(pft) : "memory");
    O_OUT();
    if (isG) { WAIT_V(0); BAR(); }
    STAMP(20);
    STAMP(21);
    const int wm = wc >> 1, wn = wc & 1, g = lane >> 4, t = lane & 15, q4 = (lane >> 2) & 3, p4 = lane & 3;
    lptr w2b[4];
#pragma unroll
    for (int ft = 0; ft < 4; ++ft) w2b[ft] = lds + 3 * K2_STG + 16384 + wn * 1024 + (g >> 1) * 2048 + (4 * (g & 1) + q4) * 128 + 32 * (ft ^ (2 * (g & 1) + (q4 >> 1))) + 8 * p4;
    lptr ar = lds + 3 * K2_STG + wm * 8192 + lane * 16;
    f32x4 ac2[4][4];
#pragma unroll
    for (int m = 0; m < 4; ++m)
#pragma unroll
      for (int n = 0; n < 4; ++n) ac2[m][n] = (f32x4){0.f, 0.f, 0.f, 0.f};
    bf16x8 Af[2][4];
    int dlt = 0;
    bf16x4 Wh[2][4][2];
#define K2_RD(set, ks, i) do { if ((i) == 0) Af[set][0] = ldb128(ar + (ks) * 1024); else if ((i) >= 3 && (i) < 6) Af[set][(i) - 2] = ldb128(ar + ((i) - 2) * 2048 + (ks) * 1024); \
      else { const int f_ = (i) < 3 ? 0 : ((i) - 4) >> 1, h_ = (i) < 3 ? (i) - 1 : (i) & 1; \
             Wh[set][f_][h_] = __builtin_amdgcn_ds_read_tr16_b64_v4bf16((LAS bf16x4*)(w2b[f_] + (ks) * 8192 + h_ * 4096)); } } while (0)
#define K2_W(set, m) __builtin_shufflevector(Wh[set][m][0], Wh[set][m][1], 0, 1, 2, 3, 4, 5, 6, 7)
    BAR();
    STAMP(22);
#pragma unroll
    for (int i = 0; i < 12; ++i) { K2_RD(0, 0, i); SCHED(); }
    int buf = 3;
#define K2_TILE() do { SCHED(); \
      _Pragma("unroll") for (int i = 0; i < 16; ++i) {         \
        ac2[i >> 2][i & 3] = MFMA16(K2_W(0, i >> 2), Af[0][i & 3], ac2[i >> 2][i & 3]); \
        if (i < 12) K2_RD(1, 1, i); \
        if (i == 12) { dlt = (buf == 4) ? -4 * K2_STG : K2_STG; buf = (buf == 4) ? 0 : buf + 1; ar += dlt; asm volatile("" : "+v"(ar)); } \
        if (i == 13) { w2b[0] += dlt; w2b[1] += dlt; asm volatile("" : "+v"(w2b[0]), "+v"(w2b[1])); } \
        if (i == 14) { w2b[2] += dlt; w2b[3] += dlt; asm volatile("" : "+v"(w2b[2]), "+v"(w2b[3])); } \
        if (i == 15) BAR();                  \
        SCHED(); } \
      _Pragma("unroll") for (int i = 0; i < 16; ++i) {         \
        ac2[i >> 2][i & 3] = MFMA16(K2_W(1, i >> 2), Af[1][i & 3], ac2[i >> 2][i & 3]); \
        if (i < 12) K2_RD(0, 0, i); \
        SCHED(); } } while (0)
#pragma unroll 1
    for (int kt = 0; kt < 6; ++kt) K2_TILE();
    WAIT_V(0);
#pragma unroll 1
    for (int kt = 6; kt < nt; ++kt) K2_TILE();
    STAMP(23);
    const lptr cw = lds + (isG ? 0 : 65536) + (wm * 64 + t) * 512;
#pragma unroll
    for (int m = 0; m < 4; ++m)
#pragma unroll
      for (int n = 0; n < 4; ++n) *(LAS f32x4*)(cw + n * 8192 + (((wn * 16 + m * 4 + g) ^ (t & 7)) << 4)) = ac2[m][n];
    WAIT_L(0); BAR();
    if (isG) BAR();
  }
  STAMP(24);
  STAMP_OUT(ws + (57 << 20), 30);
#undef lane
}

extern "C" void kernel_launch(void* const* d_in, const int* in_sizes, int n_in, void* d_out, int out_size, void* d_ws, size_t ws_size, hipStream_t stream) {
  const char* x = (const char*)d_in[0]; const char* kc = (const char*)d_in[1]; const char* vc = (const char*)d_in[2];
  const char* wq = (const char*)d_in[3]; const char* wo = (const char*)d_in[4]; const char* w1 = (const char*)d_in[6]; const char* w2 = (const char*)d_in[7];
  (void)hipFuncSetAttribute((const void*)mega_kernel, hipFuncAttributeMaxDynamicSharedMemorySize, K1_LDS);
  mega_kernel<<<256, 512, K1_LDS, stream>>>(x, kc, vc, wq, w1, wo, w2, (char*)d_ws, (char*)d_out);
}
```

```cpp
#define PROBE_X 7
#include <hip/hip_runtime.h>
#include <stdint.h>

typedef unsigned short u16;
typedef __bf16 bf16x2 __attribute__((ext_vector_type(2)));
typedef __bf16 bf16x4 __attribute__((ext_vector_type(4)));
typedef __bf16 bf16x8 __attribute__((ext_vector_type(8)));
typedef float f32x2 __attribute__((ext_vector_type(2)));
typedef float f32x4 __attribute__((ext_vector_type(4)));
typedef float f32x16 __attribute__((ext_vector_type(16)));
typedef unsigned u32x2 __attribute__((ext_vector_type(2)));
typedef unsigned u32x4 __attribute__((ext_vector_type(4)));
#define LAS __attribute__((address_space(3)))
typedef LAS char* lptr;
#if defined(__HIP_DEVICE_COMPILE__) && !defined(USEPK)
#define NOPK __attribute__((target("no-packed-fp32-ops")))
#else
#define NOPK
#endif
#define DI NOPK __device__ __forceinline__
#define F2U(x) __builtin_bit_cast(unsigned, (float)(x))
#define U2F(x) __builtin_bit_cast(float, (unsigned)(x))
#define TID ((int)__builtin_amdgcn_workitem_id_x())
#define BID ((int)__builtin_amdgcn_workgroup_id_x())
#define SCHED() __builtin_amdgcn_sched_barrier(0)
#define BAR() asm volatile("s_barrier" ::: "memory")
#define WAIT_V(n) asm volatile("s_waitcnt vmcnt(%0)" :: "n"(n) : "memory")
#define WAIT_L(n) do { asm volatile("" ::: "memory"); __builtin_amdgcn_s_waitcnt(0xC07F | ((n) << 8)); asm volatile("" ::: "memory"); } while (0)
#define MFMA16(a, b, c) __builtin_amdgcn_mfma_f32_16x16x32_bf16(a, b, c, 0, 0, 0)
#define MFMA32(a, b, c) __builtin_amdgcn_mfma_f32_32x32x16_bf16(a, b, c, 0, 0, 0)

DI void glds(unsigned voff, const char* sbase, unsigned ldsaddr) {
  asm volatile("s_mov_b32 m0, %2\n\ts_nop 0\n\tglobal_load_lds_dwordx4 %0, %1" ::"v"(voff), "s"(sbase), "s"(ldsaddr) : "memory");
}
DI void glds1(unsigned voff, const char* sbase, unsigned ldsaddr) {
  asm volatile("s_mov_b32 m0, %2\n\ts_nop 0\n\tglobal_load_lds_dwordx4 %0, %1 sc1" ::"v"(voff), "s"(sbase), "s"(ldsaddr) : "memory");
}
DI unsigned pk2(float lo, float hi) { f32x2 v = {lo, hi}; return __builtin_bit_cast(unsigned, __builtin_convertvector(v, bf16x2)); }
DI float bf2f(unsigned b) { return U2F(b << 16); }
DI bf16x8 ldb128(lptr p) { return *(const LAS bf16x8*)p; }
DI bf16x8 ldtr2(lptr p, int d) {
  bf16x4 a = __builtin_amdgcn_ds_read_tr16_b64_v4bf16((LAS bf16x4*)p);
  bf16x4 b = __builtin_amdgcn_ds_read_tr16_b64_v4bf16((LAS bf16x4*)(p + d));
  return __builtin_shufflevector(a, b, 0, 1, 2, 3, 4, 5, 6, 7);
}
DI void ST128_(__amdgpu_buffer_rsrc_t r, u32x4 v, unsigned voff, int soff) { __builtin_amdgcn_raw_buffer_store_b128(v, r, voff + (unsigned)soff, 0, 16); }
DI float silu(float v) { return v * __builtin_amdgcn_rcpf(1.0f + __builtin_amdgcn_exp2f(v * -1.4426950408889634f)); }

#define STAMP_DECL const unsigned long long pa_ = __builtin_amdgcn_s_memrealtime(); if (BID == 0 && TID == 0) __hip_atomic_store((unsigned long long*)(ws + 2048), pa_, __ATOMIC_RELAXED, __HIP_MEMORY_SCOPE_AGENT);
#define LSTAMP(i)
#define STAMP(i)
#define STAMP_OUT(buf, n) do { if (BID == PROBE_X && wid == 0) { const unsigned long long t0_ = __hip_atomic_load((unsigned long long*)(ws + 2048), __ATOMIC_RELAXED, __HIP_MEMORY_SCOPE_AGENT); \
    const long long d_ = 10000 + (long long)(pa_ - t0_) * 8; \
    if (d_ > 0 && d_ < 100000) while ((long long)(__builtin_amdgcn_s_memrealtime() - t0_) < d_) __builtin_amdgcn_s_sleep(1); } } while (0)
template <int CTRL> DI float dpp_add(float v) { return v + U2F(__builtin_amdgcn_update_dpp(0u, F2U(v), CTRL, 0xf, 0xf, true)); }
DI float half_sum(float v) {
  v = dpp_add<0xB1>(v); v = dpp_add<0x4E>(v); v = dpp_add<0x141>(v); v = dpp_add<0x140>(v);
  auto rr = __builtin_amdgcn_permlane16_swap(F2U(v), F2U(v), false, false);
  return U2F(rr[0]) + U2F(rr[1]);
}
typedef __attribute__((address_space(1))) unsigned long long gu64;

#define SX(b, h) (98304 + (b) * 32768 + (h) * 16384)
constexpr int SLOT_B = 0, SLOT_E = 32768, SLOT_A = 65536, SLOT_C = 98304, SLOT_D = 131072, K1_LDS = 163840;

#define XSWAP 0
constexpr int WS_HFLAG = 4096;
constexpr int WS_GFLAG = 65536;
constexpr int WS_OFLAG = 131072;
#define OFS 16
constexpr int WS_STATS = 1 << 20;
constexpr int WS_G = 5 << 20, WS_O = 9 << 20, WS_H = 25 << 20, WS_END = 39 << 20;
constexpr int K2_STG = 32768;

NOPK __global__ __launch_bounds__(512, 2) void mega_kernel(const char* __restrict__ xn, const char* __restrict__ kc, const char* __restrict__ vc,
                                                           const char* __restrict__ wq, const char* __restrict__ w1, const char* __restrict__ wo, const char* __restrict__ w2,
                                                           char* ws, char* __restrict__ out) {
  STAMP_DECL
  STAMP(0);
  extern __shared__ __attribute__((aligned(16))) char shm[];
  const lptr lds = (lptr)shm;
  const unsigned lds0 = (unsigned)(uintptr_t)lds;
  const int tid = TID, wid = __builtin_amdgcn_readfirstlane(tid >> 6), lane = tid & 63, wr = wid >> 2, wc = wid & 3;
  const int bid = BID ^ XSWAP, xcd = bid & 7, idx = bid >> 3, hd = 4 * xcd + (idx & 3), rb = idx >> 2;
  const int g = lane >> 4, t = lane & 15, q4 = (lane >> 2) & 3, p4 = lane & 3;

  const bool light = xcd >= 4;
  unsigned voffX[4], voffQ[2], voffH[2];
  {
    const int r = lane >> 3, c = lane & 7;
#pragma unroll
    for (int i = 0; i < 4; ++i) voffX[i] = (unsigned)((8 * i + r) * 2048 + ((c ^ (4 * (i & 1) + (r >> 1))) << 4) - i * 1024);
    const int k = 32 * (wid >> 2) + 16 * (wid & 1) + 4 * ((wid >> 1) & 1) + 8 * (r >> 2) + (r & 3);
    const int cw = c ^ (((r >> 1) & 3) << 1);
    voffQ[0] = (unsigned)(k * 8192 + cw * 16 + 1024); voffQ[1] = voffQ[0] + 128 - 1024;
    voffH[0] = (unsigned)(k * 7168 + 128 + (light ? (cw & 3) : cw) * 16 + 1024);
    voffH[1] = (unsigned)(k * 7168 + cw * 16);
  }
  const char* xb = xn + (size_t)rb * 256 * 2048 + wid * 65536;
  const char* qb = wq + hd * 256 - 1024;
  const char* hb = w1 + (light ? 4096 + 192 * (hd - 16) : 256 * hd) - 1024;
  const bool full = wr == 1 || !light;
#define GLDS2(v0_, v1_, sb_, ld_) asm volatile("s_mov_b32 m0, %3\n\ts_nop 0\n\tglobal_load_lds_dwordx4 %0, %2\n\tglobal_load_lds_dwordx4 %1, %2 offset:1024" \
    ::"v"(v0_), "v"(v1_), "s"(sb_), "s"(ld_) : "memory")
#define GLDS4(v0_, v1_, v2_, v3_, sb_, ld_) asm volatile("s_mov_b32 m0, %5\n\ts_nop 0\n\tglobal_load_lds_dwordx4 %0, %4\n\tglobal_load_lds_dwordx4 %1, %4 offset:1024\n\t" \
    "global_load_lds_dwordx4 %2, %4 offset:2048\n\tglobal_load_lds_dwordx4 %3, %4 offset:3072" ::"v"(v0_), "v"(v1_), "v"(v2_), "v"(v3_), "s"(sb_), "s"(ld_) : "memory")
#define ST_X(b, kt) GLDS4(voffX[0], voffX[1], voffX[2], voffX[3], xb + (kt) * 128, lds0 + SX(b, 0) + wid * 4096)
#define ST_W0(wo_, kt) GLDS2(voffQ[0], voffQ[1], qb + (size_t)(kt) * 524288, lds0 + (wo_) + wid * 2048)
#define ST_W1(wo_, kt) GLDS2(voffH[0], voffH[1], hb + (size_t)(kt) * 458752, lds0 + (wo_) + 16384 + wid * 2048)

  lptr wb[4], xr[2];
#pragma unroll
  for (int m = 0; m < 4; ++m) wb[m] = lds + wr * 1024 + (g >> 1) * 2048 + (4 * (g & 1) + q4) * 128 + 32 * ((m ^ wc) ^ (2 * (g & 1) + (q4 >> 1))) + 8 * p4;
#pragma unroll
  for (int ks = 0; ks < 2; ++ks) xr[ks] = lds + 98304 + (wc * 32 + t) * 128 + 16 * ((4 * ks + g) ^ (t >> 1));
#define LDW(dst, h) do { _Pragma("unroll") for (int m = 0; m < 4; ++m) _Pragma("unroll") for (int k = 0; k < 2; ++k) dst[m][k] = ldtr2(wb[m] + (h) * 16384 + k * 8192, 4096); } while (0)
#define LDX(dst, b, h) do { _Pragma("unroll") for (int n = 0; n < 2; ++n) _Pragma("unroll") for (int k = 0; k < 2; ++k) dst[n][k] = ldb128(xr[k] + (SX(b, h) - 98304) + n * 2048); } while (0)

  f32x4 acc[2][2][4][2];
#pragma unroll
  for (int a = 0; a < 2; ++a)
#pragma unroll
    for (int b = 0; b < 2; ++b)
#pragma unroll
      for (int m = 0; m < 4; ++m)
#pragma unroll
        for (int n = 0; n < 2; ++n) acc[a][b][m][n] = (f32x4){0.f, 0.f, 0.f, 0.f};
  bf16x8 At[4][2], B0[2][2], B1[2][2];

#define MM4(ai, m, k) do { _Pragma("unroll") for (int n = 0; n < 2; ++n) { \
      acc[ai][0][m][n] = MFMA16(At[m][k], B0[n][k], acc[ai][0][m][n]); acc[ai][1][m][n] = MFMA16(At[m][k], B1[n][k], acc[ai][1][m][n]); } } while (0)
#define MMA2(ai, CLOSE) do { __builtin_amdgcn_s_setprio(1); \
    MM4(ai, 0, 0); cs[ai] = MFMA16(At[0][0], ones, cs[ai]); MM4(ai, 1, 0); MM4(ai, 0, 1); cs[ai] = MFMA16(At[0][1], ones, cs[ai]); MM4(ai, 1, 1); \
    asm volatile("" : "+v"(cs[ai])); \
    if ((ai) == 0 || full) { MM4(ai, 2, 0); MM4(ai, 3, 0); MM4(ai, 2, 1); MM4(ai, 3, 1); } \
    SCHED(); CLOSE; __builtin_amdgcn_s_setprio(0); SCHED(); } while (0)
  char* const stats = ws + WS_STATS;
  const __amdgpu_buffer_rsrc_t wrs = __builtin_amdgcn_make_buffer_rsrc(ws, 0, WS_END, 0x00020000);
  f32x4 cs[2] = {{0.f, 0.f, 0.f, 0.f}, {0.f, 0.f, 0.f, 0.f}};
  bf16x8 ones = __builtin_bit_cast(bf16x8, (u32x4){0x3f803f80u, 0x3f803f80u, 0x3f803f80u, 0x3f803f80u});
#define PIN_SETUP() do { asm volatile("" : "+v"(ones), "+v"(cs[0]), "+v"(cs[1]), "+v"(wb[0]), "+v"(wb[1]), "+v"(wb[2]), "+v"(wb[3]), "+v"(xr[0]), "+v"(xr[1])); \
    _Pragma("unroll") for (int a = 0; a < 2; ++a) _Pragma("unroll") for (int b = 0; b < 2; ++b) \
      asm volatile("" : "+v"(acc[a][b][0][0]), "+v"(acc[a][b][0][1]), "+v"(acc[a][b][1][0]), "+v"(acc[a][b][1][1]), "+v"(acc[a][b][2][0]), "+v"(acc[a][b][2][1]), "+v"(acc[a][b][3][0]), "+v"(acc[a][b][3][1])); } while (0)
  unsigned tag;
  {
    if (wr == 0) {
      unsigned ep;
      ST_X(0, 0); ST_W0(0, 0);
      asm volatile("global_load_dword %0, %1, %2 sc1" : "=&v"(ep) : "v"(0), "s"(ws) : "memory");
      ST_W1(0, 0); ST_W0(32768, 1); ST_X(1, 1);
      PIN_SETUP();
      asm volatile("s_waitcnt vmcnt(8)" : "+v"(ep) :: "memory");
      tag = __builtin_amdgcn_readfirstlane(ep) + 1u;
      BAR();
    } else {
      const int row = rb * 256 + hd * 8 + wc * 2 + (lane >> 5);
      u32x4 a[4];
      unsigned ep;
      asm volatile("global_load_dword %0, %1, %2 sc1" : "=&v"(ep) : "v"(0), "s"(ws) : "memory");
      asm volatile("global_load_dwordx4 %0, %4, %5\n\tglobal_load_dwordx4 %1, %4, %5 offset:512\n\tglobal_load_dwordx4 %2, %4, %5 offset:1024\n\tglobal_load_dwordx4 %3, %4, %5 offset:1536"
                   : "=&v"(a[0]), "=&v"(a[1]), "=&v"(a[2]), "=&v"(a[3]) : "v"(row * 2048 + (lane & 31) * 16), "s"(xn) : "memory");
      ST_X(0, 0); ST_W0(0, 0);
      ST_W1(0, 0); ST_W0(32768, 1); ST_X(1, 1);
      PIN_SETUP();
      asm volatile("s_waitcnt vmcnt(8)" : "+v"(ep), "+v"(a[0]), "+v"(a[1]), "+v"(a[2]), "+v"(a[3]) :: "memory");
      BAR();
      tag = __builtin_amdgcn_readfirstlane(ep) + 1u;
      float v[32];
#pragma unroll
      for (int i = 0; i < 16; ++i) { v[2 * i] = bf2f(a[i >> 2][i & 3] & 0xffffu); v[2 * i + 1] = U2F(a[i >> 2][i & 3] & 0xffff0000u); }
      float s0 = 0.f, s1 = 0.f, s2 = 0.f, s3 = 0.f;
#pragma unroll
      for (int i = 0; i < 8; ++i) { s0 += v[i]; s1 += v[8 + i]; s2 += v[16 + i]; s3 += v[24 + i]; }
      const float mean = half_sum((s0 + s1) + (s2 + s3)) * (1.0f / 1024.0f);
      s0 = s1 = s2 = s3 = 0.f;
#pragma unroll
      for (int i = 0; i < 8; ++i) { const float d0 = v[i] - mean, d1 = v[8 + i] - mean, d2 = v[16 + i] - mean, d3 = v[24 + i] - mean; s0 += d0 * d0; s1 += d1 * d1; s2 += d2 * d2; s3 += d3 * d3; }
      const float sd = __builtin_amdgcn_sqrtf(half_sum((s0 + s1) + (s2 + s3)) * (1.0f / 1024.0f) + 1e-5f);
      if ((lane & 31) < 2) __hip_atomic_store((gu64*)(stats + row * 16 + (lane & 1) * 8), ((unsigned long long)tag << 32) | F2U((lane & 1) ? sd : mean), __ATOMIC_RELAXED, __HIP_MEMORY_SCOPE_AGENT);
    }
  }
  if (wr == 1) BAR();
  SCHED();
  STAMP(1);
  const int kvg = hd >> 3;
  const char* kb_g = kc + ((size_t)kvg * 2048 + 1536) * 256;
  const char* vb_g = vc + ((size_t)kvg * 2048 + 1536) * 256;
#define VOFFK ((unsigned)(((lane >> 2) & 7) * 256 + (4 * (lane >> 5) + ((lane & 3) ^ ((2 * (wid & 1) + ((lane >> 4) & 1)) & 3))) * 16))
#define VOFFV ((unsigned)(((lane >> 2) & 7) * 256 + (lane >> 5) * 64 + (lane & 3) * 16))
#define ST_KV8(voff, gsrc, slot) do { glds(voff, (gsrc) + wid * 2048, lds0 + (slot) + wid * 2048); glds(voff, (gsrc) + wid * 2048 + 128, lds0 + (slot) + wid * 2048 + 1024); } while (0)
  int wprv = 65536, wnxt = 32768;
#define W_ADV() do { const int d_ = (wnxt == 0) ? -65536 : 32768, cur_ = wnxt - d_; wnxt = wprv; wprv = cur_; \
    _Pragma("unroll") for (int f = 0; f < 4; ++f) wb[f] += d_; \
    asm volatile("" : "+v"(wb[0]), "+v"(wb[1]), "+v"(wb[2]), "+v"(wb[3])); } while (0)
#define PH_A(b, STAGE) do { LDX(B0, b, 0); LDX(B1, b, 1); SCHED(); LDW(At, 0); STAGE; WAIT_V(10); WAIT_L(12); BAR(); SCHED(); MMA2(0, WAIT_L(0); BAR()); } while (0)
#define PH_B(STAGE) do { LDW(At, 1); STAGE; W_ADV(); WAIT_V(8); BAR(); SCHED(); MMA2(1, WAIT_L(0); BAR()); } while (0)
#pragma unroll 1
  for (int kt = 0; kt < 14; kt += 2) {
    PH_A(0, ST_W1(wnxt, kt + 1); ST_W0(wprv, kt + 2));
    PH_B(ST_X(0, kt + 2));
    PH_A(1, ST_W1(wnxt, kt + 2); ST_W0(wprv, kt + 3));
    PH_B(ST_X(1, kt + 3));
  }
  STAMP(2);
  const unsigned voffK = VOFFK, voffV = VOFFV;
  PH_A(0, ST_W1(0, 15); ST_KV8(voffK, kb_g, SLOT_E));
  PH_B(ST_KV8(voffV, vb_g, SLOT_C); ST_KV8(voffV, vb_g + 16384, SLOT_C + 16384));
  const unsigned soff = (unsigned)((rb * 256 + (g >> 1) * 128 + wc * 32 + (g & 1) * 16 + t) * 16);
  u32x4 sg;
  LDX(B0, 1, 0); LDX(B1, 1, 1); SCHED(); LDW(At, 0);
  asm volatile("global_load_dwordx4 %0, %1, %2 sc1" : "=&v"(sg) : "v"(soff), "s"(stats) : "memory");
  ST_KV8(voffK, kb_g + 16384, SLOT_E + 16384); ST_KV8(voffK, kb_g + 32768, SLOT_A);
  WAIT_V(11); BAR(); SCHED(); MMA2(0, WAIT_L(0); BAR());
  LDW(At, 1);
  ST_KV8(voffK, kb_g + 49152, SLOT_A + 16384);
  BAR(); SCHED();
  const lptr csw = lds + SLOT_D + (t & 3) * 8192 + wr * 4096 + wc * 64 + g * 16;
  MMA2(1, *(LAS f32x4*)csw = cs[0]; *(LAS f32x4*)(csw + 256) = cs[1]; asm volatile("s_waitcnt vmcnt(4)" : "+v"(sg) :: "memory"); WAIT_L(0); BAR());
  f32x4 cv[2][4];
#pragma unroll
  for (int a = 0; a < 2; ++a)
#pragma unroll
    for (int m = 0; m < 4; ++m) cv[a][m] = *(const LAS f32x4*)(lds + SLOT_D + wc * 8192 + wr * 4096 + a * 256 + (m ^ wc) * 64 + g * 16);
  if (__builtin_expect(__builtin_amdgcn_ballot_w64(sg[1] != tag || sg[3] != tag) != 0ull, 0)) {
    for (unsigned spins = 0; spins < 4000000u; ++spins) {
      __builtin_amdgcn_s_sleep(2);
      asm volatile("" ::: "memory");
      sg = __builtin_amdgcn_raw_buffer_load_b128(wrs, soff, WS_STATS, 16);
      if (__builtin_amdgcn_ballot_w64(sg[1] != tag || sg[3] != tag) == 0ull) break;
    }
  }
  float mu[2][2], rs[2][2], sd[2][2];
  {
    auto m16 = __builtin_amdgcn_permlane16_swap(sg[0], sg[0], false, false);
    auto s16 = __builtin_amdgcn_permlane16_swap(sg[2], sg[2], false, false);
#pragma unroll
    for (int n = 0; n < 2; ++n) {
      auto m32 = __builtin_amdgcn_permlane32_swap(m16[n], m16[n], false, false);
      auto s32 = __builtin_amdgcn_permlane32_swap(s16[n], s16[n], false, false);
#pragma unroll
      for (int bj = 0; bj < 2; ++bj) { mu[bj][n] = -U2F(m32[bj]); sd[bj][n] = U2F(s32[bj]); rs[bj][n] = __builtin_amdgcn_rcpf(sd[bj][n]) * 1.4426950408889634f; }
    }
  }
  WAIT_L(0);
  STAMP(3);
  {
    const lptr qw = lds + wc * 8192 + wr * 4096 + ((g >> 1) * 32 + t) * 16 + (g & 1) * 8;
#pragma unroll
    for (int bj = 0; bj < 2; ++bj) {
      lptr qwb = qw + (bj ? SLOT_D : SLOT_B);
      asm volatile("" : "+v"(qwb));
#pragma unroll
      for (int m = 0; m < 4; ++m)
#pragma unroll
        for (int n = 0; n < 2; ++n) {
          const f32x4 v = (acc[0][bj][m][n] + cv[0][m] * mu[bj][n]) * (rs[bj][n] * 0.08838834764831845f);
          u32x2 w = {pk2(v[0], v[1]), pk2(v[2], v[3])};
          *(LAS u32x2*)(qwb + (m ^ wc) * 1024 + n * 256) = w;
        }
    }
  }
  WAIT_L(0); BAR(); SCHED();
  STAMP(4);
  bf16x8 qf[8];
  const lptr qr = lds + (wr ? SLOT_D : SLOT_B) + wc * 8192 + lane * 16;
  if (wr == 1) {
#pragma unroll
    for (int s = 0; s < 8; ++s) qf[s] = ldb128(qr + s * 1024);
  }
  {
#define ST128(...) ST128_(wrs, __VA_ARGS__)
    u32x2 hw[2][2][4];
#define SILU16(m) do { \
      const f32x4 a0 = acc[1][0][m][0] + cv[1][m] * mu[0][0], a1 = acc[1][0][m][1] + cv[1][m] * mu[0][1], a2 = acc[1][1][m][0] + cv[1][m] * mu[1][0], a3 = acc[1][1][m][1] + cv[1][m] * mu[1][1]; \
      f32x4 e0 = a0 * -rs[0][0], e1 = a1 * -rs[0][1], e2 = a2 * -rs[1][0], e3 = a3 * -rs[1][1]; SCHED(); \
      _Pragma("unroll") for (int i = 0; i < 4; ++i) { e0[i] = __builtin_amdgcn_exp2f(e0[i]); e1[i] = __builtin_amdgcn_exp2f(e1[i]); e2[i] = __builtin_amdgcn_exp2f(e2[i]); e3[i] = __builtin_amdgcn_exp2f(e3[i]); } SCHED(); \
      e0 = e0 * sd[0][0] + sd[0][0]; e1 = e1 * sd[0][1] + sd[0][1]; e2 = e2 * sd[1][0] + sd[1][0]; e3 = e3 * sd[1][1] + sd[1][1]; SCHED(); \
      _Pragma("unroll") for (int i = 0; i < 4; ++i) { e0[i] = __builtin_amdgcn_rcpf(e0[i]); e1[i] = __builtin_amdgcn_rcpf(e1[i]); e2[i] = __builtin_amdgcn_rcpf(e2[i]); e3[i] = __builtin_amdgcn_rcpf(e3[i]); } SCHED(); \
      e0 *= a0; e1 *= a1; e2 *= a2; e3 *= a3; SCHED(); \
      hw[0][0][m] = (u32x2){pk2(e0[0], e0[1]), pk2(e0[2], e0[3])}; hw[0][1][m] = (u32x2){pk2(e1[0], e1[1]), pk2(e1[2], e1[3])}; \
      hw[1][0][m] = (u32x2){pk2(e2[0], e2[1]), pk2(e2[2], e2[3])}; hw[1][1][m] = (u32x2){pk2(e3[0], e3[1]), pk2(e3[2], e3[3])}; SCHED(); } while (0)
    SILU16(0);
    if (wr == 0) {
      BAR(); SCHED();
#pragma unroll
      for (int s = 0; s < 8; ++s) qf[s] = ldb128(qr + s * 1024);
    }
    SILU16(1);
    const unsigned hoff = (unsigned)((rb * 16 + wc * 2) * 2048 + lane * 16);
    const int pb = (light ? 16 + 3 * hd : 4 * hd) + (wr ? 0 : 2), sw = full ? (wc >> 1) : 0, p0 = pb + sw, p1 = pb + (sw ^ 1);
#define H_OUT1(p_, a_, b_) do { _Pragma("unroll") for (int bj = 0; bj < 2; ++bj) _Pragma("unroll") for (int n = 0; n < 2; ++n) \
      ST128((u32x4){hw[bj][n][a_][0], hw[bj][n][a_][1], hw[bj][n][b_][0], hw[bj][n][b_][1]}, hoff + n * 2048, WS_H + ((p_) >> 1) * 262144 + ((p_) & 1) * 1024 + bj * 16384); } while (0)
#define H_OUT(p_, m_) do { if (wc & 1) H_OUT1(p_, (m_) + 1, m_); else H_OUT1(p_, m_, (m_) + 1); } while (0)
    H_OUT(p0, 0);
    if (full) { SILU16(2); SILU16(3); H_OUT(p1, 2); }
  }
  STAMP(5);
  if (wr == 1) { WAIT_L(0); BAR(); }
  __builtin_amdgcn_s_setprio(1);
  SCHED();
  STAMP(6);
  const int r32 = lane & 31, hh = lane >> 5;
  lptr kr[2];
#pragma unroll
  for (int e = 0; e < 2; ++e) kr[e] = lds + 2048 * (r32 >> 3) + 64 * (r32 & 7) + 16 * ((2 * e + hh) ^ ((r32 >> 2) & 3));
  const lptr vr = lds + ((lane & 3) << 3) + (((lane >> 2) & 3) << 6) + (((lane >> 4) & 1) << 5) + (hh << 8);
  f32x16 o[4], s[4];
#pragma unroll
  for (int d = 0; d < 4; ++d)
#pragma unroll
    for (int i = 0; i < 16; ++i) o[d][i] = 0.f;
  bf16x8 pf[8];
  constexpr int RD = 6;
  bf16x8 fr[RD];
  f32x4 lacc = {0.f, 0.f, 0.f, 0.f};
  const unsigned selw = (((lane >> 4) ^ (lane >> 2)) & 1) ? 0u : 0x3f803f80u;
  bf16x8 sel = __builtin_bit_cast(bf16x8, (u32x4){selw, selw, selw, selw});
  asm volatile("" : "+v"(sel));
  f32x16 negm = {0.f, 0.f, 0.f, 0.f, 0.f, 0.f, 0.f, 0.f, 0.f, 0.f, 0.f, 0.f, 0.f, 0.f, 0.f, 0.f};
#define SM_THR 60.0f
#define FRAG(i) ((i) < 32 ? ldtr2(v0 + (((i) >> 2) * 8 + ((i) & 3)) * 512, 2048) : ldb128((((((i) - 32) >> 2) & 1) ? k1 : k0) + ((i) & 3) * 8192 + (((i) - 32) >> 3) * 512))
#define XEARLY 3
#define XPRIO 1
#define YPRIO 0
#define XSEG(lo, hi, CLOSE) do { __builtin_amdgcn_s_setprio(XPRIO); \
    _Pragma("unroll") for (int i = (lo); i < (hi); ++i) { \
      if (i < 32) o[i & 3] = MFMA32(fr[i % RD], pf[(i >> 2) & 7], o[i & 3]); \
      else s[i & 3] = MFMA32(fr[i % RD], qf[((i - 32) >> 2) & 7], i < 36 ? negm : s[i & 3]); \
      if (i < 32 && (i & 3) == 1) lacc = MFMA16(sel, pf[(i >> 2) & 7], lacc); \
      if (i + RD < (hi)) fr[i % RD] = FRAG(i + RD); \
      SCHED(); \
      if (i == 63 - XEARLY) { __builtin_amdgcn_s_setprio(2); CLOSE; WAIT_L(0); BAR(); SCHED(); } } \
    __builtin_amdgcn_s_setprio(YPRIO); } while (0)
#define PREF(lo) do { _Pragma("unroll") for (int i = (lo); i < (lo) + RD; ++i) fr[i % RD] = FRAG(i); SCHED(); } while (0)
#define MX3(a, b, c) __builtin_fmaxf(__builtin_fmaxf((a), (b)), (c))
#define SOFTMAX() do { \
      float m0 = MX3(s[0][0], s[0][1], s[0][2]), m1 = MX3(s[1][0], s[1][1], s[1][2]), m2 = MX3(s[2][0], s[2][1], s[2][2]), m3 = MX3(s[3][0], s[3][1], s[3][2]); \
      _Pragma("unroll") for (int i = 3; i < 15; i += 2) { m0 = MX3(m0, s[0][i], s[0][i + 1]); m1 = MX3(m1, s[1][i], s[1][i + 1]); m2 = MX3(m2, s[2][i], s[2][i + 1]); m3 = MX3(m3, s[3][i], s[3][i + 1]); } \
      m0 = MX3(m0, s[0][15], s[1][15]); m2 = MX3(m2, s[2][15], s[3][15]); \
      float mx = __builtin_fmaxf(MX3(m0, m1, m2), m3); \
        \
      if (__builtin_expect(__builtin_amdgcn_ballot_w64(!(mx <= SM_THR)) != 0ull, 0)) { \
        auto rr = __builtin_amdgcn_permlane32_swap(F2U(mx), F2U(mx), false, false); \
        const float dl = MX3(U2F(rr[0]), U2F(rr[1]), 0.f); \
        _Pragma("unroll") for (int kb = 0; kb < 4; ++kb) s[kb] -= dl; \
        negm -= dl; \
        const float alpha = __builtin_amdgcn_exp2f(-dl); lacc *= alpha; _Pragma("unroll") for (int d = 0; d < 4; ++d) o[d] *= alpha; \
      } \
      SCHED(); \
      _Pragma("unroll") for (int kb = 0; kb < 4; ++kb) { \
        _Pragma("unroll") for (int e = 0; e < 2; ++e) { \
          _Pragma("unroll") for (int i = 0; i < 8; ++i) s[kb][8 * e + i] = __builtin_amdgcn_exp2f(s[kb][8 * e + i]); \
          u32x4 w = {pk2(s[kb][8 * e], s[kb][8 * e + 1]), pk2(s[kb][8 * e + 2], s[kb][8 * e + 3]), pk2(s[kb][8 * e + 4], s[kb][8 * e + 5]), pk2(s[kb][8 * e + 6], s[kb][8 * e + 7])}; \
          pf[2 * kb + e] = __builtin_bit_cast(bf16x8, w); } \
        asm volatile("" : "+v"(pf[2 * kb]), "+v"(pf[2 * kb + 1])); \
        SCHED(); } } while (0)
  {
    const lptr k0 = kr[0] + SLOT_E, k1 = kr[1] + SLOT_E, v0 = vr;
    {
      bf16x8 kf[8];
#pragma unroll
      for (int st = 0; st < 8; ++st) kf[st] = FRAG(32 + 4 * st);
      PREF(32);
#pragma unroll
      for (int st = 0; st < 8; ++st) negm = MFMA32(kf[st], qf[st], negm);
      float m0 = MX3(negm[0], negm[1], negm[2]), m1 = MX3(negm[3], negm[4], negm[5]);
      m0 = MX3(m0, negm[6], negm[7]); m1 = MX3(m1, negm[8], negm[9]); m0 = MX3(m0, negm[10], negm[11]); m1 = MX3(m1, negm[12], negm[13]);
      m0 = __builtin_fmaxf(MX3(m0, negm[14], negm[15]), m1);
      auto rr = __builtin_amdgcn_permlane32_swap(F2U(m0), F2U(m0), false, false);
      m0 = -__builtin_fmaxf(U2F(rr[0]), U2F(rr[1]));
#pragma unroll
      for (int i = 0; i < 16; ++i) negm[i] = m0;
      asm volatile("" : "+v"(negm));
      SCHED();
    }
    XSEG(32, 64, WAIT_V(4));
  }
  STAMP(7);
  const char* kvsrc = (wr ? kb_g + 65536 : vb_g + 32768) + wc * 8192 - 1024;
  unsigned kvo[2][2];
#pragma unroll
  for (int par = 0; par < 2; ++par) {
    const int xx = wr ? ((lane & 3) ^ ((2 * par + ((lane >> 4) & 1)) & 3)) : (lane & 3);
    kvo[par][0] = (unsigned)(((lane >> 2) & 7) * 256 + (lane >> 5) * 64 + xx * 16 + 1024);
    kvo[par][1] = kvo[par][0] + 128 - 1024;
  }
  const unsigned pfo = (unsigned)((lane < 21 ? lane : 20) * 128);
  unsigned pft = 0;
#pragma unroll 1
  for (int c = 0; c < 3; ++c) {
    if (c == 0) STAMP(8); else if (c == 1) STAMP(12);
    if (c == 1 && wid == 4 && lane == 0) __builtin_amdgcn_raw_buffer_store_b32(tag, wrs, 0, WS_HFLAG + (rb * 32 + hd) * 128, 16);
    if (c + wr < 3) {
      const int slot = wr ? (c == 0 ? SLOT_B : SLOT_C) : (c == 0 ? SLOT_D : (c == 1 ? SLOT_E : SLOT_A));
#pragma unroll
      for (int hf = 0; hf < 2; ++hf)
        asm volatile("s_mov_b32 m0, %5\n\ts_nop 0\n\t"
                     "global_load_lds_dwordx4 %0, %4\n\tglobal_load_lds_dwordx4 %1, %4 offset:1024\n\t"
                     "global_load_lds_dwordx4 %2, %4 offset:2048\n\tglobal_load_lds_dwordx4 %3, %4 offset:3072"
                     ::"v"(kvo[0][0]), "v"(kvo[0][1]), "v"(kvo[1][0]), "v"(kvo[1][1]), "s"(kvsrc + c * 32768 + hf * 4096), "s"(lds0 + slot + wc * 8192 + hf * 4096) : "memory");
    }
    {
      const int sl = (bid * 8 + wid) * 3 + c;
      const int o1 = sl * 2688, o2 = (sl - 3121) * 2688;
      const char* pp = sl < 3121 ? wo + (o1 < 8385920 ? o1 : 8385920) : w2 + (o2 < 7337344 ? o2 : 7337344);
      asm volatile("global_load_dword %0, %1, %2" : "+v"(pft) : "v"(pfo), "s"(pp) : "memory");
    }
    SOFTMAX();
    const int kslot = (c == 0) ? SLOT_A : ((c == 1) ? SLOT_B : SLOT_C);
    const int vslot = (c == 0) ? SLOT_C : ((c == 1) ? SLOT_D : SLOT_E);
    const lptr k0 = kr[0] + kslot, k1 = kr[1] + kslot, v0 = vr + vslot;
    PREF(0);
    if (c == 0) STAMP(9); else if (c == 1) STAMP(13);
    BAR(); SCHED();
    if (c == 0) STAMP(10); else if (c == 1) STAMP(14);
    XSEG(0, 64, WAIT_V(1); if (c == 0) WAIT_V(0));
    if (c == 0) STAMP(11); else if (c == 2) STAMP(15);
  }
  STAMP(16);
  SOFTMAX();
  {
    const lptr k0 = kr[0], k1 = kr[1], v0 = vr + SLOT_A;
    PREF(0);
    STAMP(17);
    if (wr == 0) BAR();
    SCHED();
    STAMP(18);
    XSEG(0, 32, );
  }
  STAMP(19);
  int lane2 = __builtin_amdgcn_mbcnt_hi(~0u, __builtin_amdgcn_mbcnt_lo(~0u, 0u));
  asm volatile("" : "+v"(lane2));
#define lane lane2
  const int isG = xcd >> 2, j2 = xcd & 3, tm = 8 * (j2 >> 1) + (idx & 7), tn = 4 * (j2 & 1) + (idx >> 3), nt = isG ? 56 : 64;
#define O_OUT() do { \
    const float inv = __builtin_amdgcn_rcpf(lacc[0]); \
    const unsigned oo = (unsigned)((rb * 16 + wr * 8 + wc * 2 + (r32 >> 4)) * 2048 + (hh * 16 + (r32 & 15)) * 16); \
    _Pragma("unroll") for (int d = 0; d < 4; ++d) _Pragma("unroll") for (int e = 0; e < 2; ++e) { \
        u32x4 w = {pk2(o[d][8 * e] * inv, o[d][8 * e + 1] * inv), pk2(o[d][8 * e + 2] * inv, o[d][8 * e + 3] * inv), \
                   pk2(o[d][8 * e + 4] * inv, o[d][8 * e + 5] * inv), pk2(o[d][8 * e + 6] * inv, o[d][8 * e + 7] * inv)}; \
        ST128(w, oo + (d & 1) * 1024 + e * 512, WS_O + (2 * hd + (d >> 1)) * 262144); } } while (0)
#define O_FLAG() do { if (wid == 0 && lane == 0) __builtin_amdgcn_raw_buffer_store_b32(tag, wrs, 0, WS_OFLAG + (rb * 32 + hd) * OFS, 16); } while (0)
#define POLL32(off_, slp) do { for (unsigned spins = 0; spins < 2000000u; ++spins) { \
      if (__builtin_amdgcn_ballot_w64(__builtin_amdgcn_raw_buffer_load_b32(wrs, (off_), 0, 16) != tag) == 0ull) break; \
      __builtin_amdgcn_s_sleep(slp); asm volatile("" ::: "memory"); } \
    asm volatile("" ::: "memory"); } while (0)
  if (wr == 0) {
    const char* ab = ws + (isG ? WS_H : WS_O) + tm * 16384 + wc * 4096;
    const char* wbp = (isG ? w2 : wo) + tn * 256 - 1024 + (wc >> 1) * 65536 + (wc & 1) * (isG ? 32768 : 16384);
    const unsigned voffA = lane * 16;
    unsigned voffW[2][2];
    {
      const int r = lane >> 3, c = lane & 7;
      voffW[0][0] = (unsigned)(r * 2048 + ((c ^ (((r >> 1) & 3) << 1)) << 4) + 1024);
      voffW[0][1] = voffW[0][0] + 128 - 1024;
      voffW[1][0] = voffW[0][0] + (isG ? 16384 : 32768) - 2048;
      voffW[1][1] = voffW[1][0] + 128 - 1024;
    }
#define K2_ISSUE(p_, buf_) do { const int k_ = isG ? ((p_) + 14 * j2 >= 56 ? (p_) + 14 * j2 - 56 : (p_) + 14 * j2) : ((((p_) + 8 * j2) & 31) | (~(p_) & 32)); \
      const unsigned d_ = lds0 + (buf_) * K2_STG + wc * 4096; \
      asm volatile("s_mov_b32 m0, %2\n\ts_nop 0\n\tglobal_load_lds_dwordx4 %0, %1 sc1\n\tglobal_load_lds_dwordx4 %0, %1 offset:1024 sc1\n\t" \
                   "global_load_lds_dwordx4 %0, %1 offset:2048 sc1\n\tglobal_load_lds_dwordx4 %0, %1 offset:3072 sc1" \
                   ::"v"(voffA), "s"(ab + (size_t)k_ * 262144), "s"(d_) : "memory"); \
      asm volatile("s_mov_b32 m0, %5\n\ts_nop 0\n\tglobal_load_lds_dwordx4 %0, %4\n\tglobal_load_lds_dwordx4 %1, %4 offset:1024\n\t" \
                   "global_load_lds_dwordx4 %2, %4 offset:2048\n\tglobal_load_lds_dwordx4 %3, %4 offset:3072" \
                   ::"v"(voffW[0][0]), "v"(voffW[0][1]), "v"(voffW[1][0]), "v"(voffW[1][1]), "s"(wbp + (size_t)k_ * 131072), "s"(d_ + 16384) : "memory"); } while (0)
    const unsigned f1 = isG ? (unsigned)(WS_HFLAG + ((tm >> 1) * 32 + (lane & 31)) * 128) : (unsigned)(WS_OFLAG + ((tm >> 1) * 32 + 16 + (lane & 15)) * OFS);
    const unsigned f2 = (unsigned)(WS_OFLAG + ((tm >> 1) * 32 + (lane & 15)) * OFS);
    O_OUT();
    asm volatile("s_waitcnt vmcnt(0)" :: "v"(pft) : "memory");
    STAMP(20);
    POLL32(f1, 4);
    if (isG) { BAR(); O_FLAG(); }
    K2_ISSUE(0, 3); K2_ISSUE(1, 4);
    STAMP(21);
    WAIT_V(8); BAR();
    K2_ISSUE(2, 0); K2_ISSUE(3, 1);
    STAMP(22);
    int buf = 2;
    unsigned pol = 0;
#pragma unroll 1
    for (int b = 0; b < nt - 4; ++b) {
      WAIT_V(16); BAR();
      if (b == 6 && !isG) O_FLAG();
      if (b == 24) asm volatile("buffer_load_dword %0, %1, %2, 0 offen sc1" : "+v"(pol) : "v"(f2), "s"(wrs) : "memory");
      if (b == 28 && !isG) {
        asm volatile("" : "+v"(pol));
        if (__builtin_amdgcn_ballot_w64(pol != tag) != 0ull) POLL32(f2, 4);
      }
      K2_ISSUE(b + 4, buf);
      buf = (buf == 4) ? 0 : buf + 1;
    }
    const int c8 = lane & 15, r0 = wc * 4 + (lane >> 4);
    const unsigned goff = (unsigned)((tm * 128 + r0) * 2048 + tn * 256 + c8 * 16);
    const lptr cb = lds + (isG ? 0 : 65536) + r0 * 512;
    WAIT_V(16); BAR();
    WAIT_V(8); BAR();
    WAIT_V(0); BAR();
    if (isG) {
      BAR();
      STAMP(23);
      BAR();
#pragma unroll
      for (int i = 0; i < 8; ++i) {
        const f32x4 a0 = *(const LAS f32x4*)(cb + i * 8192 + (((2 * c8) ^ (r0 & 7)) << 4)), a1 = *(const LAS f32x4*)(cb + i * 8192 + (((2 * c8 + 1) ^ (r0 & 7)) << 4));
        ST128((u32x4){pk2(silu(a0[0]), silu(a0[1])), pk2(silu(a0[2]), silu(a0[3])), pk2(silu(a1[0]), silu(a1[1])), pk2(silu(a1[2]), silu(a1[3]))}, goff + i * 32768, WS_G);
      }
    } else {
      u32x4 xv[8], gv[8];
      const unsigned gf = __builtin_amdgcn_raw_buffer_load_b32(wrs, 0, WS_GFLAG + (tm * 8 + tn) * 128, 16);
#pragma unroll
      for (int i = 0; i < 8; ++i) xv[i] = *(const u32x4*)(xn + goff + i * 32768);
      BAR();
      if (__builtin_amdgcn_readfirstlane(gf) != tag) POLL32((unsigned)(WS_GFLAG + (tm * 8 + tn) * 128), 1);
      asm volatile("" ::: "memory");
#pragma unroll
      for (int i = 0; i < 8; ++i) gv[i] = __builtin_amdgcn_raw_buffer_load_b128(wrs, goff + i * 32768, WS_G, 16);
      float xf[8][8], gf2[8][8];
#pragma unroll
      for (int i = 0; i < 8; ++i) {
#pragma unroll
        for (int e = 0; e < 4; ++e) { xf[i][2 * e] = bf2f(xv[i][e] & 0xffffu); xf[i][2 * e + 1] = U2F(xv[i][e] & 0xffff0000u); }
        asm volatile("" : "+v"(xf[i][0]), "+v"(xf[i][1]), "+v"(xf[i][2]), "+v"(xf[i][3]), "+v"(xf[i][4]), "+v"(xf[i][5]), "+v"(xf[i][6]), "+v"(xf[i][7]));
      }
#pragma unroll
      for (int i = 0; i < 8; ++i) {
#pragma unroll
        for (int e = 0; e < 4; ++e) { gf2[i][2 * e] = bf2f(gv[i][e] & 0xffffu); gf2[i][2 * e + 1] = U2F(gv[i][e] & 0xffff0000u); }
        asm volatile("" : "+v"(gf2[i][0]), "+v"(gf2[i][1]), "+v"(gf2[i][2]), "+v"(gf2[i][3]), "+v"(gf2[i][4]), "+v"(gf2[i][5]), "+v"(gf2[i][6]), "+v"(gf2[i][7]));
      }
      STAMP(23);
      BAR();
      f32x4 a0[8], a1[8];
#pragma unroll
      for (int i = 0; i < 8; ++i) { a0[i] = *(const LAS f32x4*)(cb + i * 8192 + (((2 * c8) ^ (r0 & 7)) << 4)); a1[i] = *(const LAS f32x4*)(cb + i * 8192 + (((2 * c8 + 1) ^ (r0 & 7)) << 4)); }
      SCHED();
#pragma unroll
      for (int i = 0; i < 8; ++i) {
        const u32x4 w = {pk2(xf[i][0] + a0[i][0] * gf2[i][0], xf[i][1] + a0[i][1] * gf2[i][1]), pk2(xf[i][2] + a0[i][2] * gf2[i][2], xf[i][3] + a0[i][3] * gf2[i][3]),
                         pk2(xf[i][4] + a1[i][0] * gf2[i][4], xf[i][5] + a1[i][1] * gf2[i][5]), pk2(xf[i][6] + a1[i][2] * gf2[i][6], xf[i][7] + a1[i][3] * gf2[i][7])};
        *(u32x4*)(out + goff + i * 32768) = w;
      }
    }
    if (isG) {
      WAIT_V(0); BAR();
      if (wid == 0 && lane == 0) __builtin_amdgcn_raw_buffer_store_b32(tag, wrs, 0, WS_GFLAG + (tm * 8 + tn) * 128, 16);
      if (bid == 4 && wid == 0) {
        for (unsigned spins = 0; spins < 2000000u; ++spins) {
          bool ok = true;
#pragma unroll
          for (int i = 0; i < 4; ++i) ok = ok && __builtin_amdgcn_raw_buffer_load_b32(wrs, (unsigned)((lane + i * 64) * 128), WS_HFLAG, 16) == tag;
          if (__builtin_amdgcn_ballot_w64(!ok) == 0ull) break;
          __builtin_amdgcn_s_sleep(4);
          asm volatile("" ::: "memory");
        }
        asm volatile("" ::: "memory");
        if (lane == 0) __builtin_amdgcn_raw_buffer_store_b32(tag, wrs, 0, 0, 16);
      }
    }
  } else {
    asm volatile("s_waitcnt vmcnt(0)" :: "v"(pft) : "memory");
    O_OUT();
    if (isG) { WAIT_V(0); BAR(); }
    STAMP(20);
    STAMP(21);
    const int wm = wc >> 1, wn = wc & 1, g = lane >> 4, t = lane & 15, q4 = (lane >> 2) & 3, p4 = lane & 3;
    lptr w2b[4];
#pragma unroll
    for (int ft = 0; ft < 4; ++ft) w2b[ft] = lds + 3 * K2_STG + 16384 + wn * 1024 + (g >> 1) * 2048 + (4 * (g & 1) + q4) * 128 + 32 * (ft ^ (2 * (g & 1) + (q4 >> 1))) + 8 * p4;
    lptr ar = lds + 3 * K2_STG + wm * 8192 + lane * 16;
    f32x4 ac2[4][4];
#pragma unroll
    for (int m = 0; m < 4; ++m)
#pragma unroll
      for (int n = 0; n < 4; ++n) ac2[m][n] = (f32x4){0.f, 0.f, 0.f, 0.f};
    bf16x8 Af[2][4];
    int dlt = 0;
    bf16x4 Wh[2][4][2];
#define K2_RD(set, ks, i) do { if ((i) == 0) Af[set][0] = ldb128(ar + (ks) * 1024); else if ((i) >= 3 && (i) < 6) Af[set][(i) - 2] = ldb128(ar + ((i) - 2) * 2048 + (ks) * 1024); \
      else { const int f_ = (i) < 3 ? 0 : ((i) - 4) >> 1, h_ = (i) < 3 ? (i) - 1 : (i) & 1; \
             Wh[set][f_][h_] = __builtin_amdgcn_ds_read_tr16_b64_v4bf16((LAS bf16x4*)(w2b[f_] + (ks) * 8192 + h_ * 4096)); } } while (0)
#define K2_W(set, m) __builtin_shufflevector(Wh[set][m][0], Wh[set][m][1], 0, 1, 2, 3, 4, 5, 6, 7)
    BAR();
    STAMP(22);
#pragma unroll
    for (int i = 0; i < 12; ++i) { K2_RD(0, 0, i); SCHED(); }
    int buf = 3;
#define K2_TILE() do { SCHED(); \
      _Pragma("unroll") for (int i = 0; i < 16; ++i) {         \
        ac2[i >> 2][i & 3] = MFMA16(K2_W(0, i >> 2), Af[0][i & 3], ac2[i >> 2][i & 3]); \
        if (i < 12) K2_RD(1, 1, i); \
        if (i == 12) { dlt = (buf == 4) ? -4 * K2_STG : K2_STG; buf = (buf == 4) ? 0 : buf + 1; ar += dlt; asm volatile("" : "+v"(ar)); } \
        if (i == 13) { w2b[0] += dlt; w2b[1] += dlt; asm volatile("" : "+v"(w2b[0]), "+v"(w2b[1])); } \
        if (i == 14) { w2b[2] += dlt; w2b[3] += dlt; asm volatile("" : "+v"(w2b[2]), "+v"(w2b[3])); } \
        if (i == 15) BAR();                  \
        SCHED(); } \
      _Pragma("unroll") for (int i = 0; i < 16; ++i) {         \
        ac2[i >> 2][i & 3] = MFMA16(K2_W(1, i >> 2), Af[1][i & 3], ac2[i >> 2][i & 3]); \
        if (i < 12) K2_RD(0, 0, i); \
        SCHED(); } } while (0)
#pragma unroll 1
    for (int kt = 0; kt < 6; ++kt) K2_TILE();
    WAIT_V(0);
#pragma unroll 1
    for (int kt = 6; kt < nt; ++kt) K2_TILE();
    STAMP(23);
    const lptr cw = lds + (isG ? 0 : 65536) + (wm * 64 + t) * 512;
#pragma unroll
    for (int m = 0; m < 4; ++m)
#pragma unroll
      for (int n = 0; n < 4; ++n) *(LAS f32x4*)(cw + n * 8192 + (((wn * 16 + m * 4 + g) ^ (t & 7)) << 4)) = ac2[m][n];
    WAIT_L(0); BAR();
    if (isG) BAR();
  }
  STAMP(24);
  STAMP_OUT(ws + (57 << 20), 30);
#undef lane
}

extern "C" void kernel_launch(void* const* d_in, const int* in_sizes, int n_in, void* d_out, int out_size, void* d_ws, size_t ws_size, hipStream_t stream) {
  const char* x = (const char*)d_in[0]; const char* kc = (const char*)d_in[1]; const char* vc = (const char*)d_in[2];
  const char* wq = (const char*)d_in[3]; const char* wo = (const char*)d_in[4]; const char* w1 = (const char*)d_in[6]; const char* w2 = (const char*)d_in[7];
  (void)hipFuncSetAttribute((const void*)mega_kernel, hipFuncAttributeMaxDynamicSharedMemorySize, K1_LDS);
  mega_kernel<<<256, 512, K1_LDS, stream>>>(x, kc, vc, wq, w1, wo, w2, (char*)d_ws, (char*)d_out);
}
```

```cpp
#include <hip/hip_runtime.h>
#include <stdint.h>

typedef unsigned short u16;
typedef __bf16 bf16x2 __attribute__((ext_vector_type(2)));
typedef __bf16 bf16x4 __attribute__((ext_vector_type(4)));
typedef __bf16 bf16x8 __attribute__((ext_vector_type(8)));
typedef float f32x2 __attribute__((ext_vector_type(2)));
typedef float f32x4 __attribute__((ext_vector_type(4)));
typedef float f32x16 __attribute__((ext_vector_type(16)));
typedef unsigned u32x2 __attribute__((ext_vector_type(2)));
typedef unsigned u32x4 __attribute__((ext_vector_type(4)));
#define LAS __attribute__((address_space(3)))
typedef LAS char* lptr;
#if defined(__HIP_DEVICE_COMPILE__) && !defined(USEPK)
#define NOPK __attribute__((target("no-packed-fp32-ops")))
#else
#define NOPK
#endif
#define DI NOPK __device__ __forceinline__
#define F2U(x) __builtin_bit_cast(unsigned, (float)(x))
#define U2F(x) __builtin_bit_cast(float, (unsigned)(x))
#define TID ((int)__builtin_amdgcn_workitem_id_x())
#define BID ((int)__builtin_amdgcn_workgroup_id_x())
#define SCHED() __builtin_amdgcn_sched_barrier(0)
#define BAR() asm volatile("s_barrier" ::: "memory")
#define WAIT_V(n) asm volatile("s_waitcnt vmcnt(%0)" :: "n"(n) : "memory")
#define WAIT_L(n) do { asm volatile("" ::: "memory"); __builtin_amdgcn_s_waitcnt(0xC07F | ((n) << 8)); asm volatile("" ::: "memory"); } while (0)
#define MFMA16(a, b, c) __builtin_amdgcn_mfma_f32_16x16x32_bf16(a, b, c, 0, 0, 0)
#define MFMA32(a, b, c) __builtin_amdgcn_mfma_f32_32x32x16_bf16(a, b, c, 0, 0, 0)

DI void glds(unsigned voff, const char* sbase, unsigned ldsaddr) {
  asm volatile("s_mov_b32 m0, %2\n\ts_nop 0\n\tglobal_load_lds_dwordx4 %0, %1" ::"v"(voff), "s"(sbase), "s"(ldsaddr) : "memory");
}
DI void glds1(unsigned voff, const char* sbase, unsigned ldsaddr) {
  asm volatile("s_mov_b32 m0, %2\n\ts_nop 0\n\tglobal_load_lds_dwordx4 %0, %1 sc1" ::"v"(voff), "s"(sbase), "s"(ldsaddr) : "memory");
}
DI unsigned pk2(float lo, float hi) { f32x2 v = {lo, hi}; return __builtin_bit_cast(unsigned, __builtin_convertvector(v, bf16x2)); }
DI float bf2f(unsigned b) { return U2F(b << 16); }
DI bf16x8 ldb128(lptr p) { return *(const LAS bf16x8*)p; }
DI bf16x8 ldtr2(lptr p, int d) {
  bf16x4 a = __builtin_amdgcn_ds_read_tr16_b64_v4bf16((LAS bf16x4*)p);
  bf16x4 b = __builtin_amdgcn_ds_read_tr16_b64_v4bf16((LAS bf16x4*)(p + d));
  return __builtin_shufflevector(a, b, 0, 1, 2, 3, 4, 5, 6, 7);
}
DI void ST128_(__amdgpu_buffer_rsrc_t r, u32x4 v, unsigned voff, int soff) { __builtin_amdgcn_raw_buffer_store_b128(v, r, voff + (unsigned)soff, 0, 16); }
DI float silu(float v) { return v * __builtin_amdgcn_rcpf(1.0f + __builtin_amdgcn_exp2f(v * -1.4426950408889634f)); }

#define STAMP_DECL
#define LSTAMP(i)
#define STAMP(i)
#define STAMP_OUT(buf, n)
template <int CTRL> DI float dpp_add(float v) { return v + U2F(__builtin_amdgcn_update_dpp(0u, F2U(v), CTRL, 0xf, 0xf, true)); }
DI float half_sum(float v) {
  v = dpp_add<0xB1>(v); v = dpp_add<0x4E>(v); v = dpp_add<0x141>(v); v = dpp_add<0x140>(v);
  auto rr = __builtin_amdgcn_permlane16_swap(F2U(v), F2U(v), false, false);
  return U2F(rr[0]) + U2F(rr[1]);
}
typedef __attribute__((address_space(1))) unsigned long long gu64;

#define SX(b, h) (98304 + (b) * 32768 + (h) * 16384)
constexpr int SLOT_B = 0, SLOT_E = 32768, SLOT_A = 65536, SLOT_C = 98304, SLOT_D = 131072, K1_LDS = 163840;

#define XSWAP 0
constexpr int WS_HFLAG = 4096;
constexpr int WS_GFLAG = 65536;
constexpr int WS_OFLAG = 131072;
#define OFS 16
constexpr int WS_STATS = 1 << 20;
constexpr int WS_G = 5 << 20, WS_O = 9 << 20, WS_H = 25 << 20, WS_END = 39 << 20;
constexpr int K2_STG = 32768;

NOPK __global__ __launch_bounds__(512, 2) void mega_kernel(const char* __restrict__ xn, const char* __restrict__ kc, const char* __restrict__ vc,
                                                           const char* __restrict__ wq, const char* __restrict__ w1, const char* __restrict__ wo, const char* __restrict__ w2,
                                                           char* ws, char* __restrict__ out) {
  STAMP_DECL
  STAMP(0);
  extern __shared__ __attribute__((aligned(16))) char shm[];
  const lptr lds = (lptr)shm;
  const unsigned lds0 = (unsigned)(uintptr_t)lds;
  const int tid = TID, wid = __builtin_amdgcn_readfirstlane(tid >> 6), lane = tid & 63, wr = wid >> 2, wc = wid & 3;
  const int bid = BID ^ XSWAP, xcd = bid & 7, idx = bid >> 3, hd = 4 * xcd + (idx & 3), rb = idx >> 2;
  const int g = lane >> 4, t = lane & 15, q4 = (lane >> 2) & 3, p4 = lane & 3;

  const bool light = xcd >= 4;
  unsigned voffX[4], voffQ[2], voffH[2];
  {
    const int r = lane >> 3, c = lane & 7;
#pragma unroll
    for (int i = 0; i < 4; ++i) voffX[i] = (unsigned)((8 * i + r) * 2048 + ((c ^ (4 * (i & 1) + (r >> 1))) << 4) - i * 1024);
    const int k = 32 * (wid >> 2) + 16 * (wid & 1) + 4 * ((wid >> 1) & 1) + 8 * (r >> 2) + (r & 3);
    const int cw = c ^ (((r >> 1) & 3) << 1);
    voffQ[0] = (unsigned)(k * 8192 + cw * 16 + 1024); voffQ[1] = voffQ[0] + 128 - 1024;
    voffH[0] = (unsigned)(k * 7168 + 128 + (light ? (cw & 3) : cw) * 16 + 1024);
    voffH[1] = (unsigned)(k * 7168 + cw * 16);
  }
  const char* xb = xn + (size_t)rb * 256 * 2048 + wid * 65536;
  const char* qb = wq + hd * 256 - 1024;
  const char* hb = w1 + (light ? 4096 + 192 * (hd - 16) : 256 * hd) - 1024;
  const bool full = wr == 1 || !light;
#define GLDS2(v0_, v1_, sb_, ld_) asm volatile("s_mov_b32 m0, %3\n\ts_nop 0\n\tglobal_load_lds_dwordx4 %0, %2\n\tglobal_load_lds_dwordx4 %1, %2 offset:1024" \
    ::"v"(v0_), "v"(v1_), "s"(sb_), "s"(ld_) : "memory")
#define GLDS4(v0_, v1_, v2_, v3_, sb_, ld_) asm volatile("s_mov_b32 m0, %5\n\ts_nop 0\n\tglobal_load_lds_dwordx4 %0, %4\n\tglobal_load_lds_dwordx4 %1, %4 offset:1024\n\t" \
    "global_load_lds_dwordx4 %2, %4 offset:2048\n\tglobal_load_lds_dwordx4 %3, %4 offset:3072" ::"v"(v0_), "v"(v1_), "v"(v2_), "v"(v3_), "s"(sb_), "s"(ld_) : "memory")
#define ST_X(b, kt) GLDS4(voffX[0], voffX[1], voffX[2], voffX[3], xb + (kt) * 128, lds0 + SX(b, 0) + wid * 4096)
#define ST_W0(wo_, kt) GLDS2(voffQ[0], voffQ[1], qb + (size_t)(kt) * 524288, lds0 + (wo_) + wid * 2048)
#define ST_W1(wo_, kt) GLDS2(voffH[0], voffH[1], hb + (size_t)(kt) * 458752, lds0 + (wo_) + 16384 + wid * 2048)

  lptr wb[4], xr[2];
#pragma unroll
  for (int m = 0; m < 4; ++m) wb[m] = lds + wr * 1024 + (g >> 1) * 2048 + (4 * (g & 1) + q4) * 128 + 32 * ((m ^ wc) ^ (2 * (g & 1) + (q4 >> 1))) + 8 * p4;
#pragma unroll
  for (int ks = 0; ks < 2; ++ks) xr[ks] = lds + 98304 + (wc * 32 + t) * 128 + 16 * ((4 * ks + g) ^ (t >> 1));
#define LDW(dst, h) do { _Pragma("unroll") for (int m = 0; m < 4; ++m) _Pragma("unroll") for (int k = 0; k < 2; ++k) dst[m][k] = ldtr2(wb[m] + (h) * 16384 + k * 8192, 4096); } while (0)
#define LDX(dst, b, h) do { _Pragma("unroll") for (int n = 0; n < 2; ++n) _Pragma("unroll") for (int k = 0; k < 2; ++k) dst[n][k] = ldb128(xr[k] + (SX(b, h) - 98304) + n * 2048); } while (0)

  f32x4 acc[2][2][4][2];
#pragma unroll
  for (int a = 0; a < 2; ++a)
#pragma unroll
    for (int b = 0; b < 2; ++b)
#pragma unroll
      for (int m = 0; m < 4; ++m)
#pragma unroll
        for (int n = 0; n < 2; ++n) acc[a][b][m][n] = (f32x4){0.f, 0.f, 0.f, 0.f};
  bf16x8 At[4][2], B0[2][2], B1[2][2];

#define MM4(ai, m, k) do { _Pragma("unroll") for (int n = 0; n < 2; ++n) { \
      acc[ai][0][m][n] = MFMA16(At[m][k], B0[n][k], acc[ai][0][m][n]); acc[ai][1][m][n] = MFMA16(At[m][k], B1[n][k], acc[ai][1][m][n]); } } while (0)
#define MMA2(ai, CLOSE) do { __builtin_amdgcn_s_setprio(1); \
    MM4(ai, 0, 0); cs[ai] = MFMA16(At[0][0], ones, cs[ai]); MM4(ai, 1, 0); MM4(ai, 0, 1); cs[ai] = MFMA16(At[0][1], ones, cs[ai]); MM4(ai, 1, 1); \
    asm volatile("" : "+v"(cs[ai])); \
    if ((ai) == 0 || full) { MM4(ai, 2, 0); MM4(ai, 3, 0); MM4(ai, 2, 1); MM4(ai, 3, 1); } \
    SCHED(); CLOSE; __builtin_amdgcn_s_setprio(0); SCHED(); } while (0)
  char* const stats = ws + WS_STATS;
  const __amdgpu_buffer_rsrc_t wrs = __builtin_amdgcn_make_buffer_rsrc(ws, 0, WS_END, 0x00020000);
  f32x4 cs[2] = {{0.f, 0.f, 0.f, 0.f}, {0.f, 0.f, 0.f, 0.f}};
  bf16x8 ones = __builtin_bit_cast(bf16x8, (u32x4){0x3f803f80u, 0x3f803f80u, 0x3f803f80u, 0x3f803f80u});
#define PIN_SETUP() do { asm volatile("" : "+v"(ones), "+v"(cs[0]), "+v"(cs[1]), "+v"(wb[0]), "+v"(wb[1]), "+v"(wb[2]), "+v"(wb[3]), "+v"(xr[0]), "+v"(xr[1])); \
    _Pragma("unroll") for (int a = 0; a < 2; ++a) _Pragma("unroll") for (int b = 0; b < 2; ++b) \
      asm volatile("" : "+v"(acc[a][b][0][0]), "+v"(acc[a][b][0][1]), "+v"(acc[a][b][1][0]), "+v"(acc[a][b][1][1]), "+v"(acc[a][b][2][0]), "+v"(acc[a][b][2][1]), "+v"(acc[a][b][3][0]), "+v"(acc[a][b][3][1])); } while (0)
  unsigned tag;
  {
    if (wr == 0) {
      unsigned ep;
      ST_X(0, 0); ST_W0(0, 0);
      asm volatile("global_load_dword %0, %1, %2 sc1" : "=&v"(ep) : "v"(0), "s"(ws) : "memory");
      ST_W1(0, 0); ST_W0(32768, 1); ST_X(1, 1);
      PIN_SETUP();
      asm volatile("s_waitcnt vmcnt(8)" : "+v"(ep) :: "memory");
      tag = __builtin_amdgcn_readfirstlane(ep) + 1u;
      BAR();
    } else {
      const int row = rb * 256 + hd * 8 + wc * 2 + (lane >> 5);
      u32x4 a[4];
      unsigned ep;
      asm volatile("global_load_dword %0, %1, %2 sc1" : "=&v"(ep) : "v"(0), "s"(ws) : "memory");
      asm volatile("global_load_dwordx4 %0, %4, %5\n\tglobal_load_dwordx4 %1, %4, %5 offset:512\n\tglobal_load_dwordx4 %2, %4, %5 offset:1024\n\tglobal_load_dwordx4 %3, %4, %5 offset:1536"
                   : "=&v"(a[0]), "=&v"(a[1]), "=&v"(a[2]), "=&v"(a[3]) : "v"(row * 2048 + (lane & 31) * 16), "s"(xn) : "memory");
      ST_X(0, 0); ST_W0(0, 0);
      ST_W1(0, 0); ST_W0(32768, 1); ST_X(1, 1);
      PIN_SETUP();
      asm volatile("s_waitcnt vmcnt(8)" : "+v"(ep), "+v"(a[0]), "+v"(a[1]), "+v"(a[2]), "+v"(a[3]) :: "memory");
      BAR();
      tag = __builtin_amdgcn_readfirstlane(ep) + 1u;
      float v[32];
#pragma unroll
      for (int i = 0; i < 16; ++i) { v[2 * i] = bf2f(a[i >> 2][i & 3] & 0xffffu); v[2 * i + 1] = U2F(a[i >> 2][i & 3] & 0xffff0000u); }
      float s0 = 0.f, s1 = 0.f, s2 = 0.f, s3 = 0.f;
#pragma unroll
      for (int i = 0; i < 8; ++i) { s0 += v[i]; s1 += v[8 + i]; s2 += v[16 + i]; s3 += v[24 + i]; }
      const float mean = half_sum((s0 + s1) + (s2 + s3)) * (1.0f / 1024.0f);
      s0 = s1 = s2 = s3 = 0.f;
#pragma unroll
      for (int i = 0; i < 8; ++i) { const float d0 = v[i] - mean, d1 = v[8 + i] - mean, d2 = v[16 + i] - mean, d3 = v[24 + i] - mean; s0 += d0 * d0; s1 += d1 * d1; s2 += d2 * d2; s3 += d3 * d3; }
      const float sd = __builtin_amdgcn_sqrtf(half_sum((s0 + s1) + (s2 + s3)) * (1.0f / 1024.0f) + 1e-5f);
      if ((lane & 31) < 2) __hip_atomic_store((gu64*)(stats + row * 16 + (lane & 1) * 8), ((unsigned long long)tag << 32) | F2U((lane & 1) ? sd : mean), __ATOMIC_RELAXED, __HIP_MEMORY_SCOPE_AGENT);
    }
  }
  if (wr == 1) BAR();
  SCHED();
  STAMP(1);
  const int kvg = hd >> 3;
  const char* kb_g = kc + ((size_t)kvg * 2048 + 1536) * 256;
  const char* vb_g = vc + ((size_t)kvg * 2048 + 1536) * 256;
#define VOFFK ((unsigned)(((lane >> 2) & 7) * 256 + (4 * (lane >> 5) + ((lane & 3) ^ ((2 * (wid & 1) + ((lane >> 4) & 1)) & 3))) * 16))
#define VOFFV ((unsigned)(((lane >> 2) & 7) * 256 + (lane >> 5) * 64 + (lane & 3) * 16))
#define ST_KV8(voff, gsrc, slot) do { glds(voff, (gsrc) + wid * 2048, lds0 + (slot) + wid * 2048); glds(voff, (gsrc) + wid * 2048 + 128, lds0 + (slot) + wid * 2048 + 1024); } while (0)
  int wprv = 65536, wnxt = 32768;
#define W_ADV() do { const int d_ = (wnxt == 0) ? -65536 : 32768, cur_ = wnxt - d_; wnxt = wprv; wprv = cur_; \
    _Pragma("unroll") for (int f = 0; f < 4; ++f) wb[f] += d_; \
    asm volatile("" : "+v"(wb[0]), "+v"(wb[1]), "+v"(wb[2]), "+v"(wb[3])); } while (0)
#define PH_A(b, STAGE) do { LDX(B0, b, 0); LDX(B1, b, 1); SCHED(); LDW(At, 0); STAGE; WAIT_V(10); WAIT_L(12); BAR(); SCHED(); MMA2(0, WAIT_L(0); BAR()); } while (0)
#define PH_B(STAGE) do { LDW(At, 1); STAGE; W_ADV(); WAIT_V(8); BAR(); SCHED(); MMA2(1, WAIT_L(0); BAR()); } while (0)
#pragma unroll 1
  for (int kt = 0; kt < 14; kt += 2) {
    PH_A(0, ST_W1(wnxt, kt + 1); ST_W0(wprv, kt + 2));
    PH_B(ST_X(0, kt + 2));
    PH_A(1, ST_W1(wnxt, kt + 2); ST_W0(wprv, kt + 3));
    PH_B(ST_X(1, kt + 3));
  }
  STAMP(2);
  const unsigned voffK = VOFFK, voffV = VOFFV;
  PH_A(0, ST_W1(0, 15); ST_KV8(voffK, kb_g, SLOT_E));
  PH_B(ST_KV8(voffV, vb_g, SLOT_C); ST_KV8(voffV, vb_g + 16384, SLOT_C + 16384));
  const unsigned soff = (unsigned)((rb * 256 + (g >> 1) * 128 + wc * 32 + (g & 1) * 16 + t) * 16);
  u32x4 sg;
  LDX(B0, 1, 0); LDX(B1, 1, 1); SCHED(); LDW(At, 0);
  asm volatile("global_load_dwordx4 %0, %1, %2 sc1" : "=&v"(sg) : "v"(soff), "s"(stats) : "memory");
  ST_KV8(voffK, kb_g + 16384, SLOT_E + 16384); ST_KV8(voffK, kb_g + 32768, SLOT_A);
  WAIT_V(11); BAR(); SCHED(); MMA2(0, WAIT_L(0); BAR());
  LDW(At, 1);
  ST_KV8(voffK, kb_g + 49152, SLOT_A + 16384);
  BAR(); SCHED();
  const lptr csw = lds + SLOT_D + (t & 3) * 8192 + wr * 4096 + wc * 64 + g * 16;
  MMA2(1, *(LAS f32x4*)csw = cs[0]; *(LAS f32x4*)(csw + 256) = cs[1]; asm volatile("s_waitcnt vmcnt(4)" : "+v"(sg) :: "memory"); WAIT_L(0); BAR());
  f32x4 cv[2][4];
#pragma unroll
  for (int a = 0; a < 2; ++a)
#pragma unroll
    for (int m = 0; m < 4; ++m) cv[a][m] = *(const LAS f32x4*)(lds + SLOT_D + wc * 8192 + wr * 4096 + a * 256 + (m ^ wc) * 64 + g * 16);
  if (__builtin_expect(__builtin_amdgcn_ballot_w64(sg[1] != tag || sg[3] != tag) != 0ull, 0)) {
    for (unsigned spins = 0; spins < 4000000u; ++spins) {
      __builtin_amdgcn_s_sleep(2);
      asm volatile("" ::: "memory");
      sg = __builtin_amdgcn_raw_buffer_load_b128(wrs, soff, WS_STATS, 16);
      if (__builtin_amdgcn_ballot_w64(sg[1] != tag || sg[3] != tag) == 0ull) break;
    }
  }
  float mu[2][2], rs[2][2], sd[2][2];
  {
    auto m16 = __builtin_amdgcn_permlane16_swap(sg[0], sg[0], false, false);
    auto s16 = __builtin_amdgcn_permlane16_swap(sg[2], sg[2], false, false);
#pragma unroll
    for (int n = 0; n < 2; ++n) {
      auto m32 = __builtin_amdgcn_permlane32_swap(m16[n], m16[n], false, false);
      auto s32 = __builtin_amdgcn_permlane32_swap(s16[n], s16[n], false, false);
#pragma unroll
      for (int bj = 0; bj < 2; ++bj) { mu[bj][n] = -U2F(m32[bj]); sd[bj][n] = U2F(s32[bj]); rs[bj][n] = __builtin_amdgcn_rcpf(sd[bj][n]) * 1.4426950408889634f; }
    }
  }
  WAIT_L(0);
  STAMP(3);
  {
    const lptr qw = lds + wc * 8192 + wr * 4096 + ((g >> 1) * 32 + t) * 16 + (g & 1) * 8;
#pragma unroll
    for (int bj = 0; bj < 2; ++bj) {
      lptr qwb = qw + (bj ? SLOT_D : SLOT_B);
      asm volatile("" : "+v"(qwb));
#pragma unroll
      for (int m = 0; m < 4; ++m)
#pragma unroll
        for (int n = 0; n < 2; ++n) {
          const f32x4 v = (acc[0][bj][m][n] + cv[0][m] * mu[bj][n]) * (rs[bj][n] * 0.08838834764831845f);
          u32x2 w = {pk2(v[0], v[1]), pk2(v[2], v[3])};
          *(LAS u32x2*)(qwb + (m ^ wc) * 1024 + n * 256) = w;
        }
    }
  }
  WAIT_L(0); BAR(); SCHED();
  STAMP(4);
  bf16x8 qf[8];
  const lptr qr = lds + (wr ? SLOT_D : SLOT_B) + wc * 8192 + lane * 16;
  if (wr == 1) {
#pragma unroll
    for (int s = 0; s < 8; ++s) qf[s] = ldb128(qr + s * 1024);
  }
  {
#define ST128(...) ST128_(wrs, __VA_ARGS__)
    u32x2 hw[2][2][4];
#define SILU16(m) do { \
      const f32x4 a0 = acc[1][0][m][0] + cv[1][m] * mu[0][0], a1 = acc[1][0][m][1] + cv[1][m] * mu[0][1], a2 = acc[1][1][m][0] + cv[1][m] * mu[1][0], a3 = acc[1][1][m][1] + cv[1][m] * mu[1][1]; \
      f32x4 e0 = a0 * -rs[0][0], e1 = a1 * -rs[0][1], e2 = a2 * -rs[1][0], e3 = a3 * -rs[1][1]; SCHED(); \
      _Pragma("unroll") for (int i = 0; i < 4; ++i) { e0[i] = __builtin_amdgcn_exp2f(e0[i]); e1[i] = __builtin_amdgcn_exp2f(e1[i]); e2[i] = __builtin_amdgcn_exp2f(e2[i]); e3[i] = __builtin_amdgcn_exp2f(e3[i]); } SCHED(); \
      e0 = e0 * sd[0][0] + sd[0][0]; e1 = e1 * sd[0][1] + sd[0][1]; e2 = e2 * sd[1][0] + sd[1][0]; e3 = e3 * sd[1][1] + sd[1][1]; SCHED(); \
      _Pragma("unroll") for (int i = 0; i < 4; ++i) { e0[i] = __builtin_amdgcn_rcpf(e0[i]); e1[i] = __builtin_amdgcn_rcpf(e1[i]); e2[i] = __builtin_amdgcn_rcpf(e2[i]); e3[i] = __builtin_amdgcn_rcpf(e3[i]); } SCHED(); \
      e0 *= a0; e1 *= a1; e2 *= a2; e3 *= a3; SCHED(); \
      hw[0][0][m] = (u32x2){pk2(e0[0], e0[1]), pk2(e0[2], e0[3])}; hw[0][1][m] = (u32x2){pk2(e1[0], e1[1]), pk2(e1[2], e1[3])}; \
      hw[1][0][m] = (u32x2){pk2(e2[0], e2[1]), pk2(e2[2], e2[3])}; hw[1][1][m] = (u32x2){pk2(e3[0], e3[1]), pk2(e3[2], e3[3])}; SCHED(); } while (0)
    SILU16(0);
    if (wr == 0) {
      BAR(); SCHED();
#pragma unroll
      for (int s = 0; s < 8; ++s) qf[s] = ldb128(qr + s * 1024);
    }
    SILU16(1);
    const unsigned hoff = (unsigned)((rb * 16 + wc * 2) * 2048 + lane * 16);
    const int pb = (light ? 16 + 3 * hd : 4 * hd) + (wr ? 0 : 2), sw = full ? (wc >> 1) : 0, p0 = pb + sw, p1 = pb + (sw ^ 1);
#define H_OUT1(p_, a_, b_) do { _Pragma("unroll") for (int bj = 0; bj < 2; ++bj) _Pragma("unroll") for (int n = 0; n < 2; ++n) \
      ST128((u32x4){hw[bj][n][a_][0], hw[bj][n][a_][1], hw[bj][n][b_][0], hw[bj][n][b_][1]}, hoff + n * 2048, WS_H + ((p_) >> 1) * 262144 + ((p_) & 1) * 1024 + bj * 16384); } while (0)
#define H_OUT(p_, m_) do { if (wc & 1) H_OUT1(p_, (m_) + 1, m_); else H_OUT1(p_, m_, (m_) + 1); } while (0)
    H_OUT(p0, 0);
    if (full) { SILU16(2); SILU16(3); H_OUT(p1, 2); }
  }
  STAMP(5);
  if (wr == 1) { WAIT_L(0); BAR(); }
  __builtin_amdgcn_s_setprio(1);
  SCHED();
  STAMP(6);
  const int r32 = lane & 31, hh = lane >> 5;
  lptr kr[2];
#pragma unroll
  for (int e = 0; e < 2; ++e) kr[e] = lds + 2048 * (r32 >> 3) + 64 * (r32 & 7) + 16 * ((2 * e + hh) ^ ((r32 >> 2) & 3));
  const lptr vr = lds + ((lane & 3) << 3) + (((lane >> 2) & 3) << 6) + (((lane >> 4) & 1) << 5) + (hh << 8);
  f32x16 o[4], s[4];
#pragma unroll
  for (int d = 0; d < 4; ++d)
#pragma unroll
    for (int i = 0; i < 16; ++i) o[d][i] = 0.f;
  bf16x8 pf[8];
  constexpr int RD = 6;
  bf16x8 fr[RD];
  f32x4 lacc = {0.f, 0.f, 0.f, 0.f};
  const unsigned selw = (((lane >> 4) ^ (lane >> 2)) & 1) ? 0u : 0x3f803f80u;
  bf16x8 sel = __builtin_bit_cast(bf16x8, (u32x4){selw, selw, selw, selw});
  asm volatile("" : "+v"(sel));
  f32x16 negm = {0.f, 0.f, 0.f, 0.f, 0.f, 0.f, 0.f, 0.f, 0.f, 0.f, 0.f, 0.f, 0.f, 0.f, 0.f, 0.f};
#define SM_THR 60.0f
#define FRAG(i) ((i) < 32 ? ldtr2(v0 + (((i) >> 2) * 8 + ((i) & 3)) * 512, 2048) : ldb128((((((i) - 32) >> 2) & 1) ? k1 : k0) + ((i) & 3) * 8192 + (((i) - 32) >> 3) * 512))
#define XEARLY 3
#define XPRIO 1
#define YPRIO 0
#define XSEG(lo, hi, CLOSE) do { __builtin_amdgcn_s_setprio(XPRIO); \
    _Pragma("unroll") for (int i = (lo); i < (hi); ++i) { \
      if (i < 32) o[i & 3] = MFMA32(fr[i % RD], pf[(i >> 2) & 7], o[i & 3]); \
      else s[i & 3] = MFMA32(fr[i % RD], qf[((i - 32) >> 2) & 7], i < 36 ? negm : s[i & 3]); \
      if (i < 32 && (i & 3) == 1) lacc = MFMA16(sel, pf[(i >> 2) & 7], lacc); \
      if (i + RD < (hi)) fr[i % RD] = FRAG(i + RD); \
      SCHED(); \
      if (i == 63 - XEARLY) { __builtin_amdgcn_s_setprio(2); CLOSE; WAIT_L(0); BAR(); SCHED(); } } \
    __builtin_amdgcn_s_setprio(YPRIO); } while (0)
#define PREF(lo) do { _Pragma("unroll") for (int i = (lo); i < (lo) + RD; ++i) fr[i % RD] = FRAG(i); SCHED(); } while (0)
#define MX3(a, b, c) __builtin_fmaxf(__builtin_fmaxf((a), (b)), (c))
#define SOFTMAX() do { \
      float m0 = MX3(s[0][0], s[0][1], s[0][2]), m1 = MX3(s[1][0], s[1][1], s[1][2]), m2 = MX3(s[2][0], s[2][1], s[2][2]), m3 = MX3(s[3][0], s[3][1], s[3][2]); \
      _Pragma("unroll") for (int i = 3; i < 15; i += 2) { m0 = MX3(m0, s[0][i], s[0][i + 1]); m1 = MX3(m1, s[1][i], s[1][i + 1]); m2 = MX3(m2, s[2][i], s[2][i + 1]); m3 = MX3(m3, s[3][i], s[3][i + 1]); } \
      m0 = MX3(m0, s[0][15], s[1][15]); m2 = MX3(m2, s[2][15], s[3][15]); \
      float mx = __builtin_fmaxf(MX3(m0, m1, m2), m3); \
        \
      if (__builtin_expect(__builtin_amdgcn_ballot_w64(!(mx <= SM_THR)) != 0ull, 0)) { \
        auto rr = __builtin_amdgcn_permlane32_swap(F2U(mx), F2U(mx), false, false); \
        const float dl = MX3(U2F(rr[0]), U2F(rr[1]), 0.f); \
        _Pragma("unroll") for (int kb = 0; kb < 4; ++kb) s[kb] -= dl; \
        negm -= dl; \
        const float alpha = __builtin_amdgcn_exp2f(-dl); lacc *= alpha; _Pragma("unroll") for (int d = 0; d < 4; ++d) o[d] *= alpha; \
      } \
      SCHED(); \
      _Pragma("unroll") for (int kb = 0; kb < 4; ++kb) { \
        _Pragma("unroll") for (int e = 0; e < 2; ++e) { \
          _Pragma("unroll") for (int i = 0; i < 8; ++i) s[kb][8 * e + i] = __builtin_amdgcn_exp2f(s[kb][8 * e + i]); \
          u32x4 w = {pk2(s[kb][8 * e], s[kb][8 * e + 1]), pk2(s[kb][8 * e + 2], s[kb][8 * e + 3]), pk2(s[kb][8 * e + 4], s[kb][8 * e + 5]), pk2(s[kb][8 * e + 6], s[kb][8 * e + 7])}; \
          pf[2 * kb + e] = __builtin_bit_cast(bf16x8, w); } \
        asm volatile("" : "+v"(pf[2 * kb]), "+v"(pf[2 * kb + 1])); \
        SCHED(); } } while (0)
  {
    const lptr k0 = kr[0] + SLOT_E, k1 = kr[1] + SLOT_E, v0 = vr;
    {
      bf16x8 kf[8];
#pragma unroll
      for (int st = 0; st < 8; ++st) kf[st] = FRAG(32 + 4 * st);
      PREF(32);
#pragma unroll
      for (int st = 0; st < 8; ++st) negm = MFMA32(kf[st], qf[st], negm);
      float m0 = MX3(negm[0], negm[1], negm[2]), m1 = MX3(negm[3], negm[4], negm[5]);
      m0 = MX3(m0, negm[6], negm[7]); m1 = MX3(m1, negm[8], negm[9]); m0 = MX3(m0, negm[10], negm[11]); m1 = MX3(m1, negm[12], negm[13]);
      m0 = __builtin_fmaxf(MX3(m0, negm[14], negm[15]), m1);
      auto rr = __builtin_amdgcn_permlane32_swap(F2U(m0), F2U(m0), false, false);
      m0 = -__builtin_fmaxf(U2F(rr[0]), U2F(rr[1]));
#pragma unroll
      for (int i = 0; i < 16; ++i) negm[i] = m0;
      asm volatile("" : "+v"(negm));
      SCHED();
    }
    XSEG(32, 64, WAIT_V(4));
  }
  STAMP(7);
  const char* kvsrc = (wr ? kb_g + 65536 : vb_g + 32768) + wc * 8192 - 1024;
  unsigned kvo[2][2];
#pragma unroll
  for (int par = 0; par < 2; ++par) {
    const int xx = wr ? ((lane & 3) ^ ((2 * par + ((lane >> 4) & 1)) & 3)) : (lane & 3);
    kvo[par][0] = (unsigned)(((lane >> 2) & 7) * 256 + (lane >> 5) * 64 + xx * 16 + 1024);
    kvo[par][1] = kvo[par][0] + 128 - 1024;
  }
  const unsigned pfo = (unsigned)((lane < 21 ? lane : 20) * 128);
  unsigned pft = 0;
#pragma unroll 1
  for (int c = 0; c < 3; ++c) {
    if (c == 0) STAMP(8); else if (c == 1) STAMP(12);
    if (c == 1 && wid == 4 && lane == 0) __builtin_amdgcn_raw_buffer_store_b32(tag, wrs, 0, WS_HFLAG + (rb * 32 + hd) * 128, 16);
    if (c + wr < 3) {
      const int slot = wr ? (c == 0 ? SLOT_B : SLOT_C) : (c == 0 ? SLOT_D : (c == 1 ? SLOT_E : SLOT_A));
#pragma unroll
      for (int hf = 0; hf < 2; ++hf)
        asm volatile("s_mov_b32 m0, %5\n\ts_nop 0\n\t"
                     "global_load_lds_dwordx4 %0, %4\n\tglobal_load_lds_dwordx4 %1, %4 offset:1024\n\t"
                     "global_load_lds_dwordx4 %2, %4 offset:2048\n\tglobal_load_lds_dwordx4 %3, %4 offset:3072"
                     ::"v"(kvo[0][0]), "v"(kvo[0][1]), "v"(kvo[1][0]), "v"(kvo[1][1]), "s"(kvsrc + c * 32768 + hf * 4096), "s"(lds0 + slot + wc * 8192 + hf * 4096) : "memory");
    }
    {
      const int sl = (bid * 8 + wid) * 3 + c;
      const int o1 = sl * 2688, o2 = (sl - 3121) * 2688;
      const char* pp = sl < 3121 ? wo + (o1 < 8385920 ? o1 : 8385920) : w2 + (o2 < 7337344 ? o2 : 7337344);
      asm volatile("global_load_dword %0, %1, %2" : "+v"(pft) : "v"(pfo), "s"(pp) : "memory");
    }
    SOFTMAX();
    const int kslot = (c == 0) ? SLOT_A : ((c == 1) ? SLOT_B : SLOT_C);
    const int vslot = (c == 0) ? SLOT_C : ((c == 1) ? SLOT_D : SLOT_E);
    const lptr k0 = kr[0] + kslot, k1 = kr[1] + kslot, v0 = vr + vslot;
    PREF(0);
    if (c == 0) STAMP(9); else if (c == 1) STAMP(13);
    BAR(); SCHED();
    if (c == 0) STAMP(10); else if (c == 1) STAMP(14);
    XSEG(0, 64, WAIT_V(1); if (c == 0) WAIT_V(0));
    if (c == 0) STAMP(11); else if (c == 2) STAMP(15);
  }
  STAMP(16);
  SOFTMAX();
  {
    const lptr k0 = kr[0], k1 = kr[1], v0 = vr + SLOT_A;
    PREF(0);
    STAMP(17);
    if (wr == 0) BAR();
    SCHED();
    STAMP(18);
    XSEG(0, 32, );
  }
  STAMP(19);
  int lane2 = __builtin_amdgcn_mbcnt_hi(~0u, __builtin_amdgcn_mbcnt_lo(~0u, 0u));
  asm volatile("" : "+v"(lane2));
#define lane lane2
  const int isG = xcd >> 2, j2 = xcd & 3, tm = 8 * (j2 >> 1) + (idx & 7), tn = 4 * (j2 & 1) + (idx >> 3), nt = isG ? 56 : 64;
#define O_OUT() do { \
    const float inv = __builtin_amdgcn_rcpf(lacc[0]); \
    const unsigned oo = (unsigned)((rb * 16 + wr * 8 + wc * 2 + (r32 >> 4)) * 2048 + (hh * 16 + (r32 & 15)) * 16); \
    _Pragma("unroll") for (int d = 0; d < 4; ++d) _Pragma("unroll") for (int e = 0; e < 2; ++e) { \
        u32x4 w = {pk2(o[d][8 * e] * inv, o[d][8 * e + 1] * inv), pk2(o[d][8 * e + 2] * inv, o[d][8 * e + 3] * inv), \
                   pk2(o[d][8 * e + 4] * inv, o[d][8 * e + 5] * inv), pk2(o[d][8 * e + 6] * inv, o[d][8 * e + 7] * inv)}; \
        ST128(w, oo + (d & 1) * 1024 + e * 512, WS_O + (2 * hd + (d >> 1)) * 262144); } } while (0)
#define O_FLAG() do { if (wid == 0 && lane == 0) __builtin_amdgcn_raw_buffer_store_b32(tag, wrs, 0, WS_OFLAG + (rb * 32 + hd) * OFS, 16); } while (0)
#define POLL32(off_, slp) do { for (unsigned spins = 0; spins < 2000000u; ++spins) { \
      if (__builtin_amdgcn_ballot_w64(__builtin_amdgcn_raw_buffer_load_b32(wrs, (off_), 0, 16) != tag) == 0ull) break; \
      __builtin_amdgcn_s_sleep(slp); asm volatile("" ::: "memory"); } \
    asm volatile("" ::: "memory"); } while (0)
  if (wr == 0) {
    const char* ab = ws + (isG ? WS_H : WS_O) + tm * 16384 + wc * 4096;
    const char* wbp = (isG ? w2 : wo) + tn * 256 - 1024 + (wc >> 1) * 65536 + (wc & 1) * (isG ? 32768 : 16384);
    const unsigned voffA = lane * 16;
    unsigned voffW[2][2];
    {
      const int r = lane >> 3, c = lane & 7;
      voffW[0][0] = (unsigned)(r * 2048 + ((c ^ (((r >> 1) & 3) << 1)) << 4) + 1024);
      voffW[0][1] = voffW[0][0] + 128 - 1024;
      voffW[1][0] = voffW[0][0] + (isG ? 16384 : 32768) - 2048;
      voffW[1][1] = voffW[1][0] + 128 - 1024;
    }
#define K2_ISSUE(p_, buf_) do { const int k_ = isG ? ((p_) + 14 * j2 >= 56 ? (p_) + 14 * j2 - 56 : (p_) + 14 * j2) : ((((p_) + 8 * j2) & 31) | (~(p_) & 32)); \
      const unsigned d_ = lds0 + (buf_) * K2_STG + wc * 4096; \
      asm volatile("s_mov_b32 m0, %2\n\ts_nop 0\n\tglobal_load_lds_dwordx4 %0, %1 sc1\n\tglobal_load_lds_dwordx4 %0, %1 offset:1024 sc1\n\t" \
                   "global_load_lds_dwordx4 %0, %1 offset:2048 sc1\n\tglobal_load_lds_dwordx4 %0, %1 offset:3072 sc1" \
                   ::"v"(voffA), "s"(ab + (size_t)k_ * 262144), "s"(d_) : "memory"); \
      asm volatile("s_mov_b32 m0, %5\n\ts_nop 0\n\tglobal_load_lds_dwordx4 %0, %4\n\tglobal_load_lds_dwordx4 %1, %4 offset:1024\n\t" \
                   "global_load_lds_dwordx4 %2, %4 offset:2048\n\tglobal_load_lds_dwordx4 %3, %4 offset:3072" \
                   ::"v"(voffW[0][0]), "v"(voffW[0][1]), "v"(voffW[1][0]), "v"(voffW[1][1]), "s"(wbp + (size_t)k_ * 131072), "s"(d_ + 16384) : "memory"); } while (0)
    const unsigned f1 = isG ? (unsigned)(WS_HFLAG + ((tm >> 1) * 32 + (lane & 31)) * 128) : (unsigned)(WS_OFLAG + ((tm >> 1) * 32 + 16 + (lane & 15)) * OFS);
    const unsigned f2 = (unsigned)(WS_OFLAG + ((tm >> 1) * 32 + (lane & 15)) * OFS);
    O_OUT();
    asm volatile("s_waitcnt vmcnt(0)" :: "v"(pft) : "memory");
    STAMP(20);
    POLL32(f1, 4);
    if (isG) { BAR(); O_FLAG(); }
    K2_ISSUE(0, 3); K2_ISSUE(1, 4);
    STAMP(21);
    WAIT_V(8); BAR();
    K2_ISSUE(2, 0); K2_ISSUE(3, 1);
    STAMP(22);
    int buf = 2;
    unsigned pol = 0;
#pragma unroll 1
    for (int b = 0; b < nt - 4; ++b) {
      WAIT_V(16); BAR();
      if (b == 6 && !isG) O_FLAG();
      if (b == 24) asm volatile("buffer_load_dword %0, %1, %2, 0 offen sc1" : "+v"(pol) : "v"(f2), "s"(wrs) : "memory");
      if (b == 28 && !isG) {
        asm volatile("" : "+v"(pol));
        if (__builtin_amdgcn_ballot_w64(pol != tag) != 0ull) POLL32(f2, 4);
      }
      K2_ISSUE(b + 4, buf);
      buf = (buf == 4) ? 0 : buf + 1;
    }
    const int c8 = lane & 15, r0 = wc * 4 + (lane >> 4);
    const unsigned goff = (unsigned)((tm * 128 + r0) * 2048 + tn * 256 + c8 * 16);
    const lptr cb = lds + (isG ? 0 : 65536) + r0 * 512;
    WAIT_V(16); BAR();
    WAIT_V(8); BAR();
    WAIT_V(0); BAR();
    if (isG) {
      BAR();
      STAMP(23);
      BAR();
#pragma unroll
      for (int i = 0; i < 8; ++i) {
        const f32x4 a0 = *(const LAS f32x4*)(cb + i * 8192 + (((2 * c8) ^ (r0 & 7)) << 4)), a1 = *(const LAS f32x4*)(cb + i * 8192 + (((2 * c8 + 1) ^ (r0 & 7)) << 4));
        ST128((u32x4){pk2(silu(a0[0]), silu(a0[1])), pk2(silu(a0[2]), silu(a0[3])), pk2(silu(a1[0]), silu(a1[1])), pk2(silu(a1[2]), silu(a1[3]))}, goff + i * 32768, WS_G);
      }
    } else {
      u32x4 xv[8], gv[8];
      const unsigned gf = __builtin_amdgcn_raw_buffer_load_b32(wrs, 0, WS_GFLAG + (tm * 8 + tn) * 128, 16);
#pragma unroll
      for (int i = 0; i < 8; ++i) xv[i] = *(const u32x4*)(xn + goff + i * 32768);
      BAR();
      if (__builtin_amdgcn_readfirstlane(gf) != tag) POLL32((unsigned)(WS_GFLAG + (tm * 8 + tn) * 128), 1);
      asm volatile("" ::: "memory");
#pragma unroll
      for (int i = 0; i < 8; ++i) gv[i] = __builtin_amdgcn_raw_buffer_load_b128(wrs, goff + i * 32768, WS_G, 16);
      float xf[8][8], gf2[8][8];
#pragma unroll
      for (int i = 0; i < 8; ++i) {
#pragma unroll
        for (int e = 0; e < 4; ++e) { xf[i][2 * e] = bf2f(xv[i][e] & 0xffffu); xf[i][2 * e + 1] = U2F(xv[i][e] & 0xffff0000u); }
        asm volatile("" : "+v"(xf[i][0]), "+v"(xf[i][1]), "+v"(xf[i][2]), "+v"(xf[i][3]), "+v"(xf[i][4]), "+v"(xf[i][5]), "+v"(xf[i][6]), "+v"(xf[i][7]));
      }
#pragma unroll
      for (int i = 0; i < 8; ++i) {
#pragma unroll
        for (int e = 0; e < 4; ++e) { gf2[i][2 * e] = bf2f(gv[i][e] & 0xffffu); gf2[i][2 * e + 1] = U2F(gv[i][e] & 0xffff0000u); }
        asm volatile("" : "+v"(gf2[i][0]), "+v"(gf2[i][1]), "+v"(gf2[i][2]), "+v"(gf2[i][3]), "+v"(gf2[i][4]), "+v"(gf2[i][5]), "+v"(gf2[i][6]), "+v"(gf2[i][7]));
      }
      STAMP(23);
      BAR();
      f32x4 a0[8], a1[8];
#pragma unroll
      for (int i = 0; i < 8; ++i) { a0[i] = *(const LAS f32x4*)(cb + i * 8192 + (((2 * c8) ^ (r0 & 7)) << 4)); a1[i] = *(const LAS f32x4*)(cb + i * 8192 + (((2 * c8 + 1) ^ (r0 & 7)) << 4)); }
      SCHED();
#pragma unroll
      for (int i = 0; i < 8; ++i) {
        const u32x4 w = {pk2(xf[i][0] + a0[i][0] * gf2[i][0], xf[i][1] + a0[i][1] * gf2[i][1]), pk2(xf[i][2] + a0[i][2] * gf2[i][2], xf[i][3] + a0[i][3] * gf2[i][3]),
                         pk2(xf[i][4] + a1[i][0] * gf2[i][4], xf[i][5] + a1[i][1] * gf2[i][5]), pk2(xf[i][6] + a1[i][2] * gf2[i][6], xf[i][7] + a1[i][3] * gf2[i][7])};
        __builtin_amdgcn_raw_buffer_store_b128(w, __builtin_amdgcn_make_buffer_rsrc(out, 0, 4194304, 0x00020000), goff + i * 32768, 0, 16);
      }
    }
    if (isG) {
      WAIT_V(0); BAR();
      if (wid == 0 && lane == 0) __builtin_amdgcn_raw_buffer_store_b32(tag, wrs, 0, WS_GFLAG + (tm * 8 + tn) * 128, 16);
      if (bid == 4 && wid == 0) {
        for (unsigned spins = 0; spins < 2000000u; ++spins) {
          bool ok = true;
#pragma unroll
          for (int i = 0; i < 4; ++i) ok = ok && __builtin_amdgcn_raw_buffer_load_b32(wrs, (unsigned)((lane + i * 64) * 128), WS_HFLAG, 16) == tag;
          if (__builtin_amdgcn_ballot_w64(!ok) == 0ull) break;
          __builtin_amdgcn_s_sleep(4);
          asm volatile("" ::: "memory");
        }
        asm volatile("" ::: "memory");
        if (lane == 0) __builtin_amdgcn_raw_buffer_store_b32(tag, wrs, 0, 0, 16);
      }
    }
  } else {
    asm volatile("s_waitcnt vmcnt(0)" :: "v"(pft) : "memory");
    O_OUT();
    if (isG) { WAIT_V(0); BAR(); }
    STAMP(20);
    STAMP(21);
    const int wm = wc >> 1, wn = wc & 1, g = lane >> 4, t = lane & 15, q4 = (lane >> 2) & 3, p4 = lane & 3;
    lptr w2b[4];
#pragma unroll
    for (int ft = 0; ft < 4; ++ft) w2b[ft] = lds + 3 * K2_STG + 16384 + wn * 1024 + (g >> 1) * 2048 + (4 * (g & 1) + q4) * 128 + 32 * (ft ^ (2 * (g & 1) + (q4 >> 1))) + 8 * p4;
    lptr ar = lds + 3 * K2_STG + wm * 8192 + lane * 16;
    f32x4 ac2[4][4];
#pragma unroll
    for (int m = 0; m < 4; ++m)
#pragma unroll
      for (int n = 0; n < 4; ++n) ac2[m][n] = (f32x4){0.f, 0.f, 0.f, 0.f};
    bf16x8 Af[2][4];
    int dlt = 0;
    bf16x4 Wh[2][4][2];
#define K2_RD(set, ks, i) do { if ((i) == 0) Af[set][0] = ldb128(ar + (ks) * 1024); else if ((i) >= 3 && (i) < 6) Af[set][(i) - 2] = ldb128(ar + ((i) - 2) * 2048 + (ks) * 1024); \
      else { const int f_ = (i) < 3 ? 0 : ((i) - 4) >> 1, h_ = (i) < 3 ? (i) - 1 : (i) & 1; \
             Wh[set][f_][h_] = __builtin_amdgcn_ds_read_tr16_b64_v4bf16((LAS bf16x4*)(w2b[f_] + (ks) * 8192 + h_ * 4096)); } } while (0)
#define K2_W(set, m) __builtin_shufflevector(Wh[set][m][0], Wh[set][m][1], 0, 1, 2, 3, 4, 5, 6, 7)
    BAR();
    STAMP(22);
#pragma unroll
    for (int i = 0; i < 12; ++i) { K2_RD(0, 0, i); SCHED(); }
    int buf = 3;
#define K2_TILE() do { SCHED(); \
      _Pragma("unroll") for (int i = 0; i < 16; ++i) {         \
        ac2[i >> 2][i & 3] = MFMA16(K2_W(0, i >> 2), Af[0][i & 3], ac2[i >> 2][i & 3]); \
        if (i < 12) K2_RD(1, 1, i); \
        if (i == 12) { dlt = (buf == 4) ? -4 * K2_STG : K2_STG; buf = (buf == 4) ? 0 : buf + 1; ar += dlt; asm volatile("" : "+v"(ar)); } \
        if (i == 13) { w2b[0] += dlt; w2b[1] += dlt; asm volatile("" : "+v"(w2b[0]), "+v"(w2b[1])); } \
        if (i == 14) { w2b[2] += dlt; w2b[3] += dlt; asm volatile("" : "+v"(w2b[2]), "+v"(w2b[3])); } \
        if (i == 15) BAR();                  \
        SCHED(); } \
      _Pragma("unroll") for (int i = 0; i < 16; ++i) {         \
        ac2[i >> 2][i & 3] = MFMA16(K2_W(1, i >> 2), Af[1][i & 3], ac2[i >> 2][i & 3]); \
        if (i < 12) K2_RD(0, 0, i); \
        SCHED(); } } while (0)
#pragma unroll 1
    for (int kt = 0; kt < 6; ++kt) K2_TILE();
    WAIT_V(0);
#pragma unroll 1
    for (int kt = 6; kt < nt; ++kt) K2_TILE();
    STAMP(23);
    const lptr cw = lds + (isG ? 0 : 65536) + (wm * 64 + t) * 512;
#pragma unroll
    for (int m = 0; m < 4; ++m)
#pragma unroll
      for (int n = 0; n < 4; ++n) *(LAS f32x4*)(cw + n * 8192 + (((wn * 16 + m * 4 + g) ^ (t & 7)) << 4)) = ac2[m][n];
    WAIT_L(0); BAR();
    if (isG) BAR();
  }
  STAMP(24);
  STAMP_OUT(ws + (57 << 20), 30);
#undef lane
}

extern "C" void kernel_launch(void* const* d_in, const int* in_sizes, int n_in, void* d_out, int out_size, void* d_ws, size_t ws_size, hipStream_t stream) {
  const char* x = (const char*)d_in[0]; const char* kc = (const char*)d_in[1]; const char* vc = (const char*)d_in[2];
  const char* wq = (const char*)d_in[3]; const char* wo = (const char*)d_in[4]; const char* w1 = (const char*)d_in[6]; const char* w2 = (const char*)d_in[7];
  (void)hipFuncSetAttribute((const void*)mega_kernel, hipFuncAttributeMaxDynamicSharedMemorySize, K1_LDS);
  mega_kernel<<<256, 512, K1_LDS, stream>>>(x, kc, vc, wq, w1, wo, w2, (char*)d_ws, (char*)d_out);
}
```

```cpp
#define PK 0
#define PROBE_X 0
#include <hip/hip_runtime.h>
#include <stdint.h>

typedef unsigned short u16;
typedef __bf16 bf16x2 __attribute__((ext_vector_type(2)));
typedef __bf16 bf16x4 __attribute__((ext_vector_type(4)));
typedef __bf16 bf16x8 __attribute__((ext_vector_type(8)));
typedef float f32x2 __attribute__((ext_vector_type(2)));
typedef float f32x4 __attribute__((ext_vector_type(4)));
typedef float f32x16 __attribute__((ext_vector_type(16)));
typedef unsigned u32x2 __attribute__((ext_vector_type(2)));
typedef unsigned u32x4 __attribute__((ext_vector_type(4)));
#define LAS __attribute__((address_space(3)))
typedef LAS char* lptr;
#if defined(__HIP_DEVICE_COMPILE__) && !defined(USEPK)
#define NOPK __attribute__((target("no-packed-fp32-ops")))
#else
#define NOPK
#endif
#define DI NOPK __device__ __forceinline__
#define F2U(x) __builtin_bit_cast(unsigned, (float)(x))
#define U2F(x) __builtin_bit_cast(float, (unsigned)(x))
#define TID ((int)__builtin_amdgcn_workitem_id_x())
#define BID ((int)__builtin_amdgcn_workgroup_id_x())
#define SCHED() __builtin_amdgcn_sched_barrier(0)
#define BAR() asm volatile("s_barrier" ::: "memory")
#define WAIT_V(n) asm volatile("s_waitcnt vmcnt(%0)" :: "n"(n) : "memory")
#define WAIT_L(n) do { asm volatile("" ::: "memory"); __builtin_amdgcn_s_waitcnt(0xC07F | ((n) << 8)); asm volatile("" ::: "memory"); } while (0)
#define MFMA16(a, b, c) __builtin_amdgcn_mfma_f32_16x16x32_bf16(a, b, c, 0, 0, 0)
#define MFMA32(a, b, c) __builtin_amdgcn_mfma_f32_32x32x16_bf16(a, b, c, 0, 0, 0)

DI void glds(unsigned voff, const char* sbase, unsigned ldsaddr) {
  asm volatile("s_mov_b32 m0, %2\n\ts_nop 0\n\tglobal_load_lds_dwordx4 %0, %1" ::"v"(voff), "s"(sbase), "s"(ldsaddr) : "memory");
}
DI void glds1(unsigned voff, const char* sbase, unsigned ldsaddr) {
  asm volatile("s_mov_b32 m0, %2\n\ts_nop 0\n\tglobal_load_lds_dwordx4 %0, %1 sc1" ::"v"(voff), "s"(sbase), "s"(ldsaddr) : "memory");
}
DI unsigned pk2(float lo, float hi) { f32x2 v = {lo, hi}; return __builtin_bit_cast(unsigned, __builtin_convertvector(v, bf16x2)); }
DI float bf2f(unsigned b) { return U2F(b << 16); }
DI bf16x8 ldb128(lptr p) { return *(const LAS bf16x8*)p; }
DI bf16x8 ldtr2(lptr p, int d) {
  bf16x4 a = __builtin_amdgcn_ds_read_tr16_b64_v4bf16((LAS bf16x4*)p);
  bf16x4 b = __builtin_amdgcn_ds_read_tr16_b64_v4bf16((LAS bf16x4*)(p + d));
  return __builtin_shufflevector(a, b, 0, 1, 2, 3, 4, 5, 6, 7);
}
DI void ST128_(__amdgpu_buffer_rsrc_t r, u32x4 v, unsigned voff, int soff) { __builtin_amdgcn_raw_buffer_store_b128(v, r, voff + (unsigned)soff, 0, 16); }
DI float silu(float v) { return v * __builtin_amdgcn_rcpf(1.0f + __builtin_amdgcn_exp2f(v * -1.4426950408889634f)); }

#define STAMP_DECL const unsigned long long pa_ = __builtin_amdgcn_s_memrealtime(); if (BID == 0 && TID == 0) __hip_atomic_store((unsigned long long*)(ws + 2048), pa_, __ATOMIC_RELAXED, __HIP_MEMORY_SCOPE_AGENT);
#define LSTAMP(i)
#define STAMP(i)
#define STAMP_OUT(buf, n) do { if (BID == PROBE_X && wid == 0) { const unsigned long long t0_ = __hip_atomic_load((unsigned long long*)(ws + 2048), __ATOMIC_RELAXED, __HIP_MEMORY_SCOPE_AGENT); \
    const long long d_ = 12000 + (long long)(pa_ - t0_) * PK; \
    if (d_ > 0 && d_ < 100000) while ((long long)(__builtin_amdgcn_s_memrealtime() - t0_) < d_) __builtin_amdgcn_s_sleep(1); } } while (0)
template <int CTRL> DI float dpp_add(float v) { return v + U2F(__builtin_amdgcn_update_dpp(0u, F2U(v), CTRL, 0xf, 0xf, true)); }
DI float half_sum(float v) {
  v = dpp_add<0xB1>(v); v = dpp_add<0x4E>(v); v = dpp_add<0x141>(v); v = dpp_add<0x140>(v);
  auto rr = __builtin_amdgcn_permlane16_swap(F2U(v), F2U(v), false, false);
  return U2F(rr[0]) + U2F(rr[1]);
}
typedef __attribute__((address_space(1))) unsigned long long gu64;

#define SX(b, h) (98304 + (b) * 32768 + (h) * 16384)
constexpr int SLOT_B = 0, SLOT_E = 32768, SLOT_A = 65536, SLOT_C = 98304, SLOT_D = 131072, K1_LDS = 163840;

#define XSWAP 0
constexpr int WS_HFLAG = 4096;
constexpr int WS_GFLAG = 65536;
constexpr int WS_OFLAG = 131072;
#define OFS 16
constexpr int WS_STATS = 1 << 20;
constexpr int WS_G = 5 << 20, WS_O = 9 << 20, WS_H = 25 << 20, WS_END = 39 << 20;
constexpr int K2_STG = 32768;

NOPK __global__ __launch_bounds__(512, 2) void mega_kernel(const char* __restrict__ xn, const char* __restrict__ kc, const char* __restrict__ vc,
                                                           const char* __restrict__ wq, const char* __restrict__ w1, const char* __restrict__ wo, const char* __restrict__ w2,
                                                           char* ws, char* __restrict__ out) {
  STAMP_DECL
  STAMP(0);
  extern __shared__ __attribute__((aligned(16))) char shm[];
  const lptr lds = (lptr)shm;
  const unsigned lds0 = (unsigned)(uintptr_t)lds;
  const int tid = TID, wid = __builtin_amdgcn_readfirstlane(tid >> 6), lane = tid & 63, wr = wid >> 2, wc = wid & 3;
  const int bid = BID ^ XSWAP, xcd = bid & 7, idx = bid >> 3, hd = 4 * xcd + (idx & 3), rb = idx >> 2;
  const int g = lane >> 4, t = lane & 15, q4 = (lane >> 2) & 3, p4 = lane & 3;

  const bool light = xcd >= 4;
  unsigned voffX[4], voffQ[2], voffH[2];
  {
    const int r = lane >> 3, c = lane & 7;
#pragma unroll
    for (int i = 0; i < 4; ++i) voffX[i] = (unsigned)((8 * i + r) * 2048 + ((c ^ (4 * (i & 1) + (r >> 1))) << 4) - i * 1024);
    const int k = 32 * (wid >> 2) + 16 * (wid & 1) + 4 * ((wid >> 1) & 1) + 8 * (r >> 2) + (r & 3);
    const int cw = c ^ (((r >> 1) & 3) << 1);
    voffQ[0] = (unsigned)(k * 8192 + cw * 16 + 1024); voffQ[1] = voffQ[0] + 128 - 1024;
    voffH[0] = (unsigned)(k * 7168 + 128 + (light ? (cw & 3) : cw) * 16 + 1024);
    voffH[1] = (unsigned)(k * 7168 + cw * 16);
  }
  const char* xb = xn + (size_t)rb * 256 * 2048 + wid * 65536;
  const char* qb = wq + hd * 256 - 1024;
  const char* hb = w1 + (light ? 4096 + 192 * (hd - 16) : 256 * hd) - 1024;
  const bool full = wr == 1 || !light;
#define GLDS2(v0_, v1_, sb_, ld_) asm volatile("s_mov_b32 m0, %3\n\ts_nop 0\n\tglobal_load_lds_dwordx4 %0, %2\n\tglobal_load_lds_dwordx4 %1, %2 offset:1024" \
    ::"v"(v0_), "v"(v1_), "s"(sb_), "s"(ld_) : "memory")
#define GLDS4(v0_, v1_, v2_, v3_, sb_, ld_) asm volatile("s_mov_b32 m0, %5\n\ts_nop 0\n\tglobal_load_lds_dwordx4 %0, %4\n\tglobal_load_lds_dwordx4 %1, %4 offset:1024\n\t" \
    "global_load_lds_dwordx4 %2, %4 offset:2048\n\tglobal_load_lds_dwordx4 %3, %4 offset:3072" ::"v"(v0_), "v"(v1_), "v"(v2_), "v"(v3_), "s"(sb_), "s"(ld_) : "memory")
#define ST_X(b, kt) GLDS4(voffX[0], voffX[1], voffX[2], voffX[3], xb + (kt) * 128, lds0 + SX(b, 0) + wid * 4096)
#define ST_W0(wo_, kt) GLDS2(voffQ[0], voffQ[1], qb + (size_t)(kt) * 524288, lds0 + (wo_) + wid * 2048)
#define ST_W1(wo_, kt) GLDS2(voffH[0], voffH[1], hb + (size_t)(kt) * 458752, lds0 + (wo_) + 16384 + wid * 2048)

  lptr wb[4], xr[2];
#pragma unroll
  for (int m = 0; m < 4; ++m) wb[m] = lds + wr * 1024 + (g >> 1) * 2048 + (4 * (g & 1) + q4) * 128 + 32 * ((m ^ wc) ^ (2 * (g & 1) + (q4 >> 1))) + 8 * p4;
#pragma unroll
  for (int ks = 0; ks < 2; ++ks) xr[ks] = lds + 98304 + (wc * 32 + t) * 128 + 16 * ((4 * ks + g) ^ (t >> 1));
#define LDW(dst, h) do { _Pragma("unroll") for (int m = 0; m < 4; ++m) _Pragma("unroll") for (int k = 0; k < 2; ++k) dst[m][k] = ldtr2(wb[m] + (h) * 16384 + k * 8192, 4096); } while (0)
#define LDX(dst, b, h) do { _Pragma("unroll") for (int n = 0; n < 2; ++n) _Pragma("unroll") for (int k = 0; k < 2; ++k) dst[n][k] = ldb128(xr[k] + (SX(b, h) - 98304) + n * 2048); } while (0)

  f32x4 acc[2][2][4][2];
#pragma unroll
  for (int a = 0; a < 2; ++a)
#pragma unroll
    for (int b = 0; b < 2; ++b)
#pragma unroll
      for (int m = 0; m < 4; ++m)
#pragma unroll
        for (int n = 0; n < 2; ++n) acc[a][b][m][n] = (f32x4){0.f, 0.f, 0.f, 0.f};
  bf16x8 At[4][2], B0[2][2], B1[2][2];

#define MM4(ai, m, k) do { _Pragma("unroll") for (int n = 0; n < 2; ++n) { \
      acc[ai][0][m][n] = MFMA16(At[m][k], B0[n][k], acc[ai][0][m][n]); acc[ai][1][m][n] = MFMA16(At[m][k], B1[n][k], acc[ai][1][m][n]); } } while (0)
#define MMA2(ai, CLOSE) do { __builtin_amdgcn_s_setprio(1); \
    MM4(ai, 0, 0); cs[ai] = MFMA16(At[0][0], ones, cs[ai]); MM4(ai, 1, 0); MM4(ai, 0, 1); cs[ai] = MFMA16(At[0][1], ones, cs[ai]); MM4(ai, 1, 1); \
    asm volatile("" : "+v"(cs[ai])); \
    if ((ai) == 0 || full) { MM4(ai, 2, 0); MM4(ai, 3, 0); MM4(ai, 2, 1); MM4(ai, 3, 1); } \
    SCHED(); CLOSE; __builtin_amdgcn_s_setprio(0); SCHED(); } while (0)
  char* const stats = ws + WS_STATS;
  const __amdgpu_buffer_rsrc_t wrs = __builtin_amdgcn_make_buffer_rsrc(ws, 0, WS_END, 0x00020000);
  f32x4 cs[2] = {{0.f, 0.f, 0.f, 0.f}, {0.f, 0.f, 0.f, 0.f}};
  bf16x8 ones = __builtin_bit_cast(bf16x8, (u32x4){0x3f803f80u, 0x3f803f80u, 0x3f803f80u, 0x3f803f80u});
#define PIN_SETUP() do { asm volatile("" : "+v"(ones), "+v"(cs[0]), "+v"(cs[1]), "+v"(wb[0]), "+v"(wb[1]), "+v"(wb[2]), "+v"(wb[3]), "+v"(xr[0]), "+v"(xr[1])); \
    _Pragma("unroll") for (int a = 0; a < 2; ++a) _Pragma("unroll") for (int b = 0; b < 2; ++b) \
      asm volatile("" : "+v"(acc[a][b][0][0]), "+v"(acc[a][b][0][1]), "+v"(acc[a][b][1][0]), "+v"(acc[a][b][1][1]), "+v"(acc[a][b][2][0]), "+v"(acc[a][b][2][1]), "+v"(acc[a][b][3][0]), "+v"(acc[a][b][3][1])); } while (0)
  unsigned tag;
  {
    if (wr == 0) {
      unsigned ep;
      ST_X(0, 0); ST_W0(0, 0);
      asm volatile("global_load_dword %0, %1, %2 sc1" : "=&v"(ep) : "v"(0), "s"(ws) : "memory");
      ST_W1(0, 0); ST_W0(32768, 1); ST_X(1, 1);
      PIN_SETUP();
      asm volatile("s_waitcnt vmcnt(8)" : "+v"(ep) :: "memory");
      tag = __builtin_amdgcn_readfirstlane(ep) + 1u;
      BAR();
    } else {
      const int row = rb * 256 + hd * 8 + wc * 2 + (lane >> 5);
      u32x4 a[4];
      unsigned ep;
      asm volatile("global_load_dword %0, %1, %2 sc1" : "=&v"(ep) : "v"(0), "s"(ws) : "memory");
      asm volatile("global_load_dwordx4 %0, %4, %5\n\tglobal_load_dwordx4 %1, %4, %5 offset:512\n\tglobal_load_dwordx4 %2, %4, %5 offset:1024\n\tglobal_load_dwordx4 %3, %4, %5 offset:1536"
                   : "=&v"(a[0]), "=&v"(a[1]), "=&v"(a[2]), "=&v"(a[3]) : "v"(row * 2048 + (lane & 31) * 16), "s"(xn) : "memory");
      ST_X(0, 0); ST_W0(0, 0);
      ST_W1(0, 0); ST_W0(32768, 1); ST_X(1, 1);
      PIN_SETUP();
      asm volatile("s_waitcnt vmcnt(8)" : "+v"(ep), "+v"(a[0]), "+v"(a[1]), "+v"(a[2]), "+v"(a[3]) :: "memory");
      BAR();
      tag = __builtin_amdgcn_readfirstlane(ep) + 1u;
      float v[32];
#pragma unroll
      for (int i = 0; i < 16; ++i) { v[2 * i] = bf2f(a[i >> 2][i & 3] & 0xffffu); v[2 * i + 1] = U2F(a[i >> 2][i & 3] & 0xffff0000u); }
      float s0 = 0.f, s1 = 0.f, s2 = 0.f, s3 = 0.f;
#pragma unroll
      for (int i = 0; i < 8; ++i) { s0 += v[i]; s1 += v[8 + i]; s2 += v[16 + i]; s3 += v[24 + i]; }
      const float mean = half_sum((s0 + s1) + (s2 + s3)) * (1.0f / 1024.0f);
      s0 = s1 = s2 = s3 = 0.f;
#pragma unroll
      for (int i = 0; i < 8; ++i) { const float d0 = v[i] - mean, d1 = v[8 + i] - mean, d2 = v[16 + i] - mean, d3 = v[24 + i] - mean; s0 += d0 * d0; s1 += d1 * d1; s2 += d2 * d2; s3 += d3 * d3; }
      const float sd = __builtin_amdgcn_sqrtf(half_sum((s0 + s1) + (s2 + s3)) * (1.0f / 1024.0f) + 1e-5f);
      if ((lane & 31) < 2) __hip_atomic_store((gu64*)(stats + row * 16 + (lane & 1) * 8), ((unsigned long long)tag << 32) | F2U((lane & 1) ? sd : mean), __ATOMIC_RELAXED, __HIP_MEMORY_SCOPE_AGENT);
    }
  }
  if (wr == 1) BAR();
  SCHED();
  STAMP(1);
  const int kvg = hd >> 3;
  const char* kb_g = kc + ((size_t)kvg * 2048 + 1536) * 256;
  const char* vb_g = vc + ((size_t)kvg * 2048 + 1536) * 256;
#define VOFFK ((unsigned)(((lane >> 2) & 7) * 256 + (4 * (lane >> 5) + ((lane & 3) ^ ((2 * (wid & 1) + ((lane >> 4) & 1)) & 3))) * 16))
#define VOFFV ((unsigned)(((lane >> 2) & 7) * 256 + (lane >> 5) * 64 + (lane & 3) * 16))
#define ST_KV8(voff, gsrc, slot) do { glds(voff, (gsrc) + wid * 2048, lds0 + (slot) + wid * 2048); glds(voff, (gsrc) + wid * 2048 + 128, lds0 + (slot) + wid * 2048 + 1024); } while (0)
  int wprv = 65536, wnxt = 32768;
#define W_ADV() do { const int d_ = (wnxt == 0) ? -65536 : 32768, cur_ = wnxt - d_; wnxt = wprv; wprv = cur_; \
    _Pragma("unroll") for (int f = 0; f < 4; ++f) wb[f] += d_; \
    asm volatile("" : "+v"(wb[0]), "+v"(wb[1]), "+v"(wb[2]), "+v"(wb[3])); } while (0)
#define PH_A(b, STAGE) do { LDX(B0, b, 0); LDX(B1, b, 1); SCHED(); LDW(At, 0); STAGE; WAIT_V(10); WAIT_L(12); BAR(); SCHED(); MMA2(0, WAIT_L(0); BAR()); } while (0)
#define PH_B(STAGE) do { LDW(At, 1); STAGE; W_ADV(); WAIT_V(8); BAR(); SCHED(); MMA2(1, WAIT_L(0); BAR()); } while (0)
#pragma unroll 1
  for (int kt = 0; kt < 14; kt += 2) {
    PH_A(0, ST_W1(wnxt, kt + 1); ST_W0(wprv, kt + 2));
    PH_B(ST_X(0, kt + 2));
    PH_A(1, ST_W1(wnxt, kt + 2); ST_W0(wprv, kt + 3));
    PH_B(ST_X(1, kt + 3));
  }
  STAMP(2);
  const unsigned voffK = VOFFK, voffV = VOFFV;
  PH_A(0, ST_W1(0, 15); ST_KV8(voffK, kb_g, SLOT_E));
  PH_B(ST_KV8(voffV, vb_g, SLOT_C); ST_KV8(voffV, vb_g + 16384, SLOT_C + 16384));
  const unsigned soff = (unsigned)((rb * 256 + (g >> 1) * 128 + wc * 32 + (g & 1) * 16 + t) * 16);
  u32x4 sg;
  LDX(B0, 1, 0); LDX(B1, 1, 1); SCHED(); LDW(At, 0);
  asm volatile("global_load_dwordx4 %0, %1, %2 sc1" : "=&v"(sg) : "v"(soff), "s"(stats) : "memory");
  ST_KV8(voffK, kb_g + 16384, SLOT_E + 16384); ST_KV8(voffK, kb_g + 32768, SLOT_A);
  WAIT_V(11); BAR(); SCHED(); MMA2(0, WAIT_L(0); BAR());
  LDW(At, 1);
  ST_KV8(voffK, kb_g + 49152, SLOT_A + 16384);
  BAR(); SCHED();
  const lptr csw = lds + SLOT_D + (t & 3) * 8192 + wr * 4096 + wc * 64 + g * 16;
  MMA2(1, *(LAS f32x4*)csw = cs[0]; *(LAS f32x4*)(csw + 256) = cs[1]; asm volatile("s_waitcnt vmcnt(4)" : "+v"(sg) :: "memory"); WAIT_L(0); BAR());
  f32x4 cv[2][4];
#pragma unroll
  for (int a = 0; a < 2; ++a)
#pragma unroll
    for (int m = 0; m < 4; ++m) cv[a][m] = *(const LAS f32x4*)(lds + SLOT_D + wc * 8192 + wr * 4096 + a * 256 + (m ^ wc) * 64 + g * 16);
  if (__builtin_expect(__builtin_amdgcn_ballot_w64(sg[1] != tag || sg[3] != tag) != 0ull, 0)) {
    for (unsigned spins = 0; spins < 4000000u; ++spins) {
      __builtin_amdgcn_s_sleep(2);
      asm volatile("" ::: "memory");
      sg = __builtin_amdgcn_raw_buffer_load_b128(wrs, soff, WS_STATS, 16);
      if (__builtin_amdgcn_ballot_w64(sg[1] != tag || sg[3] != tag) == 0ull) break;
    }
  }
  float mu[2][2], rs[2][2], sd[2][2];
  {
    auto m16 = __builtin_amdgcn_permlane16_swap(sg[0], sg[0], false, false);
    auto s16 = __builtin_amdgcn_permlane16_swap(sg[2], sg[2], false, false);
#pragma unroll
    for (int n = 0; n < 2; ++n) {
      auto m32 = __builtin_amdgcn_permlane32_swap(m16[n], m16[n], false, false);
      auto s32 = __builtin_amdgcn_permlane32_swap(s16[n], s16[n], false, false);
#pragma unroll
      for (int bj = 0; bj < 2; ++bj) { mu[bj][n] = -U2F(m32[bj]); sd[bj][n] = U2F(s32[bj]); rs[bj][n] = __builtin_amdgcn_rcpf(sd[bj][n]) * 1.4426950408889634f; }
    }
  }
  WAIT_L(0);
  STAMP(3);
  {
    const lptr qw = lds + wc * 8192 + wr * 4096 + ((g >> 1) * 32 + t) * 16 + (g & 1) * 8;
#pragma unroll
    for (int bj = 0; bj < 2; ++bj) {
      lptr qwb = qw + (bj ? SLOT_D : SLOT_B);
      asm volatile("" : "+v"(qwb));
#pragma unroll
      for (int m = 0; m < 4; ++m)
#pragma unroll
        for (int n = 0; n < 2; ++n) {
          const f32x4 v = (acc[0][bj][m][n] + cv[0][m] * mu[bj][n]) * (rs[bj][n] * 0.08838834764831845f);
          u32x2 w = {pk2(v[0], v[1]), pk2(v[2], v[3])};
          *(LAS u32x2*)(qwb + (m ^ wc) * 1024 + n * 256) = w;
        }
    }
  }
  WAIT_L(0); BAR(); SCHED();
  STAMP(4);
  bf16x8 qf[8];
  const lptr qr = lds + (wr ? SLOT_D : SLOT_B) + wc * 8192 + lane * 16;
  if (wr == 1) {
#pragma unroll
    for (int s = 0; s < 8; ++s) qf[s] = ldb128(qr + s * 1024);
  }
  {
#define ST128(...) ST128_(wrs, __VA_ARGS__)
    u32x2 hw[2][2][4];
#define SILU16(m) do { \
      const f32x4 a0 = acc[1][0][m][0] + cv[1][m] * mu[0][0], a1 = acc[1][0][m][1] + cv[1][m] * mu[0][1], a2 = acc[1][1][m][0] + cv[1][m] * mu[1][0], a3 = acc[1][1][m][1] + cv[1][m] * mu[1][1]; \
      f32x4 e0 = a0 * -rs[0][0], e1 = a1 * -rs[0][1], e2 = a2 * -rs[1][0], e3 = a3 * -rs[1][1]; SCHED(); \
      _Pragma("unroll") for (int i = 0; i < 4; ++i) { e0[i] = __builtin_amdgcn_exp2f(e0[i]); e1[i] = __builtin_amdgcn_exp2f(e1[i]); e2[i] = __builtin_amdgcn_exp2f(e2[i]); e3[i] = __builtin_amdgcn_exp2f(e3[i]); } SCHED(); \
      e0 = e0 * sd[0][0] + sd[0][0]; e1 = e1 * sd[0][1] + sd[0][1]; e2 = e2 * sd[1][0] + sd[1][0]; e3 = e3 * sd[1][1] + sd[1][1]; SCHED(); \
      _Pragma("unroll") for (int i = 0; i < 4; ++i) { e0[i] = __builtin_amdgcn_rcpf(e0[i]); e1[i] = __builtin_amdgcn_rcpf(e1[i]); e2[i] = __builtin_amdgcn_rcpf(e2[i]); e3[i] = __builtin_amdgcn_rcpf(e3[i]); } SCHED(); \
      e0 *= a0; e1 *= a1; e2 *= a2; e3 *= a3; SCHED(); \
      hw[0][0][m] = (u32x2){pk2(e0[0], e0[1]), pk2(e0[2], e0[3])}; hw[0][1][m] = (u32x2){pk2(e1[0], e1[1]), pk2(e1[2], e1[3])}; \
      hw[1][0][m] = (u32x2){pk2(e2[0], e2[1]), pk2(e2[2], e2[3])}; hw[1][1][m] = (u32x2){pk2(e3[0], e3[1]), pk2(e3[2], e3[3])}; SCHED(); } while (0)
    SILU16(0);
    if (wr == 0) {
      BAR(); SCHED();
#pragma unroll
      for (int s = 0; s < 8; ++s) qf[s] = ldb128(qr + s * 1024);
    }
    SILU16(1);
    const unsigned hoff = (unsigned)((rb * 16 + wc * 2) * 2048 + lane * 16);
    const int pb = (light ? 16 + 3 * hd : 4 * hd) + (wr ? 0 : 2), sw = full ? (wc >> 1) : 0, p0 = pb + sw, p1 = pb + (sw ^ 1);
#define H_OUT1(p_, a_, b_) do { _Pragma("unroll") for (int bj = 0; bj < 2; ++bj) _Pragma("unroll") for (int n = 0; n < 2; ++n) \
      ST128((u32x4){hw[bj][n][a_][0], hw[bj][n][a_][1], hw[bj][n][b_][0], hw[bj][n][b_][1]}, hoff + n * 2048, WS_H + ((p_) >> 1) * 262144 + ((p_) & 1) * 1024 + bj * 16384); } while (0)
#define H_OUT(p_, m_) do { if (wc & 1) H_OUT1(p_, (m_) + 1, m_); else H_OUT1(p_, m_, (m_) + 1); } while (0)
    H_OUT(p0, 0);
    if (full) { SILU16(2); SILU16(3); H_OUT(p1, 2); }
  }
  STAMP(5);
  if (wr == 1) { WAIT_L(0); BAR(); }
  __builtin_amdgcn_s_setprio(1);
  SCHED();
  STAMP(6);
  const int r32 = lane & 31, hh = lane >> 5;
  lptr kr[2];
#pragma unroll
  for (int e = 0; e < 2; ++e) kr[e] = lds + 2048 * (r32 >> 3) + 64 * (r32 & 7) + 16 * ((2 * e + hh) ^ ((r32 >> 2) & 3));
  const lptr vr = lds + ((lane & 3) << 3) + (((lane >> 2) & 3) << 6) + (((lane >> 4) & 1) << 5) + (hh << 8);
  f32x16 o[4], s[4];
#pragma unroll
  for (int d = 0; d < 4; ++d)
#pragma unroll
    for (int i = 0; i < 16; ++i) o[d][i] = 0.f;
  bf16x8 pf[8];
  constexpr int RD = 6;
  bf16x8 fr[RD];
  f32x4 lacc = {0.f, 0.f, 0.f, 0.f};
  const unsigned selw = (((lane >> 4) ^ (lane >> 2)) & 1) ? 0u : 0x3f803f80u;
  bf16x8 sel = __builtin_bit_cast(bf16x8, (u32x4){selw, selw, selw, selw});
  asm volatile("" : "+v"(sel));
  f32x16 negm = {0.f, 0.f, 0.f, 0.f, 0.f, 0.f, 0.f, 0.f, 0.f, 0.f, 0.f, 0.f, 0.f, 0.f, 0.f, 0.f};
#define SM_THR 60.0f
#define FRAG(i) ((i) < 32 ? ldtr2(v0 + (((i) >> 2) * 8 + ((i) & 3)) * 512, 2048) : ldb128((((((i) - 32) >> 2) & 1) ? k1 : k0) + ((i) & 3) * 8192 + (((i) - 32) >> 3) * 512))
#define XEARLY 3
#define XPRIO 1
#define YPRIO 0
#define XSEG(lo, hi, CLOSE) do { __builtin_amdgcn_s_setprio(XPRIO); \
    _Pragma("unroll") for (int i = (lo); i < (hi); ++i) { \
      if (i < 32) o[i & 3] = MFMA32(fr[i % RD], pf[(i >> 2) & 7], o[i & 3]); \
      else s[i & 3] = MFMA32(fr[i % RD], qf[((i - 32) >> 2) & 7], i < 36 ? negm : s[i & 3]); \
      if (i < 32 && (i & 3) == 1) lacc = MFMA16(sel, pf[(i >> 2) & 7], lacc); \
      if (i + RD < (hi)) fr[i % RD] = FRAG(i + RD); \
      SCHED(); \
      if (i == 63 - XEARLY) { __builtin_amdgcn_s_setprio(2); CLOSE; WAIT_L(0); BAR(); SCHED(); } } \
    __builtin_amdgcn_s_setprio(YPRIO); } while (0)
#define PREF(lo) do { _Pragma("unroll") for (int i = (lo); i < (lo) + RD; ++i) fr[i % RD] = FRAG(i); SCHED(); } while (0)
#define MX3(a, b, c) __builtin_fmaxf(__builtin_fmaxf((a), (b)), (c))
#define SOFTMAX() do { \
      float m0 = MX3(s[0][0], s[0][1], s[0][2]), m1 = MX3(s[1][0], s[1][1], s[1][2]), m2 = MX3(s[2][0], s[2][1], s[2][2]), m3 = MX3(s[3][0], s[3][1], s[3][2]); \
      _Pragma("unroll") for (int i = 3; i < 15; i += 2) { m0 = MX3(m0, s[0][i], s[0][i + 1]); m1 = MX3(m1, s[1][i], s[1][i + 1]); m2 = MX3(m2, s[2][i], s[2][i + 1]); m3 = MX3(m3, s[3][i], s[3][i + 1]); } \
      m0 = MX3(m0, s[0][15], s[1][15]); m2 = MX3(m2, s[2][15], s[3][15]); \
      float mx = __builtin_fmaxf(MX3(m0, m1, m2), m3); \
        \
      if (__builtin_expect(__builtin_amdgcn_ballot_w64(!(mx <= SM_THR)) != 0ull, 0)) { \
        auto rr = __builtin_amdgcn_permlane32_swap(F2U(mx), F2U(mx), false, false); \
        const float dl = MX3(U2F(rr[0]), U2F(rr[1]), 0.f); \
        _Pragma("unroll") for (int kb = 0; kb < 4; ++kb) s[kb] -= dl; \
        negm -= dl; \
        const float alpha = __builtin_amdgcn_exp2f(-dl); lacc *= alpha; _Pragma("unroll") for (int d = 0; d < 4; ++d) o[d] *= alpha; \
      } \
      SCHED(); \
      _Pragma("unroll") for (int kb = 0; kb < 4; ++kb) { \
        _Pragma("unroll") for (int e = 0; e < 2; ++e) { \
          _Pragma("unroll") for (int i = 0; i < 8; ++i) s[kb][8 * e + i] = __builtin_amdgcn_exp2f(s[kb][8 * e + i]); \
          u32x4 w = {pk2(s[kb][8 * e], s[kb][8 * e + 1]), pk2(s[kb][8 * e + 2], s[kb][8 * e + 3]), pk2(s[kb][8 * e + 4], s[kb][8 * e + 5]), pk2(s[kb][8 * e + 6], s[kb][8 * e + 7])}; \
          pf[2 * kb + e] = __builtin_bit_cast(bf16x8, w); } \
        asm volatile("" : "+v"(pf[2 * kb]), "+v"(pf[2 * kb + 1])); \
        SCHED(); } } while (0)
  {
    const lptr k0 = kr[0] + SLOT_E, k1 = kr[1] + SLOT_E, v0 = vr;
    {
      bf16x8 kf[8];
#pragma unroll
      for (int st = 0; st < 8; ++st) kf[st] = FRAG(32 + 4 * st);
      PREF(32);
#pragma unroll
      for (int st = 0; st < 8; ++st) negm = MFMA32(kf[st], qf[st], negm);
      float m0 = MX3(negm[0], negm[1], negm[2]), m1 = MX3(negm[3], negm[4], negm[5]);
      m0 = MX3(m0, negm[6], negm[7]); m1 = MX3(m1, negm[8], negm[9]); m0 = MX3(m0, negm[10], negm[11]); m1 = MX3(m1, negm[12], negm[13]);
      m0 = __builtin_fmaxf(MX3(m0, negm[14], negm[15]), m1);
      auto rr = __builtin_amdgcn_permlane32_swap(F2U(m0), F2U(m0), false, false);
      m0 = -__builtin_fmaxf(U2F(rr[0]), U2F(rr[1]));
#pragma unroll
      for (int i = 0; i < 16; ++i) negm[i] = m0;
      asm volatile("" : "+v"(negm));
      SCHED();
    }
    XSEG(32, 64, WAIT_V(4));
  }
  STAMP(7);
  const char* kvsrc = (wr ? kb_g + 65536 : vb_g + 32768) + wc * 8192 - 1024;
  unsigned kvo[2][2];
#pragma unroll
  for (int par = 0; par < 2; ++par) {
    const int xx = wr ? ((lane & 3) ^ ((2 * par + ((lane >> 4) & 1)) & 3)) : (lane & 3);
    kvo[par][0] = (unsigned)(((lane >> 2) & 7) * 256 + (lane >> 5) * 64 + xx * 16 + 1024);
    kvo[par][1] = kvo[par][0] + 128 - 1024;
  }
  const unsigned pfo = (unsigned)((lane < 21 ? lane : 20) * 128);
  unsigned pft = 0;
#pragma unroll 1
  for (int c = 0; c < 3; ++c) {
    if (c == 0) STAMP(8); else if (c == 1) STAMP(12);
    if (c == 1 && wid == 4 && lane == 0) __builtin_amdgcn_raw_buffer_store_b32(tag, wrs, 0, WS_HFLAG + (rb * 32 + hd) * 128, 16);
    if (c + wr < 3) {
      const int slot = wr ? (c == 0 ? SLOT_B : SLOT_C) : (c == 0 ? SLOT_D : (c == 1 ? SLOT_E : SLOT_A));
#pragma unroll
      for (int hf = 0; hf < 2; ++hf)
        asm volatile("s_mov_b32 m0, %5\n\ts_nop 0\n\t"
                     "global_load_lds_dwordx4 %0, %4\n\tglobal_load_lds_dwordx4 %1, %4 offset:1024\n\t"
                     "global_load_lds_dwordx4 %2, %4 offset:2048\n\tglobal_load_lds_dwordx4 %3, %4 offset:3072"
                     ::"v"(kvo[0][0]), "v"(kvo[0][1]), "v"(kvo[1][0]), "v"(kvo[1][1]), "s"(kvsrc + c * 32768 + hf * 4096), "s"(lds0 + slot + wc * 8192 + hf * 4096) : "memory");
    }
    {
      const int sl = (bid * 8 + wid) * 3 + c;
      const int o1 = sl * 2688, o2 = (sl - 3121) * 2688;
      const char* pp = sl < 3121 ? wo + (o1 < 8385920 ? o1 : 8385920) : w2 + (o2 < 7337344 ? o2 : 7337344);
      asm volatile("global_load_dword %0, %1, %2" : "+v"(pft) : "v"(pfo), "s"(pp) : "memory");
    }
    SOFTMAX();
    const int kslot = (c == 0) ? SLOT_A : ((c == 1) ? SLOT_B : SLOT_C);
    const int vslot = (c == 0) ? SLOT_C : ((c == 1) ? SLOT_D : SLOT_E);
    const lptr k0 = kr[0] + kslot, k1 = kr[1] + kslot, v0 = vr + vslot;
    PREF(0);
    if (c == 0) STAMP(9); else if (c == 1) STAMP(13);
    BAR(); SCHED();
    if (c == 0) STAMP(10); else if (c == 1) STAMP(14);
    XSEG(0, 64, WAIT_V(1); if (c == 0) WAIT_V(0));
    if (c == 0) STAMP(11); else if (c == 2) STAMP(15);
  }
  STAMP(16);
  SOFTMAX();
  {
    const lptr k0 = kr[0], k1 = kr[1], v0 = vr + SLOT_A;
    PREF(0);
    STAMP(17);
    if (wr == 0) BAR();
    SCHED();
    STAMP(18);
    XSEG(0, 32, );
  }
  STAMP(19);
  int lane2 = __builtin_amdgcn_mbcnt_hi(~0u, __builtin_amdgcn_mbcnt_lo(~0u, 0u));
  asm volatile("" : "+v"(lane2));
#define lane lane2
  const int isG = xcd >> 2, j2 = xcd & 3, tm = 8 * (j2 >> 1) + (idx & 7), tn = 4 * (j2 & 1) + (idx >> 3), nt = isG ? 56 : 64;
#define O_OUT() do { \
    const float inv = __builtin_amdgcn_rcpf(lacc[0]); \
    const unsigned oo = (unsigned)((rb * 16 + wr * 8 + wc * 2 + (r32 >> 4)) * 2048 + (hh * 16 + (r32 & 15)) * 16); \
    _Pragma("unroll") for (int d = 0; d < 4; ++d) _Pragma("unroll") for (int e = 0; e < 2; ++e) { \
        u32x4 w = {pk2(o[d][8 * e] * inv, o[d][8 * e + 1] * inv), pk2(o[d][8 * e + 2] * inv, o[d][8 * e + 3] * inv), \
                   pk2(o[d][8 * e + 4] * inv, o[d][8 * e + 5] * inv), pk2(o[d][8 * e + 6] * inv, o[d][8 * e + 7] * inv)}; \
        ST128(w, oo + (d & 1) * 1024 + e * 512, WS_O + (2 * hd + (d >> 1)) * 262144); } } while (0)
#define O_FLAG() do { if (wid == 0 && lane == 0) __builtin_amdgcn_raw_buffer_store_b32(tag, wrs, 0, WS_OFLAG + (rb * 32 + hd) * OFS, 16); } while (0)
#define POLL32(off_, slp) do { for (unsigned spins = 0; spins < 2000000u; ++spins) { \
      if (__builtin_amdgcn_ballot_w64(__builtin_amdgcn_raw_buffer_load_b32(wrs, (off_), 0, 16) != tag) == 0ull) break; \
      __builtin_amdgcn_s_sleep(slp); asm volatile("" ::: "memory"); } \
    asm volatile("" ::: "memory"); } while (0)
  if (wr == 0) {
    const char* ab = ws + (isG ? WS_H : WS_O) + tm * 16384 + wc * 4096;
    const char* wbp = (isG ? w2 : wo) + tn * 256 - 1024 + (wc >> 1) * 65536 + (wc & 1) * (isG ? 32768 : 16384);
    const unsigned voffA = lane * 16;
    unsigned voffW[2][2];
    {
      const int r = lane >> 3, c = lane & 7;
      voffW[0][0] = (unsigned)(r * 2048 + ((c ^ (((r >> 1) & 3) << 1)) << 4) + 1024);
      voffW[0][1] = voffW[0][0] + 128 - 1024;
      voffW[1][0] = voffW[0][0] + (isG ? 16384 : 32768) - 2048;
      voffW[1][1] = voffW[1][0] + 128 - 1024;
    }
#define K2_ISSUE(p_, buf_) do { const int k_ = isG ? ((p_) + 14 * j2 >= 56 ? (p_) + 14 * j2 - 56 : (p_) + 14 * j2) : ((((p_) + 8 * j2) & 31) | (~(p_) & 32)); \
      const unsigned d_ = lds0 + (buf_) * K2_STG + wc * 4096; \
      asm volatile("s_mov_b32 m0, %2\n\ts_nop 0\n\tglobal_load_lds_dwordx4 %0, %1 sc1\n\tglobal_load_lds_dwordx4 %0, %1 offset:1024 sc1\n\t" \
                   "global_load_lds_dwordx4 %0, %1 offset:2048 sc1\n\tglobal_load_lds_dwordx4 %0, %1 offset:3072 sc1" \
                   ::"v"(voffA), "s"(ab + (size_t)k_ * 262144), "s"(d_) : "memory"); \
      asm volatile("s_mov_b32 m0, %5\n\ts_nop 0\n\tglobal_load_lds_dwordx4 %0, %4\n\tglobal_load_lds_dwordx4 %1, %4 offset:1024\n\t" \
                   "global_load_lds_dwordx4 %2, %4 offset:2048\n\tglobal_load_lds_dwordx4 %3, %4 offset:3072" \
                   ::"v"(voffW[0][0]), "v"(voffW[0][1]), "v"(voffW[1][0]), "v"(voffW[1][1]), "s"(wbp + (size_t)k_ * 131072), "s"(d_ + 16384) : "memory"); } while (0)
    const unsigned f1 = isG ? (unsigned)(WS_HFLAG + ((tm >> 1) * 32 + (lane & 31)) * 128) : (unsigned)(WS_OFLAG + ((tm >> 1) * 32 + 16 + (lane & 15)) * OFS);
    const unsigned f2 = (unsigned)(WS_OFLAG + ((tm >> 1) * 32 + (lane & 15)) * OFS);
    O_OUT();
    asm volatile("s_waitcnt vmcnt(0)" :: "v"(pft) : "memory");
    STAMP(20);
    POLL32(f1, 4);
    if (isG) { BAR(); O_FLAG(); }
    K2_ISSUE(0, 3); K2_ISSUE(1, 4);
    STAMP(21);
    WAIT_V(8); BAR();
    K2_ISSUE(2, 0); K2_ISSUE(3, 1);
    STAMP(22);
    int buf = 2;
    unsigned pol = 0;
#pragma unroll 1
    for (int b = 0; b < nt - 4; ++b) {
      WAIT_V(16); BAR();
      if (b == 6 && !isG) O_FLAG();
      if (b == 24) asm volatile("buffer_load_dword %0, %1, %2, 0 offen sc1" : "+v"(pol) : "v"(f2), "s"(wrs) : "memory");
      if (b == 28 && !isG) {
        asm volatile("" : "+v"(pol));
        if (__builtin_amdgcn_ballot_w64(pol != tag) != 0ull) POLL32(f2, 4);
      }
      K2_ISSUE(b + 4, buf);
      buf = (buf == 4) ? 0 : buf + 1;
    }
    const int c8 = lane & 15, r0 = wc * 4 + (lane >> 4);
    const unsigned goff = (unsigned)((tm * 128 + r0) * 2048 + tn * 256 + c8 * 16);
    const lptr cb = lds + (isG ? 0 : 65536) + r0 * 512;
    WAIT_V(16); BAR();
    WAIT_V(8); BAR();
    WAIT_V(0); BAR();
    if (isG) {
      BAR();
      STAMP(23);
      BAR();
#pragma unroll
      for (int i = 0; i < 8; ++i) {
        const f32x4 a0 = *(const LAS f32x4*)(cb + i * 8192 + (((2 * c8) ^ (r0 & 7)) << 4)), a1 = *(const LAS f32x4*)(cb + i * 8192 + (((2 * c8 + 1) ^ (r0 & 7)) << 4));
        ST128((u32x4){pk2(silu(a0[0]), silu(a0[1])), pk2(silu(a0[2]), silu(a0[3])), pk2(silu(a1[0]), silu(a1[1])), pk2(silu(a1[2]), silu(a1[3]))}, goff + i * 32768, WS_G);
      }
    } else {
      u32x4 xv[8], gv[8];
      const unsigned gf = __builtin_amdgcn_raw_buffer_load_b32(wrs, 0, WS_GFLAG + (tm * 8 + tn) * 128, 16);
#pragma unroll
      for (int i = 0; i < 8; ++i) xv[i] = *(const u32x4*)(xn + goff + i * 32768);
      BAR();
      if (__builtin_amdgcn_readfirstlane(gf) != tag) POLL32((unsigned)(WS_GFLAG + (tm * 8 + tn) * 128), 1);
      asm volatile("" ::: "memory");
#pragma unroll
      for (int i = 0; i < 8; ++i) gv[i] = __builtin_amdgcn_raw_buffer_load_b128(wrs, goff + i * 32768, WS_G, 16);
      float xf[8][8], gf2[8][8];
#pragma unroll
      for (int i = 0; i < 8; ++i) {
#pragma unroll
        for (int e = 0; e < 4; ++e) { xf[i][2 * e] = bf2f(xv[i][e] & 0xffffu); xf[i][2 * e + 1] = U2F(xv[i][e] & 0xffff0000u); }
        asm volatile("" : "+v"(xf[i][0]), "+v"(xf[i][1]), "+v"(xf[i][2]), "+v"(xf[i][3]), "+v"(xf[i][4]), "+v"(xf[i][5]), "+v"(xf[i][6]), "+v"(xf[i][7]));
      }
#pragma unroll
      for (int i = 0; i < 8; ++i) {
#pragma unroll
        for (int e = 0; e < 4; ++e) { gf2[i][2 * e] = bf2f(gv[i][e] & 0xffffu); gf2[i][2 * e + 1] = U2F(gv[i][e] & 0xffff0000u); }
        asm volatile("" : "+v"(gf2[i][0]), "+v"(gf2[i][1]), "+v"(gf2[i][2]), "+v"(gf2[i][3]), "+v"(gf2[i][4]), "+v"(gf2[i][5]), "+v"(gf2[i][6]), "+v"(gf2[i][7]));
      }
      STAMP(23);
      BAR();
      f32x4 a0[8], a1[8];
#pragma unroll
      for (int i = 0; i < 8; ++i) { a0[i] = *(const LAS f32x4*)(cb + i * 8192 + (((2 * c8) ^ (r0 & 7)) << 4)); a1[i] = *(const LAS f32x4*)(cb + i * 8192 + (((2 * c8 + 1) ^ (r0 & 7)) << 4)); }
      SCHED();
#pragma unroll
      for (int i = 0; i < 8; ++i) {
        const u32x4 w = {pk2(xf[i][0] + a0[i][0] * gf2[i][0], xf[i][1] + a0[i][1] * gf2[i][1]), pk2(xf[i][2] + a0[i][2] * gf2[i][2], xf[i][3] + a0[i][3] * gf2[i][3]),
                         pk2(xf[i][4] + a1[i][0] * gf2[i][4], xf[i][5] + a1[i][1] * gf2[i][5]), pk2(xf[i][6] + a1[i][2] * gf2[i][6], xf[i][7] + a1[i][3] * gf2[i][7])};
        __builtin_amdgcn_raw_buffer_store_b128(w, __builtin_amdgcn_make_buffer_rsrc(out, 0, 4194304, 0x00020000), goff + i * 32768, 0, 16);
      }
    }
    if (isG) {
      WAIT_V(0); BAR();
      if (wid == 0 && lane == 0) __builtin_amdgcn_raw_buffer_store_b32(tag, wrs, 0, WS_GFLAG + (tm * 8 + tn) * 128, 16);
      if (bid == 4 && wid == 0) {
        for (unsigned spins = 0; spins < 2000000u; ++spins) {
          bool ok = true;
#pragma unroll
          for (int i = 0; i < 4; ++i) ok = ok && __builtin_amdgcn_raw_buffer_load_b32(wrs, (unsigned)((lane + i * 64) * 128), WS_HFLAG, 16) == tag;
          if (__builtin_amdgcn_ballot_w64(!ok) == 0ull) break;
          __builtin_amdgcn_s_sleep(4);
          asm volatile("" ::: "memory");
        }
        asm volatile("" ::: "memory");
        if (lane == 0) __builtin_amdgcn_raw_buffer_store_b32(tag, wrs, 0, 0, 16);
      }
    }
  } else {
    asm volatile("s_waitcnt vmcnt(0)" :: "v"(pft) : "memory");
    O_OUT();
    if (isG) { WAIT_V(0); BAR(); }
    STAMP(20);
    STAMP(21);
    const int wm = wc >> 1, wn = wc & 1, g = lane >> 4, t = lane & 15, q4 = (lane >> 2) & 3, p4 = lane & 3;
    lptr w2b[4];
#pragma unroll
    for (int ft = 0; ft < 4; ++ft) w2b[ft] = lds + 3 * K2_STG + 16384 + wn * 1024 + (g >> 1) * 2048 + (4 * (g & 1) + q4) * 128 + 32 * (ft ^ (2 * (g & 1) + (q4 >> 1))) + 8 * p4;
    lptr ar = lds + 3 * K2_STG + wm * 8192 + lane * 16;
    f32x4 ac2[4][4];
#pragma unroll
    for (int m = 0; m < 4; ++m)
#pragma unroll
      for (int n = 0; n < 4; ++n) ac2[m][n] = (f32x4){0.f, 0.f, 0.f, 0.f};
    bf16x8 Af[2][4];
    int dlt = 0;
    bf16x4 Wh[2][4][2];
#define K2_RD(set, ks, i) do { if ((i) == 0) Af[set][0] = ldb128(ar + (ks) * 1024); else if ((i) >= 3 && (i) < 6) Af[set][(i) - 2] = ldb128(ar + ((i) - 2) * 2048 + (ks) * 1024); \
      else { const int f_ = (i) < 3 ? 0 : ((i) - 4) >> 1, h_ = (i) < 3 ? (i) - 1 : (i) & 1; \
             Wh[set][f_][h_] = __builtin_amdgcn_ds_read_tr16_b64_v4bf16((LAS bf16x4*)(w2b[f_] + (ks) * 8192 + h_ * 4096)); } } while (0)
#define K2_W(set, m) __builtin_shufflevector(Wh[set][m][0], Wh[set][m][1], 0, 1, 2, 3, 4, 5, 6, 7)
    BAR();
    STAMP(22);
#pragma unroll
    for (int i = 0; i < 12; ++i) { K2_RD(0, 0, i); SCHED(); }
    int buf = 3;
#define K2_TILE() do { SCHED(); \
      _Pragma("unroll") for (int i = 0; i < 16; ++i) {         \
        ac2[i >> 2][i & 3] = MFMA16(K2_W(0, i >> 2), Af[0][i & 3], ac2[i >> 2][i & 3]); \
        if (i < 12) K2_RD(1, 1, i); \
        if (i == 12) { dlt = (buf == 4) ? -4 * K2_STG : K2_STG; buf = (buf == 4) ? 0 : buf + 1; ar += dlt; asm volatile("" : "+v"(ar)); } \
        if (i == 13) { w2b[0] += dlt; w2b[1] += dlt; asm volatile("" : "+v"(w2b[0]), "+v"(w2b[1])); } \
        if (i == 14) { w2b[2] += dlt; w2b[3] += dlt; asm volatile("" : "+v"(w2b[2]), "+v"(w2b[3])); } \
        if (i == 15) BAR();                  \
        SCHED(); } \
      _Pragma("unroll") for (int i = 0; i < 16; ++i) {         \
        ac2[i >> 2][i & 3] = MFMA16(K2_W(1, i >> 2), Af[1][i & 3], ac2[i >> 2][i & 3]); \
        if (i < 12) K2_RD(0, 0, i); \
        SCHED(); } } while (0)
#pragma unroll 1
    for (int kt = 0; kt < 6; ++kt) K2_TILE();
    WAIT_V(0);
#pragma unroll 1
    for (int kt = 6; kt < nt; ++kt) K2_TILE();
    STAMP(23);
    const lptr cw = lds + (isG ? 0 : 65536) + (wm * 64 + t) * 512;
#pragma unroll
    for (int m = 0; m < 4; ++m)
#pragma unroll
      for (int n = 0; n < 4; ++n) *(LAS f32x4*)(cw + n * 8192 + (((wn * 16 + m * 4 + g) ^ (t & 7)) << 4)) = ac2[m][n];
    WAIT_L(0); BAR();
    if (isG) BAR();
  }
  STAMP(24);
  STAMP_OUT(ws + (57 << 20), 30);
#undef lane
}

__global__ void tiny_first(char* ws) { if (threadIdx.x == 0) __hip_atomic_store((unsigned long long*)(ws + 2048), __builtin_amdgcn_s_memrealtime(), __ATOMIC_RELAXED, __HIP_MEMORY_SCOPE_AGENT); }
__global__ void tiny_last(char* ws) { const unsigned long long t0_ = __hip_atomic_load((unsigned long long*)(ws + 2048), __ATOMIC_RELAXED, __HIP_MEMORY_SCOPE_AGENT);
  for (int i = 0; i < 1000000; ++i) { if ((long long)(__builtin_amdgcn_s_memrealtime() - t0_) >= 12000) break; __builtin_amdgcn_s_sleep(1); } }
extern "C" void kernel_launch(void* const* d_in, const int* in_sizes, int n_in, void* d_out, int out_size, void* d_ws, size_t ws_size, hipStream_t stream) {
  const char* x = (const char*)d_in[0]; const char* kc = (const char*)d_in[1]; const char* vc = (const char*)d_in[2];
  const char* wq = (const char*)d_in[3]; const char* wo = (const char*)d_in[4]; const char* w1 = (const char*)d_in[6]; const char* w2 = (const char*)d_in[7];
  (void)hipFuncSetAttribute((const void*)mega_kernel, hipFuncAttributeMaxDynamicSharedMemorySize, K1_LDS);
  mega_kernel<<<256, 512, K1_LDS, stream>>>(x, kc, vc, wq, w1, wo, w2, (char*)d_ws, (char*)d_out);
}
```

```cpp
#include <hip/hip_runtime.h>
#include <stdint.h>

typedef unsigned short u16;
typedef __bf16 bf16x2 __attribute__((ext_vector_type(2)));
typedef __bf16 bf16x4 __attribute__((ext_vector_type(4)));
typedef __bf16 bf16x8 __attribute__((ext_vector_type(8)));
typedef float f32x2 __attribute__((ext_vector_type(2)));
typedef float f32x4 __attribute__((ext_vector_type(4)));
typedef float f32x16 __attribute__((ext_vector_type(16)));
typedef unsigned u32x2 __attribute__((ext_vector_type(2)));
typedef unsigned u32x4 __attribute__((ext_vector_type(4)));
#define LAS __attribute__((address_space(3)))
typedef LAS char* lptr;
#if defined(__HIP_DEVICE_COMPILE__) && !defined(USEPK)
#define NOPK __attribute__((target("no-packed-fp32-ops")))
#else
#define NOPK
#endif
#define DI NOPK __device__ __forceinline__
#define F2U(x) __builtin_bit_cast(unsigned, (float)(x))
#define U2F(x) __builtin_bit_cast(float, (unsigned)(x))
#define TID ((int)__builtin_amdgcn_workitem_id_x())
#define BID ((int)__builtin_amdgcn_workgroup_id_x())
#define SCHED() __builtin_amdgcn_sched_barrier(0)
#define BAR() asm volatile("s_barrier" ::: "memory")
#define WAIT_V(n) asm volatile("s_waitcnt vmcnt(%0)" :: "n"(n) : "memory")
#define WAIT_L(n) do { asm volatile("" ::: "memory"); __builtin_amdgcn_s_waitcnt(0xC07F | ((n) << 8)); asm volatile("" ::: "memory"); } while (0)
#define MFMA16(a, b, c) __builtin_amdgcn_mfma_f32_16x16x32_bf16(a, b, c, 0, 0, 0)
#define MFMA32(a, b, c) __builtin_amdgcn_mfma_f32_32x32x16_bf16(a, b, c, 0, 0, 0)

DI void glds(unsigned voff, const char* sbase, unsigned ldsaddr) {
  asm volatile("s_mov_b32 m0, %2\n\ts_nop 0\n\tglobal_load_lds_dwordx4 %0, %1" ::"v"(voff), "s"(sbase), "s"(ldsaddr) : "memory");
}
DI void glds1(unsigned voff, const char* sbase, unsigned ldsaddr) {
  asm volatile("s_mov_b32 m0, %2\n\ts_nop 0\n\tglobal_load_lds_dwordx4 %0, %1 sc1" ::"v"(voff), "s"(sbase), "s"(ldsaddr) : "memory");
}
DI unsigned pk2(float lo, float hi) { f32x2 v = {lo, hi}; return __builtin_bit_cast(unsigned, __builtin_convertvector(v, bf16x2)); }
DI float bf2f(unsigned b) { return U2F(b << 16); }
DI bf16x8 ldb128(lptr p) { return *(const LAS bf16x8*)p; }
DI bf16x8 ldtr2(lptr p, int d) {
  bf16x4 a = __builtin_amdgcn_ds_read_tr16_b64_v4bf16((LAS bf16x4*)p);
  bf16x4 b = __builtin_amdgcn_ds_read_tr16_b64_v4bf16((LAS bf16x4*)(p + d));
  return __builtin_shufflevector(a, b, 0, 1, 2, 3, 4, 5, 6, 7);
}
DI void ST128_(__amdgpu_buffer_rsrc_t r, u32x4 v, unsigned voff, int soff) { __builtin_amdgcn_raw_buffer_store_b128(v, r, voff + (unsigned)soff, 0, 16); }
DI float silu(float v) { return v * __builtin_amdgcn_rcpf(1.0f + __builtin_amdgcn_exp2f(v * -1.4426950408889634f)); }

#define STAMP_DECL
#define LSTAMP(i)
#define STAMP(i)
#define STAMP_OUT(buf, n)
template <int CTRL> DI float dpp_add(float v) { return v + U2F(__builtin_amdgcn_update_dpp(0u, F2U(v), CTRL, 0xf, 0xf, true)); }
DI float half_sum(float v) {
  v = dpp_add<0xB1>(v); v = dpp_add<0x4E>(v); v = dpp_add<0x141>(v); v = dpp_add<0x140>(v);
  auto rr = __builtin_amdgcn_permlane16_swap(F2U(v), F2U(v), false, false);
  return U2F(rr[0]) + U2F(rr[1]);
}
typedef __attribute__((address_space(1))) unsigned long long gu64;

#define SX(b, h) (98304 + (b) * 32768 + (h) * 16384)
constexpr int SLOT_B = 0, SLOT_E = 32768, SLOT_A = 65536, SLOT_C = 98304, SLOT_D = 131072, K1_LDS = 163840;

#define XSWAP 0
constexpr int WS_HFLAG = 4096;
constexpr int WS_GFLAG = 65536;
constexpr int WS_OFLAG = 131072;
#define OFS 16
constexpr int WS_STATS = 1 << 20;
constexpr int WS_PFLAG = 196608;
constexpr int WS_G = 5 << 20, WS_O = 9 << 20, WS_H = 25 << 20, WS_P = 39 << 20, WS_END = 47 << 20;
constexpr int K2_STG = 24576, K2_C = 98304;

NOPK __global__ __launch_bounds__(512, 2) void mega_kernel(const char* __restrict__ xn, const char* __restrict__ kc, const char* __restrict__ vc,
                                                           const char* __restrict__ wq, const char* __restrict__ w1, const char* __restrict__ wo, const char* __restrict__ w2,
                                                           char* ws, char* __restrict__ out) {
  STAMP_DECL
  STAMP(0);
  extern __shared__ __attribute__((aligned(16))) char shm[];
  const lptr lds = (lptr)shm;
  const unsigned lds0 = (unsigned)(uintptr_t)lds;
  const int tid = TID, wid = __builtin_amdgcn_readfirstlane(tid >> 6), lane = tid & 63, wr = wid >> 2, wc = wid & 3;
  const int bid = BID ^ XSWAP, xcd = bid & 7, idx = bid >> 3, hd = (idx & 2) * 8 + 2 * xcd + (idx & 1), rb = idx >> 2;
  const int g = lane >> 4, t = lane & 15, q4 = (lane >> 2) & 3, p4 = lane & 3;

  const bool light = (idx & 2) != 0;
  unsigned voffX[4], voffQ[2], voffH[2];
  {
    const int r = lane >> 3, c = lane & 7;
#pragma unroll
    for (int i = 0; i < 4; ++i) voffX[i] = (unsigned)((8 * i + r) * 2048 + ((c ^ (4 * (i & 1) + (r >> 1))) << 4) - i * 1024);
    const int k = 32 * (wid >> 2) + 16 * (wid & 1) + 4 * ((wid >> 1) & 1) + 8 * (r >> 2) + (r & 3);
    const int cw = c ^ (((r >> 1) & 3) << 1);
    voffQ[0] = (unsigned)(k * 8192 + cw * 16 + 1024); voffQ[1] = voffQ[0] + 128 - 1024;
    voffH[0] = (unsigned)(k * 7168 + 128 + (light ? (cw & 3) : cw) * 16 + 1024);
    voffH[1] = (unsigned)(k * 7168 + cw * 16);
  }
  const char* xb = xn + (size_t)rb * 256 * 2048 + wid * 65536;
  const char* qb = wq + hd * 256 - 1024;
  const char* hb = w1 + (light ? 4096 + 192 * (hd - 16) : 256 * hd) - 1024;
  const bool full = wr == 1 || !light;
#define GLDS2(v0_, v1_, sb_, ld_) asm volatile("s_mov_b32 m0, %3\n\ts_nop 0\n\tglobal_load_lds_dwordx4 %0, %2\n\tglobal_load_lds_dwordx4 %1, %2 offset:1024" \
    ::"v"(v0_), "v"(v1_), "s"(sb_), "s"(ld_) : "memory")
#define GLDS4(v0_, v1_, v2_, v3_, sb_, ld_) asm volatile("s_mov_b32 m0, %5\n\ts_nop 0\n\tglobal_load_lds_dwordx4 %0, %4\n\tglobal_load_lds_dwordx4 %1, %4 offset:1024\n\t" \
    "global_load_lds_dwordx4 %2, %4 offset:2048\n\tglobal_load_lds_dwordx4 %3, %4 offset:3072" ::"v"(v0_), "v"(v1_), "v"(v2_), "v"(v3_), "s"(sb_), "s"(ld_) : "memory")
#define ST_X(b, kt) GLDS4(voffX[0], voffX[1], voffX[2], voffX[3], xb + (kt) * 128, lds0 + SX(b, 0) + wid * 4096)
#define ST_W0(wo_, kt) GLDS2(voffQ[0], voffQ[1], qb + (size_t)(kt) * 524288, lds0 + (wo_) + wid * 2048)
#define ST_W1(wo_, kt) GLDS2(voffH[0], voffH[1], hb + (size_t)(kt) * 458752, lds0 + (wo_) + 16384 + wid * 2048)

  lptr wb[4], xr[2];
#pragma unroll
  for (int m = 0; m < 4; ++m) wb[m] = lds + wr * 1024 + (g >> 1) * 2048 + (4 * (g & 1) + q4) * 128 + 32 * ((m ^ wc) ^ (2 * (g & 1) + (q4 >> 1))) + 8 * p4;
#pragma unroll
  for (int ks = 0; ks < 2; ++ks) xr[ks] = lds + 98304 + (wc * 32 + t) * 128 + 16 * ((4 * ks + g) ^ (t >> 1));
#define LDW(dst, h) do { _Pragma("unroll") for (int m = 0; m < 4; ++m) _Pragma("unroll") for (int k = 0; k < 2; ++k) dst[m][k] = ldtr2(wb[m] + (h) * 16384 + k * 8192, 4096); } while (0)
#define LDX(dst, b, h) do { _Pragma("unroll") for (int n = 0; n < 2; ++n) _Pragma("unroll") for (int k = 0; k < 2; ++k) dst[n][k] = ldb128(xr[k] + (SX(b, h) - 98304) + n * 2048); } while (0)

  f32x4 acc[2][2][4][2];
#pragma unroll
  for (int a = 0; a < 2; ++a)
#pragma unroll
    for (int b = 0; b < 2; ++b)
#pragma unroll
      for (int m = 0; m < 4; ++m)
#pragma unroll
        for (int n = 0; n < 2; ++n) acc[a][b][m][n] = (f32x4){0.f, 0.f, 0.f, 0.f};
  bf16x8 At[4][2], B0[2][2], B1[2][2];

#define MM4(ai, m, k) do { _Pragma("unroll") for (int n = 0; n < 2; ++n) { \
      acc[ai][0][m][n] = MFMA16(At[m][k], B0[n][k], acc[ai][0][m][n]); acc[ai][1][m][n] = MFMA16(At[m][k], B1[n][k], acc[ai][1][m][n]); } } while (0)
#define MMA2(ai, CLOSE) do { __builtin_amdgcn_s_setprio(1); \
    MM4(ai, 0, 0); cs[ai] = MFMA16(At[0][0], ones, cs[ai]); MM4(ai, 1, 0); MM4(ai, 0, 1); cs[ai] = MFMA16(At[0][1], ones, cs[ai]); MM4(ai, 1, 1); \
    asm volatile("" : "+v"(cs[ai])); \
    if ((ai) == 0 || full) { MM4(ai, 2, 0); MM4(ai, 3, 0); MM4(ai, 2, 1); MM4(ai, 3, 1); } \
    SCHED(); CLOSE; __builtin_amdgcn_s_setprio(0); SCHED(); } while (0)
  char* const stats = ws + WS_STATS;
  const __amdgpu_buffer_rsrc_t wrs = __builtin_amdgcn_make_buffer_rsrc(ws, 0, WS_END, 0x00020000);
  f32x4 cs[2] = {{0.f, 0.f, 0.f, 0.f}, {0.f, 0.f, 0.f, 0.f}};
  bf16x8 ones = __builtin_bit_cast(bf16x8, (u32x4){0x3f803f80u, 0x3f803f80u, 0x3f803f80u, 0x3f803f80u});
#define PIN_SETUP() do { asm volatile("" : "+v"(ones), "+v"(cs[0]), "+v"(cs[1]), "+v"(wb[0]), "+v"(wb[1]), "+v"(wb[2]), "+v"(wb[3]), "+v"(xr[0]), "+v"(xr[1])); \
    _Pragma("unroll") for (int a = 0; a < 2; ++a) _Pragma("unroll") for (int b = 0; b < 2; ++b) \
      asm volatile("" : "+v"(acc[a][b][0][0]), "+v"(acc[a][b][0][1]), "+v"(acc[a][b][1][0]), "+v"(acc[a][b][1][1]), "+v"(acc[a][b][2][0]), "+v"(acc[a][b][2][1]), "+v"(acc[a][b][3][0]), "+v"(acc[a][b][3][1])); } while (0)
  unsigned tag;
  {
    if (wr == 0) {
      unsigned ep;
      ST_X(0, 0); ST_W0(0, 0);
      asm volatile("global_load_dword %0, %1, %2 sc1" : "=&v"(ep) : "v"(0), "s"(ws) : "memory");
      ST_W1(0, 0); ST_W0(32768, 1); ST_X(1, 1);
      PIN_SETUP();
      asm volatile("s_waitcnt vmcnt(8)" : "+v"(ep) :: "memory");
      tag = __builtin_amdgcn_readfirstlane(ep) + 1u;
      BAR();
    } else {
      const int row = rb * 256 + hd * 8 + wc * 2 + (lane >> 5);
      u32x4 a[4];
      unsigned ep;
      asm volatile("global_load_dword %0, %1, %2 sc1" : "=&v"(ep) : "v"(0), "s"(ws) : "memory");
      asm volatile("global_load_dwordx4 %0, %4, %5\n\tglobal_load_dwordx4 %1, %4, %5 offset:512\n\tglobal_load_dwordx4 %2, %4, %5 offset:1024\n\tglobal_load_dwordx4 %3, %4, %5 offset:1536"
                   : "=&v"(a[0]), "=&v"(a[1]), "=&v"(a[2]), "=&v"(a[3]) : "v"(row * 2048 + (lane & 31) * 16), "s"(xn) : "memory");
      ST_X(0, 0); ST_W0(0, 0);
      ST_W1(0, 0); ST_W0(32768, 1); ST_X(1, 1);
      PIN_SETUP();
      asm volatile("s_waitcnt vmcnt(8)" : "+v"(ep), "+v"(a[0]), "+v"(a[1]), "+v"(a[2]), "+v"(a[3]) :: "memory");
      BAR();
      tag = __builtin_amdgcn_readfirstlane(ep) + 1u;
      float v[32];
#pragma unroll
      for (int i = 0; i < 16; ++i) { v[2 * i] = bf2f(a[i >> 2][i & 3] & 0xffffu); v[2 * i + 1] = U2F(a[i >> 2][i & 3] & 0xffff0000u); }
      float s0 = 0.f, s1 = 0.f, s2 = 0.f, s3 = 0.f;
#pragma unroll
      for (int i = 0; i < 8; ++i) { s0 += v[i]; s1 += v[8 + i]; s2 += v[16 + i]; s3 += v[24 + i]; }
      const float mean = half_sum((s0 + s1) + (s2 + s3)) * (1.0f / 1024.0f);
      s0 = s1 = s2 = s3 = 0.f;
#pragma unroll
      for (int i = 0; i < 8; ++i) { const float d0 = v[i] - mean, d1 = v[8 + i] - mean, d2 = v[16 + i] - mean, d3 = v[24 + i] - mean; s0 += d0 * d0; s1 += d1 * d1; s2 += d2 * d2; s3 += d3 * d3; }
      const float sd = __builtin_amdgcn_sqrtf(half_sum((s0 + s1) + (s2 + s3)) * (1.0f / 1024.0f) + 1e-5f);
      if ((lane & 31) < 2) __hip_atomic_store((gu64*)(stats + row * 16 + (lane & 1) * 8), ((unsigned long long)tag << 32) | F2U((lane & 1) ? sd : mean), __ATOMIC_RELAXED, __HIP_MEMORY_SCOPE_AGENT);
    }
  }
  if (wr == 1) BAR();
  SCHED();
  STAMP(1);
  const int kvg = hd >> 3;
  const char* kb_g = kc + ((size_t)kvg * 2048 + 1536) * 256;
  const char* vb_g = vc + ((size_t)kvg * 2048 + 1536) * 256;
#define VOFFK ((unsigned)(((lane >> 2) & 7) * 256 + (4 * (lane >> 5) + ((lane & 3) ^ ((2 * (wid & 1) + ((lane >> 4) & 1)) & 3))) * 16))
#define VOFFV ((unsigned)(((lane >> 2) & 7) * 256 + (lane >> 5) * 64 + (lane & 3) * 16))
#define ST_KV8(voff, gsrc, slot) do { glds(voff, (gsrc) + wid * 2048, lds0 + (slot) + wid * 2048); glds(voff, (gsrc) + wid * 2048 + 128, lds0 + (slot) + wid * 2048 + 1024); } while (0)
  int wprv = 65536, wnxt = 32768;
#define W_ADV() do { const int d_ = (wnxt == 0) ? -65536 : 32768, cur_ = wnxt - d_; wnxt = wprv; wprv = cur_; \
    _Pragma("unroll") for (int f = 0; f < 4; ++f) wb[f] += d_; \
    asm volatile("" : "+v"(wb[0]), "+v"(wb[1]), "+v"(wb[2]), "+v"(wb[3])); } while (0)
#define PH_A(b, STAGE) do { LDX(B0, b, 0); LDX(B1, b, 1); SCHED(); LDW(At, 0); STAGE; WAIT_V(10); WAIT_L(12); BAR(); SCHED(); MMA2(0, WAIT_L(0); BAR()); } while (0)
#define PH_B(STAGE) do { LDW(At, 1); STAGE; W_ADV(); WAIT_V(8); BAR(); SCHED(); MMA2(1, WAIT_L(0); BAR()); } while (0)
#pragma unroll 1
  for (int kt = 0; kt < 14; kt += 2) {
    PH_A(0, ST_W1(wnxt, kt + 1); ST_W0(wprv, kt + 2));
    PH_B(ST_X(0, kt + 2));
    PH_A(1, ST_W1(wnxt, kt + 2); ST_W0(wprv, kt + 3));
    PH_B(ST_X(1, kt + 3));
  }
  STAMP(2);
  const unsigned voffK = VOFFK, voffV = VOFFV;
  PH_A(0, ST_W1(0, 15); ST_KV8(voffK, kb_g, SLOT_E));
  PH_B(ST_KV8(voffV, vb_g, SLOT_C); ST_KV8(voffV, vb_g + 16384, SLOT_C + 16384));
  const unsigned soff = (unsigned)((rb * 256 + (g >> 1) * 128 + wc * 32 + (g & 1) * 16 + t) * 16);
  u32x4 sg;
  LDX(B0, 1, 0); LDX(B1, 1, 1); SCHED(); LDW(At, 0);
  asm volatile("global_load_dwordx4 %0, %1, %2 sc1" : "=&v"(sg) : "v"(soff), "s"(stats) : "memory");
  ST_KV8(voffK, kb_g + 16384, SLOT_E + 16384); ST_KV8(voffK, kb_g + 32768, SLOT_A);
  WAIT_V(11); BAR(); SCHED(); MMA2(0, WAIT_L(0); BAR());
  LDW(At, 1);
  ST_KV8(voffK, kb_g + 49152, SLOT_A + 16384);
  BAR(); SCHED();
  const lptr csw = lds + SLOT_D + (t & 3) * 8192 + wr * 4096 + wc * 64 + g * 16;
  MMA2(1, *(LAS f32x4*)csw = cs[0]; *(LAS f32x4*)(csw + 256) = cs[1]; asm volatile("s_waitcnt vmcnt(4)" : "+v"(sg) :: "memory"); WAIT_L(0); BAR());
  f32x4 cv[2][4];
#pragma unroll
  for (int a = 0; a < 2; ++a)
#pragma unroll
    for (int m = 0; m < 4; ++m) cv[a][m] = *(const LAS f32x4*)(lds + SLOT_D + wc * 8192 + wr * 4096 + a * 256 + (m ^ wc) * 64 + g * 16);
  if (__builtin_expect(__builtin_amdgcn_ballot_w64(sg[1] != tag || sg[3] != tag) != 0ull, 0)) {
    for (unsigned spins = 0; spins < 4000000u; ++spins) {
      __builtin_amdgcn_s_sleep(2);
      asm volatile("" ::: "memory");
      sg = __builtin_amdgcn_raw_buffer_load_b128(wrs, soff, WS_STATS, 16);
      if (__builtin_amdgcn_ballot_w64(sg[1] != tag || sg[3] != tag) == 0ull) break;
    }
  }
  float mu[2][2], rs[2][2], sd[2][2];
  {
    auto m16 = __builtin_amdgcn_permlane16_swap(sg[0], sg[0], false, false);
    auto s16 = __builtin_amdgcn_permlane16_swap(sg[2], sg[2], false, false);
#pragma unroll
    for (int n = 0; n < 2; ++n) {
      auto m32 = __builtin_amdgcn_permlane32_swap(m16[n], m16[n], false, false);
      auto s32 = __builtin_amdgcn_permlane32_swap(s16[n], s16[n], false, false);
#pragma unroll
      for (int bj = 0; bj < 2; ++bj) { mu[bj][n] = -U2F(m32[bj]); sd[bj][n] = U2F(s32[bj]); rs[bj][n] = __builtin_amdgcn_rcpf(sd[bj][n]) * 1.4426950408889634f; }
    }
  }
  WAIT_L(0);
  STAMP(3);
  {
    const lptr qw = lds + wc * 8192 + wr * 4096 + ((g >> 1) * 32 + t) * 16 + (g & 1) * 8;
#pragma unroll
    for (int bj = 0; bj < 2; ++bj) {
      lptr qwb = qw + (bj ? SLOT_D : SLOT_B);
      asm volatile("" : "+v"(qwb));
#pragma unroll
      for (int m = 0; m < 4; ++m)
#pragma unroll
        for (int n = 0; n < 2; ++n) {
          const f32x4 v = (acc[0][bj][m][n] + cv[0][m] * mu[bj][n]) * (rs[bj][n] * 0.08838834764831845f);
          u32x2 w = {pk2(v[0], v[1]), pk2(v[2], v[3])};
          *(LAS u32x2*)(qwb + (m ^ wc) * 1024 + n * 256) = w;
        }
    }
  }
  WAIT_L(0); BAR(); SCHED();
  STAMP(4);
  bf16x8 qf[8];
  const lptr qr = lds + (wr ? SLOT_D : SLOT_B) + wc * 8192 + lane * 16;
  if (wr == 1) {
#pragma unroll
    for (int s = 0; s < 8; ++s) qf[s] = ldb128(qr + s * 1024);
  }
  {
#define ST128(...) ST128_(wrs, __VA_ARGS__)
    u32x2 hw[2][2][4];
#define SILU16(m) do { \
      const f32x4 a0 = acc[1][0][m][0] + cv[1][m] * mu[0][0], a1 = acc[1][0][m][1] + cv[1][m] * mu[0][1], a2 = acc[1][1][m][0] + cv[1][m] * mu[1][0], a3 = acc[1][1][m][1] + cv[1][m] * mu[1][1]; \
      f32x4 e0 = a0 * -rs[0][0], e1 = a1 * -rs[0][1], e2 = a2 * -rs[1][0], e3 = a3 * -rs[1][1]; SCHED(); \
      if (!light) { \
      _Pragma("unroll") for (int i = 0; i < 4; ++i) { e0[i] = __builtin_amdgcn_exp2f(e0[i]); e1[i] = __builtin_amdgcn_exp2f(e1[i]); e2[i] = __builtin_amdgcn_exp2f(e2[i]); e3[i] = __builtin_amdgcn_exp2f(e3[i]); } SCHED(); \
      e0 = e0 * sd[0][0] + sd[0][0]; e1 = e1 * sd[0][1] + sd[0][1]; e2 = e2 * sd[1][0] + sd[1][0]; e3 = e3 * sd[1][1] + sd[1][1]; SCHED(); \
      _Pragma("unroll") for (int i = 0; i < 4; ++i) { e0[i] = __builtin_amdgcn_rcpf(e0[i]); e1[i] = __builtin_amdgcn_rcpf(e1[i]); e2[i] = __builtin_amdgcn_rcpf(e2[i]); e3[i] = __builtin_amdgcn_rcpf(e3[i]); } SCHED(); \
      e0 *= a0; e1 *= a1; e2 *= a2; e3 *= a3; SCHED(); } \
      hw[0][0][m] = (u32x2){pk2(e0[0], e0[1]), pk2(e0[2], e0[3])}; hw[0][1][m] = (u32x2){pk2(e1[0], e1[1]), pk2(e1[2], e1[3])}; \
      hw[1][0][m] = (u32x2){pk2(e2[0], e2[1]), pk2(e2[2], e2[3])}; hw[1][1][m] = (u32x2){pk2(e3[0], e3[1]), pk2(e3[2], e3[3])}; SCHED(); } while (0)
    SILU16(0);
    if (wr == 0) {
      BAR(); SCHED();
#pragma unroll
      for (int s = 0; s < 8; ++s) qf[s] = ldb128(qr + s * 1024);
    }
    SILU16(1);
    const unsigned hoff = (unsigned)((rb * 16 + wc * 2) * 2048 + lane * 16);
    const int pb = (light ? 16 + 3 * hd : 4 * hd) + (wr ? 0 : 2), sw = full ? (wc >> 1) : 0, p0 = pb + sw, p1 = pb + (sw ^ 1);
#define H_OUT1(p_, a_, b_) do { _Pragma("unroll") for (int bj = 0; bj < 2; ++bj) _Pragma("unroll") for (int n = 0; n < 2; ++n) \
      ST128((u32x4){hw[bj][n][a_][0], hw[bj][n][a_][1], hw[bj][n][b_][0], hw[bj][n][b_][1]}, hoff + n * 2048, WS_H + ((p_) >> 1) * 262144 + ((p_) & 1) * 1024 + bj * 16384); } while (0)
#define H_OUT(p_, m_) do { if (wc & 1) H_OUT1(p_, (m_) + 1, m_); else H_OUT1(p_, m_, (m_) + 1); } while (0)
    H_OUT(p0, 0);
    if (full) { SILU16(2); SILU16(3); H_OUT(p1, 2); }
  }
  STAMP(5);
  if (wr == 1) { WAIT_L(0); BAR(); }
  __builtin_amdgcn_s_setprio(1);
  SCHED();
  STAMP(6);
  const int r32 = lane & 31, hh = lane >> 5;
  lptr kr[2];
#pragma unroll
  for (int e = 0; e < 2; ++e) kr[e] = lds + 2048 * (r32 >> 3) + 64 * (r32 & 7) + 16 * ((2 * e + hh) ^ ((r32 >> 2) & 3));
  const lptr vr = lds + ((lane & 3) << 3) + (((lane >> 2) & 3) << 6) + (((lane >> 4) & 1) << 5) + (hh << 8);
  f32x16 o[4], s[4];
#pragma unroll
  for (int d = 0; d < 4; ++d)
#pragma unroll
    for (int i = 0; i < 16; ++i) o[d][i] = 0.f;
  bf16x8 pf[8];
  constexpr int RD = 6;
  bf16x8 fr[RD];
  f32x4 lacc = {0.f, 0.f, 0.f, 0.f};
  const unsigned selw = (((lane >> 4) ^ (lane >> 2)) & 1) ? 0u : 0x3f803f80u;
  bf16x8 sel = __builtin_bit_cast(bf16x8, (u32x4){selw, selw, selw, selw});
  asm volatile("" : "+v"(sel));
  f32x16 negm = {0.f, 0.f, 0.f, 0.f, 0.f, 0.f, 0.f, 0.f, 0.f, 0.f, 0.f, 0.f, 0.f, 0.f, 0.f, 0.f};
#define SM_THR 60.0f
#define FRAG(i) ((i) < 32 ? ldtr2(v0 + (((i) >> 2) * 8 + ((i) & 3)) * 512, 2048) : ldb128((((((i) - 32) >> 2) & 1) ? k1 : k0) + ((i) & 3) * 8192 + (((i) - 32) >> 3) * 512))
#define XEARLY 3
#define XPRIO 1
#define YPRIO 0
#define XSEG(lo, hi, CLOSE) do { __builtin_amdgcn_s_setprio(XPRIO); \
    _Pragma("unroll") for (int i = (lo); i < (hi); ++i) { \
      if (i < 32) o[i & 3] = MFMA32(fr[i % RD], pf[(i >> 2) & 7], o[i & 3]); \
      else s[i & 3] = MFMA32(fr[i % RD], qf[((i - 32) >> 2) & 7], i < 36 ? negm : s[i & 3]); \
      if (i < 32 && (i & 3) == 1) lacc = MFMA16(sel, pf[(i >> 2) & 7], lacc); \
      if (i + RD < (hi)) fr[i % RD] = FRAG(i + RD); \
      SCHED(); \
      if (i == 63 - XEARLY) { __builtin_amdgcn_s_setprio(2); CLOSE; WAIT_L(0); BAR(); SCHED(); } } \
    __builtin_amdgcn_s_setprio(YPRIO); } while (0)
#define PREF(lo) do { _Pragma("unroll") for (int i = (lo); i < (lo) + RD; ++i) fr[i % RD] = FRAG(i); SCHED(); } while (0)
#define MX3(a, b, c) __builtin_fmaxf(__builtin_fmaxf((a), (b)), (c))
#define SOFTMAX() do { \
      float m0 = MX3(s[0][0], s[0][1], s[0][2]), m1 = MX3(s[1][0], s[1][1], s[1][2]), m2 = MX3(s[2][0], s[2][1], s[2][2]), m3 = MX3(s[3][0], s[3][1], s[3][2]); \
      _Pragma("unroll") for (int i = 3; i < 15; i += 2) { m0 = MX3(m0, s[0][i], s[0][i + 1]); m1 = MX3(m1, s[1][i], s[1][i + 1]); m2 = MX3(m2, s[2][i], s[2][i + 1]); m3 = MX3(m3, s[3][i], s[3][i + 1]); } \
      m0 = MX3(m0, s[0][15], s[1][15]); m2 = MX3(m2, s[2][15], s[3][15]); \
      float mx = __builtin_fmaxf(MX3(m0, m1, m2), m3); \
        \
      if (__builtin_expect(__builtin_amdgcn_ballot_w64(!(mx <= SM_THR)) != 0ull, 0)) { \
        auto rr = __builtin_amdgcn_permlane32_swap(F2U(mx), F2U(mx), false, false); \
        const float dl = MX3(U2F(rr[0]), U2F(rr[1]), 0.f); \
        _Pragma("unroll") for (int kb = 0; kb < 4; ++kb) s[kb] -= dl; \
        negm -= dl; \
        const float alpha = __builtin_amdgcn_exp2f(-dl); lacc *= alpha; _Pragma("unroll") for (int d = 0; d < 4; ++d) o[d] *= alpha; \
      } \
      SCHED(); \
      _Pragma("unroll") for (int kb = 0; kb < 4; ++kb) { \
        _Pragma("unroll") for (int e = 0; e < 2; ++e) { \
          _Pragma("unroll") for (int i = 0; i < 8; ++i) s[kb][8 * e + i] = __builtin_amdgcn_exp2f(s[kb][8 * e + i]); \
          u32x4 w = {pk2(s[kb][8 * e], s[kb][8 * e + 1]), pk2(s[kb][8 * e + 2], s[kb][8 * e + 3]), pk2(s[kb][8 * e + 4], s[kb][8 * e + 5]), pk2(s[kb][8 * e + 6], s[kb][8 * e + 7])}; \
          pf[2 * kb + e] = __builtin_bit_cast(bf16x8, w); } \
        asm volatile("" : "+v"(pf[2 * kb]), "+v"(pf[2 * kb + 1])); \
        SCHED(); } } while (0)
  {
    const lptr k0 = kr[0] + SLOT_E, k1 = kr[1] + SLOT_E, v0 = vr;
    {
      bf16x8 kf[8];
#pragma unroll
      for (int st = 0; st < 8; ++st) kf[st] = FRAG(32 + 4 * st);
      PREF(32);
#pragma unroll
      for (int st = 0; st < 8; ++st) negm = MFMA32(kf[st], qf[st], negm);
      float m0 = MX3(negm[0], negm[1], negm[2]), m1 = MX3(negm[3], negm[4], negm[5]);
      m0 = MX3(m0, negm[6], negm[7]); m1 = MX3(m1, negm[8], negm[9]); m0 = MX3(m0, negm[10], negm[11]); m1 = MX3(m1, negm[12], negm[13]);
      m0 = __builtin_fmaxf(MX3(m0, negm[14], negm[15]), m1);
      auto rr = __builtin_amdgcn_permlane32_swap(F2U(m0), F2U(m0), false, false);
      m0 = -__builtin_fmaxf(U2F(rr[0]), U2F(rr[1]));
#pragma unroll
      for (int i = 0; i < 16; ++i) negm[i] = m0;
      asm volatile("" : "+v"(negm));
      SCHED();
    }
    XSEG(32, 64, WAIT_V(4));
  }
  STAMP(7);
  const char* kvsrc = (wr ? kb_g + 65536 : vb_g + 32768) + wc * 8192 - 1024;
  unsigned kvo[2][2];
#pragma unroll
  for (int par = 0; par < 2; ++par) {
    const int xx = wr ? ((lane & 3) ^ ((2 * par + ((lane >> 4) & 1)) & 3)) : (lane & 3);
    kvo[par][0] = (unsigned)(((lane >> 2) & 7) * 256 + (lane >> 5) * 64 + xx * 16 + 1024);
    kvo[par][1] = kvo[par][0] + 128 - 1024;
  }
  const unsigned pfo = (unsigned)((lane < 21 ? lane : 20) * 128);
  unsigned pft = 0;
#pragma unroll 1
  for (int c = 0; c < 3; ++c) {
    if (c == 0) STAMP(8); else if (c == 1) STAMP(12);
    if (c == 1 && !light && wid == 4 && lane == 0) __builtin_amdgcn_raw_buffer_store_b32(tag, wrs, 0, WS_HFLAG + (rb * 32 + hd) * 128, 16);
    if (c + wr < 3) {
      const int slot = wr ? (c == 0 ? SLOT_B : SLOT_C) : (c == 0 ? SLOT_D : (c == 1 ? SLOT_E : SLOT_A));
#pragma unroll
      for (int hf = 0; hf < 2; ++hf)
        asm volatile("s_mov_b32 m0, %5\n\ts_nop 0\n\t"
                     "global_load_lds_dwordx4 %0, %4\n\tglobal_load_lds_dwordx4 %1, %4 offset:1024\n\t"
                     "global_load_lds_dwordx4 %2, %4 offset:2048\n\tglobal_load_lds_dwordx4 %3, %4 offset:3072"
                     ::"v"(kvo[0][0]), "v"(kvo[0][1]), "v"(kvo[1][0]), "v"(kvo[1][1]), "s"(kvsrc + c * 32768 + hf * 4096), "s"(lds0 + slot + wc * 8192 + hf * 4096) : "memory");
    }
    {
      const int sl = (bid * 8 + wid) * 3 + c;
      const int o1 = sl * 2688, o2 = (sl - 3121) * 2688;
      const char* pp = sl < 3121 ? wo + (o1 < 8385920 ? o1 : 8385920) : w2 + (o2 < 7337344 ? o2 : 7337344);
      asm volatile("global_load_dword %0, %1, %2" : "+v"(pft) : "v"(pfo), "s"(pp) : "memory");
    }
    SOFTMAX();
    const int kslot = (c == 0) ? SLOT_A : ((c == 1) ? SLOT_B : SLOT_C);
    const int vslot = (c == 0) ? SLOT_C : ((c == 1) ? SLOT_D : SLOT_E);
    const lptr k0 = kr[0] + kslot, k1 = kr[1] + kslot, v0 = vr + vslot;
    PREF(0);
    if (c == 0) STAMP(9); else if (c == 1) STAMP(13);
    BAR(); SCHED();
    if (c == 0) STAMP(10); else if (c == 1) STAMP(14);
    XSEG(0, 64, WAIT_V(1); if (c == 0) WAIT_V(0));
    if (c == 0) STAMP(11); else if (c == 2) STAMP(15);
  }
  STAMP(16);
  SOFTMAX();
  {
    const lptr k0 = kr[0], k1 = kr[1], v0 = vr + SLOT_A;
    PREF(0);
    STAMP(17);
    if (wr == 0) BAR();
    SCHED();
    STAMP(18);
    XSEG(0, 32, );
  }
  STAMP(19);
  int lane2 = __builtin_amdgcn_mbcnt_hi(~0u, __builtin_amdgcn_mbcnt_lo(~0u, 0u));
  asm volatile("" : "+v"(lane2));
#define lane lane2
  const int isG = (idx >> 1) & 1, kh = xcd & 1, tm = 4 * (xcd >> 1) + ((idx >> 1) & 2) + (idx & 1), tn = 2 * (idx >> 3) + kh;
  const int npw = isG ? 48 : 56;
#define O_OUT() do { \
    const float inv = __builtin_amdgcn_rcpf(lacc[0]); \
    const unsigned oo = (unsigned)((rb * 16 + wr * 8 + wc * 2 + (r32 >> 4)) * 2048 + (hh * 16 + (r32 & 15)) * 16); \
    _Pragma("unroll") for (int d = 0; d < 4; ++d) _Pragma("unroll") for (int e = 0; e < 2; ++e) { \
        u32x4 w = {pk2(o[d][8 * e] * inv, o[d][8 * e + 1] * inv), pk2(o[d][8 * e + 2] * inv, o[d][8 * e + 3] * inv), \
                   pk2(o[d][8 * e + 4] * inv, o[d][8 * e + 5] * inv), pk2(o[d][8 * e + 6] * inv, o[d][8 * e + 7] * inv)}; \
        ST128(w, oo + (d & 1) * 1024 + e * 512, WS_O + (2 * hd + (d >> 1)) * 262144); } } while (0)
#define O_FLAG() do { if (wid == 0 && lane == 0) __builtin_amdgcn_raw_buffer_store_b32(tag, wrs, 0, WS_OFLAG + (rb * 32 + hd) * OFS, 16); } while (0)
#define POLL32(off_, slp) do { for (unsigned spins = 0; spins < 2000000u; ++spins) { \
      if (__builtin_amdgcn_ballot_w64(__builtin_amdgcn_raw_buffer_load_b32(wrs, (off_), 0, 16) != tag) == 0ull) break; \
      __builtin_amdgcn_s_sleep(slp); asm volatile("" ::: "memory"); } \
    asm volatile("" ::: "memory"); } while (0)
  const int c8 = lane & 15, r0 = wc * 4 + (lane >> 4);
  const unsigned goff = (unsigned)((tm * 128 + r0) * 2048 + tn * 256 + c8 * 16);
  const lptr cb = lds + K2_C + r0 * 512;
#define G_FIN() do { f32x4 a0[4], a1[4]; \
      _Pragma("unroll") for (int i = 0; i < 4; ++i) { a0[i] = *(const LAS f32x4*)(cb + (wr * 4 + i) * 8192 + (((2 * c8) ^ (r0 & 7)) << 4)); a1[i] = *(const LAS f32x4*)(cb + (wr * 4 + i) * 8192 + (((2 * c8 + 1) ^ (r0 & 7)) << 4)); } \
      _Pragma("unroll") for (int i = 0; i < 4; ++i) \
        ST128((u32x4){pk2(silu(a0[i][0]), silu(a0[i][1])), pk2(silu(a0[i][2]), silu(a0[i][3])), pk2(silu(a1[i][0]), silu(a1[i][1])), pk2(silu(a1[i][2]), silu(a1[i][3]))}, goff + i * 32768, WS_G + wr * 131072); \
      WAIT_V(0); BAR(); } while (0)
  const unsigned f1 = isG ? (unsigned)(WS_HFLAG + ((tm >> 1) * 32 + (lane & 15)) * 128) : (unsigned)(WS_OFLAG + ((tm >> 1) * 32 + 16 + (lane & 15)) * OFS);
  asm volatile("s_waitcnt vmcnt(0)" :: "v"(pft) : "memory");
  O_OUT();
  STAMP(20);
  if (wr == 0) {
    const char* ab = ws + (isG ? WS_H : WS_O) + tm * 16384 + wc * 4096;
    const char* wbp = (isG ? w2 : wo) + (tn >> 1) * 512 - 3072 + (isG ? wc : (((wc & 1) << 1) | (wc >> 1))) * 16384;
    const unsigned voffA0 = lane * 16, voffA1 = lane * 16 + 1024;
    unsigned voffW[4];
    {
      const int r = lane >> 3, c = lane & 7;
      const unsigned lp = (unsigned)(r * 2048 + ((c ^ (((r >> 1) & 3) << 1)) << 4) + 3072);
      voffW[0] = lp + kh * 256; voffW[1] = voffW[0] + 128 - 1024; voffW[2] = lp + (kh ^ 1) * 256 - 2048; voffW[3] = voffW[2] + 128 - 1024;
    }
    const char *ap, *wp;
    unsigned dst = lds0 + 4 * K2_STG;
#define K2_RUN(k_) do { ap = ab + (size_t)(k_) * 262144; wp = wbp + (size_t)(k_) * 131072; } while (0)
#define K2_ISSUE(WIDE, ks) do { \
      asm volatile("s_mov_b32 m0, %3\n\ts_nop 0\n\tglobal_load_lds_dwordx4 %0, %2 sc1\n\tglobal_load_lds_dwordx4 %1, %2 offset:1024 sc1" \
                   ::"v"(voffA0), "v"(voffA1), "s"(ap + (ks) * 1024), "s"(dst + wc * 2048) : "memory"); \
      if (WIDE) GLDS4(voffW[0], voffW[1], voffW[2], voffW[3], wp + (ks) * 65536, dst + 8192 + wc * 4096); \
      else GLDS2(voffW[0], voffW[1], wp + (ks) * 65536, dst + 8192 + wc * 4096); \
      dst = (dst == lds0 + 5 * K2_STG) ? lds0 : dst + K2_STG; \
      if (ks) { ap += 262144; wp += 131072; } } while (0)
#define K2_STEP(WIDE, ks, EVENT) do { WAIT_V((WIDE) ? 18 : 12); BAR(); EVENT; K2_ISSUE(WIDE, ks); } while (0)
#define K2_TILES(WIDE, n_) do { _Pragma("unroll 1") for (int i_ = 0; i_ < (n_); ++i_) { K2_STEP(WIDE, 0, ); K2_STEP(WIDE, 1, ); } } while (0)
#define LOOK(f_) asm volatile("buffer_load_dword %0, %1, %2, 0 offen sc1" : "+v"(pol) : "v"(f_), "s"(wrs) : "memory")
    const unsigned f2 = isG ? f1 + 16 * 128 : f1 - 16 * OFS;
    const unsigned f3 = (unsigned)(WS_PFLAG + (bid ^ 1) * 128);
    u32x4 xh[12];
#define HPO(i) ((unsigned)(WS_H + ((16 + 3 * hd + ((i) >> 2)) >> 1) * 262144 + ((16 + 3 * hd + ((i) >> 2)) & 1) * 1024 + (((i) >> 1) & 1) * 16384 + (rb * 16 + wc * 2 + ((i) & 1)) * 2048) + lane * 16)
#define HP_SILU(i) do { if (isG) { float tv[8], ev[8]; \
      _Pragma("unroll") for (int e = 0; e < 4; ++e) { tv[2 * e] = bf2f(xh[i][e] & 0xffffu); tv[2 * e + 1] = U2F(xh[i][e] & 0xffff0000u); } SCHED(); \
      _Pragma("unroll") for (int e = 0; e < 8; ++e) ev[e] = __builtin_amdgcn_exp2f(tv[e]); SCHED(); \
      _Pragma("unroll") for (int e = 0; e < 8; ++e) ev[e] = ev[e] * -1.4426950408889634f - 1.4426950408889634f; SCHED(); \
      _Pragma("unroll") for (int e = 0; e < 8; ++e) ev[e] = __builtin_amdgcn_rcpf(ev[e]); SCHED(); \
      ST128((u32x4){pk2(ev[0] * tv[0], ev[1] * tv[1]), pk2(ev[2] * tv[2], ev[3] * tv[3]), pk2(ev[4] * tv[4], ev[5] * tv[5]), pk2(ev[6] * tv[6], ev[7] * tv[7])}, HPO(i), 0); SCHED(); } } while (0)
    if (isG) {
#pragma unroll
      for (int i = 0; i < 12; ++i) xh[i] = __builtin_amdgcn_raw_buffer_load_b128(wrs, HPO(i), 0, 16);
    } else {
#pragma unroll
      for (int i = 0; i < 8; ++i) xh[i] = *(const u32x4*)(xn + goff + i * 32768);
    }
    POLL32(f1, 4);
    asm volatile("s_waitcnt vmcnt(0)" : "+v"(xh[0]), "+v"(xh[1]), "+v"(xh[2]), "+v"(xh[3]), "+v"(xh[4]), "+v"(xh[5]), "+v"(xh[6]), "+v"(xh[7]), "+v"(xh[8]), "+v"(xh[9]), "+v"(xh[10]), "+v"(xh[11]) :: "memory");
    if (isG) { BAR(); O_FLAG(); }
    K2_RUN(isG ? 16 * kh : 32 + 14 * kh);
    K2_ISSUE(1, 0); K2_ISSUE(1, 1);
    STAMP(21);
    WAIT_V(6); BAR();
    K2_ISSUE(1, 0); K2_ISSUE(1, 1); K2_ISSUE(1, 0);
    STAMP(22);
    unsigned pol = 0;
    K2_STEP(1, 1, );
#pragma unroll
    for (int i = 0; i < 12; i += 2) {
      K2_STEP(1, 0, if (i == 8 && !isG) O_FLAG()); HP_SILU(i);
      K2_STEP(1, 1, ); HP_SILU(i + 1);
    }
    K2_TILES(1, 1);
    if (isG) WAIT_V(0);
    K2_TILES(1, 1);
    if (isG && wid == 0 && lane == 0) __builtin_amdgcn_raw_buffer_store_b32(tag, wrs, 0, WS_HFLAG + (rb * 32 + hd) * 128, 16);
    K2_TILES(1, isG ? 3 : 1);
    LOOK(f2);
    K2_TILES(1, 2);
    K2_RUN(isG ? 32 + 8 * kh : 14 * kh);
    K2_STEP(1, 0, asm volatile("" : "+v"(pol)); if (__builtin_amdgcn_ballot_w64(pol != tag) != 0ull) POLL32(f2, 4));
    K2_STEP(1, 1, );
    K2_TILES(1, isG ? 7 : 13);
    K2_RUN(isG ? 48 : 60);
    K2_TILES(0, 4);
    if (!isG) K2_RUN(28);
    K2_STEP(0, 0, );
    K2_STEP(0, 1, if (wid == 0 && lane == 0) __builtin_amdgcn_raw_buffer_store_b32(tag, wrs, 0, WS_PFLAG + bid * 128, 16));
    K2_STEP(0, 0, ); K2_STEP(0, 1, );
    const unsigned f4 = (lane & 1) ? (unsigned)(WS_GFLAG + (tm * 8 + tn) * 128) : f3;
    K2_STEP(0, 0, LOOK(f4)); K2_STEP(0, 1, ); K2_STEP(0, 0, );
    WAIT_V(12);
    asm volatile("" : "+v"(pol));
    const unsigned long long miss = __builtin_amdgcn_ballot_w64(pol != tag);
    if (wid == 0 && (miss & 1ull)) POLL32(f3, 2);
    BAR();
    K2_ISSUE(0, 1);
    WAIT_V(12); BAR();
    WAIT_V(8); BAR();
    if (isG) {
      WAIT_V(4); BAR();
      WAIT_V(0); BAR();
      BAR();
      STAMP(23);
      BAR();
    } else {
      u32x4 gv[8];
      const __amdgpu_buffer_rsrc_t ors = __builtin_amdgcn_make_buffer_rsrc(out, 0, 2048 * 2048, 0x00020000);
#define LD8(dst_, rs_, so_, pol_) asm volatile("buffer_load_dwordx4 %0, %8, %10, %11 offen" pol_ "\n\tbuffer_load_dwordx4 %1, %9, %10, %11 offen" pol_ "\n\t" \
        "buffer_load_dwordx4 %2, %8, %10, %12 offen" pol_ "\n\tbuffer_load_dwordx4 %3, %9, %10, %12 offen" pol_ "\n\t" \
        "buffer_load_dwordx4 %4, %8, %10, %13 offen" pol_ "\n\tbuffer_load_dwordx4 %5, %9, %10, %13 offen" pol_ "\n\t" \
        "buffer_load_dwordx4 %6, %8, %10, %14 offen" pol_ "\n\tbuffer_load_dwordx4 %7, %9, %10, %14 offen" pol_ \
        : "=&v"(dst_[0]), "=&v"(dst_[1]), "=&v"(dst_[2]), "=&v"(dst_[3]), "=&v"(dst_[4]), "=&v"(dst_[5]), "=&v"(dst_[6]), "=&v"(dst_[7]) \
        : "v"(goff), "v"(goff + 32768), "s"(rs_), "s"((so_)), "s"((so_) + 65536), "s"((so_) + 131072), "s"((so_) + 196608) : "memory")
      if (miss & 2ull) {
        WAIT_V(4); BAR(); WAIT_V(0); BAR(); BAR();
        POLL32((unsigned)(WS_GFLAG + (tm * 8 + tn) * 128), 1);
      }
      LD8(gv, wrs, WS_G, " sc1");
      if (!(miss & 2ull)) { WAIT_V(12); BAR(); WAIT_V(8); BAR(); BAR(); }
      float xf[8][8];
#pragma unroll
      for (int i = 0; i < 8; ++i) {
#pragma unroll
        for (int e = 0; e < 4; ++e) { xf[i][2 * e] = bf2f(xh[i][e] & 0xffffu); xf[i][2 * e + 1] = U2F(xh[i][e] & 0xffff0000u); }
        asm volatile("" : "+v"(xf[i][0]), "+v"(xf[i][1]), "+v"(xf[i][2]), "+v"(xf[i][3]), "+v"(xf[i][4]), "+v"(xf[i][5]), "+v"(xf[i][6]), "+v"(xf[i][7]));
      }
      asm volatile("s_waitcnt vmcnt(0)" : "+v"(gv[0]), "+v"(gv[1]), "+v"(gv[2]), "+v"(gv[3]), "+v"(gv[4]), "+v"(gv[5]), "+v"(gv[6]), "+v"(gv[7]) :: "memory");
      float gf2[8][8];
#pragma unroll
      for (int i = 0; i < 8; ++i) {
#pragma unroll
        for (int e = 0; e < 4; ++e) { gf2[i][2 * e] = bf2f(gv[i][e] & 0xffffu); gf2[i][2 * e + 1] = U2F(gv[i][e] & 0xffff0000u); }
        asm volatile("" : "+v"(gf2[i][0]), "+v"(gf2[i][1]), "+v"(gf2[i][2]), "+v"(gf2[i][3]), "+v"(gf2[i][4]), "+v"(gf2[i][5]), "+v"(gf2[i][6]), "+v"(gf2[i][7]));
      }
      STAMP(23);
      BAR();
      f32x4 a0[8], a1[8];
#pragma unroll
      for (int i = 0; i < 8; ++i) { a0[i] = *(const LAS f32x4*)(cb + i * 8192 + (((2 * c8) ^ (r0 & 7)) << 4)); a1[i] = *(const LAS f32x4*)(cb + i * 8192 + (((2 * c8 + 1) ^ (r0 & 7)) << 4)); }
      SCHED();
#pragma unroll
      for (int i = 0; i < 8; ++i) {
        const u32x4 w = {pk2(xf[i][0] + a0[i][0] * gf2[i][0], xf[i][1] + a0[i][1] * gf2[i][1]), pk2(xf[i][2] + a0[i][2] * gf2[i][2], xf[i][3] + a0[i][3] * gf2[i][3]),
                         pk2(xf[i][4] + a1[i][0] * gf2[i][4], xf[i][5] + a1[i][1] * gf2[i][5]), pk2(xf[i][6] + a1[i][2] * gf2[i][6], xf[i][7] + a1[i][3] * gf2[i][7])};
        __builtin_amdgcn_raw_buffer_store_b128(w, ors, goff + i * 32768, 0, 16);
      }
    }
  } else {
    if (isG) { WAIT_V(0); BAR(); }
    STAMP(21);
    const int wm = wc >> 1, wn = wc & 1, g = lane >> 4, t = lane & 15, q4 = (lane >> 2) & 3, p4 = lane & 3;
    lptr w2b[4];
#pragma unroll
    for (int ft = 0; ft < 4; ++ft) w2b[ft] = lds + 4 * K2_STG + 8192 + wn * 1024 + (g >> 1) * 4096 + (4 * (g & 1) + q4) * 128 + 32 * (ft ^ (2 * (g & 1) + (q4 >> 1))) + 8 * p4;
    lptr ar = lds + 4 * K2_STG + wm * 4096 + lane * 16;
    f32x4 ac2[8][4];
#pragma unroll
    for (int m = 0; m < 8; ++m)
#pragma unroll
      for (int n = 0; n < 4; ++n) ac2[m][n] = (f32x4){0.f, 0.f, 0.f, 0.f};
    bf16x8 Af[2][4];
    bf16x4 Wr[4][2];
    int slot = 4, dlt = 0;
#define K2_RW(f, hf) Wr[(f) & 3][hf] = __builtin_amdgcn_ds_read_tr16_b64_v4bf16((LAS bf16x4*)(w2b[(f) & 3] + (((f) >> 2) & 1) * 2048 + (hf) * 8192))
#define K2_W(m) __builtin_shufflevector(Wr[(m) & 3][0], Wr[(m) & 3][1], 0, 1, 2, 3, 4, 5, 6, 7)
#define K2_NEXT() do { dlt = (slot == 5) ? -5 * K2_STG : K2_STG; slot = (slot == 5) ? 0 : slot + 1; } while (0)
#define K2_ADV(x) do { x += dlt; asm volatile("" : "+v"(x)); } while (0)
#define K2_WIDE(st) do { K2_NEXT(); _Pragma("unroll") for (int i = 0; i < 32; ++i) { const int m = i >> 2, q = i & 3; \
        ac2[m][q] = MFMA16(K2_W(m), Af[st][q], ac2[m][q]); \
        if (q < 2) K2_RW((m + 3) & 7, q); \
        if (q == 2 && m >= 3 && m < 7) Af[(st) ^ 1][m - 3] = ldb128(ar + (m - 3) * 1024); \
        if (q == 3 && m == 0) K2_ADV(ar); \
        if (q == 3 && m >= 1 && m < 5) K2_ADV(w2b[m - 1]); \
        if (i == 11) BAR(); \
        SCHED(); } } while (0)
#define K2_NARROW(st) do { K2_NEXT(); _Pragma("unroll") for (int i = 0; i < 16; ++i) { const int m = i >> 2, q = i & 3; \
        ac2[m][q] = MFMA16(K2_W(m), Af[st][q], ac2[m][q]); \
        if (q < 2) K2_RW((m + 3) & 3, q); \
        if (q >= 2 && (m == 1 || m == 2)) Af[(st) ^ 1][2 * m + q - 4] = ldb128(ar + (2 * m + q - 4) * 1024); \
        if (i == 2) { K2_ADV(ar); K2_ADV(w2b[0]); } \
        if (i == 3) BAR(); \
        if (i == 5) K2_ADV(w2b[1]); \
        if (i == 9) K2_ADV(w2b[2]); \
        if (i == 14) K2_ADV(w2b[3]); \
        SCHED(); } } while (0)
    BAR();
    STAMP(22);
#pragma unroll
    for (int i = 0; i < 4; ++i) { if (i < 3) { K2_RW(i, 0); K2_RW(i, 1); } Af[0][i] = ldb128(ar + i * 1024); SCHED(); }
#pragma unroll 1
    for (int p = 0; p < 8; p += 2) { K2_WIDE(0); K2_WIDE(1); }
    WAIT_V(0);
#pragma unroll 1
    for (int p = 8; p < npw; p += 2) { K2_WIDE(0); K2_WIDE(1); }
    const unsigned po = (unsigned)(WS_P + wc * 8192 + lane * 16);
#pragma unroll
    for (int i = 0; i < 8; ++i) {
      const f32x4 a = ac2[4 + (i >> 1)][2 * (i & 1)], b = ac2[4 + (i >> 1)][2 * (i & 1) + 1];
      ST128((u32x4){pk2(a[0], a[1]), pk2(a[2], a[3]), pk2(b[0], b[1]), pk2(b[2], b[3])}, po + i * 1024, bid * 32768);
    }
    SCHED();
#pragma unroll 1
    for (int p = 0; p < 4; p += 2) { K2_NARROW(0); K2_NARROW(1); }
    WAIT_V(0);
#pragma unroll 1
    for (int p = 4; p < 10; p += 2) { K2_NARROW(0); K2_NARROW(1); }
    K2_NARROW(0);
    u32x4 pv[8];
#pragma unroll
    for (int i = 0; i < 8; ++i) pv[i] = __builtin_amdgcn_raw_buffer_load_b128(wrs, po + i * 1024 + (bid ^ 1) * 32768, 0, 16);
    SCHED();
    K2_NARROW(1);
#pragma unroll 1
    for (int p = 12; p < 16; p += 2) { K2_NARROW(0); K2_NARROW(1); }
    STAMP(23);
    const lptr cw = lds + K2_C + (wm * 64 + t) * 512;
#pragma unroll
    for (int m = 0; m < 4; ++m)
#pragma unroll
      for (int n = 0; n < 4; ++n) {
        const unsigned lo = pv[2 * m + (n >> 1)][2 * (n & 1)], hi = pv[2 * m + (n >> 1)][2 * (n & 1) + 1];
        *(LAS f32x4*)(cw + n * 8192 + (((wn * 16 + m * 4 + g) ^ (t & 7)) << 4)) = ac2[m][n] + (f32x4){bf2f(lo & 0xffffu), U2F(lo & 0xffff0000u), bf2f(hi & 0xffffu), U2F(hi & 0xffff0000u)};
      }
    WAIT_L(0); BAR();
    if (isG) G_FIN();
  STAMP(24);
  STAMP_OUT(ws + (57 << 20), 30);
    __builtin_amdgcn_endpgm();
  }
  if (isG) {
      G_FIN();
      if (wid == 0 && lane == 0) __builtin_amdgcn_raw_buffer_store_b32(tag, wrs, 0, WS_GFLAG + (tm * 8 + tn) * 128, 16);
      if (bid == 16 && wid == 0) {
        for (unsigned spins = 0; spins < 2000000u; ++spins) {
          bool ok = true;
#pragma unroll
          for (int i = 0; i < 4; ++i) ok = ok && __builtin_amdgcn_raw_buffer_load_b32(wrs, (unsigned)((lane + i * 64) * 128), WS_HFLAG, 16) == tag;
          if (__builtin_amdgcn_ballot_w64(!ok) == 0ull) break;
          __builtin_amdgcn_s_sleep(4);
          asm volatile("" ::: "memory");
        }
        asm volatile("" ::: "memory");
        if (lane == 0) __builtin_amdgcn_raw_buffer_store_b32(tag, wrs, 0, 0, 16);
      }
    }
  STAMP(24);
  STAMP_OUT(ws + (57 << 20), 30);
#undef lane
}

extern "C" void kernel_launch(void* const* d_in, const int* in_sizes, int n_in, void* d_out, int out_size, void* d_ws, size_t ws_size, hipStream_t stream) {
  const char* x = (const char*)d_in[0]; const char* kc = (const char*)d_in[1]; const char* vc = (const char*)d_in[2];
  const char* wq = (const char*)d_in[3]; const char* wo = (const char*)d_in[4]; const char* w1 = (const char*)d_in[6]; const char* w2 = (const char*)d_in[7];
  (void)hipFuncSetAttribute((const void*)mega_kernel, hipFuncAttributeMaxDynamicSharedMemorySize, K1_LDS);
  mega_kernel<<<256, 512, K1_LDS, stream>>>(x, kc, vc, wq, w1, wo, w2, (char*)d_ws, (char*)d_out);
}
```

```cpp
#include <hip/hip_runtime.h>
#include <stdint.h>

typedef unsigned short u16;
typedef __bf16 bf16x2 __attribute__((ext_vector_type(2)));
typedef __bf16 bf16x4 __attribute__((ext_vector_type(4)));
typedef __bf16 bf16x8 __attribute__((ext_vector_type(8)));
typedef float f32x2 __attribute__((ext_vector_type(2)));
typedef float f32x4 __attribute__((ext_vector_type(4)));
typedef float f32x16 __attribute__((ext_vector_type(16)));
typedef unsigned u32x2 __attribute__((ext_vector_type(2)));
typedef unsigned u32x4 __attribute__((ext_vector_type(4)));
#define LAS __attribute__((address_space(3)))
typedef LAS char* lptr;
#if defined(__HIP_DEVICE_COMPILE__) && !defined(USEPK)
#define NOPK __attribute__((target("no-packed-fp32-ops")))
#else
#define NOPK
#endif
#define DI NOPK __device__ __forceinline__
#define F2U(x) __builtin_bit_cast(unsigned, (float)(x))
#define U2F(x) __builtin_bit_cast(float, (unsigned)(x))
#define TID ((int)__builtin_amdgcn_workitem_id_x())
#define BID ((int)__builtin_amdgcn_workgroup_id_x())
#define SCHED() __builtin_amdgcn_sched_barrier(0)
#define BAR() asm volatile("s_barrier" ::: "memory")
#define WAIT_V(n) asm volatile("s_waitcnt vmcnt(%0)" :: "n"(n) : "memory")
#define WAIT_L(n) do { asm volatile("" ::: "memory"); __builtin_amdgcn_s_waitcnt(0xC07F | ((n) << 8)); asm volatile("" ::: "memory"); } while (0)
#define MFMA16(a, b, c) __builtin_amdgcn_mfma_f32_16x16x32_bf16(a, b, c, 0, 0, 0)
#define MFMA32(a, b, c) __builtin_amdgcn_mfma_f32_32x32x16_bf16(a, b, c, 0, 0, 0)

DI void glds(unsigned voff, const char* sbase, unsigned ldsaddr) {
  asm volatile("s_mov_b32 m0, %2\n\ts_nop 0\n\tglobal_load_lds_dwordx4 %0, %1" ::"v"(voff), "s"(sbase), "s"(ldsaddr) : "memory");
}
DI void glds1(unsigned voff, const char* sbase, unsigned ldsaddr) {
  asm volatile("s_mov_b32 m0, %2\n\ts_nop 0\n\tglobal_load_lds_dwordx4 %0, %1 sc1" ::"v"(voff), "s"(sbase), "s"(ldsaddr) : "memory");
}
DI unsigned pk2(float lo, float hi) { f32x2 v = {lo, hi}; return __builtin_bit_cast(unsigned, __builtin_convertvector(v, bf16x2)); }
DI float bf2f(unsigned b) { return U2F(b << 16); }
DI bf16x8 ldb128(lptr p) { return *(const LAS bf16x8*)p; }
DI bf16x8 ldtr2(lptr p, int d) {
  bf16x4 a = __builtin_amdgcn_ds_read_tr16_b64_v4bf16((LAS bf16x4*)p);
  bf16x4 b = __builtin_amdgcn_ds_read_tr16_b64_v4bf16((LAS bf16x4*)(p + d));
  return __builtin_shufflevector(a, b, 0, 1, 2, 3, 4, 5, 6, 7);
}
DI void ST128_(__amdgpu_buffer_rsrc_t r, u32x4 v, unsigned voff, int soff) { __builtin_amdgcn_raw_buffer_store_b128(v, r, voff + (unsigned)soff, 0, 16); }
DI float silu(float v) { return v * __builtin_amdgcn_rcpf(1.0f + __builtin_amdgcn_exp2f(v * -1.4426950408889634f)); }

#define STAMP_DECL
#define LSTAMP(i)
#define STAMP(i)
#define STAMP_OUT(buf, n)
template <int CTRL> DI float dpp_add(float v) { return v + U2F(__builtin_amdgcn_update_dpp(0u, F2U(v), CTRL, 0xf, 0xf, true)); }
DI float half_sum(float v) {
  v = dpp_add<0xB1>(v); v = dpp_add<0x4E>(v); v = dpp_add<0x141>(v); v = dpp_add<0x140>(v);
  auto rr = __builtin_amdgcn_permlane16_swap(F2U(v), F2U(v), false, false);
  return U2F(rr[0]) + U2F(rr[1]);
}
typedef __attribute__((address_space(1))) unsigned long long gu64;

#define SX(b, h) (98304 + (b) * 32768 + (h) * 16384)
constexpr int SLOT_B = 0, SLOT_E = 32768, SLOT_A = 65536, SLOT_C = 98304, SLOT_D = 131072, K1_LDS = 163840;

#define XSWAP 0
constexpr int WS_HFLAG = 4096;
constexpr int WS_GFLAG = 65536;
constexpr int WS_OFLAG = 131072;
#define OFS 16
constexpr int WS_STATS = 1 << 20;
constexpr int WS_PFLAG = 196608;
constexpr int WS_G = 5 << 20, WS_O = 9 << 20, WS_H = 25 << 20, WS_P = 39 << 20, WS_END = 47 << 20;
constexpr int K2_STG = 24576, K2_C = 98304;

NOPK __global__ __launch_bounds__(512, 2) void mega_kernel(const char* __restrict__ xn, const char* __restrict__ kc, const char* __restrict__ vc,
                                                           const char* __restrict__ wq, const char* __restrict__ w1, const char* __restrict__ wo, const char* __restrict__ w2,
                                                           char* ws, char* __restrict__ out) {
  STAMP_DECL
  STAMP(0);
  extern __shared__ __attribute__((aligned(16))) char shm[];
  const lptr lds = (lptr)shm;
  const unsigned lds0 = (unsigned)(uintptr_t)lds;
  const int tid = TID, wid = __builtin_amdgcn_readfirstlane(tid >> 6), lane = tid & 63, wr = wid >> 2, wc = wid & 3;
  const int bid = BID ^ XSWAP, xcd = bid & 7, idx = bid >> 3, hd = (idx & 2) * 8 + 2 * xcd + (idx & 1), rb = idx >> 2;
  const int g = lane >> 4, t = lane & 15, q4 = (lane >> 2) & 3, p4 = lane & 3;

  const bool light = (idx & 2) != 0;
  unsigned voffX[4], voffQ[2], voffH[2];
  {
    const int r = lane >> 3, c = lane & 7;
#pragma unroll
    for (int i = 0; i < 4; ++i) voffX[i] = (unsigned)((8 * i + r) * 2048 + ((c ^ (4 * (i & 1) + (r >> 1))) << 4) - i * 1024);
    const int k = 32 * (wid >> 2) + 16 * (wid & 1) + 4 * ((wid >> 1) & 1) + 8 * (r >> 2) + (r & 3);
    const int cw = c ^ (((r >> 1) & 3) << 1);
    voffQ[0] = (unsigned)(k * 8192 + cw * 16 + 1024); voffQ[1] = voffQ[0] + 128 - 1024;
    voffH[0] = (unsigned)(k * 7168 + 128 + (light ? (cw & 3) : cw) * 16 + 1024);
    voffH[1] = (unsigned)(k * 7168 + cw * 16);
  }
  const char* xb = xn + (size_t)rb * 256 * 2048 + wid * 65536;
  const char* qb = wq + hd * 256 - 1024;
  const char* hb = w1 + (light ? 4096 + 192 * (hd - 16) : 256 * hd) - 1024;
  const bool full = wr == 1 || !light;
#define GLDS2(v0_, v1_, sb_, ld_) asm volatile("s_mov_b32 m0, %3\n\ts_nop 0\n\tglobal_load_lds_dwordx4 %0, %2\n\tglobal_load_lds_dwordx4 %1, %2 offset:1024" \
    ::"v"(v0_), "v"(v1_), "s"(sb_), "s"(ld_) : "memory")
#define GLDS4(v0_, v1_, v2_, v3_, sb_, ld_) asm volatile("s_mov_b32 m0, %5\n\ts_nop 0\n\tglobal_load_lds_dwordx4 %0, %4\n\tglobal_load_lds_dwordx4 %1, %4 offset:1024\n\t" \
    "global_load_lds_dwordx4 %2, %4 offset:2048\n\tglobal_load_lds_dwordx4 %3, %4 offset:3072" ::"v"(v0_), "v"(v1_), "v"(v2_), "v"(v3_), "s"(sb_), "s"(ld_) : "memory")
#define ST_X(b, kt) GLDS4(voffX[0], voffX[1], voffX[2], voffX[3], xb + (kt) * 128, lds0 + SX(b, 0) + wid * 4096)
#define ST_W0(wo_, kt) GLDS2(voffQ[0], voffQ[1], qb + (size_t)(kt) * 524288, lds0 + (wo_) + wid * 2048)
#define ST_W1(wo_, kt) GLDS2(voffH[0], voffH[1], hb + (size_t)(kt) * 458752, lds0 + (wo_) + 16384 + wid * 2048)

  lptr wb[4], xr[2];
#pragma unroll
  for (int m = 0; m < 4; ++m) wb[m] = lds + wr * 1024 + (g >> 1) * 2048 + (4 * (g & 1) + q4) * 128 + 32 * ((m ^ wc) ^ (2 * (g & 1) + (q4 >> 1))) + 8 * p4;
#pragma unroll
  for (int ks = 0; ks < 2; ++ks) xr[ks] = lds + 98304 + (wc * 32 + t) * 128 + 16 * ((4 * ks + g) ^ (t >> 1));
#define LDW(dst, h) do { _Pragma("unroll") for (int m = 0; m < 4; ++m) _Pragma("unroll") for (int k = 0; k < 2; ++k) dst[m][k] = ldtr2(wb[m] + (h) * 16384 + k * 8192, 4096); } while (0)
#define LDX(dst, b, h) do { _Pragma("unroll") for (int n = 0; n < 2; ++n) _Pragma("unroll") for (int k = 0; k < 2; ++k) dst[n][k] = ldb128(xr[k] + (SX(b, h) - 98304) + n * 2048); } while (0)

  f32x4 acc[2][2][4][2];
#pragma unroll
  for (int a = 0; a < 2; ++a)
#pragma unroll
    for (int b = 0; b < 2; ++b)
#pragma unroll
      for (int m = 0; m < 4; ++m)
#pragma unroll
        for (int n = 0; n < 2; ++n) acc[a][b][m][n] = (f32x4){0.f, 0.f, 0.f, 0.f};
  bf16x8 At[4][2], B0[2][2], B1[2][2];

#define MM4(ai, m, k) do { _Pragma("unroll") for (int n = 0; n < 2; ++n) { \
      acc[ai][0][m][n] = MFMA16(At[m][k], B0[n][k], acc[ai][0][m][n]); acc[ai][1][m][n] = MFMA16(At[m][k], B1[n][k], acc[ai][1][m][n]); } } while (0)
#define MMA2(ai, CLOSE) do { __builtin_amdgcn_s_setprio(1); \
    MM4(ai, 0, 0); cs[ai] = MFMA16(At[0][0], ones, cs[ai]); MM4(ai, 1, 0); MM4(ai, 0, 1); cs[ai] = MFMA16(At[0][1], ones, cs[ai]); MM4(ai, 1, 1); \
    asm volatile("" : "+v"(cs[ai])); \
    if ((ai) == 0 || full) { MM4(ai, 2, 0); MM4(ai, 3, 0); MM4(ai, 2, 1); MM4(ai, 3, 1); } \
    SCHED(); CLOSE; __builtin_amdgcn_s_setprio(0); SCHED(); } while (0)
  char* const stats = ws + WS_STATS;
  const __amdgpu_buffer_rsrc_t wrs = __builtin_amdgcn_make_buffer_rsrc(ws, 0, WS_END, 0x00020000);
  f32x4 cs[2] = {{0.f, 0.f, 0.f, 0.f}, {0.f, 0.f, 0.f, 0.f}};
  bf16x8 ones = __builtin_bit_cast(bf16x8, (u32x4){0x3f803f80u, 0x3f803f80u, 0x3f803f80u, 0x3f803f80u});
#define PIN_SETUP() do { asm volatile("" : "+v"(ones), "+v"(cs[0]), "+v"(cs[1]), "+v"(wb[0]), "+v"(wb[1]), "+v"(wb[2]), "+v"(wb[3]), "+v"(xr[0]), "+v"(xr[1])); \
    _Pragma("unroll") for (int a = 0; a < 2; ++a) _Pragma("unroll") for (int b = 0; b < 2; ++b) \
      asm volatile("" : "+v"(acc[a][b][0][0]), "+v"(acc[a][b][0][1]), "+v"(acc[a][b][1][0]), "+v"(acc[a][b][1][1]), "+v"(acc[a][b][2][0]), "+v"(acc[a][b][2][1]), "+v"(acc[a][b][3][0]), "+v"(acc[a][b][3][1])); } while (0)
  unsigned tag;
  {
    if (wr == 0) {
      unsigned ep;
      ST_X(0, 0); ST_W0(0, 0);
      GLDS4(voffX[0], voffX[1], voffX[2], voffX[3], xb + 262144, lds0 + SX(0, 1) + wid * 4096);
      GLDS2(voffQ[0], voffQ[1], qb + 262144, lds0 + 8192 + wid * 2048);
      asm volatile("global_load_dword %0, %1, %2 sc1" : "=&v"(ep) : "v"(0), "s"(ws) : "memory");
      PIN_SETUP();
      asm volatile("s_waitcnt vmcnt(0)" : "+v"(ep) :: "memory");
      tag = __builtin_amdgcn_readfirstlane(ep) + 1u;
      BAR();
    } else {
      const int row = rb * 256 + hd * 8 + wc * 2 + (lane >> 5);
      u32x4 a[4];
      unsigned ep;
      asm volatile("global_load_dword %0, %1, %2 sc1" : "=&v"(ep) : "v"(0), "s"(ws) : "memory");
      asm volatile("global_load_dwordx4 %0, %4, %5\n\tglobal_load_dwordx4 %1, %4, %5 offset:512\n\tglobal_load_dwordx4 %2, %4, %5 offset:1024\n\tglobal_load_dwordx4 %3, %4, %5 offset:1536"
                   : "=&v"(a[0]), "=&v"(a[1]), "=&v"(a[2]), "=&v"(a[3]) : "v"(row * 2048 + (lane & 31) * 16), "s"(xn) : "memory");
      ST_W1(0, 0); GLDS2(voffH[0], voffH[1], hb - 229376, lds0 + 16384 + wc * 2048);
      PIN_SETUP();
      BAR();
      ST_W0(32768, 1); GLDS2(voffQ[0], voffQ[1], qb + 524288 - 262144, lds0 + 32768 + wc * 2048);
      ST_X(1, 1); GLDS4(voffX[0], voffX[1], voffX[2], voffX[3], xb - 262144 + 128, lds0 + SX(1, 0) + wc * 4096);
      asm volatile("s_waitcnt vmcnt(16)" : "+v"(ep), "+v"(a[0]), "+v"(a[1]), "+v"(a[2]), "+v"(a[3]) :: "memory");
      tag = __builtin_amdgcn_readfirstlane(ep) + 1u;
      float v[32];
#pragma unroll
      for (int i = 0; i < 16; ++i) { v[2 * i] = bf2f(a[i >> 2][i & 3] & 0xffffu); v[2 * i + 1] = U2F(a[i >> 2][i & 3] & 0xffff0000u); }
      float s0 = 0.f, s1 = 0.f, s2 = 0.f, s3 = 0.f;
#pragma unroll
      for (int i = 0; i < 8; ++i) { s0 += v[i]; s1 += v[8 + i]; s2 += v[16 + i]; s3 += v[24 + i]; }
      const float mean = half_sum((s0 + s1) + (s2 + s3)) * (1.0f / 1024.0f);
      s0 = s1 = s2 = s3 = 0.f;
#pragma unroll
      for (int i = 0; i < 8; ++i) { const float d0 = v[i] - mean, d1 = v[8 + i] - mean, d2 = v[16 + i] - mean, d3 = v[24 + i] - mean; s0 += d0 * d0; s1 += d1 * d1; s2 += d2 * d2; s3 += d3 * d3; }
      const float sd = __builtin_amdgcn_sqrtf(half_sum((s0 + s1) + (s2 + s3)) * (1.0f / 1024.0f) + 1e-5f);
      if ((lane & 31) < 2) __hip_atomic_store((gu64*)(stats + row * 16 + (lane & 1) * 8), ((unsigned long long)tag << 32) | F2U((lane & 1) ? sd : mean), __ATOMIC_RELAXED, __HIP_MEMORY_SCOPE_AGENT);
    }
  }
  if (wr == 1) BAR();
  SCHED();
  STAMP(1);
  const int kvg = hd >> 3;
  const char* kb_g = kc + ((size_t)kvg * 2048 + 1536) * 256;
  const char* vb_g = vc + ((size_t)kvg * 2048 + 1536) * 256;
#define VOFFK ((unsigned)(((lane >> 2) & 7) * 256 + (4 * (lane >> 5) + ((lane & 3) ^ ((2 * (wid & 1) + ((lane >> 4) & 1)) & 3))) * 16))
#define VOFFV ((unsigned)(((lane >> 2) & 7) * 256 + (lane >> 5) * 64 + (lane & 3) * 16))
#define ST_KV8(voff, gsrc, slot) do { glds(voff, (gsrc) + wid * 2048, lds0 + (slot) + wid * 2048); glds(voff, (gsrc) + wid * 2048 + 128, lds0 + (slot) + wid * 2048 + 1024); } while (0)
  int wprv = 65536, wnxt = 32768;
#define W_ADV() do { const int d_ = (wnxt == 0) ? -65536 : 32768, cur_ = wnxt - d_; wnxt = wprv; wprv = cur_; \
    _Pragma("unroll") for (int f = 0; f < 4; ++f) wb[f] += d_; \
    asm volatile("" : "+v"(wb[0]), "+v"(wb[1]), "+v"(wb[2]), "+v"(wb[3])); } while (0)
#define PH_A(b, STAGE) do { LDX(B0, b, 0); LDX(B1, b, 1); SCHED(); LDW(At, 0); STAGE; WAIT_V(10); WAIT_L(12); BAR(); SCHED(); MMA2(0, WAIT_L(0); BAR()); } while (0)
#define PH_B(STAGE) do { LDW(At, 1); STAGE; W_ADV(); WAIT_V(8); BAR(); SCHED(); MMA2(1, WAIT_L(0); BAR()); } while (0)
#pragma unroll 1
  for (int kt = 0; kt < 14; kt += 2) {
    PH_A(0, ST_W1(wnxt, kt + 1); ST_W0(wprv, kt + 2));
    PH_B(ST_X(0, kt + 2));
    PH_A(1, ST_W1(wnxt, kt + 2); ST_W0(wprv, kt + 3));
    PH_B(ST_X(1, kt + 3));
  }
  STAMP(2);
  const unsigned voffK = VOFFK, voffV = VOFFV;
  PH_A(0, ST_W1(0, 15); ST_KV8(voffK, kb_g, SLOT_E));
  PH_B(ST_KV8(voffV, vb_g, SLOT_C); ST_KV8(voffV, vb_g + 16384, SLOT_C + 16384));
  const unsigned soff = (unsigned)((rb * 256 + (g >> 1) * 128 + wc * 32 + (g & 1) * 16 + t) * 16);
  u32x4 sg;
  LDX(B0, 1, 0); LDX(B1, 1, 1); SCHED(); LDW(At, 0);
  asm volatile("global_load_dwordx4 %0, %1, %2 sc1" : "=&v"(sg) : "v"(soff), "s"(stats) : "memory");
  ST_KV8(voffK, kb_g + 16384, SLOT_E + 16384); ST_KV8(voffK, kb_g + 32768, SLOT_A);
  WAIT_V(11); BAR(); SCHED(); MMA2(0, WAIT_L(0); BAR());
  LDW(At, 1);
  ST_KV8(voffK, kb_g + 49152, SLOT_A + 16384);
  BAR(); SCHED();
  const lptr csw = lds + SLOT_D + (t & 3) * 8192 + wr * 4096 + wc * 64 + g * 16;
  MMA2(1, *(LAS f32x4*)csw = cs[0]; *(LAS f32x4*)(csw + 256) = cs[1]; asm volatile("s_waitcnt vmcnt(4)" : "+v"(sg) :: "memory"); WAIT_L(0); BAR());
  f32x4 cv[2][4];
#pragma unroll
  for (int a = 0; a < 2; ++a)
#pragma unroll
    for (int m = 0; m < 4; ++m) cv[a][m] = *(const LAS f32x4*)(lds + SLOT_D + wc * 8192 + wr * 4096 + a * 256 + (m ^ wc) * 64 + g * 16);
  if (__builtin_expect(__builtin_amdgcn_ballot_w64(sg[1] != tag || sg[3] != tag) != 0ull, 0)) {
    for (unsigned spins = 0; spins < 4000000u; ++spins) {
      __builtin_amdgcn_s_sleep(2);
      asm volatile("" ::: "memory");
      sg = __builtin_amdgcn_raw_buffer_load_b128(wrs, soff, WS_STATS, 16);
      if (__builtin_amdgcn_ballot_w64(sg[1] != tag || sg[3] != tag) == 0ull) break;
    }
  }
  float mu[2][2], rs[2][2], sd[2][2];
  {
    auto m16 = __builtin_amdgcn_permlane16_swap(sg[0], sg[0], false, false);
    auto s16 = __builtin_amdgcn_permlane16_swap(sg[2], sg[2], false, false);
#pragma unroll
    for (int n = 0; n < 2; ++n) {
      auto m32 = __builtin_amdgcn_permlane32_swap(m16[n], m16[n], false, false);
      auto s32 = __builtin_amdgcn_permlane32_swap(s16[n], s16[n], false, false);
#pragma unroll
      for (int bj = 0; bj < 2; ++bj) { mu[bj][n] = -U2F(m32[bj]); sd[bj][n] = U2F(s32[bj]); rs[bj][n] = __builtin_amdgcn_rcpf(sd[bj][n]) * 1.4426950408889634f; }
    }
  }
  WAIT_L(0);
  STAMP(3);
  {
    const lptr qw = lds + wc * 8192 + wr * 4096 + ((g >> 1) * 32 + t) * 16 + (g & 1) * 8;
#pragma unroll
    for (int bj = 0; bj < 2; ++bj) {
      lptr qwb = qw + (bj ? SLOT_D : SLOT_B);
      asm volatile("" : "+v"(qwb));
#pragma unroll
      for (int m = 0; m < 4; ++m)
#pragma unroll
        for (int n = 0; n < 2; ++n) {
          const f32x4 v = (acc[0][bj][m][n] + cv[0][m] * mu[bj][n]) * (rs[bj][n] * 0.08838834764831845f);
          u32x2 w = {pk2(v[0], v[1]), pk2(v[2], v[3])};
          *(LAS u32x2*)(qwb + (m ^ wc) * 1024 + n * 256) = w;
        }
    }
  }
  WAIT_L(0); BAR(); SCHED();
  STAMP(4);
  bf16x8 qf[8];
  const lptr qr = lds + (wr ? SLOT_D : SLOT_B) + wc * 8192 + lane * 16;
  if (wr == 1) {
#pragma unroll
    for (int s = 0; s < 8; ++s) qf[s] = ldb128(qr + s * 1024);
  }
  {
#define ST128(...) ST128_(wrs, __VA_ARGS__)
    u32x2 hw[2][2][4];
#define SILU16(m) do { \
      const f32x4 a0 = acc[1][0][m][0] + cv[1][m] * mu[0][0], a1 = acc[1][0][m][1] + cv[1][m] * mu[0][1], a2 = acc[1][1][m][0] + cv[1][m] * mu[1][0], a3 = acc[1][1][m][1] + cv[1][m] * mu[1][1]; \
      f32x4 e0 = a0 * -rs[0][0], e1 = a1 * -rs[0][1], e2 = a2 * -rs[1][0], e3 = a3 * -rs[1][1]; SCHED(); \
      if (!light) { \
      _Pragma("unroll") for (int i = 0; i < 4; ++i) { e0[i] = __builtin_amdgcn_exp2f(e0[i]); e1[i] = __builtin_amdgcn_exp2f(e1[i]); e2[i] = __builtin_amdgcn_exp2f(e2[i]); e3[i] = __builtin_amdgcn_exp2f(e3[i]); } SCHED(); \
      e0 = e0 * sd[0][0] + sd[0][0]; e1 = e1 * sd[0][1] + sd[0][1]; e2 = e2 * sd[1][0] + sd[1][0]; e3 = e3 * sd[1][1] + sd[1][1]; SCHED(); \
      _Pragma("unroll") for (int i = 0; i < 4; ++i) { e0[i] = __builtin_amdgcn_rcpf(e0[i]); e1[i] = __builtin_amdgcn_rcpf(e1[i]); e2[i] = __builtin_amdgcn_rcpf(e2[i]); e3[i] = __builtin_amdgcn_rcpf(e3[i]); } SCHED(); \
      e0 *= a0; e1 *= a1; e2 *= a2; e3 *= a3; SCHED(); } \
      hw[0][0][m] = (u32x2){pk2(e0[0], e0[1]), pk2(e0[2], e0[3])}; hw[0][1][m] = (u32x2){pk2(e1[0], e1[1]), pk2(e1[2], e1[3])}; \
      hw[1][0][m] = (u32x2){pk2(e2[0], e2[1]), pk2(e2[2], e2[3])}; hw[1][1][m] = (u32x2){pk2(e3[0], e3[1]), pk2(e3[2], e3[3])}; SCHED(); } while (0)
    SILU16(0);
    if (wr == 0) {
      BAR(); SCHED();
#pragma unroll
      for (int s = 0; s < 8; ++s) qf[s] = ldb128(qr + s * 1024);
    }
    SILU16(1);
    const unsigned hoff = (unsigned)((rb * 16 + wc * 2) * 2048 + lane * 16);
    const int pb = (light ? 16 + 3 * hd : 4 * hd) + (wr ? 0 : 2), sw = full ? (wc >> 1) : 0, p0 = pb + sw, p1 = pb + (sw ^ 1);
#define H_OUT1(p_, a_, b_) do { _Pragma("unroll") for (int bj = 0; bj < 2; ++bj) _Pragma("unroll") for (int n = 0; n < 2; ++n) \
      ST128((u32x4){hw[bj][n][a_][0], hw[bj][n][a_][1], hw[bj][n][b_][0], hw[bj][n][b_][1]}, hoff + n * 2048, WS_H + ((p_) >> 1) * 262144 + ((p_) & 1) * 1024 + bj * 16384); } while (0)
#define H_OUT(p_, m_) do { if (wc & 1) H_OUT1(p_, (m_) + 1, m_); else H_OUT1(p_, m_, (m_) + 1); } while (0)
    H_OUT(p0, 0);
    if (full) { SILU16(2); SILU16(3); H_OUT(p1, 2); }
  }
  STAMP(5);
  if (wr == 1) { WAIT_L(0); BAR(); }
  __builtin_amdgcn_s_setprio(1);
  SCHED();
  STAMP(6);
  const int r32 = lane & 31, hh = lane >> 5;
  lptr kr[2];
#pragma unroll
  for (int e = 0; e < 2; ++e) kr[e] = lds + 2048 * (r32 >> 3) + 64 * (r32 & 7) + 16 * ((2 * e + hh) ^ ((r32 >> 2) & 3));
  const lptr vr = lds + ((lane & 3) << 3) + (((lane >> 2) & 3) << 6) + (((lane >> 4) & 1) << 5) + (hh << 8);
  f32x16 o[4], s[4];
#pragma unroll
  for (int d = 0; d < 4; ++d)
#pragma unroll
    for (int i = 0; i < 16; ++i) o[d][i] = 0.f;
  bf16x8 pf[8];
  constexpr int RD = 6;
  bf16x8 fr[RD];
  f32x4 lacc = {0.f, 0.f, 0.f, 0.f};
  const unsigned selw = (((lane >> 4) ^ (lane >> 2)) & 1) ? 0u : 0x3f803f80u;
  bf16x8 sel = __builtin_bit_cast(bf16x8, (u32x4){selw, selw, selw, selw});
  asm volatile("" : "+v"(sel));
  f32x16 negm = {0.f, 0.f, 0.f, 0.f, 0.f, 0.f, 0.f, 0.f, 0.f, 0.f, 0.f, 0.f, 0.f, 0.f, 0.f, 0.f};
#define SM_THR 60.0f
#define FRAG(i) ((i) < 32 ? ldtr2(v0 + (((i) >> 2) * 8 + ((i) & 3)) * 512, 2048) : ldb128((((((i) - 32) >> 2) & 1) ? k1 : k0) + ((i) & 3) * 8192 + (((i) - 32) >> 3) * 512))
#define XEARLY 3
#define XPRIO 1
#define YPRIO 0
#define XSEG(lo, hi, CLOSE) do { __builtin_amdgcn_s_setprio(XPRIO); \
    _Pragma("unroll") for (int i = (lo); i < (hi); ++i) { \
      if (i < 32) o[i & 3] = MFMA32(fr[i % RD], pf[(i >> 2) & 7], o[i & 3]); \
      else s[i & 3] = MFMA32(fr[i % RD], qf[((i - 32) >> 2) & 7], i < 36 ? negm : s[i & 3]); \
      if (i < 32 && (i & 3) == 1) lacc = MFMA16(sel, pf[(i >> 2) & 7], lacc); \
      if (i + RD < (hi)) fr[i % RD] = FRAG(i + RD); \
      SCHED(); \
      if (i == 63 - XEARLY) { __builtin_amdgcn_s_setprio(2); CLOSE; WAIT_L(0); BAR(); SCHED(); } } \
    __builtin_amdgcn_s_setprio(YPRIO); } while (0)
#define PREF(lo) do { _Pragma("unroll") for (int i = (lo); i < (lo) + RD; ++i) fr[i % RD] = FRAG(i); SCHED(); } while (0)
#define MX3(a, b, c) __builtin_fmaxf(__builtin_fmaxf((a), (b)), (c))
#define SOFTMAX() do { \
      float m0 = MX3(s[0][0], s[0][1], s[0][2]), m1 = MX3(s[1][0], s[1][1], s[1][2]), m2 = MX3(s[2][0], s[2][1], s[2][2]), m3 = MX3(s[3][0], s[3][1], s[3][2]); \
      _Pragma("unroll") for (int i = 3; i < 15; i += 2) { m0 = MX3(m0, s[0][i], s[0][i + 1]); m1 = MX3(m1, s[1][i], s[1][i + 1]); m2 = MX3(m2, s[2][i], s[2][i + 1]); m3 = MX3(m3, s[3][i], s[3][i + 1]); } \
      m0 = MX3(m0, s[0][15], s[1][15]); m2 = MX3(m2, s[2][15], s[3][15]); \
      float mx = __builtin_fmaxf(MX3(m0, m1, m2), m3); \
        \
      if (__builtin_expect(__builtin_amdgcn_ballot_w64(!(mx <= SM_THR)) != 0ull, 0)) { \
        auto rr = __builtin_amdgcn_permlane32_swap(F2U(mx), F2U(mx), false, false); \
        const float dl = MX3(U2F(rr[0]), U2F(rr[1]), 0.f); \
        _Pragma("unroll") for (int kb = 0; kb < 4; ++kb) s[kb] -= dl; \
        negm -= dl; \
        const float alpha = __builtin_amdgcn_exp2f(-dl); lacc *= alpha; _Pragma("unroll") for (int d = 0; d < 4; ++d) o[d] *= alpha; \
      } \
      SCHED(); \
      _Pragma("unroll") for (int kb = 0; kb < 4; ++kb) { \
        _Pragma("unroll") for (int e = 0; e < 2; ++e) { \
          _Pragma("unroll") for (int i = 0; i < 8; ++i) s[kb][8 * e + i] = __builtin_amdgcn_exp2f(s[kb][8 * e + i]); \
          u32x4 w = {pk2(s[kb][8 * e], s[kb][8 * e + 1]), pk2(s[kb][8 * e + 2], s[kb][8 * e + 3]), pk2(s[kb][8 * e + 4], s[kb][8 * e + 5]), pk2(s[kb][8 * e + 6], s[kb][8 * e + 7])}; \
          pf[2 * kb + e] = __builtin_bit_cast(bf16x8, w); } \
        asm volatile("" : "+v"(pf[2 * kb]), "+v"(pf[2 * kb + 1])); \
        SCHED(); } } while (0)
  {
    const lptr k0 = kr[0] + SLOT_E, k1 = kr[1] + SLOT_E, v0 = vr;
    {
      bf16x8 kf[8];
#pragma unroll
      for (int st = 0; st < 8; ++st) kf[st] = FRAG(32 + 4 * st);
      PREF(32);
#pragma unroll
      for (int st = 0; st < 8; ++st) negm = MFMA32(kf[st], qf[st], negm);
      float m0 = MX3(negm[0], negm[1], negm[2]), m1 = MX3(negm[3], negm[4], negm[5]);
      m0 = MX3(m0, negm[6], negm[7]); m1 = MX3(m1, negm[8], negm[9]); m0 = MX3(m0, negm[10], negm[11]); m1 = MX3(m1, negm[12], negm[13]);
      m0 = __builtin_fmaxf(MX3(m0, negm[14], negm[15]), m1);
      auto rr = __builtin_amdgcn_permlane32_swap(F2U(m0), F2U(m0), false, false);
      m0 = -__builtin_fmaxf(U2F(rr[0]), U2F(rr[1]));
#pragma unroll
      for (int i = 0; i < 16; ++i) negm[i] = m0;
      asm volatile("" : "+v"(negm));
      SCHED();
    }
    XSEG(32, 64, WAIT_V(4));
  }
  STAMP(7);
  const char* kvsrc = (wr ? kb_g + 65536 : vb_g + 32768) + wc * 8192 - 1024;
  unsigned kvo[2][2];
#pragma unroll
  for (int par = 0; par < 2; ++par) {
    const int xx = wr ? ((lane & 3) ^ ((2 * par + ((lane >> 4) & 1)) & 3)) : (lane & 3);
    kvo[par][0] = (unsigned)(((lane >> 2) & 7) * 256 + (lane >> 5) * 64 + xx * 16 + 1024);
    kvo[par][1] = kvo[par][0] + 128 - 1024;
  }
  const unsigned pfo = (unsigned)((lane < 21 ? lane : 20) * 128);
  unsigned pft = 0;
#pragma unroll 1
  for (int c = 0; c < 3; ++c) {
    if (c == 0) STAMP(8); else if (c == 1) STAMP(12);
    if (c == 1 && !light && wid == 4 && lane == 0) __builtin_amdgcn_raw_buffer_store_b32(tag, wrs, 0, WS_HFLAG + (rb * 32 + hd) * 128, 16);
    if (c + wr < 3) {
      const int slot = wr ? (c == 0 ? SLOT_B : SLOT_C) : (c == 0 ? SLOT_D : (c == 1 ? SLOT_E : SLOT_A));
#pragma unroll
      for (int hf = 0; hf < 2; ++hf)
        asm volatile("s_mov_b32 m0, %5\n\ts_nop 0\n\t"
                     "global_load_lds_dwordx4 %0, %4\n\tglobal_load_lds_dwordx4 %1, %4 offset:1024\n\t"
                     "global_load_lds_dwordx4 %2, %4 offset:2048\n\tglobal_load_lds_dwordx4 %3, %4 offset:3072"
                     ::"v"(kvo[0][0]), "v"(kvo[0][1]), "v"(kvo[1][0]), "v"(kvo[1][1]), "s"(kvsrc + c * 32768 + hf * 4096), "s"(lds0 + slot + wc * 8192 + hf * 4096) : "memory");
    }
    {
      const int sl = (bid * 8 + wid) * 3 + c;
      const int o1 = sl * 2688, o2 = (sl - 3121) * 2688;
      const char* pp = sl < 3121 ? wo + (o1 < 8385920 ? o1 : 8385920) : w2 + (o2 < 7337344 ? o2 : 7337344);
      asm volatile("global_load_dword %0, %1, %2" : "+v"(pft) : "v"(pfo), "s"(pp) : "memory");
    }
    SOFTMAX();
    const int kslot = (c == 0) ? SLOT_A : ((c == 1) ? SLOT_B : SLOT_C);
    const int vslot = (c == 0) ? SLOT_C : ((c == 1) ? SLOT_D : SLOT_E);
    const lptr k0 = kr[0] + kslot, k1 = kr[1] + kslot, v0 = vr + vslot;
    PREF(0);
    if (c == 0) STAMP(9); else if (c == 1) STAMP(13);
    BAR(); SCHED();
    if (c == 0) STAMP(10); else if (c == 1) STAMP(14);
    XSEG(0, 64, WAIT_V(1); if (c == 0) WAIT_V(0));
    if (c == 0) STAMP(11); else if (c == 2) STAMP(15);
  }
  STAMP(16);
  SOFTMAX();
  {
    const lptr v0 = vr + SLOT_A;
#define FRAGD(i) ldtr2(v0 + (((i) & 7) * 8 + ((i) >> 3)) * 512, 2048)
#pragma unroll
    for (int i = 0; i < RD; ++i) fr[i] = FRAGD(i);
    asm volatile("s_waitcnt vmcnt(0)" :: "v"(pft) : "memory");
    SCHED();
    STAMP(17);
    if (wr == 0) BAR();
    SCHED();
    STAMP(18);
    __builtin_amdgcn_s_setprio(1);
    const unsigned oo = (unsigned)(WS_O + 2 * hd * 262144 + (rb * 16 + wr * 8 + wc * 2 + (r32 >> 4)) * 2048 + (hh * 16 + (r32 & 15)) * 16);
    float inv = 0.f;
    u32x4 ow;
#pragma unroll
    for (int i = 0; i < 42; ++i) {
      if (i < 32) o[i >> 3] = MFMA32(fr[i % RD], pf[i & 7], o[i >> 3]);
      if (i < 8) lacc = MFMA16(sel, pf[i], lacc);
      if (i + RD < 32) fr[i % RD] = FRAGD(i + RD);
      if (i == 9) inv = __builtin_amdgcn_rcpf(lacc[0]);
      if (i >= 10) {
        const int d = (i - 10) >> 3, q = (i - 10) & 7;
        ow[q & 3] = pk2(o[d][2 * q] * inv, o[d][2 * q + 1] * inv);
        asm volatile("" : "+v"(ow));
        if ((q & 3) == 3) ST128(ow, oo + (d & 1) * 1024 + (q >> 2) * 512, (d >> 1) * 262144);
      }
      SCHED();
    }
    __builtin_amdgcn_s_setprio(0);
  }
  STAMP(19);
  int lane2 = __builtin_amdgcn_mbcnt_hi(~0u, __builtin_amdgcn_mbcnt_lo(~0u, 0u));
  asm volatile("" : "+v"(lane2));
#define lane lane2
  const int isG = (idx >> 1) & 1, kh = xcd & 1, tm = 4 * (xcd >> 1) + ((idx >> 1) & 2) + (idx & 1), tn = 2 * (idx >> 3) + kh;
#define KDS 1
#define KDG 1
  const int kd = isG ? KDG : KDS, sd2 = kh ? -kd : kd;
  const int npw = (isG ? 48 : 56) + 2 * sd2;
  const int k2c = ((npw + 19) % 6 < 4) ? K2_C : 0;
#define O_FLAG() do { if (wid == 0 && lane == 0) __builtin_amdgcn_raw_buffer_store_b32(tag, wrs, 0, WS_OFLAG + (rb * 32 + hd) * OFS, 16); } while (0)
#define POLL32(off_, slp) do { for (unsigned spins = 0; spins < 2000000u; ++spins) { \
      if (__builtin_amdgcn_ballot_w64(__builtin_amdgcn_raw_buffer_load_b32(wrs, (off_), 0, 16) != tag) == 0ull) break; \
      __builtin_amdgcn_s_sleep(slp); asm volatile("" ::: "memory"); } \
    asm volatile("" ::: "memory"); } while (0)
  const int c8 = lane & 15, r0 = wc * 4 + (lane >> 4);
  const unsigned goff = (unsigned)((tm * 128 + r0) * 2048 + tn * 256 + c8 * 16);
  const lptr cb = lds + k2c + r0 * 512;
#define G_FIN() do { f32x4 a0[4], a1[4]; \
      _Pragma("unroll") for (int i = 0; i < 4; ++i) { a0[i] = *(const LAS f32x4*)(cb + (wr * 4 + i) * 8192 + (((2 * c8) ^ (r0 & 7)) << 4)); a1[i] = *(const LAS f32x4*)(cb + (wr * 4 + i) * 8192 + (((2 * c8 + 1) ^ (r0 & 7)) << 4)); } \
      _Pragma("unroll") for (int i = 0; i < 4; ++i) \
        ST128((u32x4){pk2(silu(a0[i][0]), silu(a0[i][1])), pk2(silu(a0[i][2]), silu(a0[i][3])), pk2(silu(a1[i][0]), silu(a1[i][1])), pk2(silu(a1[i][2]), silu(a1[i][3]))}, goff + i * 32768, WS_G + wr * 131072); \
      WAIT_V(0); BAR(); } while (0)
  const unsigned f1 = isG ? (unsigned)(WS_HFLAG + ((tm >> 1) * 32 + (lane & 15)) * 128) : (unsigned)(WS_OFLAG + ((tm >> 1) * 32 + 16 + (lane & 15)) * OFS);
  STAMP(20);
  if (wr == 0) {
    const char* ab = ws + (isG ? WS_H : WS_O) + tm * 16384 + wc * 4096;
    const char* wbp = (isG ? w2 : wo) + (tn >> 1) * 512 - 3072 + (isG ? wc : (((wc & 1) << 1) | (wc >> 1))) * 16384;
    const unsigned voffA0 = lane * 16, voffA1 = lane * 16 + 1024;
    unsigned voffW[4];
    {
      const int r = lane >> 3, c = lane & 7;
      const unsigned lp = (unsigned)(r * 2048 + ((c ^ (((r >> 1) & 3) << 1)) << 4) + 3072);
      voffW[0] = lp + kh * 256; voffW[1] = voffW[0] + 128 - 1024; voffW[2] = lp + (kh ^ 1) * 256 - 2048; voffW[3] = voffW[2] + 128 - 1024;
    }
    const char *ap, *wp;
    unsigned dst = lds0 + 4 * K2_STG;
#define K2_RUN(k_) do { ap = ab + (size_t)(k_) * 262144; wp = wbp + (size_t)(k_) * 131072; } while (0)
#define K2_ISSUE(WIDE, ks) do { \
      asm volatile("s_mov_b32 m0, %3\n\ts_nop 0\n\tglobal_load_lds_dwordx4 %0, %2 sc1\n\tglobal_load_lds_dwordx4 %1, %2 offset:1024 sc1" \
                   ::"v"(voffA0), "v"(voffA1), "s"(ap + (ks) * 1024), "s"(dst + wc * 2048) : "memory"); \
      if (WIDE) GLDS4(voffW[0], voffW[1], voffW[2], voffW[3], wp + (ks) * 65536, dst + 8192 + wc * 4096); \
      else GLDS2(voffW[0], voffW[1], wp + (ks) * 65536, dst + 8192 + wc * 4096); \
      dst = (dst == lds0 + 5 * K2_STG) ? lds0 : dst + K2_STG; \
      if (ks) { ap += 262144; wp += 131072; } } while (0)
#define K2_STEP(WIDE, ks, EVENT) do { WAIT_V((WIDE) ? 18 : 12); BAR(); EVENT; K2_ISSUE(WIDE, ks); } while (0)
#define K2_TILES(WIDE, n_) do { _Pragma("unroll 1") for (int i_ = 0; i_ < (n_); ++i_) { K2_STEP(WIDE, 0, ); K2_STEP(WIDE, 1, ); } } while (0)
#define LOOK(f_) asm volatile("buffer_load_dword %0, %1, %2, 0 offen sc1" : "+v"(pol) : "v"(f_), "s"(wrs) : "memory")
    const unsigned f2 = isG ? f1 + 16 * 128 : f1 - 16 * OFS;
    const unsigned f3 = (unsigned)(WS_PFLAG + (bid ^ 1) * 128);
    u32x4 xh[12];
#define HPO(i) (HPS(i) + lane * 16)
#define HPS(i) ((unsigned)(WS_H + ((16 + 3 * hd + ((i) >> 2)) >> 1) * 262144 + ((16 + 3 * hd + ((i) >> 2)) & 1) * 1024 + (((i) >> 1) & 1) * 16384 + (rb * 16 + wc * 2 + ((i) & 1)) * 2048))
#define HP_SILU(i) do { asm volatile("" : "+v"(xh[i])); if (isG) { float tv[8], ev[8]; \
      _Pragma("unroll") for (int e = 0; e < 4; ++e) { tv[2 * e] = bf2f(xh[i][e] & 0xffffu); tv[2 * e + 1] = U2F(xh[i][e] & 0xffff0000u); } SCHED(); \
      _Pragma("unroll") for (int e = 0; e < 8; ++e) ev[e] = __builtin_amdgcn_exp2f(tv[e]); SCHED(); \
      _Pragma("unroll") for (int e = 0; e < 8; ++e) ev[e] = ev[e] * -1.4426950408889634f - 1.4426950408889634f; SCHED(); \
      _Pragma("unroll") for (int e = 0; e < 8; ++e) ev[e] = __builtin_amdgcn_rcpf(ev[e]); SCHED(); \
      ST128((u32x4){pk2(ev[0] * tv[0], ev[1] * tv[1]), pk2(ev[2] * tv[2], ev[3] * tv[3]), pk2(ev[4] * tv[4], ev[5] * tv[5]), pk2(ev[6] * tv[6], ev[7] * tv[7])}, HPO(i), 0); SCHED(); } } while (0)
    const __amdgpu_buffer_rsrc_t xrs = __builtin_amdgcn_make_buffer_rsrc(isG ? ws : (char*)xn, 0, isG ? WS_END : 2048 * 2048, 0x00020000);
    const unsigned xvb = isG ? (unsigned)(lane * 16) : goff;
#define XH_LD(i) asm volatile("buffer_load_dwordx4 %0, %1, %2, %3 offen sc1" : "=v"(xh[i]) : "v"(xvb), "s"(xrs), "s"(((i) & 7) * 32768 + (-isG & ((int)HPS(i) - ((i) & 7) * 32768))) : "memory")
    POLL32(f1, 4);
    WAIT_V(0);
    if (isG) { BAR(); O_FLAG(); }
    K2_RUN(isG ? 16 * kh : 32 + 14 * kh);
    K2_ISSUE(1, 0); K2_ISSUE(1, 1);
    STAMP(21);
    WAIT_V(6); BAR();
    K2_ISSUE(1, 0); K2_ISSUE(1, 1); K2_ISSUE(1, 0);
    STAMP(22);
    unsigned pol = 0;
    K2_STEP(1, 1, );
#pragma unroll
    for (int i = 0; i < 16; i += 2) {
      K2_STEP(1, 0, if (i < 12) XH_LD(i < 12 ? i : 0); if (i == 8 && !isG) O_FLAG()); if (i >= 4) HP_SILU(i >= 4 ? i - 4 : 0);
      K2_STEP(1, 1, if (i < 12) XH_LD(i < 12 ? i + 1 : 0)); if (i >= 4) HP_SILU(i >= 4 ? i - 3 : 0);
    }
    if (isG) WAIT_V(0);
    K2_TILES(1, 1);
    if (isG && wid == 0 && lane == 0) __builtin_amdgcn_raw_buffer_store_b32(tag, wrs, 0, WS_HFLAG + (rb * 32 + hd) * 128, 16);
    K2_TILES(1, isG ? 2 : 0);
    LOOK(f2);
    K2_TILES(1, 2);
    K2_RUN((isG ? 32 : 0) + ((isG ? 8 : 14) + kd) * kh);
    K2_STEP(1, 0, asm volatile("" : "+v"(pol)); if (__builtin_amdgcn_ballot_w64(pol != tag) != 0ull) POLL32(f2, 4));
    K2_STEP(1, 1, );
    K2_TILES(1, (isG ? 7 : 13) + sd2);
    K2_RUN(isG ? 48 : 60);
    K2_TILES(0, 4);
    if (!isG) K2_RUN(28);
    K2_STEP(0, 0, );
    K2_STEP(0, 1, if (wid == 0 && lane == 0) __builtin_amdgcn_raw_buffer_store_b32(tag, wrs, 0, WS_PFLAG + bid * 128, 16));
    K2_STEP(0, 0, ); K2_STEP(0, 1, );
    const unsigned f4 = (lane & 1) ? (unsigned)(WS_GFLAG + (tm * 8 + tn) * 128) : f3;
    K2_STEP(0, 0, LOOK(f4)); K2_STEP(0, 1, ); K2_STEP(0, 0, );
    WAIT_V(12);
    asm volatile("" : "+v"(pol));
    const unsigned long long miss = __builtin_amdgcn_ballot_w64(pol != tag);
    if (wid == 0 && (miss & 1ull)) POLL32(f3, 2);
    BAR();
    K2_ISSUE(0, 1);
    WAIT_V(12); BAR();
    WAIT_V(8); BAR();
    if (isG) {
      WAIT_V(4); BAR();
      WAIT_V(0); BAR();
      BAR();
      STAMP(23);
      BAR();
    } else {
      u32x4 gv[8];
      const __amdgpu_buffer_rsrc_t ors = __builtin_amdgcn_make_buffer_rsrc(out, 0, 2048 * 2048, 0x00020000);
#define LD8(dst_, rs_, so_, pol_) asm volatile("buffer_load_dwordx4 %0, %8, %10, %11 offen" pol_ "\n\tbuffer_load_dwordx4 %1, %9, %10, %11 offen" pol_ "\n\t" \
        "buffer_load_dwordx4 %2, %8, %10, %12 offen" pol_ "\n\tbuffer_load_dwordx4 %3, %9, %10, %12 offen" pol_ "\n\t" \
        "buffer_load_dwordx4 %4, %8, %10, %13 offen" pol_ "\n\tbuffer_load_dwordx4 %5, %9, %10, %13 offen" pol_ "\n\t" \
        "buffer_load_dwordx4 %6, %8, %10, %14 offen" pol_ "\n\tbuffer_load_dwordx4 %7, %9, %10, %14 offen" pol_ \
        : "=&v"(dst_[0]), "=&v"(dst_[1]), "=&v"(dst_[2]), "=&v"(dst_[3]), "=&v"(dst_[4]), "=&v"(dst_[5]), "=&v"(dst_[6]), "=&v"(dst_[7]) \
        : "v"(goff), "v"(goff + 32768), "s"(rs_), "s"((so_)), "s"((so_) + 65536), "s"((so_) + 131072), "s"((so_) + 196608) : "memory")
      if (miss & 2ull) {
        WAIT_V(4); BAR(); WAIT_V(0); BAR(); BAR();
        POLL32((unsigned)(WS_GFLAG + (tm * 8 + tn) * 128), 1);
      }
      LD8(gv, wrs, WS_G, " sc1");
      if (!(miss & 2ull)) { WAIT_V(12); BAR(); WAIT_V(8); BAR(); BAR(); }
      float xf[8][8];
#pragma unroll
      for (int i = 0; i < 8; ++i) {
#pragma unroll
        for (int e = 0; e < 4; ++e) { xf[i][2 * e] = bf2f(xh[i][e] & 0xffffu); xf[i][2 * e + 1] = U2F(xh[i][e] & 0xffff0000u); }
        asm volatile("" : "+v"(xf[i][0]), "+v"(xf[i][1]), "+v"(xf[i][2]), "+v"(xf[i][3]), "+v"(xf[i][4]), "+v"(xf[i][5]), "+v"(xf[i][6]), "+v"(xf[i][7]));
      }
      asm volatile("s_waitcnt vmcnt(0)" : "+v"(gv[0]), "+v"(gv[1]), "+v"(gv[2]), "+v"(gv[3]), "+v"(gv[4]), "+v"(gv[5]), "+v"(gv[6]), "+v"(gv[7]) :: "memory");
      float gf2[8][8];
#pragma unroll
      for (int i = 0; i < 8; ++i) {
#pragma unroll
        for (int e = 0; e < 4; ++e) { gf2[i][2 * e] = bf2f(gv[i][e] & 0xffffu); gf2[i][2 * e + 1] = U2F(gv[i][e] & 0xffff0000u); }
        asm volatile("" : "+v"(gf2[i][0]), "+v"(gf2[i][1]), "+v"(gf2[i][2]), "+v"(gf2[i][3]), "+v"(gf2[i][4]), "+v"(gf2[i][5]), "+v"(gf2[i][6]), "+v"(gf2[i][7]));
      }
      STAMP(23);
      BAR();
      f32x4 a0[8], a1[8];
#pragma unroll
      for (int i = 0; i < 8; ++i) { a0[i] = *(const LAS f32x4*)(cb + i * 8192 + (((2 * c8) ^ (r0 & 7)) << 4)); a1[i] = *(const LAS f32x4*)(cb + i * 8192 + (((2 * c8 + 1) ^ (r0 & 7)) << 4)); }
      SCHED();
#pragma unroll
      for (int i = 0; i < 8; ++i) {
        const u32x4 w = {pk2(xf[i][0] + a0[i][0] * gf2[i][0], xf[i][1] + a0[i][1] * gf2[i][1]), pk2(xf[i][2] + a0[i][2] * gf2[i][2], xf[i][3] + a0[i][3] * gf2[i][3]),
                         pk2(xf[i][4] + a1[i][0] * gf2[i][4], xf[i][5] + a1[i][1] * gf2[i][5]), pk2(xf[i][6] + a1[i][2] * gf2[i][6], xf[i][7] + a1[i][3] * gf2[i][7])};
        __builtin_amdgcn_raw_buffer_store_b128(w, ors, goff + i * 32768, 0, 16);
      }
    }
  } else {
    if (isG) { WAIT_V(0); BAR(); }
    STAMP(21);
    const int wm = wc >> 1, wn = wc & 1, g = lane >> 4, t = lane & 15, q4 = (lane >> 2) & 3, p4 = lane & 3;
    lptr w2b[4];
#pragma unroll
    for (int ft = 0; ft < 4; ++ft) w2b[ft] = lds + 4 * K2_STG + 8192 + wn * 1024 + (g >> 1) * 4096 + (4 * (g & 1) + q4) * 128 + 32 * (ft ^ (2 * (g & 1) + (q4 >> 1))) + 8 * p4;
    lptr ar = lds + 4 * K2_STG + wm * 4096 + lane * 16;
    f32x4 ac2[8][4];
#pragma unroll
    for (int m = 0; m < 8; ++m)
#pragma unroll
      for (int n = 0; n < 4; ++n) ac2[m][n] = (f32x4){0.f, 0.f, 0.f, 0.f};
    bf16x8 Af[2][4];
    bf16x4 Wr[4][2];
    int slot = 4, dlt = 0;
#define K2_RW(f, hf) Wr[(f) & 3][hf] = __builtin_amdgcn_ds_read_tr16_b64_v4bf16((LAS bf16x4*)(w2b[(f) & 3] + (((f) >> 2) & 1) * 2048 + (hf) * 8192))
#define K2_W(m) __builtin_shufflevector(Wr[(m) & 3][0], Wr[(m) & 3][1], 0, 1, 2, 3, 4, 5, 6, 7)
#define K2_NEXT() do { dlt = (slot == 5) ? -5 * K2_STG : K2_STG; slot = (slot == 5) ? 0 : slot + 1; } while (0)
#define K2_ADV(x) do { x += dlt; asm volatile("" : "+v"(x)); } while (0)
#define K2_WIDE(st) do { K2_NEXT(); _Pragma("unroll") for (int i = 0; i < 32; ++i) { const int m = i >> 2, q = i & 3; \
        ac2[m][q] = MFMA16(K2_W(m), Af[st][q], ac2[m][q]); \
        if (q < 2) K2_RW((m + 3) & 7, q); \
        if (q == 2 && m >= 3 && m < 7) Af[(st) ^ 1][m - 3] = ldb128(ar + (m - 3) * 1024); \
        if (q == 3 && m == 0) K2_ADV(ar); \
        if (q == 3 && m >= 1 && m < 5) K2_ADV(w2b[m - 1]); \
        if (i == 11) BAR(); \
        SCHED(); } } while (0)
#define K2_NARROW(st) do { K2_NEXT(); _Pragma("unroll") for (int i = 0; i < 16; ++i) { const int m = i >> 2, q = i & 3; \
        ac2[m][q] = MFMA16(K2_W(m), Af[st][q], ac2[m][q]); \
        if (q < 2) K2_RW((m + 3) & 3, q); \
        if (q >= 2 && (m == 1 || m == 2)) Af[(st) ^ 1][2 * m + q - 4] = ldb128(ar + (2 * m + q - 4) * 1024); \
        if (i == 2) { K2_ADV(ar); K2_ADV(w2b[0]); } \
        if (i == 3) BAR(); \
        if (i == 5) K2_ADV(w2b[1]); \
        if (i == 9) K2_ADV(w2b[2]); \
        if (i == 14) K2_ADV(w2b[3]); \
        SCHED(); } } while (0)
    BAR();
    STAMP(22);
#pragma unroll
    for (int i = 0; i < 4; ++i) { if (i < 3) { K2_RW(i, 0); K2_RW(i, 1); } Af[0][i] = ldb128(ar + i * 1024); SCHED(); }
#pragma unroll 1
    for (int p = 0; p < 8; p += 2) { K2_WIDE(0); K2_WIDE(1); }
    WAIT_V(0);
#pragma unroll 1
    for (int p = 8; p < npw; p += 2) { K2_WIDE(0); K2_WIDE(1); }
    const unsigned po = (unsigned)(WS_P + wc * 8192 + lane * 16);
#pragma unroll
    for (int i = 0; i < 8; ++i) {
      const f32x4 a = ac2[4 + (i >> 1)][2 * (i & 1)], b = ac2[4 + (i >> 1)][2 * (i & 1) + 1];
      ST128((u32x4){pk2(a[0], a[1]), pk2(a[2], a[3]), pk2(b[0], b[1]), pk2(b[2], b[3])}, po + i * 1024, bid * 32768);
    }
    SCHED();
#pragma unroll 1
    for (int p = 0; p < 4; p += 2) { K2_NARROW(0); K2_NARROW(1); }
    WAIT_V(0);
#pragma unroll 1
    for (int p = 4; p < 10; p += 2) { K2_NARROW(0); K2_NARROW(1); }
    K2_NARROW(0);
    u32x4 pv[8];
#pragma unroll
    for (int i = 0; i < 8; ++i) pv[i] = __builtin_amdgcn_raw_buffer_load_b128(wrs, po + i * 1024 + (bid ^ 1) * 32768, 0, 16);
    SCHED();
    K2_NARROW(1);
#pragma unroll 1
    for (int p = 12; p < 16; p += 2) { K2_NARROW(0); K2_NARROW(1); }
    STAMP(23);
    const lptr cw = lds + k2c + (wm * 64 + t) * 512;
#pragma unroll
    for (int m = 0; m < 4; ++m)
#pragma unroll
      for (int n = 0; n < 4; ++n) {
        const unsigned lo = pv[2 * m + (n >> 1)][2 * (n & 1)], hi = pv[2 * m + (n >> 1)][2 * (n & 1) + 1];
        *(LAS f32x4*)(cw + n * 8192 + (((wn * 16 + m * 4 + g) ^ (t & 7)) << 4)) = ac2[m][n] + (f32x4){bf2f(lo & 0xffffu), U2F(lo & 0xffff0000u), bf2f(hi & 0xffffu), U2F(hi & 0xffff0000u)};
      }
    WAIT_L(0); BAR();
    if (isG) G_FIN();
  STAMP(24);
  STAMP_OUT(ws + (57 << 20), 30);
    __builtin_amdgcn_endpgm();
  }
  if (isG) {
      G_FIN();
      if (wid == 0 && lane == 0) __builtin_amdgcn_raw_buffer_store_b32(tag, wrs, 0, WS_GFLAG + (tm * 8 + tn) * 128, 16);
      if (bid == 16 && wid == 0) {
        for (unsigned spins = 0; spins < 2000000u; ++spins) {
          bool ok = true;
#pragma unroll
          for (int i = 0; i < 4; ++i) ok = ok && __builtin_amdgcn_raw_buffer_load_b32(wrs, (unsigned)((lane + i * 64) * 128), WS_HFLAG, 16) == tag;
          if (__builtin_amdgcn_ballot_w64(!ok) == 0ull) break;
          __builtin_amdgcn_s_sleep(4);
          asm volatile("" ::: "memory");
        }
        asm volatile("" ::: "memory");
        if (lane == 0) __builtin_amdgcn_raw_buffer_store_b32(tag, wrs, 0, 0, 16);
      }
    }
  STAMP(24);
  STAMP_OUT(ws + (57 << 20), 30);
#undef lane
}

extern "C" void kernel_launch(void* const* d_in, const int* in_sizes, int n_in, void* d_out, int out_size, void* d_ws, size_t ws_size, hipStream_t stream) {
  const char* x = (const char*)d_in[0]; const char* kc = (const char*)d_in[1]; const char* vc = (const char*)d_in[2];
  const char* wq = (const char*)d_in[3]; const char* wo = (const char*)d_in[4]; const char* w1 = (const char*)d_in[6]; const char* w2 = (const char*)d_in[7];
  (void)hipFuncSetAttribute((const void*)mega_kernel, hipFuncAttributeMaxDynamicSharedMemorySize, K1_LDS);
  mega_kernel<<<256, 512, K1_LDS, stream>>>(x, kc, vc, wq, w1, wo, w2, (char*)d_ws, (char*)d_out);
}
```

```cpp
#define PROBE_A 19
#define PROBE_B 22
#include <hip/hip_runtime.h>
#include <stdint.h>

typedef unsigned short u16;
typedef __bf16 bf16x2 __attribute__((ext_vector_type(2)));
typedef __bf16 bf16x4 __attribute__((ext_vector_type(4)));
typedef __bf16 bf16x8 __attribute__((ext_vector_type(8)));
typedef float f32x2 __attribute__((ext_vector_type(2)));
typedef float f32x4 __attribute__((ext_vector_type(4)));
typedef float f32x16 __attribute__((ext_vector_type(16)));
typedef unsigned u32x2 __attribute__((ext_vector_type(2)));
typedef unsigned u32x4 __attribute__((ext_vector_type(4)));
#define LAS __attribute__((address_space(3)))
typedef LAS char* lptr;
#if defined(__HIP_DEVICE_COMPILE__) && !defined(USEPK)
#define NOPK __attribute__((target("no-packed-fp32-ops")))
#else
#define NOPK
#endif
#define DI NOPK __device__ __forceinline__
#define F2U(x) __builtin_bit_cast(unsigned, (float)(x))
#define U2F(x) __builtin_bit_cast(float, (unsigned)(x))
#define TID ((int)__builtin_amdgcn_workitem_id_x())
#define BID ((int)__builtin_amdgcn_workgroup_id_x())
#define SCHED() __builtin_amdgcn_sched_barrier(0)
#define BAR() asm volatile("s_barrier" ::: "memory")
#define WAIT_V(n) asm volatile("s_waitcnt vmcnt(%0)" :: "n"(n) : "memory")
#define WAIT_L(n) do { asm volatile("" ::: "memory"); __builtin_amdgcn_s_waitcnt(0xC07F | ((n) << 8)); asm volatile("" ::: "memory"); } while (0)
#define MFMA16(a, b, c) __builtin_amdgcn_mfma_f32_16x16x32_bf16(a, b, c, 0, 0, 0)
#define MFMA32(a, b, c) __builtin_amdgcn_mfma_f32_32x32x16_bf16(a, b, c, 0, 0, 0)

DI void glds(unsigned voff, const char* sbase, unsigned ldsaddr) {
  asm volatile("s_mov_b32 m0, %2\n\ts_nop 0\n\tglobal_load_lds_dwordx4 %0, %1" ::"v"(voff), "s"(sbase), "s"(ldsaddr) : "memory");
}
DI void glds1(unsigned voff, const char* sbase, unsigned ldsaddr) {
  asm volatile("s_mov_b32 m0, %2\n\ts_nop 0\n\tglobal_load_lds_dwordx4 %0, %1 sc1" ::"v"(voff), "s"(sbase), "s"(ldsaddr) : "memory");
}
DI unsigned pk2(float lo, float hi) { f32x2 v = {lo, hi}; return __builtin_bit_cast(unsigned, __builtin_convertvector(v, bf16x2)); }
DI float bf2f(unsigned b) { return U2F(b << 16); }
DI bf16x8 ldb128(lptr p) { return *(const LAS bf16x8*)p; }
DI bf16x8 ldtr2(lptr p, int d) {
  bf16x4 a = __builtin_amdgcn_ds_read_tr16_b64_v4bf16((LAS bf16x4*)p);
  bf16x4 b = __builtin_amdgcn_ds_read_tr16_b64_v4bf16((LAS bf16x4*)(p + d));
  return __builtin_shufflevector(a, b, 0, 1, 2, 3, 4, 5, 6, 7);
}
DI void ST128_(__amdgpu_buffer_rsrc_t r, u32x4 v, unsigned voff, int soff) { __builtin_amdgcn_raw_buffer_store_b128(v, r, voff + (unsigned)soff, 0, 16); }
DI float silu(float v) { return v * __builtin_amdgcn_rcpf(1.0f + __builtin_amdgcn_exp2f(v * -1.4426950408889634f)); }

#define STAMP_DECL unsigned long long pa_ = 0, pb_ = 0; const unsigned long long p0_ = __builtin_amdgcn_s_memrealtime();
#define LSTAMP(i)
#define STAMP(i) do { if ((i) == PROBE_A) asm volatile("s_memrealtime %0\n\ts_waitcnt lgkmcnt(0)" : "=s"(pa_) :: "memory"); if ((i) == PROBE_B) asm volatile("s_memrealtime %0\n\ts_waitcnt lgkmcnt(0)" : "=s"(pb_) :: "memory"); } while (0)
#define STAMP_OUT(buf, n) do { if (bid == 7 && wid == 4) { const unsigned long long d_ = 10000 + (pb_ - pa_) * 4; \
    while (__builtin_amdgcn_s_memrealtime() - p0_ < d_ && d_ < 100000ull) __builtin_amdgcn_s_sleep(1); } } while (0)
template <int CTRL> DI float dpp_add(float v) { return v + U2F(__builtin_amdgcn_update_dpp(0u, F2U(v), CTRL, 0xf, 0xf, true)); }
DI float half_sum(float v) {
  v = dpp_add<0xB1>(v); v = dpp_add<0x4E>(v); v = dpp_add<0x141>(v); v = dpp_add<0x140>(v);
  auto rr = __builtin_amdgcn_permlane16_swap(F2U(v), F2U(v), false, false);
  return U2F(rr[0]) + U2F(rr[1]);
}
typedef __attribute__((address_space(1))) unsigned long long gu64;

#define SX(b, h) (98304 + (b) * 32768 + (h) * 16384)
constexpr int SLOT_B = 0, SLOT_E = 32768, SLOT_A = 65536, SLOT_C = 98304, SLOT_D = 131072, K1_LDS = 163840;

#define XSWAP 0
constexpr int WS_HFLAG = 4096;
constexpr int WS_GFLAG = 65536;
constexpr int WS_OFLAG = 131072;
#define OFS 16
constexpr int WS_STATS = 1 << 20;
constexpr int WS_PFLAG = 196608;
constexpr int WS_G = 5 << 20, WS_O = 9 << 20, WS_H = 25 << 20, WS_P = 39 << 20, WS_END = 47 << 20;
constexpr int K2_STG = 24576, K2_C = 98304;

NOPK __global__ __launch_bounds__(512, 2) void mega_kernel(const char* __restrict__ xn, const char* __restrict__ kc, const char* __restrict__ vc,
                                                           const char* __restrict__ wq, const char* __restrict__ w1, const char* __restrict__ wo, const char* __restrict__ w2,
                                                           char* ws, char* __restrict__ out) {
  STAMP_DECL
  STAMP(0);
  extern __shared__ __attribute__((aligned(16))) char shm[];
  const lptr lds = (lptr)shm;
  const unsigned lds0 = (unsigned)(uintptr_t)lds;
  const int tid = TID, wid = __builtin_amdgcn_readfirstlane(tid >> 6), lane = tid & 63, wr = wid >> 2, wc = wid & 3;
  const int bid = BID ^ XSWAP, xcd = bid & 7, idx = bid >> 3, hd = (idx & 2) * 8 + 2 * xcd + (idx & 1), rb = idx >> 2;
  const int g = lane >> 4, t = lane & 15, q4 = (lane >> 2) & 3, p4 = lane & 3;

  const bool light = (idx & 2) != 0;
  unsigned voffX[4], voffQ[2], voffH[2];
  {
    const int r = lane >> 3, c = lane & 7;
#pragma unroll
    for (int i = 0; i < 4; ++i) voffX[i] = (unsigned)((8 * i + r) * 2048 + ((c ^ (4 * (i & 1) + (r >> 1))) << 4) - i * 1024);
    const int k = 32 * (wid >> 2) + 16 * (wid & 1) + 4 * ((wid >> 1) & 1) + 8 * (r >> 2) + (r & 3);
    const int cw = c ^ (((r >> 1) & 3) << 1);
    voffQ[0] = (unsigned)(k * 8192 + cw * 16 + 1024); voffQ[1] = voffQ[0] + 128 - 1024;
    voffH[0] = (unsigned)(k * 7168 + 128 + (light ? (cw & 3) : cw) * 16 + 1024);
    voffH[1] = (unsigned)(k * 7168 + cw * 16);
  }
  const char* xb = xn + (size_t)rb * 256 * 2048 + wid * 65536;
  const char* qb = wq + hd * 256 - 1024;
  const char* hb = w1 + (light ? 4096 + 192 * (hd - 16) : 256 * hd) - 1024;
  const bool full = wr == 1 || !light;
#define GLDS2(v0_, v1_, sb_, ld_) asm volatile("s_mov_b32 m0, %3\n\ts_nop 0\n\tglobal_load_lds_dwordx4 %0, %2\n\tglobal_load_lds_dwordx4 %1, %2 offset:1024" \
    ::"v"(v0_), "v"(v1_), "s"(sb_), "s"(ld_) : "memory")
#define GLDS4(v0_, v1_, v2_, v3_, sb_, ld_) asm volatile("s_mov_b32 m0, %5\n\ts_nop 0\n\tglobal_load_lds_dwordx4 %0, %4\n\tglobal_load_lds_dwordx4 %1, %4 offset:1024\n\t" \
    "global_load_lds_dwordx4 %2, %4 offset:2048\n\tglobal_load_lds_dwordx4 %3, %4 offset:3072" ::"v"(v0_), "v"(v1_), "v"(v2_), "v"(v3_), "s"(sb_), "s"(ld_) : "memory")
#define ST_X(b, kt) GLDS4(voffX[0], voffX[1], voffX[2], voffX[3], xb + (kt) * 128, lds0 + SX(b, 0) + wid * 4096)
#define ST_W0(wo_, kt) GLDS2(voffQ[0], voffQ[1], qb + (size_t)(kt) * 524288, lds0 + (wo_) + wid * 2048)
#define ST_W1(wo_, kt) GLDS2(voffH[0], voffH[1], hb + (size_t)(kt) * 458752, lds0 + (wo_) + 16384 + wid * 2048)

  lptr wb[4], xr[2];
#pragma unroll
  for (int m = 0; m < 4; ++m) wb[m] = lds + wr * 1024 + (g >> 1) * 2048 + (4 * (g & 1) + q4) * 128 + 32 * ((m ^ wc) ^ (2 * (g & 1) + (q4 >> 1))) + 8 * p4;
#pragma unroll
  for (int ks = 0; ks < 2; ++ks) xr[ks] = lds + 98304 + (wc * 32 + t) * 128 + 16 * ((4 * ks + g) ^ (t >> 1));
#define LDW(dst, h) do { _Pragma("unroll") for (int m = 0; m < 4; ++m) _Pragma("unroll") for (int k = 0; k < 2; ++k) dst[m][k] = ldtr2(wb[m] + (h) * 16384 + k * 8192, 4096); } while (0)
#define LDX(dst, b, h) do { _Pragma("unroll") for (int n = 0; n < 2; ++n) _Pragma("unroll") for (int k = 0; k < 2; ++k) dst[n][k] = ldb128(xr[k] + (SX(b, h) - 98304) + n * 2048); } while (0)

  f32x4 acc[2][2][4][2];
#pragma unroll
  for (int a = 0; a < 2; ++a)
#pragma unroll
    for (int b = 0; b < 2; ++b)
#pragma unroll
      for (int m = 0; m < 4; ++m)
#pragma unroll
        for (int n = 0; n < 2; ++n) acc[a][b][m][n] = (f32x4){0.f, 0.f, 0.f, 0.f};
  bf16x8 At[4][2], B0[2][2], B1[2][2];

#define MM4(ai, m, k) do { _Pragma("unroll") for (int n = 0; n < 2; ++n) { \
      acc[ai][0][m][n] = MFMA16(At[m][k], B0[n][k], acc[ai][0][m][n]); acc[ai][1][m][n] = MFMA16(At[m][k], B1[n][k], acc[ai][1][m][n]); } } while (0)
#define MMA2(ai, CLOSE) do { __builtin_amdgcn_s_setprio(1); \
    MM4(ai, 0, 0); cs[ai] = MFMA16(At[0][0], ones, cs[ai]); MM4(ai, 1, 0); MM4(ai, 0, 1); cs[ai] = MFMA16(At[0][1], ones, cs[ai]); MM4(ai, 1, 1); \
    asm volatile("" : "+v"(cs[ai])); \
    if ((ai) == 0 || full) { MM4(ai, 2, 0); MM4(ai, 3, 0); MM4(ai, 2, 1); MM4(ai, 3, 1); } \
    SCHED(); CLOSE; __builtin_amdgcn_s_setprio(0); SCHED(); } while (0)
  char* const stats = ws + WS_STATS;
  const __amdgpu_buffer_rsrc_t wrs = __builtin_amdgcn_make_buffer_rsrc(ws, 0, WS_END, 0x00020000);
  f32x4 cs[2] = {{0.f, 0.f, 0.f, 0.f}, {0.f, 0.f, 0.f, 0.f}};
  bf16x8 ones = __builtin_bit_cast(bf16x8, (u32x4){0x3f803f80u, 0x3f803f80u, 0x3f803f80u, 0x3f803f80u});
#define PIN_SETUP() do { asm volatile("" : "+v"(ones), "+v"(cs[0]), "+v"(cs[1]), "+v"(wb[0]), "+v"(wb[1]), "+v"(wb[2]), "+v"(wb[3]), "+v"(xr[0]), "+v"(xr[1])); \
    _Pragma("unroll") for (int a = 0; a < 2; ++a) _Pragma("unroll") for (int b = 0; b < 2; ++b) \
      asm volatile("" : "+v"(acc[a][b][0][0]), "+v"(acc[a][b][0][1]), "+v"(acc[a][b][1][0]), "+v"(acc[a][b][1][1]), "+v"(acc[a][b][2][0]), "+v"(acc[a][b][2][1]), "+v"(acc[a][b][3][0]), "+v"(acc[a][b][3][1])); } while (0)
  unsigned tag;
  {
    if (wr == 0) {
      unsigned ep;
      ST_X(0, 0); ST_W0(0, 0);
      GLDS4(voffX[0], voffX[1], voffX[2], voffX[3], xb + 262144, lds0 + SX(0, 1) + wid * 4096);
      GLDS2(voffQ[0], voffQ[1], qb + 262144, lds0 + 8192 + wid * 2048);
      asm volatile("global_load_dword %0, %1, %2 sc1" : "=&v"(ep) : "v"(0), "s"(ws) : "memory");
      PIN_SETUP();
      asm volatile("s_waitcnt vmcnt(0)" : "+v"(ep) :: "memory");
      tag = __builtin_amdgcn_readfirstlane(ep) + 1u;
      BAR();
    } else {
      const int row = rb * 256 + hd * 8 + wc * 2 + (lane >> 5);
      u32x4 a[4];
      unsigned ep;
      asm volatile("global_load_dword %0, %1, %2 sc1" : "=&v"(ep) : "v"(0), "s"(ws) : "memory");
      asm volatile("global_load_dwordx4 %0, %4, %5\n\tglobal_load_dwordx4 %1, %4, %5 offset:512\n\tglobal_load_dwordx4 %2, %4, %5 offset:1024\n\tglobal_load_dwordx4 %3, %4, %5 offset:1536"
                   : "=&v"(a[0]), "=&v"(a[1]), "=&v"(a[2]), "=&v"(a[3]) : "v"(row * 2048 + (lane & 31) * 16), "s"(xn) : "memory");
      ST_W1(0, 0); GLDS2(voffH[0], voffH[1], hb - 229376, lds0 + 16384 + wc * 2048);
      PIN_SETUP();
      BAR();
      ST_W0(32768, 1); GLDS2(voffQ[0], voffQ[1], qb + 524288 - 262144, lds0 + 32768 + wc * 2048);
      ST_X(1, 1); GLDS4(voffX[0], voffX[1], voffX[2], voffX[3], xb - 262144 + 128, lds0 + SX(1, 0) + wc * 4096);
      asm volatile("s_waitcnt vmcnt(16)" : "+v"(ep), "+v"(a[0]), "+v"(a[1]), "+v"(a[2]), "+v"(a[3]) :: "memory");
      tag = __builtin_amdgcn_readfirstlane(ep) + 1u;
      float v[32];
#pragma unroll
      for (int i = 0; i < 16; ++i) { v[2 * i] = bf2f(a[i >> 2][i & 3] & 0xffffu); v[2 * i + 1] = U2F(a[i >> 2][i & 3] & 0xffff0000u); }
      float s0 = 0.f, s1 = 0.f, s2 = 0.f, s3 = 0.f;
#pragma unroll
      for (int i = 0; i < 8; ++i) { s0 += v[i]; s1 += v[8 + i]; s2 += v[16 + i]; s3 += v[24 + i]; }
      const float mean = half_sum((s0 + s1) + (s2 + s3)) * (1.0f / 1024.0f);
      s0 = s1 = s2 = s3 = 0.f;
#pragma unroll
      for (int i = 0; i < 8; ++i) { const float d0 = v[i] - mean, d1 = v[8 + i] - mean, d2 = v[16 + i] - mean, d3 = v[24 + i] - mean; s0 += d0 * d0; s1 += d1 * d1; s2 += d2 * d2; s3 += d3 * d3; }
      const float sd = __builtin_amdgcn_sqrtf(half_sum((s0 + s1) + (s2 + s3)) * (1.0f / 1024.0f) + 1e-5f);
      if ((lane & 31) < 2) __hip_atomic_store((gu64*)(stats + row * 16 + (lane & 1) * 8), ((unsigned long long)tag << 32) | F2U((lane & 1) ? sd : mean), __ATOMIC_RELAXED, __HIP_MEMORY_SCOPE_AGENT);
    }
  }
  if (wr == 1) BAR();
  SCHED();
  STAMP(1);
  const int kvg = hd >> 3;
  const char* kb_g = kc + ((size_t)kvg * 2048 + 1536) * 256;
  const char* vb_g = vc + ((size_t)kvg * 2048 + 1536) * 256;
#define VOFFK ((unsigned)(((lane >> 2) & 7) * 256 + (4 * (lane >> 5) + ((lane & 3) ^ ((2 * (wid & 1) + ((lane >> 4) & 1)) & 3))) * 16))
#define VOFFV ((unsigned)(((lane >> 2) & 7) * 256 + (lane >> 5) * 64 + (lane & 3) * 16))
#define ST_KV8(voff, gsrc, slot) do { glds(voff, (gsrc) + wid * 2048, lds0 + (slot) + wid * 2048); glds(voff, (gsrc) + wid * 2048 + 128, lds0 + (slot) + wid * 2048 + 1024); } while (0)
  int wprv = 65536, wnxt = 32768;
#define W_ADV() do { const int d_ = (wnxt == 0) ? -65536 : 32768, cur_ = wnxt - d_; wnxt = wprv; wprv = cur_; \
    _Pragma("unroll") for (int f = 0; f < 4; ++f) wb[f] += d_; \
    asm volatile("" : "+v"(wb[0]), "+v"(wb[1]), "+v"(wb[2]), "+v"(wb[3])); } while (0)
#define PH_A(b, STAGE) do { LDX(B0, b, 0); LDX(B1, b, 1); SCHED(); LDW(At, 0); STAGE; WAIT_V(10); WAIT_L(12); BAR(); SCHED(); MMA2(0, WAIT_L(0); BAR()); } while (0)
#define PH_B(STAGE) do { LDW(At, 1); STAGE; W_ADV(); WAIT_V(8); BAR(); SCHED(); MMA2(1, WAIT_L(0); BAR()); } while (0)
#pragma unroll 1
  for (int kt = 0; kt < 14; kt += 2) {
    PH_A(0, ST_W1(wnxt, kt + 1); ST_W0(wprv, kt + 2));
    PH_B(ST_X(0, kt + 2));
    PH_A(1, ST_W1(wnxt, kt + 2); ST_W0(wprv, kt + 3));
    PH_B(ST_X(1, kt + 3));
  }
  STAMP(2);
  const unsigned voffK = VOFFK, voffV = VOFFV;
  PH_A(0, ST_W1(0, 15); ST_KV8(voffK, kb_g, SLOT_E));
  PH_B(ST_KV8(voffV, vb_g, SLOT_C); ST_KV8(voffV, vb_g + 16384, SLOT_C + 16384));
  const unsigned soff = (unsigned)((rb * 256 + (g >> 1) * 128 + wc * 32 + (g & 1) * 16 + t) * 16);
  u32x4 sg;
  LDX(B0, 1, 0); LDX(B1, 1, 1); SCHED(); LDW(At, 0);
  asm volatile("global_load_dwordx4 %0, %1, %2 sc1" : "=&v"(sg) : "v"(soff), "s"(stats) : "memory");
  ST_KV8(voffK, kb_g + 16384, SLOT_E + 16384); ST_KV8(voffK, kb_g + 32768, SLOT_A);
  WAIT_V(11); BAR(); SCHED(); MMA2(0, WAIT_L(0); BAR());
  LDW(At, 1);
  ST_KV8(voffK, kb_g + 49152, SLOT_A + 16384);
  BAR(); SCHED();
  const lptr csw = lds + SLOT_D + (t & 3) * 8192 + wr * 4096 + wc * 64 + g * 16;
  MMA2(1, *(LAS f32x4*)csw = cs[0]; *(LAS f32x4*)(csw + 256) = cs[1]; asm volatile("s_waitcnt vmcnt(4)" : "+v"(sg) :: "memory"); WAIT_L(0); BAR());
  f32x4 cv[2][4];
#pragma unroll
  for (int a = 0; a < 2; ++a)
#pragma unroll
    for (int m = 0; m < 4; ++m) cv[a][m] = *(const LAS f32x4*)(lds + SLOT_D + wc * 8192 + wr * 4096 + a * 256 + (m ^ wc) * 64 + g * 16);
  if (__builtin_expect(__builtin_amdgcn_ballot_w64(sg[1] != tag || sg[3] != tag) != 0ull, 0)) {
    for (unsigned spins = 0; spins < 4000000u; ++spins) {
      __builtin_amdgcn_s_sleep(2);
      asm volatile("" ::: "memory");
      sg = __builtin_amdgcn_raw_buffer_load_b128(wrs, soff, WS_STATS, 16);
      if (__builtin_amdgcn_ballot_w64(sg[1] != tag || sg[3] != tag) == 0ull) break;
    }
  }
  float mu[2][2], rs[2][2], sd[2][2];
  {
    auto m16 = __builtin_amdgcn_permlane16_swap(sg[0], sg[0], false, false);
    auto s16 = __builtin_amdgcn_permlane16_swap(sg[2], sg[2], false, false);
#pragma unroll
    for (int n = 0; n < 2; ++n) {
      auto m32 = __builtin_amdgcn_permlane32_swap(m16[n], m16[n], false, false);
      auto s32 = __builtin_amdgcn_permlane32_swap(s16[n], s16[n], false, false);
#pragma unroll
      for (int bj = 0; bj < 2; ++bj) { mu[bj][n] = -U2F(m32[bj]); sd[bj][n] = U2F(s32[bj]); rs[bj][n] = __builtin_amdgcn_rcpf(sd[bj][n]) * 1.4426950408889634f; }
    }
  }
  WAIT_L(0);
  STAMP(3);
  {
    const lptr qw = lds + wc * 8192 + wr * 4096 + ((g >> 1) * 32 + t) * 16 + (g & 1) * 8;
#pragma unroll
    for (int bj = 0; bj < 2; ++bj) {
      lptr qwb = qw + (bj ? SLOT_D : SLOT_B);
      asm volatile("" : "+v"(qwb));
#pragma unroll
      for (int m = 0; m < 4; ++m)
#pragma unroll
        for (int n = 0; n < 2; ++n) {
          const f32x4 v = (acc[0][bj][m][n] + cv[0][m] * mu[bj][n]) * (rs[bj][n] * 0.08838834764831845f);
          u32x2 w = {pk2(v[0], v[1]), pk2(v[2], v[3])};
          *(LAS u32x2*)(qwb + (m ^ wc) * 1024 + n * 256) = w;
        }
    }
  }
  WAIT_L(0); BAR(); SCHED();
  STAMP(4);
  bf16x8 qf[8];
  const lptr qr = lds + (wr ? SLOT_D : SLOT_B) + wc * 8192 + lane * 16;
  if (wr == 1) {
#pragma unroll
    for (int s = 0; s < 8; ++s) qf[s] = ldb128(qr + s * 1024);
  }
  {
#define ST128(...) ST128_(wrs, __VA_ARGS__)
    u32x2 hw[2][2][4];
#define SILU16(m) do { \
      const f32x4 a0 = acc[1][0][m][0] + cv[1][m] * mu[0][0], a1 = acc[1][0][m][1] + cv[1][m] * mu[0][1], a2 = acc[1][1][m][0] + cv[1][m] * mu[1][0], a3 = acc[1][1][m][1] + cv[1][m] * mu[1][1]; \
      f32x4 e0 = a0 * -rs[0][0], e1 = a1 * -rs[0][1], e2 = a2 * -rs[1][0], e3 = a3 * -rs[1][1]; SCHED(); \
      if (!light) { \
      _Pragma("unroll") for (int i = 0; i < 4; ++i) { e0[i] = __builtin_amdgcn_exp2f(e0[i]); e1[i] = __builtin_amdgcn_exp2f(e1[i]); e2[i] = __builtin_amdgcn_exp2f(e2[i]); e3[i] = __builtin_amdgcn_exp2f(e3[i]); } SCHED(); \
      e0 = e0 * sd[0][0] + sd[0][0]; e1 = e1 * sd[0][1] + sd[0][1]; e2 = e2 * sd[1][0] + sd[1][0]; e3 = e3 * sd[1][1] + sd[1][1]; SCHED(); \
      _Pragma("unroll") for (int i = 0; i < 4; ++i) { e0[i] = __builtin_amdgcn_rcpf(e0[i]); e1[i] = __builtin_amdgcn_rcpf(e1[i]); e2[i] = __builtin_amdgcn_rcpf(e2[i]); e3[i] = __builtin_amdgcn_rcpf(e3[i]); } SCHED(); \
      e0 *= a0; e1 *= a1; e2 *= a2; e3 *= a3; SCHED(); } \
      hw[0][0][m] = (u32x2){pk2(e0[0], e0[1]), pk2(e0[2], e0[3])}; hw[0][1][m] = (u32x2){pk2(e1[0], e1[1]), pk2(e1[2], e1[3])}; \
      hw[1][0][m] = (u32x2){pk2(e2[0], e2[1]), pk2(e2[2], e2[3])}; hw[1][1][m] = (u32x2){pk2(e3[0], e3[1]), pk2(e3[2], e3[3])}; SCHED(); } while (0)
    SILU16(0);
    if (wr == 0) {
      BAR(); SCHED();
#pragma unroll
      for (int s = 0; s < 8; ++s) qf[s] = ldb128(qr + s * 1024);
    }
    SILU16(1);
    const unsigned hoff = (unsigned)((rb * 16 + wc * 2) * 2048 + lane * 16);
    const int pb = (light ? 16 + 3 * hd : 4 * hd) + (wr ? 0 : 2), sw = full ? (wc >> 1) : 0, p0 = pb + sw, p1 = pb + (sw ^ 1);
#define H_OUT1(p_, a_, b_) do { _Pragma("unroll") for (int bj = 0; bj < 2; ++bj) _Pragma("unroll") for (int n = 0; n < 2; ++n) \
      ST128((u32x4){hw[bj][n][a_][0], hw[bj][n][a_][1], hw[bj][n][b_][0], hw[bj][n][b_][1]}, hoff + n * 2048, WS_H + ((p_) >> 1) * 262144 + ((p_) & 1) * 1024 + bj * 16384); } while (0)
#define H_OUT(p_, m_) do { if (wc & 1) H_OUT1(p_, (m_) + 1, m_); else H_OUT1(p_, m_, (m_) + 1); } while (0)
    H_OUT(p0, 0);
    if (full) { SILU16(2); SILU16(3); H_OUT(p1, 2); }
  }
  STAMP(5);
  if (wr == 1) { WAIT_L(0); BAR(); }
  __builtin_amdgcn_s_setprio(1);
  SCHED();
  STAMP(6);
  const int r32 = lane & 31, hh = lane >> 5;
  lptr kr[2];
#pragma unroll
  for (int e = 0; e < 2; ++e) kr[e] = lds + 2048 * (r32 >> 3) + 64 * (r32 & 7) + 16 * ((2 * e + hh) ^ ((r32 >> 2) & 3));
  const lptr vr = lds + ((lane & 3) << 3) + (((lane >> 2) & 3) << 6) + (((lane >> 4) & 1) << 5) + (hh << 8);
  f32x16 o[4], s[4];
#pragma unroll
  for (int d = 0; d < 4; ++d)
#pragma unroll
    for (int i = 0; i < 16; ++i) o[d][i] = 0.f;
  bf16x8 pf[8];
  constexpr int RD = 6;
  bf16x8 fr[RD];
  f32x4 lacc = {0.f, 0.f, 0.f, 0.f};
  const unsigned selw = (((lane >> 4) ^ (lane >> 2)) & 1) ? 0u : 0x3f803f80u;
  bf16x8 sel = __builtin_bit_cast(bf16x8, (u32x4){selw, selw, selw, selw});
  asm volatile("" : "+v"(sel));
  f32x16 negm = {0.f, 0.f, 0.f, 0.f, 0.f, 0.f, 0.f, 0.f, 0.f, 0.f, 0.f, 0.f, 0.f, 0.f, 0.f, 0.f};
#define SM_THR 60.0f
#define FRAG(i) ((i) < 32 ? ldtr2(v0 + (((i) >> 2) * 8 + ((i) & 3)) * 512, 2048) : ldb128((((((i) - 32) >> 2) & 1) ? k1 : k0) + ((i) & 3) * 8192 + (((i) - 32) >> 3) * 512))
#define XEARLY 3
#define XPRIO 1
#define YPRIO 0
#define XSEG(lo, hi, CLOSE) do { __builtin_amdgcn_s_setprio(XPRIO); \
    _Pragma("unroll") for (int i = (lo); i < (hi); ++i) { \
      if (i < 32) o[i & 3] = MFMA32(fr[i % RD], pf[(i >> 2) & 7], o[i & 3]); \
      else s[i & 3] = MFMA32(fr[i % RD], qf[((i - 32) >> 2) & 7], i < 36 ? negm : s[i & 3]); \
      if (i < 32 && (i & 3) == 1) lacc = MFMA16(sel, pf[(i >> 2) & 7], lacc); \
      if (i + RD < (hi)) fr[i % RD] = FRAG(i + RD); \
      SCHED(); \
      if (i == 63 - XEARLY) { __builtin_amdgcn_s_setprio(2); CLOSE; WAIT_L(0); BAR(); SCHED(); } } \
    __builtin_amdgcn_s_setprio(YPRIO); } while (0)
#define PREF(lo) do { _Pragma("unroll") for (int i = (lo); i < (lo) + RD; ++i) fr[i % RD] = FRAG(i); SCHED(); } while (0)
#define MX3(a, b, c) __builtin_fmaxf(__builtin_fmaxf((a), (b)), (c))
#define SOFTMAX() do { \
      float m0 = MX3(s[0][0], s[0][1], s[0][2]), m1 = MX3(s[1][0], s[1][1], s[1][2]), m2 = MX3(s[2][0], s[2][1], s[2][2]), m3 = MX3(s[3][0], s[3][1], s[3][2]); \
      _Pragma("unroll") for (int i = 3; i < 15; i += 2) { m0 = MX3(m0, s[0][i], s[0][i + 1]); m1 = MX3(m1, s[1][i], s[1][i + 1]); m2 = MX3(m2, s[2][i], s[2][i + 1]); m3 = MX3(m3, s[3][i], s[3][i + 1]); } \
      m0 = MX3(m0, s[0][15], s[1][15]); m2 = MX3(m2, s[2][15], s[3][15]); \
      float mx = __builtin_fmaxf(MX3(m0, m1, m2), m3); \
        \
      if (__builtin_expect(__builtin_amdgcn_ballot_w64(!(mx <= SM_THR)) != 0ull, 0)) { \
        auto rr = __builtin_amdgcn_permlane32_swap(F2U(mx), F2U(mx), false, false); \
        const float dl = MX3(U2F(rr[0]), U2F(rr[1]), 0.f); \
        _Pragma("unroll") for (int kb = 0; kb < 4; ++kb) s[kb] -= dl; \
        negm -= dl; \
        const float alpha = __builtin_amdgcn_exp2f(-dl); lacc *= alpha; _Pragma("unroll") for (int d = 0; d < 4; ++d) o[d] *= alpha; \
      } \
      SCHED(); \
      _Pragma("unroll") for (int kb = 0; kb < 4; ++kb) { \
        _Pragma("unroll") for (int e = 0; e < 2; ++e) { \
          _Pragma("unroll") for (int i = 0; i < 8; ++i) s[kb][8 * e + i] = __builtin_amdgcn_exp2f(s[kb][8 * e + i]); \
          u32x4 w = {pk2(s[kb][8 * e], s[kb][8 * e + 1]), pk2(s[kb][8 * e + 2], s[kb][8 * e + 3]), pk2(s[kb][8 * e + 4], s[kb][8 * e + 5]), pk2(s[kb][8 * e + 6], s[kb][8 * e + 7])}; \
          pf[2 * kb + e] = __builtin_bit_cast(bf16x8, w); } \
        asm volatile("" : "+v"(pf[2 * kb]), "+v"(pf[2 * kb + 1])); \
        SCHED(); } } while (0)
  {
    const lptr k0 = kr[0] + SLOT_E, k1 = kr[1] + SLOT_E, v0 = vr;
    {
      bf16x8 kf[8];
#pragma unroll
      for (int st = 0; st < 8; ++st) kf[st] = FRAG(32 + 4 * st);
      PREF(32);
#pragma unroll
      for (int st = 0; st < 8; ++st) negm = MFMA32(kf[st], qf[st], negm);
      float m0 = MX3(negm[0], negm[1], negm[2]), m1 = MX3(negm[3], negm[4], negm[5]);
      m0 = MX3(m0, negm[6], negm[7]); m1 = MX3(m1, negm[8], negm[9]); m0 = MX3(m0, negm[10], negm[11]); m1 = MX3(m1, negm[12], negm[13]);
      m0 = __builtin_fmaxf(MX3(m0, negm[14], negm[15]), m1);
      auto rr = __builtin_amdgcn_permlane32_swap(F2U(m0), F2U(m0), false, false);
      m0 = -__builtin_fmaxf(U2F(rr[0]), U2F(rr[1]));
#pragma unroll
      for (int i = 0; i < 16; ++i) negm[i] = m0;
      asm volatile("" : "+v"(negm));
      SCHED();
    }
    XSEG(32, 64, WAIT_V(4));
  }
  STAMP(7);
  const char* kvsrc = (wr ? kb_g + 65536 : vb_g + 32768) + wc * 8192 - 1024;
  unsigned kvo[2][2];
#pragma unroll
  for (int par = 0; par < 2; ++par) {
    const int xx = wr ? ((lane & 3) ^ ((2 * par + ((lane >> 4) & 1)) & 3)) : (lane & 3);
    kvo[par][0] = (unsigned)(((lane >> 2) & 7) * 256 + (lane >> 5) * 64 + xx * 16 + 1024);
    kvo[par][1] = kvo[par][0] + 128 - 1024;
  }
  const unsigned pfo = (unsigned)((lane < 21 ? lane : 20) * 128);
  unsigned pft = 0;
#pragma unroll 1
  for (int c = 0; c < 3; ++c) {
    if (c == 0) STAMP(8); else if (c == 1) STAMP(12);
    if (c == 1 && !light && wid == 4 && lane == 0) __builtin_amdgcn_raw_buffer_store_b32(tag, wrs, 0, WS_HFLAG + (rb * 32 + hd) * 128, 16);
    if (c + wr < 3) {
      const int slot = wr ? (c == 0 ? SLOT_B : SLOT_C) : (c == 0 ? SLOT_D : (c == 1 ? SLOT_E : SLOT_A));
#pragma unroll
      for (int hf = 0; hf < 2; ++hf)
        asm volatile("s_mov_b32 m0, %5\n\ts_nop 0\n\t"
                     "global_load_lds_dwordx4 %0, %4\n\tglobal_load_lds_dwordx4 %1, %4 offset:1024\n\t"
                     "global_load_lds_dwordx4 %2, %4 offset:2048\n\tglobal_load_lds_dwordx4 %3, %4 offset:3072"
                     ::"v"(kvo[0][0]), "v"(kvo[0][1]), "v"(kvo[1][0]), "v"(kvo[1][1]), "s"(kvsrc + c * 32768 + hf * 4096), "s"(lds0 + slot + wc * 8192 + hf * 4096) : "memory");
    }
    {
      const int sl = (bid * 8 + wid) * 3 + c;
      const int o1 = sl * 2688, o2 = (sl - 3121) * 2688;
      const char* pp = sl < 3121 ? wo + (o1 < 8385920 ? o1 : 8385920) : w2 + (o2 < 7337344 ? o2 : 7337344);
      asm volatile("global_load_dword %0, %1, %2" : "+v"(pft) : "v"(pfo), "s"(pp) : "memory");
    }
    SOFTMAX();
    const int kslot = (c == 0) ? SLOT_A : ((c == 1) ? SLOT_B : SLOT_C);
    const int vslot = (c == 0) ? SLOT_C : ((c == 1) ? SLOT_D : SLOT_E);
    const lptr k0 = kr[0] + kslot, k1 = kr[1] + kslot, v0 = vr + vslot;
    PREF(0);
    if (c == 0) STAMP(9); else if (c == 1) STAMP(13);
    BAR(); SCHED();
    if (c == 0) STAMP(10); else if (c == 1) STAMP(14);
    XSEG(0, 64, WAIT_V(1); if (c == 0) WAIT_V(0));
    if (c == 0) STAMP(11); else if (c == 2) STAMP(15);
  }
  STAMP(16);
  SOFTMAX();
  {
    const lptr v0 = vr + SLOT_A;
#define FRAGD(i) ldtr2(v0 + (((i) & 7) * 8 + ((i) >> 3)) * 512, 2048)
#pragma unroll
    for (int i = 0; i < RD; ++i) fr[i] = FRAGD(i);
    asm volatile("s_waitcnt vmcnt(0)" :: "v"(pft) : "memory");
    SCHED();
    STAMP(17);
    if (wr == 0) BAR();
    SCHED();
    STAMP(18);
    __builtin_amdgcn_s_setprio(1);
    const unsigned oo = (unsigned)(WS_O + 2 * hd * 262144 + (rb * 16 + wr * 8 + wc * 2 + (r32 >> 4)) * 2048 + (hh * 16 + (r32 & 15)) * 16);
    float inv = 0.f;
    u32x4 ow;
#pragma unroll
    for (int i = 0; i < 42; ++i) {
      if (i < 32) o[i >> 3] = MFMA32(fr[i % RD], pf[i & 7], o[i >> 3]);
      if (i < 8) lacc = MFMA16(sel, pf[i], lacc);
      if (i + RD < 32) fr[i % RD] = FRAGD(i + RD);
      if (i == 9) inv = __builtin_amdgcn_rcpf(lacc[0]);
      if (i >= 10) {
        const int d = (i - 10) >> 3, q = (i - 10) & 7;
        ow[q & 3] = pk2(o[d][2 * q] * inv, o[d][2 * q + 1] * inv);
        asm volatile("" : "+v"(ow));
        if ((q & 3) == 3) ST128(ow, oo + (d & 1) * 1024 + (q >> 2) * 512, (d >> 1) * 262144);
      }
      SCHED();
    }
    __builtin_amdgcn_s_setprio(0);
  }
  STAMP(19);
  int lane2 = __builtin_amdgcn_mbcnt_hi(~0u, __builtin_amdgcn_mbcnt_lo(~0u, 0u));
  asm volatile("" : "+v"(lane2));
#define lane lane2
  const int isG = (idx >> 1) & 1, kh = xcd & 1, tm = 4 * (xcd >> 1) + ((idx >> 1) & 2) + (idx & 1), tn = 2 * (idx >> 3) + kh;
#define KDS 1
#define KDG 1
  const int kd = isG ? KDG : KDS, sd2 = kh ? -kd : kd;
  const int npw = (isG ? 48 : 56) + 2 * sd2;
  const int k2c = ((npw + 19) % 6 < 4) ? K2_C : 0;
#define O_FLAG() do { if (wid == 0 && lane == 0) __builtin_amdgcn_raw_buffer_store_b32(tag, wrs, 0, WS_OFLAG + (rb * 32 + hd) * OFS, 16); } while (0)
#define POLL32(off_, slp) do { for (unsigned spins = 0; spins < 2000000u; ++spins) { \
      if (__builtin_amdgcn_ballot_w64(__builtin_amdgcn_raw_buffer_load_b32(wrs, (off_), 0, 16) != tag) == 0ull) break; \
      __builtin_amdgcn_s_sleep(slp); asm volatile("" ::: "memory"); } \
    asm volatile("" ::: "memory"); } while (0)
  const int c8 = lane & 15, r0 = wc * 4 + (lane >> 4);
  const unsigned goff = (unsigned)((tm * 128 + r0) * 2048 + tn * 256 + c8 * 16);
  const lptr cb = lds + k2c + r0 * 512;
#define G_FIN() do { f32x4 a0[4], a1[4]; \
      _Pragma("unroll") for (int i = 0; i < 4; ++i) { a0[i] = *(const LAS f32x4*)(cb + (wr * 4 + i) * 8192 + (((2 * c8) ^ (r0 & 7)) << 4)); a1[i] = *(const LAS f32x4*)(cb + (wr * 4 + i) * 8192 + (((2 * c8 + 1) ^ (r0 & 7)) << 4)); } \
      _Pragma("unroll") for (int i = 0; i < 4; ++i) \
        ST128((u32x4){pk2(silu(a0[i][0]), silu(a0[i][1])), pk2(silu(a0[i][2]), silu(a0[i][3])), pk2(silu(a1[i][0]), silu(a1[i][1])), pk2(silu(a1[i][2]), silu(a1[i][3]))}, goff + i * 32768, WS_G + wr * 131072); \
      WAIT_V(0); BAR(); } while (0)
  const unsigned f1 = isG ? (unsigned)(WS_HFLAG + ((tm >> 1) * 32 + (lane & 15)) * 128) : (unsigned)(WS_OFLAG + ((tm >> 1) * 32 + 16 + (lane & 15)) * OFS);
  STAMP(20);
  if (wr == 0) {
    const char* ab = ws + (isG ? WS_H : WS_O) + tm * 16384 + wc * 4096;
    const char* wbp = (isG ? w2 : wo) + (tn >> 1) * 512 - 3072 + (isG ? wc : (((wc & 1) << 1) | (wc >> 1))) * 16384;
    const unsigned voffA0 = lane * 16, voffA1 = lane * 16 + 1024;
    unsigned voffW[4];
    {
      const int r = lane >> 3, c = lane & 7;
      const unsigned lp = (unsigned)(r * 2048 + ((c ^ (((r >> 1) & 3) << 1)) << 4) + 3072);
      voffW[0] = lp + kh * 256; voffW[1] = voffW[0] + 128 - 1024; voffW[2] = lp + (kh ^ 1) * 256 - 2048; voffW[3] = voffW[2] + 128 - 1024;
    }
    const char *ap, *wp;
    unsigned dst = lds0 + 4 * K2_STG;
#define K2_RUN(k_) do { ap = ab + (size_t)(k_) * 262144; wp = wbp + (size_t)(k_) * 131072; } while (0)
#define K2_ISSUE(WIDE, ks) do { \
      asm volatile("s_mov_b32 m0, %3\n\ts_nop 0\n\tglobal_load_lds_dwordx4 %0, %2 sc1\n\tglobal_load_lds_dwordx4 %1, %2 offset:1024 sc1" \
                   ::"v"(voffA0), "v"(voffA1), "s"(ap + (ks) * 1024), "s"(dst + wc * 2048) : "memory"); \
      if (WIDE) GLDS4(voffW[0], voffW[1], voffW[2], voffW[3], wp + (ks) * 65536, dst + 8192 + wc * 4096); \
      else GLDS2(voffW[0], voffW[1], wp + (ks) * 65536, dst + 8192 + wc * 4096); \
      dst = (dst == lds0 + 5 * K2_STG) ? lds0 : dst + K2_STG; \
      if (ks) { ap += 262144; wp += 131072; } } while (0)
#define K2_STEP(WIDE, ks, EVENT) do { WAIT_V((WIDE) ? 18 : 12); BAR(); EVENT; K2_ISSUE(WIDE, ks); } while (0)
#define K2_TILES(WIDE, n_) do { _Pragma("unroll 1") for (int i_ = 0; i_ < (n_); ++i_) { K2_STEP(WIDE, 0, ); K2_STEP(WIDE, 1, ); } } while (0)
#define LOOK(f_) asm volatile("buffer_load_dword %0, %1, %2, 0 offen sc1" : "+v"(pol) : "v"(f_), "s"(wrs) : "memory")
    const unsigned f2 = isG ? f1 + 16 * 128 : f1 - 16 * OFS;
    const unsigned f3 = (unsigned)(WS_PFLAG + (bid ^ 1) * 128);
    u32x4 xh[12];
#define HPO(i) (HPS(i) + lane * 16)
#define HPS(i) ((unsigned)(WS_H + ((16 + 3 * hd + ((i) >> 2)) >> 1) * 262144 + ((16 + 3 * hd + ((i) >> 2)) & 1) * 1024 + (((i) >> 1) & 1) * 16384 + (rb * 16 + wc * 2 + ((i) & 1)) * 2048))
#define HP_SILU(i) do { asm volatile("" : "+v"(xh[i])); if (isG) { float tv[8], ev[8]; \
      _Pragma("unroll") for (int e = 0; e < 4; ++e) { tv[2 * e] = bf2f(xh[i][e] & 0xffffu); tv[2 * e + 1] = U2F(xh[i][e] & 0xffff0000u); } SCHED(); \
      _Pragma("unroll") for (int e = 0; e < 8; ++e) ev[e] = __builtin_amdgcn_exp2f(tv[e]); SCHED(); \
      _Pragma("unroll") for (int e = 0; e < 8; ++e) ev[e] = ev[e] * -1.4426950408889634f - 1.4426950408889634f; SCHED(); \
      _Pragma("unroll") for (int e = 0; e < 8; ++e) ev[e] = __builtin_amdgcn_rcpf(ev[e]); SCHED(); \
      ST128((u32x4){pk2(ev[0] * tv[0], ev[1] * tv[1]), pk2(ev[2] * tv[2], ev[3] * tv[3]), pk2(ev[4] * tv[4], ev[5] * tv[5]), pk2(ev[6] * tv[6], ev[7] * tv[7])}, HPO(i), 0); SCHED(); } } while (0)
    const __amdgpu_buffer_rsrc_t xrs = __builtin_amdgcn_make_buffer_rsrc(isG ? ws : (char*)xn, 0, isG ? WS_END : 2048 * 2048, 0x00020000);
    const unsigned xvb = isG ? (unsigned)(lane * 16) : goff;
#define XH_LD(i) asm volatile("buffer_load_dwordx4 %0, %1, %2, %3 offen sc1" : "=v"(xh[i]) : "v"(xvb), "s"(xrs), "s"(((i) & 7) * 32768 + (-isG & ((int)HPS(i) - ((i) & 7) * 32768))) : "memory")
    POLL32(f1, 4);
    WAIT_V(0);
    if (isG) { BAR(); O_FLAG(); }
    K2_RUN(isG ? 16 * kh : 32 + 14 * kh);
    K2_ISSUE(1, 0); K2_ISSUE(1, 1);
    STAMP(21);
    WAIT_V(6); BAR();
    K2_ISSUE(1, 0); K2_ISSUE(1, 1); K2_ISSUE(1, 0);
    STAMP(22);
    unsigned pol = 0;
    K2_STEP(1, 1, );
#pragma unroll
    for (int i = 0; i < 16; i += 2) {
      K2_STEP(1, 0, if (i < 12) XH_LD(i < 12 ? i : 0); if (i == 8 && !isG) O_FLAG()); if (i >= 4) HP_SILU(i >= 4 ? i - 4 : 0);
      K2_STEP(1, 1, if (i < 12) XH_LD(i < 12 ? i + 1 : 0)); if (i >= 4) HP_SILU(i >= 4 ? i - 3 : 0);
    }
    if (isG) WAIT_V(0);
    K2_TILES(1, 1);
    if (isG && wid == 0 && lane == 0) __builtin_amdgcn_raw_buffer_store_b32(tag, wrs, 0, WS_HFLAG + (rb * 32 + hd) * 128, 16);
    K2_TILES(1, isG ? 2 : 0);
    LOOK(f2);
    K2_TILES(1, 2);
    K2_RUN((isG ? 32 : 0) + ((isG ? 8 : 14) + kd) * kh);
    K2_STEP(1, 0, asm volatile("" : "+v"(pol)); if (__builtin_amdgcn_ballot_w64(pol != tag) != 0ull) POLL32(f2, 4));
    K2_STEP(1, 1, );
    K2_TILES(1, (isG ? 7 : 13) + sd2);
    K2_RUN(isG ? 48 : 60);
    K2_TILES(0, 4);
    if (!isG) K2_RUN(28);
    K2_STEP(0, 0, );
    K2_STEP(0, 1, if (wid == 0 && lane == 0) __builtin_amdgcn_raw_buffer_store_b32(tag, wrs, 0, WS_PFLAG + bid * 128, 16));
    K2_STEP(0, 0, ); K2_STEP(0, 1, );
    const unsigned f4 = (lane & 1) ? (unsigned)(WS_GFLAG + (tm * 8 + tn) * 128) : f3;
    K2_STEP(0, 0, LOOK(f4)); K2_STEP(0, 1, ); K2_STEP(0, 0, );
    WAIT_V(12);
    asm volatile("" : "+v"(pol));
    const unsigned long long miss = __builtin_amdgcn_ballot_w64(pol != tag);
    if (wid == 0 && (miss & 1ull)) POLL32(f3, 2);
    BAR();
    K2_ISSUE(0, 1);
    WAIT_V(12); BAR();
    WAIT_V(8); BAR();
    if (isG) {
      WAIT_V(4); BAR();
      WAIT_V(0); BAR();
      BAR();
      STAMP(23);
      BAR();
    } else {
      u32x4 gv[8];
      const __amdgpu_buffer_rsrc_t ors = __builtin_amdgcn_make_buffer_rsrc(out, 0, 2048 * 2048, 0x00020000);
#define LD8(dst_, rs_, so_, pol_) asm volatile("buffer_load_dwordx4 %0, %8, %10, %11 offen" pol_ "\n\tbuffer_load_dwordx4 %1, %9, %10, %11 offen" pol_ "\n\t" \
        "buffer_load_dwordx4 %2, %8, %10, %12 offen" pol_ "\n\tbuffer_load_dwordx4 %3, %9, %10, %12 offen" pol_ "\n\t" \
        "buffer_load_dwordx4 %4, %8, %10, %13 offen" pol_ "\n\tbuffer_load_dwordx4 %5, %9, %10, %13 offen" pol_ "\n\t" \
        "buffer_load_dwordx4 %6, %8, %10, %14 offen" pol_ "\n\tbuffer_load_dwordx4 %7, %9, %10, %14 offen" pol_ \
        : "=&v"(dst_[0]), "=&v"(dst_[1]), "=&v"(dst_[2]), "=&v"(dst_[3]), "=&v"(dst_[4]), "=&v"(dst_[5]), "=&v"(dst_[6]), "=&v"(dst_[7]) \
        : "v"(goff), "v"(goff + 32768), "s"(rs_), "s"((so_)), "s"((so_) + 65536), "s"((so_) + 131072), "s"((so_) + 196608) : "memory")
      if (miss & 2ull) {
        WAIT_V(4); BAR(); WAIT_V(0); BAR(); BAR();
        POLL32((unsigned)(WS_GFLAG + (tm * 8 + tn) * 128), 1);
      }
      LD8(gv, wrs, WS_G, " sc1");
      if (!(miss & 2ull)) { WAIT_V(12); BAR(); WAIT_V(8); BAR(); BAR(); }
      float xf[8][8];
#pragma unroll
      for (int i = 0; i < 8; ++i) {
#pragma unroll
        for (int e = 0; e < 4; ++e) { xf[i][2 * e] = bf2f(xh[i][e] & 0xffffu); xf[i][2 * e + 1] = U2F(xh[i][e] & 0xffff0000u); }
        asm volatile("" : "+v"(xf[i][0]), "+v"(xf[i][1]), "+v"(xf[i][2]), "+v"(xf[i][3]), "+v"(xf[i][4]), "+v"(xf[i][5]), "+v"(xf[i][6]), "+v"(xf[i][7]));
      }
      asm volatile("s_waitcnt vmcnt(0)" : "+v"(gv[0]), "+v"(gv[1]), "+v"(gv[2]), "+v"(gv[3]), "+v"(gv[4]), "+v"(gv[5]), "+v"(gv[6]), "+v"(gv[7]) :: "memory");
      float gf2[8][8];
#pragma unroll
      for (int i = 0; i < 8; ++i) {
#pragma unroll
        for (int e = 0; e < 4; ++e) { gf2[i][2 * e] = bf2f(gv[i][e] & 0xffffu); gf2[i][2 * e + 1] = U2F(gv[i][e] & 0xffff0000u); }
        asm volatile("" : "+v"(gf2[i][0]), "+v"(gf2[i][1]), "+v"(gf2[i][2]), "+v"(gf2[i][3]), "+v"(gf2[i][4]), "+v"(gf2[i][5]), "+v"(gf2[i][6]), "+v"(gf2[i][7]));
      }
      STAMP(23);
      BAR();
      f32x4 a0[8], a1[8];
#pragma unroll
      for (int i = 0; i < 8; ++i) { a0[i] = *(const LAS f32x4*)(cb + i * 8192 + (((2 * c8) ^ (r0 & 7)) << 4)); a1[i] = *(const LAS f32x4*)(cb + i * 8192 + (((2 * c8 + 1) ^ (r0 & 7)) << 4)); }
      SCHED();
#pragma unroll
      for (int i = 0; i < 8; ++i) {
        const u32x4 w = {pk2(xf[i][0] + a0[i][0] * gf2[i][0], xf[i][1] + a0[i][1] * gf2[i][1]), pk2(xf[i][2] + a0[i][2] * gf2[i][2], xf[i][3] + a0[i][3] * gf2[i][3]),
                         pk2(xf[i][4] + a1[i][0] * gf2[i][4], xf[i][5] + a1[i][1] * gf2[i][5]), pk2(xf[i][6] + a1[i][2] * gf2[i][6], xf[i][7] + a1[i][3] * gf2[i][7])};
        __builtin_amdgcn_raw_buffer_store_b128(w, ors, goff + i * 32768, 0, 16);
      }
    }
  } else {
    if (isG) { WAIT_V(0); BAR(); }
    STAMP(21);
    const int wm = wc >> 1, wn = wc & 1, g = lane >> 4, t = lane & 15, q4 = (lane >> 2) & 3, p4 = lane & 3;
    lptr w2b[4];
#pragma unroll
    for (int ft = 0; ft < 4; ++ft) w2b[ft] = lds + 4 * K2_STG + 8192 + wn * 1024 + (g >> 1) * 4096 + (4 * (g & 1) + q4) * 128 + 32 * (ft ^ (2 * (g & 1) + (q4 >> 1))) + 8 * p4;
    lptr ar = lds + 4 * K2_STG + wm * 4096 + lane * 16;
    f32x4 ac2[8][4];
#pragma unroll
    for (int m = 0; m < 8; ++m)
#pragma unroll
      for (int n = 0; n < 4; ++n) ac2[m][n] = (f32x4){0.f, 0.f, 0.f, 0.f};
    bf16x8 Af[2][4];
    bf16x4 Wr[4][2];
    int slot = 4, dlt = 0;
#define K2_RW(f, hf) Wr[(f) & 3][hf] = __builtin_amdgcn_ds_read_tr16_b64_v4bf16((LAS bf16x4*)(w2b[(f) & 3] + (((f) >> 2) & 1) * 2048 + (hf) * 8192))
#define K2_W(m) __builtin_shufflevector(Wr[(m) & 3][0], Wr[(m) & 3][1], 0, 1, 2, 3, 4, 5, 6, 7)
#define K2_NEXT() do { dlt = (slot == 5) ? -5 * K2_STG : K2_STG; slot = (slot == 5) ? 0 : slot + 1; } while (0)
#define K2_ADV(x) do { x += dlt; asm volatile("" : "+v"(x)); } while (0)
#define K2_WIDE(st) do { K2_NEXT(); _Pragma("unroll") for (int i = 0; i < 32; ++i) { const int m = i >> 2, q = i & 3; \
        ac2[m][q] = MFMA16(K2_W(m), Af[st][q], ac2[m][q]); \
        if (q < 2) K2_RW((m + 3) & 7, q); \
        if (q == 2 && m >= 3 && m < 7) Af[(st) ^ 1][m - 3] = ldb128(ar + (m - 3) * 1024); \
        if (q == 3 && m == 0) K2_ADV(ar); \
        if (q == 3 && m >= 1 && m < 5) K2_ADV(w2b[m - 1]); \
        if (i == 11) BAR(); \
        SCHED(); } } while (0)
#define K2_NARROW(st) do { K2_NEXT(); _Pragma("unroll") for (int i = 0; i < 16; ++i) { const int m = i >> 2, q = i & 3; \
        ac2[m][q] = MFMA16(K2_W(m), Af[st][q], ac2[m][q]); \
        if (q < 2) K2_RW((m + 3) & 3, q); \
        if (q >= 2 && (m == 1 || m == 2)) Af[(st) ^ 1][2 * m + q - 4] = ldb128(ar + (2 * m + q - 4) * 1024); \
        if (i == 2) { K2_ADV(ar); K2_ADV(w2b[0]); } \
        if (i == 3) BAR(); \
        if (i == 5) K2_ADV(w2b[1]); \
        if (i == 9) K2_ADV(w2b[2]); \
        if (i == 14) K2_ADV(w2b[3]); \
        SCHED(); } } while (0)
    BAR();
    STAMP(22);
#pragma unroll
    for (int i = 0; i < 4; ++i) { if (i < 3) { K2_RW(i, 0); K2_RW(i, 1); } Af[0][i] = ldb128(ar + i * 1024); SCHED(); }
#pragma unroll 1
    for (int p = 0; p < 8; p += 2) { K2_WIDE(0); K2_WIDE(1); }
    WAIT_V(0);
#pragma unroll 1
    for (int p = 8; p < npw; p += 2) { K2_WIDE(0); K2_WIDE(1); }
    const unsigned po = (unsigned)(WS_P + wc * 8192 + lane * 16);
#pragma unroll
    for (int i = 0; i < 8; ++i) {
      const f32x4 a = ac2[4 + (i >> 1)][2 * (i & 1)], b = ac2[4 + (i >> 1)][2 * (i & 1) + 1];
      ST128((u32x4){pk2(a[0], a[1]), pk2(a[2], a[3]), pk2(b[0], b[1]), pk2(b[2], b[3])}, po + i * 1024, bid * 32768);
    }
    SCHED();
#pragma unroll 1
    for (int p = 0; p < 4; p += 2) { K2_NARROW(0); K2_NARROW(1); }
    WAIT_V(0);
#pragma unroll 1
    for (int p = 4; p < 10; p += 2) { K2_NARROW(0); K2_NARROW(1); }
    K2_NARROW(0);
    u32x4 pv[8];
#pragma unroll
    for (int i = 0; i < 8; ++i) pv[i] = __builtin_amdgcn_raw_buffer_load_b128(wrs, po + i * 1024 + (bid ^ 1) * 32768, 0, 16);
    SCHED();
    K2_NARROW(1);
#pragma unroll 1
    for (int p = 12; p < 16; p += 2) { K2_NARROW(0); K2_NARROW(1); }
    STAMP(23);
    const lptr cw = lds + k2c + (wm * 64 + t) * 512;
#pragma unroll
    for (int m = 0; m < 4; ++m)
#pragma unroll
      for (int n = 0; n < 4; ++n) {
        const unsigned lo = pv[2 * m + (n >> 1)][2 * (n & 1)], hi = pv[2 * m + (n >> 1)][2 * (n & 1) + 1];
        *(LAS f32x4*)(cw + n * 8192 + (((wn * 16 + m * 4 + g) ^ (t & 7)) << 4)) = ac2[m][n] + (f32x4){bf2f(lo & 0xffffu), U2F(lo & 0xffff0000u), bf2f(hi & 0xffffu), U2F(hi & 0xffff0000u)};
      }
    WAIT_L(0); BAR();
    if (isG) G_FIN();
  STAMP(24);
  STAMP_OUT(ws + (57 << 20), 30);
    __builtin_amdgcn_endpgm();
  }
  if (isG) {
      G_FIN();
      if (wid == 0 && lane == 0) __builtin_amdgcn_raw_buffer_store_b32(tag, wrs, 0, WS_GFLAG + (tm * 8 + tn) * 128, 16);
      if (bid == 16 && wid == 0) {
        for (unsigned spins = 0; spins < 2000000u; ++spins) {
          bool ok = true;
#pragma unroll
          for (int i = 0; i < 4; ++i) ok = ok && __builtin_amdgcn_raw_buffer_load_b32(wrs, (unsigned)((lane + i * 64) * 128), WS_HFLAG, 16) == tag;
          if (__builtin_amdgcn_ballot_w64(!ok) == 0ull) break;
          __builtin_amdgcn_s_sleep(4);
          asm volatile("" ::: "memory");
        }
        asm volatile("" ::: "memory");
        if (lane == 0) __builtin_amdgcn_raw_buffer_store_b32(tag, wrs, 0, 0, 16);
      }
    }
  STAMP(24);
  STAMP_OUT(ws + (57 << 20), 30);
#undef lane
}

extern "C" void kernel_launch(void* const* d_in, const int* in_sizes, int n_in, void* d_out, int out_size, void* d_ws, size_t ws_size, hipStream_t stream) {
  const char* x = (const char*)d_in[0]; const char* kc = (const char*)d_in[1]; const char* vc = (const char*)d_in[2];
  const char* wq = (const char*)d_in[3]; const char* wo = (const char*)d_in[4]; const char* w1 = (const char*)d_in[6]; const char* w2 = (const char*)d_in[7];
  (void)hipFuncSetAttribute((const void*)mega_kernel, hipFuncAttributeMaxDynamicSharedMemorySize, K1_LDS);
  mega_kernel<<<256, 512, K1_LDS, stream>>>(x, kc, vc, wq, w1, wo, w2, (char*)d_ws, (char*)d_out);
}
```

```cpp
#include <hip/hip_runtime.h>
#include <stdint.h>

typedef unsigned short u16;
typedef __bf16 bf16x2 __attribute__((ext_vector_type(2)));
typedef __bf16 bf16x4 __attribute__((ext_vector_type(4)));
typedef __bf16 bf16x8 __attribute__((ext_vector_type(8)));
typedef float f32x2 __attribute__((ext_vector_type(2)));
typedef float f32x4 __attribute__((ext_vector_type(4)));
typedef float f32x16 __attribute__((ext_vector_type(16)));
typedef unsigned u32x2 __attribute__((ext_vector_type(2)));
typedef unsigned u32x4 __attribute__((ext_vector_type(4)));
#define LAS __attribute__((address_space(3)))
typedef LAS char* lptr;
#if defined(__HIP_DEVICE_COMPILE__) && !defined(USEPK)
#define NOPK __attribute__((target("no-packed-fp32-ops")))
#else
#define NOPK
#endif
#define DI NOPK __device__ __forceinline__
#define F2U(x) __builtin_bit_cast(unsigned, (float)(x))
#define U2F(x) __builtin_bit_cast(float, (unsigned)(x))
#define TID ((int)__builtin_amdgcn_workitem_id_x())
#define BID ((int)__builtin_amdgcn_workgroup_id_x())
#define SCHED() __builtin_amdgcn_sched_barrier(0)
#define BAR() asm volatile("s_barrier" ::: "memory")
#define WAIT_V(n) asm volatile("s_waitcnt vmcnt(%0)" :: "n"(n) : "memory")
#define WAIT_L(n) do { asm volatile("" ::: "memory"); __builtin_amdgcn_s_waitcnt(0xC07F | ((n) << 8)); asm volatile("" ::: "memory"); } while (0)
#define MFMA16(a, b, c) __builtin_amdgcn_mfma_f32_16x16x32_bf16(a, b, c, 0, 0, 0)
#define MFMA32(a, b, c) __builtin_amdgcn_mfma_f32_32x32x16_bf16(a, b, c, 0, 0, 0)

DI void glds(unsigned voff, const char* sbase, unsigned ldsaddr) {
  asm volatile("s_mov_b32 m0, %2\n\ts_nop 0\n\tglobal_load_lds_dwordx4 %0, %1" ::"v"(voff), "s"(sbase), "s"(ldsaddr) : "memory");
}
DI void glds1(unsigned voff, const char* sbase, unsigned ldsaddr) {
  asm volatile("s_mov_b32 m0, %2\n\ts_nop 0\n\tglobal_load_lds_dwordx4 %0, %1 sc1" ::"v"(voff), "s"(sbase), "s"(ldsaddr) : "memory");
}
DI unsigned pk2(float lo, float hi) { f32x2 v = {lo, hi}; return __builtin_bit_cast(unsigned, __builtin_convertvector(v, bf16x2)); }
DI float bf2f(unsigned b) { return U2F(b << 16); }
DI bf16x8 ldb128(lptr p) { return *(const LAS bf16x8*)p; }
DI bf16x8 ldtr2(lptr p, int d) {
  bf16x4 a = __builtin_amdgcn_ds_read_tr16_b64_v4bf16((LAS bf16x4*)p);
  bf16x4 b = __builtin_amdgcn_ds_read_tr16_b64_v4bf16((LAS bf16x4*)(p + d));
  return __builtin_shufflevector(a, b, 0, 1, 2, 3, 4, 5, 6, 7);
}
DI void ST128_(__amdgpu_buffer_rsrc_t r, u32x4 v, unsigned voff, int soff) { __builtin_amdgcn_raw_buffer_store_b128(v, r, voff + (unsigned)soff, 0, 16); }
DI unsigned silu_pk(unsigned w) {
  const float a = U2F(w << 16), b = U2F(w & 0xffff0000u);
  const float ea = __builtin_amdgcn_exp2f(a) * -1.4426950408889634f - 1.4426950408889634f, eb = __builtin_amdgcn_exp2f(b) * -1.4426950408889634f - 1.4426950408889634f;
  return pk2(a * __builtin_amdgcn_rcpf(ea), b * __builtin_amdgcn_rcpf(eb));
}
DI float silu(float v) { return v * __builtin_amdgcn_rcpf(1.0f + __builtin_amdgcn_exp2f(v * -1.4426950408889634f)); }

#define STAMP_DECL
#define LSTAMP(i)
#define STAMP(i)
#define STAMP_OUT(buf, n)
template <int CTRL> DI float dpp_add(float v) { return v + U2F(__builtin_amdgcn_update_dpp(0u, F2U(v), CTRL, 0xf, 0xf, true)); }
DI float half_sum(float v) {
  v = dpp_add<0xB1>(v); v = dpp_add<0x4E>(v); v = dpp_add<0x141>(v); v = dpp_add<0x140>(v);
  auto rr = __builtin_amdgcn_permlane16_swap(F2U(v), F2U(v), false, false);
  return U2F(rr[0]) + U2F(rr[1]);
}
typedef __attribute__((address_space(1))) unsigned long long gu64;

#define SX(b, h) (98304 + (b) * 32768 + (h) * 16384)
constexpr int SLOT_B = 0, SLOT_E = 32768, SLOT_A = 65536, SLOT_C = 98304, SLOT_D = 131072, K1_LDS = 163840;

#define XSWAP 0
constexpr int WS_HFLAG = 4096;
constexpr int WS_GFLAG = 65536;
constexpr int WS_OFLAG = 131072;
#define OFS 16
constexpr int WS_STATS = 1 << 20;
constexpr int WS_PFLAG = 196608;
constexpr int WS_G = 5 << 20, WS_O = 9 << 20, WS_H = 25 << 20, WS_P = 39 << 20, WS_END = 47 << 20;
constexpr int K2_STG = 24576, K2_C = 98304;

NOPK __global__ __launch_bounds__(512, 2) void mega_kernel(const char* __restrict__ xn, const char* __restrict__ kc, const char* __restrict__ vc,
                                                           const char* __restrict__ wq, const char* __restrict__ w1, const char* __restrict__ wo, const char* __restrict__ w2,
                                                           char* ws, char* __restrict__ out) {
  STAMP_DECL
  STAMP(0);
  extern __shared__ __attribute__((aligned(16))) char shm[];
  const lptr lds = (lptr)shm;
  const unsigned lds0 = (unsigned)(uintptr_t)lds;
  const int tid = TID, wid = __builtin_amdgcn_readfirstlane(tid >> 6), lane = tid & 63, wr = wid >> 2, wc = wid & 3;
  const int bid = BID ^ XSWAP, xcd = bid & 7, idx = bid >> 3, hd = (idx & 2) * 8 + 2 * xcd + (idx & 1), rb = idx >> 2;
  const int g = lane >> 4, t = lane & 15, q4 = (lane >> 2) & 3, p4 = lane & 3;

  const bool light = (idx & 2) != 0;
  unsigned voffX[4], voffQ[2], voffH[2];
  {
    const int r = lane >> 3, c = lane & 7;
#pragma unroll
    for (int i = 0; i < 4; ++i) voffX[i] = (unsigned)((8 * i + r) * 2048 + ((c ^ (4 * (i & 1) + (r >> 1))) << 4) - i * 1024);
    const int k = 32 * (wid >> 2) + 16 * (wid & 1) + 4 * ((wid >> 1) & 1) + 8 * (r >> 2) + (r & 3);
    const int cw = c ^ (((r >> 1) & 3) << 1);
    voffQ[0] = (unsigned)(k * 8192 + cw * 16 + 1024); voffQ[1] = voffQ[0] + 128 - 1024;
    voffH[0] = (unsigned)(k * 7168 + 128 + (light ? (cw & 3) : cw) * 16 + 1024);
    voffH[1] = (unsigned)(k * 7168 + cw * 16);
  }
  const char* xb = xn + (size_t)rb * 256 * 2048 + wid * 65536;
  const char* qb = wq + hd * 256 - 1024;
  const char* hb = w1 + (light ? 4096 + 192 * (hd - 16) : 256 * hd) - 1024;
  const bool full = wr == 1 || !light;
#define GLDS2(v0_, v1_, sb_, ld_) asm volatile("s_mov_b32 m0, %3\n\ts_nop 0\n\tglobal_load_lds_dwordx4 %0, %2\n\tglobal_load_lds_dwordx4 %1, %2 offset:1024" \
    ::"v"(v0_), "v"(v1_), "s"(sb_), "s"(ld_) : "memory")
#define GLDS4(v0_, v1_, v2_, v3_, sb_, ld_) asm volatile("s_mov_b32 m0, %5\n\ts_nop 0\n\tglobal_load_lds_dwordx4 %0, %4\n\tglobal_load_lds_dwordx4 %1, %4 offset:1024\n\t" \
    "global_load_lds_dwordx4 %2, %4 offset:2048\n\tglobal_load_lds_dwordx4 %3, %4 offset:3072" ::"v"(v0_), "v"(v1_), "v"(v2_), "v"(v3_), "s"(sb_), "s"(ld_) : "memory")
#define ST_X(b, kt) GLDS4(voffX[0], voffX[1], voffX[2], voffX[3], xb + (kt) * 128, lds0 + SX(b, 0) + wid * 4096)
#define ST_W0(wo_, kt) GLDS2(voffQ[0], voffQ[1], qb + (size_t)(kt) * 524288, lds0 + (wo_) + wid * 2048)
#define ST_W1(wo_, kt) GLDS2(voffH[0], voffH[1], hb + (size_t)(kt) * 458752, lds0 + (wo_) + 16384 + wid * 2048)

  lptr wb[4], xr[2];
#pragma unroll
  for (int m = 0; m < 4; ++m) wb[m] = lds + wr * 1024 + (g >> 1) * 2048 + (4 * (g & 1) + q4) * 128 + 32 * ((m ^ wc) ^ (2 * (g & 1) + (q4 >> 1))) + 8 * p4;
#pragma unroll
  for (int ks = 0; ks < 2; ++ks) xr[ks] = lds + 98304 + (wc * 32 + t) * 128 + 16 * ((4 * ks + g) ^ (t >> 1));
#define LDW(dst, h) do { _Pragma("unroll") for (int m = 0; m < 4; ++m) _Pragma("unroll") for (int k = 0; k < 2; ++k) dst[m][k] = ldtr2(wb[m] + (h) * 16384 + k * 8192, 4096); } while (0)
#define LDX(dst, b, h) do { _Pragma("unroll") for (int n = 0; n < 2; ++n) _Pragma("unroll") for (int k = 0; k < 2; ++k) dst[n][k] = ldb128(xr[k] + (SX(b, h) - 98304) + n * 2048); } while (0)

  f32x4 acc[2][2][4][2];
#pragma unroll
  for (int a = 0; a < 2; ++a)
#pragma unroll
    for (int b = 0; b < 2; ++b)
#pragma unroll
      for (int m = 0; m < 4; ++m)
#pragma unroll
        for (int n = 0; n < 2; ++n) acc[a][b][m][n] = (f32x4){0.f, 0.f, 0.f, 0.f};
  bf16x8 At[4][2], B0[2][2], B1[2][2];

#define MM1(ai, m, k, j) do { if ((j) < 2) acc[ai][0][m][(j) & 1] = MFMA16(At[m][k], B0[(j) & 1][k], acc[ai][0][m][(j) & 1]); else acc[ai][1][m][(j) & 1] = MFMA16(At[m][k], B1[(j) & 1][k], acc[ai][1][m][(j) & 1]); SCHED(); } while (0)
#define MM4(ai, m, k) do { _Pragma("unroll") for (int q = 0; q < 4; ++q) MM1(ai, m, k, ((m) & 1) ? 3 - q : q); } while (0)
#define MMA2(ai, CLOSE) do { __builtin_amdgcn_s_setprio(1); \
    cs[ai] = MFMA16(At[0][0], ones, cs[ai]); SCHED(); MM4(ai, 0, 0); MM4(ai, 1, 0); \
    if ((ai) == 0) { MM4(ai, 2, 0); MM4(ai, 3, 0); } \
    cs[ai] = MFMA16(At[0][1], ones, cs[ai]); SCHED(); MM4(ai, 0, 1); MM4(ai, 1, 1); \
    asm volatile("" : "+v"(cs[ai])); \
    if ((ai) == 0 || full) { MM4(ai, 2, 1); MM4(ai, 3, 1); if ((ai) == 1) { MM4(ai, 2, 0); MM4(ai, 3, 0); } } \
    SCHED(); CLOSE; __builtin_amdgcn_s_setprio(0); SCHED(); } while (0)
  char* const stats = ws + WS_STATS;
  const __amdgpu_buffer_rsrc_t wrs = __builtin_amdgcn_make_buffer_rsrc(ws, 0, WS_END, 0x00020000);
  f32x4 cs[2] = {{0.f, 0.f, 0.f, 0.f}, {0.f, 0.f, 0.f, 0.f}};
  bf16x8 ones = __builtin_bit_cast(bf16x8, (u32x4){0x3f803f80u, 0x3f803f80u, 0x3f803f80u, 0x3f803f80u});
#define PIN_SETUP() do { asm volatile("" : "+v"(ones), "+v"(cs[0]), "+v"(cs[1]), "+v"(wb[0]), "+v"(wb[1]), "+v"(wb[2]), "+v"(wb[3]), "+v"(xr[0]), "+v"(xr[1])); \
    _Pragma("unroll") for (int a = 0; a < 2; ++a) _Pragma("unroll") for (int b = 0; b < 2; ++b) \
      asm volatile("" : "+v"(acc[a][b][0][0]), "+v"(acc[a][b][0][1]), "+v"(acc[a][b][1][0]), "+v"(acc[a][b][1][1]), "+v"(acc[a][b][2][0]), "+v"(acc[a][b][2][1]), "+v"(acc[a][b][3][0]), "+v"(acc[a][b][3][1])); } while (0)
  unsigned tag;
  {
    if (wr == 0) {
      unsigned ep;
      ST_X(0, 0); ST_W0(0, 0);
      GLDS4(voffX[0], voffX[1], voffX[2], voffX[3], xb + 262144, lds0 + SX(0, 1) + wid * 4096);
      GLDS2(voffQ[0], voffQ[1], qb + 262144, lds0 + 8192 + wid * 2048);
      asm volatile("global_load_dword %0, %1, %2 sc1" : "=&v"(ep) : "v"(0), "s"(ws) : "memory");
      PIN_SETUP();
      asm volatile("s_waitcnt vmcnt(0)" : "+v"(ep) :: "memory");
      tag = __builtin_amdgcn_readfirstlane(ep) + 1u;
      BAR();
    } else {
      const int row = rb * 256 + hd * 8 + wc * 2 + (lane >> 5);
      u32x4 a[4];
      unsigned ep;
      asm volatile("global_load_dword %0, %1, %2 sc1" : "=&v"(ep) : "v"(0), "s"(ws) : "memory");
      asm volatile("global_load_dwordx4 %0, %4, %5\n\tglobal_load_dwordx4 %1, %4, %5 offset:512\n\tglobal_load_dwordx4 %2, %4, %5 offset:1024\n\tglobal_load_dwordx4 %3, %4, %5 offset:1536"
                   : "=&v"(a[0]), "=&v"(a[1]), "=&v"(a[2]), "=&v"(a[3]) : "v"(row * 2048 + (lane & 31) * 16), "s"(xn) : "memory");
      ST_W1(0, 0); GLDS2(voffH[0], voffH[1], hb - 229376, lds0 + 16384 + wc * 2048);
      PIN_SETUP();
      BAR();
      ST_W0(32768, 1); GLDS2(voffQ[0], voffQ[1], qb + 524288 - 262144, lds0 + 32768 + wc * 2048);
      ST_X(1, 1); GLDS4(voffX[0], voffX[1], voffX[2], voffX[3], xb - 262144 + 128, lds0 + SX(1, 0) + wc * 4096);
      asm volatile("s_waitcnt vmcnt(16)" : "+v"(ep), "+v"(a[0]), "+v"(a[1]), "+v"(a[2]), "+v"(a[3]) :: "memory");
      tag = __builtin_amdgcn_readfirstlane(ep) + 1u;
      float v[32];
#pragma unroll
      for (int i = 0; i < 16; ++i) { v[2 * i] = bf2f(a[i >> 2][i & 3] & 0xffffu); v[2 * i + 1] = U2F(a[i >> 2][i & 3] & 0xffff0000u); }
      float s0 = 0.f, s1 = 0.f, s2 = 0.f, s3 = 0.f;
#pragma unroll
      for (int i = 0; i < 8; ++i) { s0 += v[i]; s1 += v[8 + i]; s2 += v[16 + i]; s3 += v[24 + i]; }
      const float mean = half_sum((s0 + s1) + (s2 + s3)) * (1.0f / 1024.0f);
      s0 = s1 = s2 = s3 = 0.f;
#pragma unroll
      for (int i = 0; i < 8; ++i) { const float d0 = v[i] - mean, d1 = v[8 + i] - mean, d2 = v[16 + i] - mean, d3 = v[24 + i] - mean; s0 += d0 * d0; s1 += d1 * d1; s2 += d2 * d2; s3 += d3 * d3; }
      const float sd = __builtin_amdgcn_sqrtf(half_sum((s0 + s1) + (s2 + s3)) * (1.0f / 1024.0f) + 1e-5f);
      if ((lane & 31) < 2) __hip_atomic_store((gu64*)(stats + row * 16 + (lane & 1) * 8), ((unsigned long long)tag << 32) | F2U((lane & 1) ? sd : mean), __ATOMIC_RELAXED, __HIP_MEMORY_SCOPE_AGENT);
    }
  }
  if (wr == 1) BAR();
  SCHED();
  STAMP(1);
  const int kvg = hd >> 3;
  const char* kb_g = kc + ((size_t)kvg * 2048 + 1536) * 256;
  const char* vb_g = vc + ((size_t)kvg * 2048 + 1536) * 256;
#define VOFFK ((unsigned)(((lane >> 2) & 7) * 256 + (4 * (lane >> 5) + ((lane & 3) ^ ((2 * (wid & 1) + ((lane >> 4) & 1)) & 3))) * 16))
#define VOFFV ((unsigned)(((lane >> 2) & 7) * 256 + (lane >> 5) * 64 + (lane & 3) * 16))
#define ST_KV8(voff, gsrc, slot) do { glds(voff, (gsrc) + wid * 2048, lds0 + (slot) + wid * 2048); glds(voff, (gsrc) + wid * 2048 + 128, lds0 + (slot) + wid * 2048 + 1024); } while (0)
  int wprv = 65536, wnxt = 32768;
#define W_ADV() do { const int d_ = (wnxt == 0) ? -65536 : 32768, cur_ = wnxt - d_; wnxt = wprv; wprv = cur_; \
    _Pragma("unroll") for (int f = 0; f < 4; ++f) wb[f] += d_; \
    asm volatile("" : "+v"(wb[0]), "+v"(wb[1]), "+v"(wb[2]), "+v"(wb[3])); } while (0)
#define PH_A(b, STAGE) do { LDX(B0, b, 0); LDX(B1, b, 1); SCHED(); LDW(At, 0); STAGE; WAIT_V(10); WAIT_L(12); BAR(); SCHED(); MMA2(0, WAIT_L(0); BAR()); } while (0)
#define PH_B(STAGE) do { LDW(At, 1); STAGE; W_ADV(); WAIT_V(8); BAR(); SCHED(); MMA2(1, WAIT_L(0); BAR()); } while (0)
#pragma unroll 1
  for (int kt = 0; kt < 14; kt += 2) {
    PH_A(0, ST_W1(wnxt, kt + 1); ST_W0(wprv, kt + 2));
    PH_B(ST_X(0, kt + 2));
    PH_A(1, ST_W1(wnxt, kt + 2); ST_W0(wprv, kt + 3));
    PH_B(ST_X(1, kt + 3));
  }
  STAMP(2);
  const unsigned voffK = VOFFK, voffV = VOFFV;
  PH_A(0, ST_W1(0, 15); ST_KV8(voffK, kb_g, SLOT_E));
  PH_B(ST_KV8(voffV, vb_g, SLOT_C); ST_KV8(voffV, vb_g + 16384, SLOT_C + 16384));
  const unsigned soff = (unsigned)((rb * 256 + (g >> 1) * 128 + wc * 32 + (g & 1) * 16 + t) * 16);
  u32x4 sg;
  LDX(B0, 1, 0); LDX(B1, 1, 1); SCHED(); LDW(At, 0);
  asm volatile("global_load_dwordx4 %0, %1, %2 sc1" : "=&v"(sg) : "v"(soff), "s"(stats) : "memory");
  ST_KV8(voffK, kb_g + 16384, SLOT_E + 16384); ST_KV8(voffK, kb_g + 32768, SLOT_A);
  WAIT_V(11); BAR(); SCHED(); MMA2(0, WAIT_L(0); BAR());
  LDW(At, 1);
  ST_KV8(voffK, kb_g + 49152, SLOT_A + 16384);
  BAR(); SCHED();
  const lptr csw = lds + SLOT_D + (t & 3) * 8192 + wr * 4096 + wc * 64 + g * 16;
  MMA2(1, *(LAS f32x4*)csw = cs[0]; *(LAS f32x4*)(csw + 256) = cs[1]; asm volatile("s_waitcnt vmcnt(4)" : "+v"(sg) :: "memory"); WAIT_L(0); BAR());
  f32x4 cv[2][4];
#pragma unroll
  for (int a = 0; a < 2; ++a)
#pragma unroll
    for (int m = 0; m < 4; ++m) cv[a][m] = *(const LAS f32x4*)(lds + SLOT_D + wc * 8192 + wr * 4096 + a * 256 + (m ^ wc) * 64 + g * 16);
  if (__builtin_expect(__builtin_amdgcn_ballot_w64(sg[1] != tag || sg[3] != tag) != 0ull, 0)) {
    for (unsigned spins = 0; spins < 4000000u; ++spins) {
      __builtin_amdgcn_s_sleep(2);
      asm volatile("" ::: "memory");
      sg = __builtin_amdgcn_raw_buffer_load_b128(wrs, soff, WS_STATS, 16);
      if (__builtin_amdgcn_ballot_w64(sg[1] != tag || sg[3] != tag) == 0ull) break;
    }
  }
  float mu[2][2], rs[2][2], sd[2][2];
  {
    auto m16 = __builtin_amdgcn_permlane16_swap(sg[0], sg[0], false, false);
    auto s16 = __builtin_amdgcn_permlane16_swap(sg[2], sg[2], false, false);
#pragma unroll
    for (int n = 0; n < 2; ++n) {
      auto m32 = __builtin_amdgcn_permlane32_swap(m16[n], m16[n], false, false);
      auto s32 = __builtin_amdgcn_permlane32_swap(s16[n], s16[n], false, false);
#pragma unroll
      for (int bj = 0; bj < 2; ++bj) { mu[bj][n] = -U2F(m32[bj]); sd[bj][n] = U2F(s32[bj]); rs[bj][n] = __builtin_amdgcn_rcpf(sd[bj][n]) * 1.4426950408889634f; }
    }
  }
  WAIT_L(0);
  STAMP(3);
  {
    const lptr qw = lds + wc * 8192 + wr * 4096 + ((g >> 1) * 32 + t) * 16 + (g & 1) * 8;
#pragma unroll
    for (int bj = 0; bj < 2; ++bj) {
      lptr qwb = qw + (bj ? SLOT_D : SLOT_B);
      asm volatile("" : "+v"(qwb));
#pragma unroll
      for (int m = 0; m < 4; ++m)
#pragma unroll
        for (int n = 0; n < 2; ++n) {
          const f32x4 v = (acc[0][bj][m][n] + cv[0][m] * mu[bj][n]) * (rs[bj][n] * 0.08838834764831845f);
          u32x2 w = {pk2(v[0], v[1]), pk2(v[2], v[3])};
          *(LAS u32x2*)(qwb + (m ^ wc) * 1024 + n * 256) = w;
        }
    }
  }
  WAIT_L(0); BAR(); SCHED();
  STAMP(4);
  bf16x8 qf[8];
  const lptr qr = lds + (wr ? SLOT_D : SLOT_B) + wc * 8192 + lane * 16;
  if (wr == 1) {
#pragma unroll
    for (int s = 0; s < 8; ++s) qf[s] = ldb128(qr + s * 1024);
  }
#define ST128(...) ST128_(wrs, __VA_ARGS__)
  u32x4 hq[3];
  unsigned hqo;
  {
    u32x2 hw[2][2][2];
#define SILU16(m, ACT) do { \
      const f32x4 a0 = acc[1][0][m][0] + cv[1][m] * mu[0][0], a1 = acc[1][0][m][1] + cv[1][m] * mu[0][1], a2 = acc[1][1][m][0] + cv[1][m] * mu[1][0], a3 = acc[1][1][m][1] + cv[1][m] * mu[1][1]; \
      f32x4 e0 = a0 * -rs[0][0], e1 = a1 * -rs[0][1], e2 = a2 * -rs[1][0], e3 = a3 * -rs[1][1]; SCHED(); \
      if (ACT) { \
      _Pragma("unroll") for (int i = 0; i < 4; ++i) { e0[i] = __builtin_amdgcn_exp2f(e0[i]); e1[i] = __builtin_amdgcn_exp2f(e1[i]); e2[i] = __builtin_amdgcn_exp2f(e2[i]); e3[i] = __builtin_amdgcn_exp2f(e3[i]); } SCHED(); \
      e0 = e0 * sd[0][0] + sd[0][0]; e1 = e1 * sd[0][1] + sd[0][1]; e2 = e2 * sd[1][0] + sd[1][0]; e3 = e3 * sd[1][1] + sd[1][1]; SCHED(); \
      _Pragma("unroll") for (int i = 0; i < 4; ++i) { e0[i] = __builtin_amdgcn_rcpf(e0[i]); e1[i] = __builtin_amdgcn_rcpf(e1[i]); e2[i] = __builtin_amdgcn_rcpf(e2[i]); e3[i] = __builtin_amdgcn_rcpf(e3[i]); } SCHED(); \
      e0 *= a0; e1 *= a1; e2 *= a2; e3 *= a3; SCHED(); } \
      else { _Pragma("unroll") for (int i = 0; i < 4; ++i) e3[i] = a3[i] * __builtin_amdgcn_rcpf(__builtin_amdgcn_exp2f(e3[i]) * sd[1][1] + sd[1][1]); } \
      hw[0][0][(m) & 1] = (u32x2){pk2(e0[0], e0[1]), pk2(e0[2], e0[3])}; hw[0][1][(m) & 1] = (u32x2){pk2(e1[0], e1[1]), pk2(e1[2], e1[3])}; \
      hw[1][0][(m) & 1] = (u32x2){pk2(e2[0], e2[1]), pk2(e2[2], e2[3])}; hw[1][1][(m) & 1] = (u32x2){pk2(e3[0], e3[1]), pk2(e3[2], e3[3])}; SCHED(); } while (0)
#define HFRAG(bj, n) ((wc & 1) ? (u32x4){hw[bj][n][1][0], hw[bj][n][1][1], hw[bj][n][0][0], hw[bj][n][0][1]} : (u32x4){hw[bj][n][0][0], hw[bj][n][0][1], hw[bj][n][1][0], hw[bj][n][1][1]})
    const unsigned hoff = (unsigned)(WS_H + (rb * 16 + wc * 2) * 2048 + lane * 16);
    const int pb = (light ? 16 + 3 * hd : 4 * hd) + (wr ? 0 : 2), sw = full ? (wc >> 1) : 0, p0 = pb + sw, p1 = pb + (sw ^ 1);
    SILU16(0, false); SILU16(1, false);
    hqo = hoff + (p0 >> 1) * 262144 + (p0 & 1) * 1024;
    hq[0] = HFRAG(0, 0); hq[1] = HFRAG(0, 1); hq[2] = HFRAG(1, 0);
    ST128(HFRAG(1, 1), hqo + 2048, 16384);
    asm volatile("" : "+v"(hq[0]), "+v"(hq[1]), "+v"(hq[2]));
    if (wr == 0) {
      BAR(); SCHED();
#pragma unroll
      for (int s = 0; s < 8; ++s) qf[s] = ldb128(qr + s * 1024);
    }
    if (full) {
      SILU16(2, true); SILU16(3, true);
#pragma unroll
      for (int bj = 0; bj < 2; ++bj)
#pragma unroll
        for (int n = 0; n < 2; ++n) ST128(HFRAG(bj, n), hoff + n * 2048, (p1 >> 1) * 262144 + (p1 & 1) * 1024 + bj * 16384);
    }
  }
  STAMP(5);
  if (wr == 1) { WAIT_L(0); BAR(); }
  __builtin_amdgcn_s_setprio(1);
  SCHED();
  STAMP(6);
  const int r32 = lane & 31, hh = lane >> 5;
  lptr kr[2];
#pragma unroll
  for (int e = 0; e < 2; ++e) kr[e] = lds + 2048 * (r32 >> 3) + 64 * (r32 & 7) + 16 * ((2 * e + hh) ^ ((r32 >> 2) & 3));
  const lptr vr = lds + ((lane & 3) << 3) + (((lane >> 2) & 3) << 6) + (((lane >> 4) & 1) << 5) + (hh << 8);
  f32x16 o[4], s[4];
#pragma unroll
  for (int d = 0; d < 4; ++d)
#pragma unroll
    for (int i = 0; i < 16; ++i) o[d][i] = 0.f;
  bf16x8 pf[8];
  constexpr int RD = 6;
  bf16x8 fr[RD];
  f32x4 lacc = {0.f, 0.f, 0.f, 0.f};
  const unsigned selw = (((lane >> 4) ^ (lane >> 2)) & 1) ? 0u : 0x3f803f80u;
  bf16x8 sel = __builtin_bit_cast(bf16x8, (u32x4){selw, selw, selw, selw});
  asm volatile("" : "+v"(sel));
  f32x16 negm = {0.f, 0.f, 0.f, 0.f, 0.f, 0.f, 0.f, 0.f, 0.f, 0.f, 0.f, 0.f, 0.f, 0.f, 0.f, 0.f};
#define SM_THR 60.0f
#define FRAG(i) ((i) < 32 ? ldtr2(v0 + (((i) >> 2) * 8 + ((i) & 3)) * 512, 2048) : ldb128((((((i) - 32) >> 2) & 1) ? k1 : k0) + ((i) & 3) * 8192 + (((i) - 32) >> 3) * 512))
#define XEARLY 3
#define XPRIO 1
#define YPRIO 0
#define XSEG(lo, hi, CLOSE) do { __builtin_amdgcn_s_setprio(XPRIO); \
    _Pragma("unroll") for (int i = (lo); i < (hi); ++i) { \
      if (i < 32) o[i & 3] = MFMA32(fr[i % RD], pf[(i >> 2) & 7], o[i & 3]); \
      else s[i & 3] = MFMA32(fr[i % RD], qf[((i - 32) >> 2) & 7], i < 36 ? negm : s[i & 3]); \
      if (i < 32 && (i & 3) == 1) lacc = MFMA16(sel, pf[(i >> 2) & 7], lacc); \
      if (i + RD < (hi)) fr[i % RD] = FRAG(i + RD); \
      SCHED(); \
      if (i == 63 - XEARLY) { __builtin_amdgcn_s_setprio(2); CLOSE; WAIT_L(0); BAR(); SCHED(); } } \
    __builtin_amdgcn_s_setprio(YPRIO); } while (0)
#define PREF(lo) do { _Pragma("unroll") for (int i = (lo); i < (lo) + RD; ++i) fr[i % RD] = FRAG(i); SCHED(); } while (0)
#define MX3(a, b, c) __builtin_fmaxf(__builtin_fmaxf((a), (b)), (c))
#define SOFTMAX() do { \
      float m0 = MX3(s[0][0], s[0][1], s[0][2]), m1 = MX3(s[1][0], s[1][1], s[1][2]), m2 = MX3(s[2][0], s[2][1], s[2][2]), m3 = MX3(s[3][0], s[3][1], s[3][2]); \
      _Pragma("unroll") for (int i = 3; i < 15; i += 2) { m0 = MX3(m0, s[0][i], s[0][i + 1]); m1 = MX3(m1, s[1][i], s[1][i + 1]); m2 = MX3(m2, s[2][i], s[2][i + 1]); m3 = MX3(m3, s[3][i], s[3][i + 1]); } \
      m0 = MX3(m0, s[0][15], s[1][15]); m2 = MX3(m2, s[2][15], s[3][15]); \
      float mx = __builtin_fmaxf(MX3(m0, m1, m2), m3); \
        \
      if (__builtin_expect(__builtin_amdgcn_ballot_w64(!(mx <= SM_THR)) != 0ull, 0)) { \
        auto rr = __builtin_amdgcn_permlane32_swap(F2U(mx), F2U(mx), false, false); \
        const float dl = MX3(U2F(rr[0]), U2F(rr[1]), 0.f); \
        _Pragma("unroll") for (int kb = 0; kb < 4; ++kb) s[kb] -= dl; \
        negm -= dl; \
        const float alpha = __builtin_amdgcn_exp2f(-dl); lacc *= alpha; _Pragma("unroll") for (int d = 0; d < 4; ++d) o[d] *= alpha; \
      } \
      SCHED(); \
      _Pragma("unroll") for (int kb = 0; kb < 4; ++kb) { \
        _Pragma("unroll") for (int e = 0; e < 2; ++e) { \
          _Pragma("unroll") for (int i = 0; i < 8; ++i) s[kb][8 * e + i] = __builtin_amdgcn_exp2f(s[kb][8 * e + i]); \
          u32x4 w = {pk2(s[kb][8 * e], s[kb][8 * e + 1]), pk2(s[kb][8 * e + 2], s[kb][8 * e + 3]), pk2(s[kb][8 * e + 4], s[kb][8 * e + 5]), pk2(s[kb][8 * e + 6], s[kb][8 * e + 7])}; \
          pf[2 * kb + e] = __builtin_bit_cast(bf16x8, w); } \
        asm volatile("" : "+v"(pf[2 * kb]), "+v"(pf[2 * kb + 1])); \
        SCHED(); } } while (0)
  {
    const lptr k0 = kr[0] + SLOT_E, k1 = kr[1] + SLOT_E, v0 = vr;
    {
      bf16x8 kf[8];
#pragma unroll
      for (int st = 0; st < 8; ++st) kf[st] = FRAG(32 + 4 * st);
      PREF(32);
#pragma unroll
      for (int st = 0; st < 8; ++st) negm = MFMA32(kf[st], qf[st], negm);
      float m0 = MX3(negm[0], negm[1], negm[2]), m1 = MX3(negm[3], negm[4], negm[5]);
      m0 = MX3(m0, negm[6], negm[7]); m1 = MX3(m1, negm[8], negm[9]); m0 = MX3(m0, negm[10], negm[11]); m1 = MX3(m1, negm[12], negm[13]);
      m0 = __builtin_fmaxf(MX3(m0, negm[14], negm[15]), m1);
      auto rr = __builtin_amdgcn_permlane32_swap(F2U(m0), F2U(m0), false, false);
      m0 = -__builtin_fmaxf(U2F(rr[0]), U2F(rr[1]));
#pragma unroll
      for (int i = 0; i < 16; ++i) negm[i] = m0;
      asm volatile("" : "+v"(negm));
      SCHED();
    }
    XSEG(32, 64, WAIT_V(1));
  }
  STAMP(7);
  const char* kvsrc = (wr ? kb_g + 65536 : vb_g + 32768) + wc * 8192 - 1024;
  unsigned kvo[2][2];
#pragma unroll
  for (int par = 0; par < 2; ++par) {
    const int xx = wr ? ((lane & 3) ^ ((2 * par + ((lane >> 4) & 1)) & 3)) : (lane & 3);
    kvo[par][0] = (unsigned)(((lane >> 2) & 7) * 256 + (lane >> 5) * 64 + xx * 16 + 1024);
    kvo[par][1] = kvo[par][0] + 128 - 1024;
  }
  const unsigned pfo = (unsigned)((lane < 21 ? lane : 20) * 128);
  unsigned pft = 0;
#pragma unroll 1
  for (int c = 0; c < 3; ++c) {
    if (c == 0) STAMP(8); else if (c == 1) STAMP(12);
    ST128((u32x4){silu_pk(hq[0][0]), silu_pk(hq[0][1]), silu_pk(hq[0][2]), silu_pk(hq[0][3])}, hqo, c == 2 ? 16384 : c * 2048);
    hq[0] = hq[1]; hq[1] = hq[2];
    SCHED();
    if (c + wr < 3) {
      const int slot = wr ? (c == 0 ? SLOT_B : SLOT_C) : (c == 0 ? SLOT_D : (c == 1 ? SLOT_E : SLOT_A));
#pragma unroll
      for (int hf = 0; hf < 2; ++hf)
        asm volatile("s_mov_b32 m0, %5\n\ts_nop 0\n\t"
                     "global_load_lds_dwordx4 %0, %4\n\tglobal_load_lds_dwordx4 %1, %4 offset:1024\n\t"
                     "global_load_lds_dwordx4 %2, %4 offset:2048\n\tglobal_load_lds_dwordx4 %3, %4 offset:3072"
                     ::"v"(kvo[0][0]), "v"(kvo[0][1]), "v"(kvo[1][0]), "v"(kvo[1][1]), "s"(kvsrc + c * 32768 + hf * 4096), "s"(lds0 + slot + wc * 8192 + hf * 4096) : "memory");
    }
    {
      const int sl = (bid * 8 + wid) * 3 + c;
      const int o1 = sl * 2688, o2 = (sl - 3121) * 2688;
      const char* pp = sl < 3121 ? wo + (o1 < 8385920 ? o1 : 8385920) : w2 + (o2 < 7337344 ? o2 : 7337344);
      asm volatile("global_load_dword %0, %1, %2" : "+v"(pft) : "v"(pfo), "s"(pp) : "memory");
    }
    SOFTMAX();
    const int kslot = (c == 0) ? SLOT_A : ((c == 1) ? SLOT_B : SLOT_C);
    const int vslot = (c == 0) ? SLOT_C : ((c == 1) ? SLOT_D : SLOT_E);
    const lptr k0 = kr[0] + kslot, k1 = kr[1] + kslot, v0 = vr + vslot;
    PREF(0);
    if (c == 0) STAMP(9); else if (c == 1) STAMP(13);
    BAR(); SCHED();
    if (c == 0) STAMP(10); else if (c == 1) STAMP(14);
    XSEG(0, 64, WAIT_V(1); if (c == 2) WAIT_V(0));
    if (c == 0) STAMP(11); else if (c == 2) STAMP(15);
  }
  if (wid == 4 && lane == 0) __builtin_amdgcn_raw_buffer_store_b32(tag, wrs, 0, WS_HFLAG + (rb * 32 + hd) * 128, 16);
  STAMP(16);
  SOFTMAX();
  {
    const lptr v0 = vr + SLOT_A;
#define FRAGD(i) ldtr2(v0 + (((i) & 7) * 8 + ((i) >> 3)) * 512, 2048)
#pragma unroll
    for (int i = 0; i < RD; ++i) fr[i] = FRAGD(i);
    asm volatile("s_waitcnt vmcnt(0)" :: "v"(pft) : "memory");
    SCHED();
    STAMP(17);
    if (wr == 0) BAR();
    SCHED();
    STAMP(18);
    __builtin_amdgcn_s_setprio(1);
    const unsigned oo = (unsigned)(WS_O + 2 * hd * 262144 + (rb * 16 + wr * 8 + wc * 2 + (r32 >> 4)) * 2048 + (hh * 16 + (r32 & 15)) * 16);
    float inv = 0.f;
    u32x4 ow;
#pragma unroll
    for (int i = 0; i < 42; ++i) {
      if (i < 32) o[i >> 3] = MFMA32(fr[i % RD], pf[i & 7], o[i >> 3]);
      if (i < 8) lacc = MFMA16(sel, pf[i], lacc);
      if (i + RD < 32) fr[i % RD] = FRAGD(i + RD);
      if (i == 9) inv = __builtin_amdgcn_rcpf(lacc[0]);
      if (i >= 10) {
        const int d = (i - 10) >> 3, q = (i - 10) & 7;
        ow[q & 3] = pk2(o[d][2 * q] * inv, o[d][2 * q + 1] * inv);
        asm volatile("" : "+v"(ow));
        if ((q & 3) == 3) ST128(ow, oo + (d & 1) * 1024 + (q >> 2) * 512, (d >> 1) * 262144);
      }
      SCHED();
    }
    __builtin_amdgcn_s_setprio(0);
  }
  STAMP(19);
  int lane2 = __builtin_amdgcn_mbcnt_hi(~0u, __builtin_amdgcn_mbcnt_lo(~0u, 0u));
  asm volatile("" : "+v"(lane2));
#define lane lane2
  const int isG = (idx >> 1) & 1, kh = xcd & 1, tm = 4 * (xcd >> 1) + ((idx >> 1) & 2) + (idx & 1), tn = 2 * (idx >> 3) + kh;
#define KDS 1
#define KDG 1
  const int kd = isG ? KDG : KDS, sd2 = kh ? -kd : kd;
  const int npw = (isG ? 48 : 56) + 2 * sd2;
  const int k2c = ((npw + 19) % 6 < 4) ? K2_C : 0;
#define O_FLAG() do { if (wid == 0 && lane == 0) __builtin_amdgcn_raw_buffer_store_b32(tag, wrs, 0, WS_OFLAG + (rb * 32 + hd) * OFS, 16); } while (0)
#define POLL32(off_, slp) do { for (unsigned spins = 0; spins < 2000000u; ++spins) { \
      if (__builtin_amdgcn_ballot_w64(__builtin_amdgcn_raw_buffer_load_b32(wrs, (off_), 0, 16) != tag) == 0ull) break; \
      __builtin_amdgcn_s_sleep(slp); asm volatile("" ::: "memory"); } \
    asm volatile("" ::: "memory"); } while (0)
  const int c8 = lane & 15, r0 = wc * 4 + (lane >> 4);
  const unsigned goff = (unsigned)((tm * 128 + r0) * 2048 + tn * 256 + c8 * 16);
  const lptr cb = lds + k2c + r0 * 512;
#define G_FIN() do { f32x4 a0[4], a1[4]; \
      _Pragma("unroll") for (int i = 0; i < 4; ++i) { a0[i] = *(const LAS f32x4*)(cb + (wr * 4 + i) * 8192 + (((2 * c8) ^ (r0 & 7)) << 4)); a1[i] = *(const LAS f32x4*)(cb + (wr * 4 + i) * 8192 + (((2 * c8 + 1) ^ (r0 & 7)) << 4)); } \
      _Pragma("unroll") for (int i = 0; i < 4; ++i) \
        ST128((u32x4){pk2(silu(a0[i][0]), silu(a0[i][1])), pk2(silu(a0[i][2]), silu(a0[i][3])), pk2(silu(a1[i][0]), silu(a1[i][1])), pk2(silu(a1[i][2]), silu(a1[i][3]))}, goff + i * 32768, WS_G + wr * 131072); \
      WAIT_V(0); BAR(); } while (0)
  const unsigned f1 = isG ? (unsigned)(WS_HFLAG + ((tm >> 1) * 32 + 16 + (lane & 15)) * 128) : (unsigned)(WS_OFLAG + ((tm >> 1) * 32 + 16 + (lane & 15)) * OFS);
  STAMP(20);
  if (wr == 0) {
    const char* ab = ws + (isG ? WS_H : WS_O) + tm * 16384 + wc * 4096;
    const char* wbp = (isG ? w2 : wo) + (tn >> 1) * 512 - 3072 + (isG ? wc : (((wc & 1) << 1) | (wc >> 1))) * 16384;
    const unsigned voffA0 = lane * 16, voffA1 = lane * 16 + 1024;
    unsigned voffW[4];
    {
      const int r = lane >> 3, c = lane & 7;
      const unsigned lp = (unsigned)(r * 2048 + ((c ^ (((r >> 1) & 3) << 1)) << 4) + 3072);
      voffW[0] = lp + kh * 256; voffW[1] = voffW[0] + 128 - 1024; voffW[2] = lp + (kh ^ 1) * 256 - 2048; voffW[3] = voffW[2] + 128 - 1024;
    }
    const char *ap, *wp;
    unsigned dst = lds0 + 4 * K2_STG;
#define K2_RUN(k_) do { ap = ab + (size_t)(k_) * 262144; wp = wbp + (size_t)(k_) * 131072; } while (0)
#define K2_ISSUE(WIDE, ks) do { \
      asm volatile("s_mov_b32 m0, %3\n\ts_nop 0\n\tglobal_load_lds_dwordx4 %0, %2 sc1\n\tglobal_load_lds_dwordx4 %1, %2 offset:1024 sc1" \
                   ::"v"(voffA0), "v"(voffA1), "s"(ap + (ks) * 1024), "s"(dst + wc * 2048) : "memory"); \
      if (WIDE) GLDS4(voffW[0], voffW[1], voffW[2], voffW[3], wp + (ks) * 65536, dst + 8192 + wc * 4096); \
      else GLDS2(voffW[0], voffW[1], wp + (ks) * 65536, dst + 8192 + wc * 4096); \
      dst = (dst == lds0 + 5 * K2_STG) ? lds0 : dst + K2_STG; \
      if (ks) { ap += 262144; wp += 131072; } } while (0)
#define K2_STEP(WIDE, ks, EVENT) do { WAIT_V((WIDE) ? 18 : 12); BAR(); EVENT; K2_ISSUE(WIDE, ks); } while (0)
#define K2_TILES(WIDE, n_) do { _Pragma("unroll 1") for (int i_ = 0; i_ < (n_); ++i_) { K2_STEP(WIDE, 0, ); K2_STEP(WIDE, 1, ); } } while (0)
#define LOOK(f_) asm volatile("buffer_load_dword %0, %1, %2, 0 offen sc1" : "+v"(pol) : "v"(f_), "s"(wrs) : "memory")
    const unsigned f2 = f1 - 16 * (isG ? 128 : OFS);
    const unsigned f3 = (unsigned)(WS_PFLAG + (bid ^ 1) * 128);
    u32x4 xh[8];
#define XH_LD(i) asm volatile("global_load_dwordx4 %0, %1, %2" : "=v"(xh[i]) : "v"(goff), "s"(xn + (i) * 32768) : "memory")
    POLL32(f1, 4);
    WAIT_V(0);
    if (isG) { BAR(); O_FLAG(); }
    K2_RUN(32 + (isG ? 12 : 14) * kh);
    K2_ISSUE(1, 0); K2_ISSUE(1, 1);
    STAMP(21);
    WAIT_V(6); BAR();
    K2_ISSUE(1, 0); K2_ISSUE(1, 1); K2_ISSUE(1, 0);
    STAMP(22);
    unsigned pol = 0;
    K2_STEP(1, 1, );
    K2_TILES(1, 4);
    if (!isG) O_FLAG();
    K2_TILES(1, isG ? 3 : 5);
    LOOK(f2);
    K2_TILES(1, 2);
    K2_RUN(((isG ? 12 : 14) + kd) * kh);
    K2_STEP(1, 0, asm volatile("" : "+v"(pol)); if (__builtin_amdgcn_ballot_w64(pol != tag) != 0ull) POLL32(f2, 4));
    K2_STEP(1, 1, );
#pragma unroll
    for (int i = 0; i < 8; ++i) { K2_STEP(1, 0, XH_LD(i)); K2_STEP(1, 1, ); }
    K2_TILES(1, (isG ? 3 : 5) + sd2);
    asm volatile("" : "+v"(xh[0]), "+v"(xh[1]), "+v"(xh[2]), "+v"(xh[3]), "+v"(xh[4]), "+v"(xh[5]), "+v"(xh[6]), "+v"(xh[7]));
    K2_RUN(isG ? 24 : 60);
    K2_TILES(0, 4);
    K2_RUN(28);
    K2_STEP(0, 0, );
    K2_STEP(0, 1, if (wid == 0 && lane == 0) __builtin_amdgcn_raw_buffer_store_b32(tag, wrs, 0, WS_PFLAG + bid * 128, 16));
    K2_STEP(0, 0, ); K2_STEP(0, 1, );
    const unsigned f4 = (lane & 1) ? (unsigned)(WS_GFLAG + (tm * 8 + tn) * 128) : f3;
    K2_STEP(0, 0, LOOK(f4)); K2_STEP(0, 1, ); K2_STEP(0, 0, );
    WAIT_V(12);
    asm volatile("" : "+v"(pol));
    const unsigned long long miss = __builtin_amdgcn_ballot_w64(pol != tag);
    if (wid == 0 && (miss & 1ull)) POLL32(f3, 2);
    BAR();
    K2_ISSUE(0, 1);
    WAIT_V(12); BAR();
    WAIT_V(8); BAR();
    if (isG) {
      WAIT_V(4); BAR();
      WAIT_V(0); BAR();
      BAR();
      STAMP(23);
      BAR();
    } else {
      u32x4 gv[8];
      const __amdgpu_buffer_rsrc_t ors = __builtin_amdgcn_make_buffer_rsrc(out, 0, 2048 * 2048, 0x00020000);
#define LD8(dst_, rs_, so_, pol_) asm volatile("buffer_load_dwordx4 %0, %8, %10, %11 offen" pol_ "\n\tbuffer_load_dwordx4 %1, %9, %10, %11 offen" pol_ "\n\t" \
        "buffer_load_dwordx4 %2, %8, %10, %12 offen" pol_ "\n\tbuffer_load_dwordx4 %3, %9, %10, %12 offen" pol_ "\n\t" \
        "buffer_load_dwordx4 %4, %8, %10, %13 offen" pol_ "\n\tbuffer_load_dwordx4 %5, %9, %10, %13 offen" pol_ "\n\t" \
        "buffer_load_dwordx4 %6, %8, %10, %14 offen" pol_ "\n\tbuffer_load_dwordx4 %7, %9, %10, %14 offen" pol_ \
        : "=&v"(dst_[0]), "=&v"(dst_[1]), "=&v"(dst_[2]), "=&v"(dst_[3]), "=&v"(dst_[4]), "=&v"(dst_[5]), "=&v"(dst_[6]), "=&v"(dst_[7]) \
        : "v"(goff), "v"(goff + 32768), "s"(rs_), "s"((so_)), "s"((so_) + 65536), "s"((so_) + 131072), "s"((so_) + 196608) : "memory")
      if (miss & 2ull) {
        WAIT_V(4); BAR(); WAIT_V(0); BAR(); BAR();
        POLL32((unsigned)(WS_GFLAG + (tm * 8 + tn) * 128), 1);
      }
      LD8(gv, wrs, WS_G, " sc1");
      if (!(miss & 2ull)) { WAIT_V(12); BAR(); WAIT_V(8); BAR(); BAR(); }
      float xf[8][8];
#pragma unroll
      for (int i = 0; i < 8; ++i) {
#pragma unroll
        for (int e = 0; e < 4; ++e) { xf[i][2 * e] = bf2f(xh[i][e] & 0xffffu); xf[i][2 * e + 1] = U2F(xh[i][e] & 0xffff0000u); }
        asm volatile("" : "+v"(xf[i][0]), "+v"(xf[i][1]), "+v"(xf[i][2]), "+v"(xf[i][3]), "+v"(xf[i][4]), "+v"(xf[i][5]), "+v"(xf[i][6]), "+v"(xf[i][7]));
      }
      asm volatile("s_waitcnt vmcnt(0)" : "+v"(gv[0]), "+v"(gv[1]), "+v"(gv[2]), "+v"(gv[3]), "+v"(gv[4]), "+v"(gv[5]), "+v"(gv[6]), "+v"(gv[7]) :: "memory");
      float gf2[8][8];
#pragma unroll
      for (int i = 0; i < 8; ++i) {
#pragma unroll
        for (int e = 0; e < 4; ++e) { gf2[i][2 * e] = bf2f(gv[i][e] & 0xffffu); gf2[i][2 * e + 1] = U2F(gv[i][e] & 0xffff0000u); }
        asm volatile("" : "+v"(gf2[i][0]), "+v"(gf2[i][1]), "+v"(gf2[i][2]), "+v"(gf2[i][3]), "+v"(gf2[i][4]), "+v"(gf2[i][5]), "+v"(gf2[i][6]), "+v"(gf2[i][7]));
      }
      STAMP(23);
      BAR();
      f32x4 a0[8], a1[8];
#pragma unroll
      for (int i = 0; i < 8; ++i) { a0[i] = *(const LAS f32x4*)(cb + i * 8192 + (((2 * c8) ^ (r0 & 7)) << 4)); a1[i] = *(const LAS f32x4*)(cb + i * 8192 + (((2 * c8 + 1) ^ (r0 & 7)) << 4)); }
      SCHED();
#pragma unroll
      for (int i = 0; i < 8; ++i) {
        const u32x4 w = {pk2(xf[i][0] + a0[i][0] * gf2[i][0], xf[i][1] + a0[i][1] * gf2[i][1]), pk2(xf[i][2] + a0[i][2] * gf2[i][2], xf[i][3] + a0[i][3] * gf2[i][3]),
                         pk2(xf[i][4] + a1[i][0] * gf2[i][4], xf[i][5] + a1[i][1] * gf2[i][5]), pk2(xf[i][6] + a1[i][2] * gf2[i][6], xf[i][7] + a1[i][3] * gf2[i][7])};
        __builtin_amdgcn_raw_buffer_store_b128(w, ors, goff + i * 32768, 0, 16);
      }
    }
  } else {
    if (isG) { WAIT_V(0); BAR(); }
    STAMP(21);
    const int wm = wc >> 1, wn = wc & 1, g = lane >> 4, t = lane & 15, q4 = (lane >> 2) & 3, p4 = lane & 3;
    lptr w2b[4];
#pragma unroll
    for (int ft = 0; ft < 4; ++ft) w2b[ft] = lds + 4 * K2_STG + 8192 + wn * 1024 + (g >> 1) * 4096 + (4 * (g & 1) + q4) * 128 + 32 * (ft ^ (2 * (g & 1) + (q4 >> 1))) + 8 * p4;
    lptr ar = lds + 4 * K2_STG + wm * 4096 + lane * 16;
    f32x4 ac2[8][4];
#pragma unroll
    for (int m = 0; m < 8; ++m)
#pragma unroll
      for (int n = 0; n < 4; ++n) ac2[m][n] = (f32x4){0.f, 0.f, 0.f, 0.f};
    bf16x8 Af[2][4];
    bf16x4 Wr[4][2];
    int slot = 4, dlt = 0;
#define K2_RW(f, hf) Wr[(f) & 3][hf] = __builtin_amdgcn_ds_read_tr16_b64_v4bf16((LAS bf16x4*)(w2b[(f) & 3] + (((f) >> 2) & 1) * 2048 + (hf) * 8192))
#define K2_W(m) __builtin_shufflevector(Wr[(m) & 3][0], Wr[(m) & 3][1], 0, 1, 2, 3, 4, 5, 6, 7)
#define K2_NEXT() do { dlt = (slot == 5) ? -5 * K2_STG : K2_STG; slot = (slot == 5) ? 0 : slot + 1; } while (0)
#define K2_ADV(x) do { x += dlt; asm volatile("" : "+v"(x)); } while (0)
#define K2_WIDE(st) do { _Pragma("unroll") for (int i = 0; i < 32; ++i) { const int m = i >> 2, q = i & 3; \
        const int n_ = (m & 1) ? 3 - q : q; ac2[m][n_] = MFMA16(K2_W(m), Af[st][n_], ac2[m][n_]); SCHED(); \
        if (q < 2) K2_RW((m + 3) & 7, q); \
        if (q == 2 && m >= 3 && m < 7) Af[(st) ^ 1][m - 3] = ldb128(ar + (m - 3) * 1024); \
        if (i == 2) K2_ADV(ar); \
        if (i == 6) K2_ADV(w2b[0]); \
        if (i == 10) K2_ADV(w2b[1]); \
        if (i == 11) BAR(); \
        if (i == 19) K2_ADV(w2b[2]); \
        if (i == 23) K2_ADV(w2b[3]); \
        if (i == 30) K2_NEXT(); \
        SCHED(); } } while (0)
#define K2_NARROW(st) do { _Pragma("unroll") for (int i = 0; i < 16; ++i) { const int m = i >> 2, q = i & 3; \
        const int n_ = (m & 1) ? 3 - q : q; ac2[m][n_] = MFMA16(K2_W(m), Af[st][n_], ac2[m][n_]); SCHED(); \
        if (q < 2) K2_RW((m + 3) & 3, q); \
        if (q >= 2 && (m == 1 || m == 2)) Af[(st) ^ 1][2 * m + q - 4] = ldb128(ar + (2 * m + q - 4) * 1024); \
        if (i == 2) { K2_ADV(ar); K2_ADV(w2b[0]); } \
        if (i == 3) BAR(); \
        if (i == 5) K2_ADV(w2b[1]); \
        if (i == 9) K2_ADV(w2b[2]); \
        if (i == 14) K2_ADV(w2b[3]); \
        if (i == 15) K2_NEXT(); \
        SCHED(); } } while (0)
    K2_NEXT();
    BAR();
    STAMP(22);
#pragma unroll
    for (int i = 0; i < 4; ++i) { if (i < 3) { K2_RW(i, 0); K2_RW(i, 1); } Af[0][i] = ldb128(ar + i * 1024); SCHED(); }
#pragma unroll 1
    for (int p = 0; p < 8; p += 2) { K2_WIDE(0); K2_WIDE(1); }
    WAIT_V(0);
#pragma unroll 1
    for (int p = 8; p < npw; p += 2) { K2_WIDE(0); K2_WIDE(1); }
    const unsigned po = (unsigned)(WS_P + wc * 8192 + lane * 16);
#pragma unroll
    for (int i = 0; i < 8; ++i) {
      const f32x4 a = ac2[4 + (i >> 1)][2 * (i & 1)], b = ac2[4 + (i >> 1)][2 * (i & 1) + 1];
      ST128((u32x4){pk2(a[0], a[1]), pk2(a[2], a[3]), pk2(b[0], b[1]), pk2(b[2], b[3])}, po + i * 1024, bid * 32768);
    }
    SCHED();
#pragma unroll 1
    for (int p = 0; p < 4; p += 2) { K2_NARROW(0); K2_NARROW(1); }
    WAIT_V(0);
#pragma unroll 1
    for (int p = 4; p < 10; p += 2) { K2_NARROW(0); K2_NARROW(1); }
    K2_NARROW(0);
    u32x4 pv[8];
#pragma unroll
    for (int i = 0; i < 8; ++i) pv[i] = __builtin_amdgcn_raw_buffer_load_b128(wrs, po + i * 1024 + (bid ^ 1) * 32768, 0, 16);
    SCHED();
    K2_NARROW(1);
#pragma unroll 1
    for (int p = 12; p < 16; p += 2) { K2_NARROW(0); K2_NARROW(1); }
    STAMP(23);
    const lptr cw = lds + k2c + (wm * 64 + t) * 512;
#pragma unroll
    for (int m = 0; m < 4; ++m)
#pragma unroll
      for (int n = 0; n < 4; ++n) {
        const unsigned lo = pv[2 * m + (n >> 1)][2 * (n & 1)], hi = pv[2 * m + (n >> 1)][2 * (n & 1) + 1];
        *(LAS f32x4*)(cw + n * 8192 + (((wn * 16 + m * 4 + g) ^ (t & 7)) << 4)) = ac2[m][n] + (f32x4){bf2f(lo & 0xffffu), U2F(lo & 0xffff0000u), bf2f(hi & 0xffffu), U2F(hi & 0xffff0000u)};
      }
    WAIT_L(0); BAR();
    if (isG) G_FIN();
  STAMP(24);
  STAMP_OUT(ws + (57 << 20), 30);
    __builtin_amdgcn_endpgm();
  }
  if (isG) {
      G_FIN();
      if (wid == 0 && lane == 0) __builtin_amdgcn_raw_buffer_store_b32(tag, wrs, 0, WS_GFLAG + (tm * 8 + tn) * 128, 16);
      if (bid == 16 && wid == 0) {
        for (unsigned spins = 0; spins < 2000000u; ++spins) {
          bool ok = true;
#pragma unroll
          for (int i = 0; i < 4; ++i) ok = ok && __builtin_amdgcn_raw_buffer_load_b32(wrs, (unsigned)((lane + i * 64) * 128), WS_HFLAG, 16) == tag;
          if (__builtin_amdgcn_ballot_w64(!ok) == 0ull) break;
          __builtin_amdgcn_s_sleep(4);
          asm volatile("" ::: "memory");
        }
        asm volatile("" ::: "memory");
        if (lane == 0) __builtin_amdgcn_raw_buffer_store_b32(tag, wrs, 0, 0, 16);
      }
    }
  STAMP(24);
  STAMP_OUT(ws + (57 << 20), 30);
#undef lane
}

extern "C" void kernel_launch(void* const* d_in, const int* in_sizes, int n_in, void* d_out, int out_size, void* d_ws, size_t ws_size, hipStream_t stream) {
  const char* x = (const char*)d_in[0]; const char* kc = (const char*)d_in[1]; const char* vc = (const char*)d_in[2];
  const char* wq = (const char*)d_in[3]; const char* wo = (const char*)d_in[4]; const char* w1 = (const char*)d_in[6]; const char* w2 = (const char*)d_in[7];
  (void)hipFuncSetAttribute((const void*)mega_kernel, hipFuncAttributeMaxDynamicSharedMemorySize, K1_LDS);
  mega_kernel<<<256, 512, K1_LDS, stream>>>(x, kc, vc, wq, w1, wo, w2, (char*)d_ws, (char*)d_out);
}
```

```cpp
#include <hip/hip_runtime.h>
#include <stdint.h>

typedef unsigned short u16;
typedef __bf16 bf16x2 __attribute__((ext_vector_type(2)));
typedef __bf16 bf16x4 __attribute__((ext_vector_type(4)));
typedef __bf16 bf16x8 __attribute__((ext_vector_type(8)));
typedef float f32x2 __attribute__((ext_vector_type(2)));
typedef float f32x4 __attribute__((ext_vector_type(4)));
typedef float f32x16 __attribute__((ext_vector_type(16)));
typedef unsigned u32x2 __attribute__((ext_vector_type(2)));
typedef unsigned u32x4 __attribute__((ext_vector_type(4)));
#define LAS __attribute__((address_space(3)))
typedef LAS char* lptr;
#if defined(__HIP_DEVICE_COMPILE__) && !defined(USEPK)
#define NOPK __attribute__((target("no-packed-fp32-ops")))
#else
#define NOPK
#endif
#define DI NOPK __device__ __forceinline__
#define F2U(x) __builtin_bit_cast(unsigned, (float)(x))
#define U2F(x) __builtin_bit_cast(float, (unsigned)(x))
#define TID ((int)__builtin_amdgcn_workitem_id_x())
#define BID ((int)__builtin_amdgcn_workgroup_id_x())
#define SCHED() __builtin_amdgcn_sched_barrier(0)
#define BAR() asm volatile("s_barrier" ::: "memory")
#define WAIT_V(n) asm volatile("s_waitcnt vmcnt(%0)" :: "n"(n) : "memory")
#define WAIT_L(n) do { asm volatile("" ::: "memory"); __builtin_amdgcn_s_waitcnt(0xC07F | ((n) << 8)); asm volatile("" ::: "memory"); } while (0)
#define MFMA16(a, b, c) __builtin_amdgcn_mfma_f32_16x16x32_bf16(a, b, c, 0, 0, 0)
#define MFMA32(a, b, c) __builtin_amdgcn_mfma_f32_32x32x16_bf16(a, b, c, 0, 0, 0)

DI void glds(unsigned voff, const char* sbase, unsigned ldsaddr) {
  asm volatile("s_mov_b32 m0, %2\n\ts_nop 0\n\tglobal_load_lds_dwordx4 %0, %1" ::"v"(voff), "s"(sbase), "s"(ldsaddr) : "memory");
}
DI void glds1(unsigned voff, const char* sbase, unsigned ldsaddr) {
  asm volatile("s_mov_b32 m0, %2\n\ts_nop 0\n\tglobal_load_lds_dwordx4 %0, %1 sc1" ::"v"(voff), "s"(sbase), "s"(ldsaddr) : "memory");
}
DI unsigned pk2(float lo, float hi) { f32x2 v = {lo, hi}; return __builtin_bit_cast(unsigned, __builtin_convertvector(v, bf16x2)); }
DI float bf2f(unsigned b) { return U2F(b << 16); }
DI bf16x8 ldb128(lptr p) { return *(const LAS bf16x8*)p; }
DI bf16x8 ldtr2(lptr p, int d) {
  bf16x4 a = __builtin_amdgcn_ds_read_tr16_b64_v4bf16((LAS bf16x4*)p);
  bf16x4 b = __builtin_amdgcn_ds_read_tr16_b64_v4bf16((LAS bf16x4*)(p + d));
  return __builtin_shufflevector(a, b, 0, 1, 2, 3, 4, 5, 6, 7);
}
DI void ST128_(__amdgpu_buffer_rsrc_t r, u32x4 v, unsigned voff, int soff) { __builtin_amdgcn_raw_buffer_store_b128(v, r, voff + (unsigned)soff, 0, 16); }
DI unsigned silu_pk(unsigned w) {
  const float a = U2F(w << 16), b = U2F(w & 0xffff0000u);
  const float ea = __builtin_amdgcn_exp2f(a) * -1.4426950408889634f - 1.4426950408889634f, eb = __builtin_amdgcn_exp2f(b) * -1.4426950408889634f - 1.4426950408889634f;
  return pk2(a * __builtin_amdgcn_rcpf(ea), b * __builtin_amdgcn_rcpf(eb));
}
DI float silu(float v) { return v * __builtin_amdgcn_rcpf(1.0f + __builtin_amdgcn_exp2f(v * -1.4426950408889634f)); }

#define STAMP_DECL
#define LSTAMP(i)
#define STAMP(i)
#define STAMP_OUT(buf, n)
template <int CTRL> DI float dpp_add(float v) { return v + U2F(__builtin_amdgcn_update_dpp(0u, F2U(v), CTRL, 0xf, 0xf, true)); }
DI float half_sum(float v) {
  v = dpp_add<0xB1>(v); v = dpp_add<0x4E>(v); v = dpp_add<0x141>(v); v = dpp_add<0x140>(v);
  auto rr = __builtin_amdgcn_permlane16_swap(F2U(v), F2U(v), false, false);
  return U2F(rr[0]) + U2F(rr[1]);
}
typedef __attribute__((address_space(1))) unsigned long long gu64;

#define SX(b, h) (98304 + (b) * 32768 + (h) * 16384)
constexpr int SLOT_B = 0, SLOT_E = 32768, SLOT_A = 65536, SLOT_C = 98304, SLOT_D = 131072, K1_LDS = 163840;

#define XSWAP 0
constexpr int WS_HFLAG = 4096;
constexpr int WS_GFLAG = 65536;
constexpr int WS_OFLAG = 131072;
#define OFS 16
constexpr int WS_STATS = 1 << 20;
constexpr int WS_PFLAG = 196608;
constexpr int WS_G = 5 << 20, WS_O = 9 << 20, WS_H = 25 << 20, WS_P = 39 << 20, WS_END = 47 << 20;
constexpr int K2_STG = 24576, K2_C = 98304;

NOPK __global__ __launch_bounds__(512, 2) void mega_kernel(const char* __restrict__ xn, const char* __restrict__ kc, const char* __restrict__ vc,
                                                           const char* __restrict__ wq, const char* __restrict__ w1, const char* __restrict__ wo, const char* __restrict__ w2,
                                                           char* ws, char* __restrict__ out) {
  STAMP_DECL
  STAMP(0);
  extern __shared__ __attribute__((aligned(16))) char shm[];
  const lptr lds = (lptr)shm;
  const unsigned lds0 = (unsigned)(uintptr_t)lds;
  const int tid = TID, wid = __builtin_amdgcn_readfirstlane(tid >> 6), lane = tid & 63, wr = wid >> 2, wc = wid & 3;
  const int bid = BID ^ XSWAP, xcd = bid & 7, idx = bid >> 3, hd = (idx & 2) * 8 + 2 * xcd + (idx & 1), rb = idx >> 2;
  const int g = lane >> 4, t = lane & 15, q4 = (lane >> 2) & 3, p4 = lane & 3;

  const bool light = (idx & 2) != 0;
  unsigned voffX[4], voffQ[2], voffH[2];
  {
    const int r = lane >> 3, c = lane & 7;
#pragma unroll
    for (int i = 0; i < 4; ++i) voffX[i] = (unsigned)((8 * i + r) * 2048 + ((c ^ (4 * (i & 1) + (r >> 1))) << 4) - i * 1024);
    const int k = 32 * (wid >> 2) + 16 * (wid & 1) + 4 * ((wid >> 1) & 1) + 8 * (r >> 2) + (r & 3);
    const int cw = c ^ (((r >> 1) & 3) << 1);
    voffQ[0] = (unsigned)(k * 8192 + cw * 16 + 1024); voffQ[1] = voffQ[0] + 128 - 1024;
    voffH[0] = (unsigned)(k * 7168 + 128 + (light ? (cw & 3) : cw) * 16 + 1024);
    voffH[1] = (unsigned)(k * 7168 + cw * 16);
  }
  const char* xb = xn + (size_t)rb * 256 * 2048 + wid * 65536;
  const char* qb = wq + hd * 256 - 1024;
  const char* hb = w1 + (light ? 4096 + 192 * (hd - 16) : 256 * hd) - 1024;
  const bool full = wr == 1 || !light;
#define GLDS2(v0_, v1_, sb_, ld_) asm volatile("s_mov_b32 m0, %3\n\ts_nop 0\n\tglobal_load_lds_dwordx4 %0, %2\n\tglobal_load_lds_dwordx4 %1, %2 offset:1024" \
    ::"v"(v0_), "v"(v1_), "s"(sb_), "s"(ld_) : "memory")
#define GLDS4(v0_, v1_, v2_, v3_, sb_, ld_) asm volatile("s_mov_b32 m0, %5\n\ts_nop 0\n\tglobal_load_lds_dwordx4 %0, %4\n\tglobal_load_lds_dwordx4 %1, %4 offset:1024\n\t" \
    "global_load_lds_dwordx4 %2, %4 offset:2048\n\tglobal_load_lds_dwordx4 %3, %4 offset:3072" ::"v"(v0_), "v"(v1_), "v"(v2_), "v"(v3_), "s"(sb_), "s"(ld_) : "memory")
#define ST_X(b, kt) GLDS4(voffX[0], voffX[1], voffX[2], voffX[3], xb + (kt) * 128, lds0 + SX(b, 0) + wid * 4096)
#define ST_W0(wo_, kt) GLDS2(voffQ[0], voffQ[1], qb + (size_t)(kt) * 524288, lds0 + (wo_) + wid * 2048)
#define ST_W1(wo_, kt) GLDS2(voffH[0], voffH[1], hb + (size_t)(kt) * 458752, lds0 + (wo_) + 16384 + wid * 2048)

  lptr wb[4], xr[2];
#pragma unroll
  for (int m = 0; m < 4; ++m) wb[m] = lds + wr * 1024 + (g >> 1) * 2048 + (4 * (g & 1) + q4) * 128 + 32 * ((m ^ wc) ^ (2 * (g & 1) + (q4 >> 1))) + 8 * p4;
#pragma unroll
  for (int ks = 0; ks < 2; ++ks) xr[ks] = lds + 98304 + (wc * 32 + t) * 128 + 16 * ((4 * ks + g) ^ (t >> 1));
#define LDW(dst, h) do { _Pragma("unroll") for (int m = 0; m < 4; ++m) _Pragma("unroll") for (int k = 0; k < 2; ++k) dst[m][k] = ldtr2(wb[m] + (h) * 16384 + k * 8192, 4096); } while (0)
#define LDX(dst, b, h) do { _Pragma("unroll") for (int n = 0; n < 2; ++n) _Pragma("unroll") for (int k = 0; k < 2; ++k) dst[n][k] = ldb128(xr[k] + (SX(b, h) - 98304) + n * 2048); } while (0)

  f32x4 acc[2][2][4][2];
#pragma unroll
  for (int a = 0; a < 2; ++a)
#pragma unroll
    for (int b = 0; b < 2; ++b)
#pragma unroll
      for (int m = 0; m < 4; ++m)
#pragma unroll
        for (int n = 0; n < 2; ++n) acc[a][b][m][n] = (f32x4){0.f, 0.f, 0.f, 0.f};
  bf16x8 At[4][2], B0[2][2], B1[2][2];

#define MM1(ai, m, k, j) do { if ((j) < 2) acc[ai][0][m][(j) & 1] = MFMA16(At[m][k], B0[(j) & 1][k], acc[ai][0][m][(j) & 1]); else acc[ai][1][m][(j) & 1] = MFMA16(At[m][k], B1[(j) & 1][k], acc[ai][1][m][(j) & 1]); SCHED(); } while (0)
#define MM4(ai, m, k) do { _Pragma("unroll") for (int q = 0; q < 4; ++q) MM1(ai, m, k, ((m) & 1) ? 3 - q : q); } while (0)
#define MMA2(ai, CLOSE) do { __builtin_amdgcn_s_setprio(1); \
    cs[ai] = MFMA16(At[0][0], ones, cs[ai]); SCHED(); MM4(ai, 0, 0); MM4(ai, 1, 0); \
    if ((ai) == 0) { MM4(ai, 2, 0); MM4(ai, 3, 0); } \
    cs[ai] = MFMA16(At[0][1], ones, cs[ai]); SCHED(); MM4(ai, 0, 1); MM4(ai, 1, 1); \
    asm volatile("" : "+v"(cs[ai])); \
    if ((ai) == 0 || full) { MM4(ai, 2, 1); MM4(ai, 3, 1); if ((ai) == 1) { MM4(ai, 2, 0); MM4(ai, 3, 0); } } \
    SCHED(); CLOSE; __builtin_amdgcn_s_setprio(0); SCHED(); } while (0)
  char* const stats = ws + WS_STATS;
  const __amdgpu_buffer_rsrc_t wrs = __builtin_amdgcn_make_buffer_rsrc(ws, 0, WS_END, 0x00020000);
  f32x4 cs[2] = {{0.f, 0.f, 0.f, 0.f}, {0.f, 0.f, 0.f, 0.f}};
  bf16x8 ones = __builtin_bit_cast(bf16x8, (u32x4){0x3f803f80u, 0x3f803f80u, 0x3f803f80u, 0x3f803f80u});
#define PIN_SETUP() do { asm volatile("" : "+v"(ones), "+v"(cs[0]), "+v"(cs[1]), "+v"(wb[0]), "+v"(wb[1]), "+v"(wb[2]), "+v"(wb[3]), "+v"(xr[0]), "+v"(xr[1])); \
    _Pragma("unroll") for (int a = 0; a < 2; ++a) _Pragma("unroll") for (int b = 0; b < 2; ++b) \
      asm volatile("" : "+v"(acc[a][b][0][0]), "+v"(acc[a][b][0][1]), "+v"(acc[a][b][1][0]), "+v"(acc[a][b][1][1]), "+v"(acc[a][b][2][0]), "+v"(acc[a][b][2][1]), "+v"(acc[a][b][3][0]), "+v"(acc[a][b][3][1])); } while (0)
  unsigned tag;
  {
    if (wr == 0) {
      unsigned ep;
      ST_X(0, 0); ST_W0(0, 0);
      GLDS4(voffX[0], voffX[1], voffX[2], voffX[3], xb + 262144, lds0 + SX(0, 1) + wid * 4096);
      GLDS2(voffQ[0], voffQ[1], qb + 262144, lds0 + 8192 + wid * 2048);
      asm volatile("global_load_dword %0, %1, %2 sc1" : "=&v"(ep) : "v"(0), "s"(ws) : "memory");
      PIN_SETUP();
      asm volatile("s_waitcnt vmcnt(0)" : "+v"(ep) :: "memory");
      tag = __builtin_amdgcn_readfirstlane(ep) + 1u;
      BAR();
    } else {
      const int row = rb * 256 + hd * 8 + wc * 2 + (lane >> 5);
      u32x4 a[4];
      unsigned ep;
      asm volatile("global_load_dword %0, %1, %2 sc1" : "=&v"(ep) : "v"(0), "s"(ws) : "memory");
      asm volatile("global_load_dwordx4 %0, %4, %5\n\tglobal_load_dwordx4 %1, %4, %5 offset:512\n\tglobal_load_dwordx4 %2, %4, %5 offset:1024\n\tglobal_load_dwordx4 %3, %4, %5 offset:1536"
                   : "=&v"(a[0]), "=&v"(a[1]), "=&v"(a[2]), "=&v"(a[3]) : "v"(row * 2048 + (lane & 31) * 16), "s"(xn) : "memory");
      ST_W1(0, 0); GLDS2(voffH[0], voffH[1], hb - 229376, lds0 + 16384 + wc * 2048);
      PIN_SETUP();
      BAR();
      ST_W0(32768, 1); GLDS2(voffQ[0], voffQ[1], qb + 524288 - 262144, lds0 + 32768 + wc * 2048);
      ST_X(1, 1); GLDS4(voffX[0], voffX[1], voffX[2], voffX[3], xb - 262144 + 128, lds0 + SX(1, 0) + wc * 4096);
      asm volatile("s_waitcnt vmcnt(16)" : "+v"(ep), "+v"(a[0]), "+v"(a[1]), "+v"(a[2]), "+v"(a[3]) :: "memory");
      tag = __builtin_amdgcn_readfirstlane(ep) + 1u;
      float v[32];
#pragma unroll
      for (int i = 0; i < 16; ++i) { v[2 * i] = bf2f(a[i >> 2][i & 3] & 0xffffu); v[2 * i + 1] = U2F(a[i >> 2][i & 3] & 0xffff0000u); }
      float s0 = 0.f, s1 = 0.f, s2 = 0.f, s3 = 0.f;
#pragma unroll
      for (int i = 0; i < 8; ++i) { s0 += v[i]; s1 += v[8 + i]; s2 += v[16 + i]; s3 += v[24 + i]; }
      const float mean = half_sum((s0 + s1) + (s2 + s3)) * (1.0f / 1024.0f);
      s0 = s1 = s2 = s3 = 0.f;
#pragma unroll
      for (int i = 0; i < 8; ++i) { const float d0 = v[i] - mean, d1 = v[8 + i] - mean, d2 = v[16 + i] - mean, d3 = v[24 + i] - mean; s0 += d0 * d0; s1 += d1 * d1; s2 += d2 * d2; s3 += d3 * d3; }
      const float sd = __builtin_amdgcn_sqrtf(half_sum((s0 + s1) + (s2 + s3)) * (1.0f / 1024.0f) + 1e-5f);
      if ((lane & 31) < 2) __hip_atomic_store((gu64*)(stats + row * 16 + (lane & 1) * 8), ((unsigned long long)tag << 32) | F2U((lane & 1) ? sd : mean), __ATOMIC_RELAXED, __HIP_MEMORY_SCOPE_AGENT);
    }
  }
  if (wr == 1) BAR();
  SCHED();
  STAMP(1);
  const int kvg = hd >> 3;
  const char* kb_g = kc + ((size_t)kvg * 2048 + 1536) * 256;
  const char* vb_g = vc + ((size_t)kvg * 2048 + 1536) * 256;
#define VOFFK ((unsigned)(((lane >> 2) & 7) * 256 + (4 * (lane >> 5) + ((lane & 3) ^ ((2 * (wid & 1) + ((lane >> 4) & 1)) & 3))) * 16))
#define VOFFV ((unsigned)(((lane >> 2) & 7) * 256 + (lane >> 5) * 64 + (lane & 3) * 16))
#define ST_KV8(voff, gsrc, slot) do { glds(voff, (gsrc) + wid * 2048, lds0 + (slot) + wid * 2048); glds(voff, (gsrc) + wid * 2048 + 128, lds0 + (slot) + wid * 2048 + 1024); } while (0)
  int wprv = 65536, wnxt = 32768;
#define W_ADV() do { const int d_ = (wnxt == 0) ? -65536 : 32768, cur_ = wnxt - d_; wnxt = wprv; wprv = cur_; \
    _Pragma("unroll") for (int f = 0; f < 4; ++f) wb[f] += d_; \
    asm volatile("" : "+v"(wb[0]), "+v"(wb[1]), "+v"(wb[2]), "+v"(wb[3])); } while (0)
#define PH_A(b, STAGE) do { LDX(B0, b, 0); LDX(B1, b, 1); SCHED(); LDW(At, 0); STAGE; WAIT_V(10); WAIT_L(12); BAR(); SCHED(); MMA2(0, WAIT_L(0); BAR()); } while (0)
#define PH_B(STAGE) do { LDW(At, 1); STAGE; W_ADV(); WAIT_V(8); BAR(); SCHED(); MMA2(1, WAIT_L(0); BAR()); } while (0)
#pragma unroll 1
  for (int kt = 0; kt < 14; kt += 2) {
    PH_A(0, ST_W1(wnxt, kt + 1); ST_W0(wprv, kt + 2));
    PH_B(ST_X(0, kt + 2));
    PH_A(1, ST_W1(wnxt, kt + 2); ST_W0(wprv, kt + 3));
    PH_B(ST_X(1, kt + 3));
  }
  STAMP(2);
  const unsigned voffK = VOFFK, voffV = VOFFV;
  PH_A(0, ST_W1(0, 15); ST_KV8(voffK, kb_g, SLOT_E));
  PH_B(ST_KV8(voffV, vb_g, SLOT_C); ST_KV8(voffV, vb_g + 16384, SLOT_C + 16384));
  const unsigned soff = (unsigned)((rb * 256 + (g >> 1) * 128 + wc * 32 + (g & 1) * 16 + t) * 16);
  u32x4 sg;
  LDX(B0, 1, 0); LDX(B1, 1, 1); SCHED(); LDW(At, 0);
  asm volatile("global_load_dwordx4 %0, %1, %2 sc1" : "=&v"(sg) : "v"(soff), "s"(stats) : "memory");
  ST_KV8(voffK, kb_g + 16384, SLOT_E + 16384); ST_KV8(voffK, kb_g + 32768, SLOT_A);
  WAIT_V(11); BAR(); SCHED(); MMA2(0, WAIT_L(0); BAR());
  LDW(At, 1);
  ST_KV8(voffK, kb_g + 49152, SLOT_A + 16384);
  BAR(); SCHED();
  const lptr csw = lds + SLOT_D + (t & 3) * 8192 + wr * 4096 + wc * 64 + g * 16;
  MMA2(1, *(LAS f32x4*)csw = cs[0]; *(LAS f32x4*)(csw + 256) = cs[1]; asm volatile("s_waitcnt vmcnt(4)" : "+v"(sg) :: "memory"); WAIT_L(0); BAR());
  f32x4 cv[2][4];
#pragma unroll
  for (int a = 0; a < 2; ++a)
#pragma unroll
    for (int m = 0; m < 4; ++m) cv[a][m] = *(const LAS f32x4*)(lds + SLOT_D + wc * 8192 + wr * 4096 + a * 256 + (m ^ wc) * 64 + g * 16);
  if (__builtin_expect(__builtin_amdgcn_ballot_w64(sg[1] != tag || sg[3] != tag) != 0ull, 0)) {
    for (unsigned spins = 0; spins < 4000000u; ++spins) {
      __builtin_amdgcn_s_sleep(2);
      asm volatile("" ::: "memory");
      sg = __builtin_amdgcn_raw_buffer_load_b128(wrs, soff, WS_STATS, 16);
      if (__builtin_amdgcn_ballot_w64(sg[1] != tag || sg[3] != tag) == 0ull) break;
    }
  }
  float mu[2][2], rs[2][2], sd[2][2];
  {
    auto m16 = __builtin_amdgcn_permlane16_swap(sg[0], sg[0], false, false);
    auto s16 = __builtin_amdgcn_permlane16_swap(sg[2], sg[2], false, false);
#pragma unroll
    for (int n = 0; n < 2; ++n) {
      auto m32 = __builtin_amdgcn_permlane32_swap(m16[n], m16[n], false, false);
      auto s32 = __builtin_amdgcn_permlane32_swap(s16[n], s16[n], false, false);
#pragma unroll
      for (int bj = 0; bj < 2; ++bj) { mu[bj][n] = -U2F(m32[bj]); sd[bj][n] = U2F(s32[bj]); rs[bj][n] = __builtin_amdgcn_rcpf(sd[bj][n]) * 1.4426950408889634f; }
    }
  }
  WAIT_L(0);
  STAMP(3);
  {
    const lptr qw = lds + wc * 8192 + wr * 4096 + ((g >> 1) * 32 + t) * 16 + (g & 1) * 8;
#pragma unroll
    for (int bj = 0; bj < 2; ++bj) {
      lptr qwb = qw + (bj ? SLOT_D : SLOT_B);
      asm volatile("" : "+v"(qwb));
#pragma unroll
      for (int m = 0; m < 4; ++m)
#pragma unroll
        for (int n = 0; n < 2; ++n) {
          const f32x4 v = (acc[0][bj][m][n] + cv[0][m] * mu[bj][n]) * (rs[bj][n] * 0.08838834764831845f);
          u32x2 w = {pk2(v[0], v[1]), pk2(v[2], v[3])};
          *(LAS u32x2*)(qwb + (m ^ wc) * 1024 + n * 256) = w;
        }
    }
  }
  WAIT_L(0); BAR(); SCHED();
  STAMP(4);
  unsigned pft = 0;
  const int sl = ((idx >> 2) * 16 + (idx & 1) * 8 + xcd) * 8 + wid;
#define TOUCH(q_) do { const int o_ = light ? ((((q_) + 2) & 3) * 916 + (sl < 916 ? sl : 915)) : ((((q_) + 2) & 3) * 1024 + sl); \
    asm volatile("global_load_dword %0, %1, %2" : "+v"(pft) : "v"((unsigned)((__builtin_amdgcn_mbcnt_lo(~0u, 0u) & 15) * 128)), "s"((light ? w2 : wo) + (light && o_ > 3583 ? 3583 : o_) * 2048) : "memory"); } while (0)
  TOUCH(0);
  bf16x8 qf[8];
  const lptr qr = lds + (wr ? SLOT_D : SLOT_B) + wc * 8192 + lane * 16;
  if (wr == 1) {
#pragma unroll
    for (int s = 0; s < 8; ++s) qf[s] = ldb128(qr + s * 1024);
  }
#define ST128(...) ST128_(wrs, __VA_ARGS__)
  u32x4 hq[3];
  unsigned hqo;
  {
    u32x2 hw[2][2][2];
#define SILU16(m, ACT) do { \
      const f32x4 a0 = acc[1][0][m][0] + cv[1][m] * mu[0][0], a1 = acc[1][0][m][1] + cv[1][m] * mu[0][1], a2 = acc[1][1][m][0] + cv[1][m] * mu[1][0], a3 = acc[1][1][m][1] + cv[1][m] * mu[1][1]; \
      f32x4 e0 = a0 * -rs[0][0], e1 = a1 * -rs[0][1], e2 = a2 * -rs[1][0], e3 = a3 * -rs[1][1]; SCHED(); \
      if (ACT) { \
      _Pragma("unroll") for (int i = 0; i < 4; ++i) { e0[i] = __builtin_amdgcn_exp2f(e0[i]); e1[i] = __builtin_amdgcn_exp2f(e1[i]); e2[i] = __builtin_amdgcn_exp2f(e2[i]); e3[i] = __builtin_amdgcn_exp2f(e3[i]); } SCHED(); \
      e0 = e0 * sd[0][0] + sd[0][0]; e1 = e1 * sd[0][1] + sd[0][1]; e2 = e2 * sd[1][0] + sd[1][0]; e3 = e3 * sd[1][1] + sd[1][1]; SCHED(); \
      _Pragma("unroll") for (int i = 0; i < 4; ++i) { e0[i] = __builtin_amdgcn_rcpf(e0[i]); e1[i] = __builtin_amdgcn_rcpf(e1[i]); e2[i] = __builtin_amdgcn_rcpf(e2[i]); e3[i] = __builtin_amdgcn_rcpf(e3[i]); } SCHED(); \
      e0 *= a0; e1 *= a1; e2 *= a2; e3 *= a3; SCHED(); } \
      else { _Pragma("unroll") for (int i = 0; i < 4; ++i) e3[i] = a3[i] * __builtin_amdgcn_rcpf(__builtin_amdgcn_exp2f(e3[i]) * sd[1][1] + sd[1][1]); } \
      hw[0][0][(m) & 1] = (u32x2){pk2(e0[0], e0[1]), pk2(e0[2], e0[3])}; hw[0][1][(m) & 1] = (u32x2){pk2(e1[0], e1[1]), pk2(e1[2], e1[3])}; \
      hw[1][0][(m) & 1] = (u32x2){pk2(e2[0], e2[1]), pk2(e2[2], e2[3])}; hw[1][1][(m) & 1] = (u32x2){pk2(e3[0], e3[1]), pk2(e3[2], e3[3])}; SCHED(); } while (0)
#define HFRAG(bj, n) ((wc & 1) ? (u32x4){hw[bj][n][1][0], hw[bj][n][1][1], hw[bj][n][0][0], hw[bj][n][0][1]} : (u32x4){hw[bj][n][0][0], hw[bj][n][0][1], hw[bj][n][1][0], hw[bj][n][1][1]})
    const unsigned hoff = (unsigned)(WS_H + (rb * 16 + wc * 2) * 2048 + lane * 16);
    const int pb = (light ? 16 + 3 * hd : 4 * hd) + (wr ? 0 : 2), sw = full ? (wc >> 1) : 0, p0 = pb + sw, p1 = pb + (sw ^ 1);
    SILU16(0, false); SILU16(1, false);
    hqo = hoff + (p0 >> 1) * 262144 + (p0 & 1) * 1024;
    hq[0] = HFRAG(0, 0); hq[1] = HFRAG(0, 1); hq[2] = HFRAG(1, 0);
    ST128(HFRAG(1, 1), hqo + 2048, 16384);
    asm volatile("" : "+v"(hq[0]), "+v"(hq[1]), "+v"(hq[2]));
    if (wr == 0) {
      BAR(); SCHED();
#pragma unroll
      for (int s = 0; s < 8; ++s) qf[s] = ldb128(qr + s * 1024);
    }
    if (full) {
      SILU16(2, true); SILU16(3, true);
#pragma unroll
      for (int bj = 0; bj < 2; ++bj)
#pragma unroll
        for (int n = 0; n < 2; ++n) ST128(HFRAG(bj, n), hoff + n * 2048, (p1 >> 1) * 262144 + (p1 & 1) * 1024 + bj * 16384);
    }
  }
  STAMP(5);
  if (wr == 1) { WAIT_L(0); BAR(); }
  __builtin_amdgcn_s_setprio(1);
  SCHED();
  STAMP(6);
  const int r32 = lane & 31, hh = lane >> 5;
  lptr kr[2];
#pragma unroll
  for (int e = 0; e < 2; ++e) kr[e] = lds + 2048 * (r32 >> 3) + 64 * (r32 & 7) + 16 * ((2 * e + hh) ^ ((r32 >> 2) & 3));
  const lptr vr = lds + ((lane & 3) << 3) + (((lane >> 2) & 3) << 6) + (((lane >> 4) & 1) << 5) + (hh << 8);
  f32x16 o[4], s[4];
#pragma unroll
  for (int d = 0; d < 4; ++d)
#pragma unroll
    for (int i = 0; i < 16; ++i) o[d][i] = 0.f;
  bf16x8 pf[8];
  constexpr int RD = 6;
  bf16x8 fr[RD];
  f32x4 lacc = {0.f, 0.f, 0.f, 0.f};
  const unsigned selw = (((lane >> 4) ^ (lane >> 2)) & 1) ? 0u : 0x3f803f80u;
  bf16x8 sel = __builtin_bit_cast(bf16x8, (u32x4){selw, selw, selw, selw});
  asm volatile("" : "+v"(sel));
  f32x16 negm = {0.f, 0.f, 0.f, 0.f, 0.f, 0.f, 0.f, 0.f, 0.f, 0.f, 0.f, 0.f, 0.f, 0.f, 0.f, 0.f};
#define SM_THR 60.0f
#define FRAG(i) ((i) < 32 ? ldtr2(v0 + (((i) >> 2) * 8 + ((i) & 3)) * 512, 2048) : ldb128((((((i) - 32) >> 2) & 1) ? k1 : k0) + ((i) & 3) * 8192 + (((i) - 32) >> 3) * 512))
#define XEARLY 3
#define XPRIO 1
#define YPRIO 0
#define XSEG(lo, hi, CLOSE) do { __builtin_amdgcn_s_setprio(XPRIO); \
    _Pragma("unroll") for (int i = (lo); i < (hi); ++i) { \
      if (i < 32) o[i & 3] = MFMA32(fr[i % RD], pf[(i >> 2) & 7], o[i & 3]); \
      else s[i & 3] = MFMA32(fr[i % RD], qf[((i - 32) >> 2) & 7], i < 36 ? negm : s[i & 3]); \
      if (i < 32 && (i & 3) == 1) lacc = MFMA16(sel, pf[(i >> 2) & 7], lacc); \
      if (i + RD < (hi)) fr[i % RD] = FRAG(i + RD); \
      SCHED(); \
      if (i == 63 - XEARLY) { __builtin_amdgcn_s_setprio(2); CLOSE; WAIT_L(0); BAR(); SCHED(); } } \
    __builtin_amdgcn_s_setprio(YPRIO); } while (0)
#define PREF(lo) do { _Pragma("unroll") for (int i = (lo); i < (lo) + RD; ++i) fr[i % RD] = FRAG(i); SCHED(); } while (0)
#define MX3(a, b, c) __builtin_fmaxf(__builtin_fmaxf((a), (b)), (c))
#define SOFTMAX() do { \
      float m0 = MX3(s[0][0], s[0][1], s[0][2]), m1 = MX3(s[1][0], s[1][1], s[1][2]), m2 = MX3(s[2][0], s[2][1], s[2][2]), m3 = MX3(s[3][0], s[3][1], s[3][2]); \
      _Pragma("unroll") for (int i = 3; i < 15; i += 2) { m0 = MX3(m0, s[0][i], s[0][i + 1]); m1 = MX3(m1, s[1][i], s[1][i + 1]); m2 = MX3(m2, s[2][i], s[2][i + 1]); m3 = MX3(m3, s[3][i], s[3][i + 1]); } \
      m0 = MX3(m0, s[0][15], s[1][15]); m2 = MX3(m2, s[2][15], s[3][15]); \
      float mx = __builtin_fmaxf(MX3(m0, m1, m2), m3); \
        \
      if (__builtin_expect(__builtin_amdgcn_ballot_w64(!(mx <= SM_THR)) != 0ull, 0)) { \
        auto rr = __builtin_amdgcn_permlane32_swap(F2U(mx), F2U(mx), false, false); \
        const float dl = MX3(U2F(rr[0]), U2F(rr[1]), 0.f); \
        _Pragma("unroll") for (int kb = 0; kb < 4; ++kb) s[kb] -= dl; \
        negm -= dl; \
        const float alpha = __builtin_amdgcn_exp2f(-dl); lacc *= alpha; _Pragma("unroll") for (int d = 0; d < 4; ++d) o[d] *= alpha; \
      } \
      SCHED(); \
      _Pragma("unroll") for (int kb = 0; kb < 4; ++kb) { \
        _Pragma("unroll") for (int e = 0; e < 2; ++e) { \
          _Pragma("unroll") for (int i = 0; i < 8; ++i) s[kb][8 * e + i] = __builtin_amdgcn_exp2f(s[kb][8 * e + i]); \
          u32x4 w = {pk2(s[kb][8 * e], s[kb][8 * e + 1]), pk2(s[kb][8 * e + 2], s[kb][8 * e + 3]), pk2(s[kb][8 * e + 4], s[kb][8 * e + 5]), pk2(s[kb][8 * e + 6], s[kb][8 * e + 7])}; \
          pf[2 * kb + e] = __builtin_bit_cast(bf16x8, w); } \
        asm volatile("" : "+v"(pf[2 * kb]), "+v"(pf[2 * kb + 1])); \
        SCHED(); } } while (0)
  {
    const lptr k0 = kr[0] + SLOT_E, k1 = kr[1] + SLOT_E, v0 = vr;
    {
      bf16x8 kf[8];
#pragma unroll
      for (int st = 0; st < 8; ++st) kf[st] = FRAG(32 + 4 * st);
      PREF(32);
#pragma unroll
      for (int st = 0; st < 8; ++st) negm = MFMA32(kf[st], qf[st], negm);
      float m0 = MX3(negm[0], negm[1], negm[2]), m1 = MX3(negm[3], negm[4], negm[5]);
      m0 = MX3(m0, negm[6], negm[7]); m1 = MX3(m1, negm[8], negm[9]); m0 = MX3(m0, negm[10], negm[11]); m1 = MX3(m1, negm[12], negm[13]);
      m0 = __builtin_fmaxf(MX3(m0, negm[14], negm[15]), m1);
      auto rr = __builtin_amdgcn_permlane32_swap(F2U(m0), F2U(m0), false, false);
      m0 = -__builtin_fmaxf(U2F(rr[0]), U2F(rr[1]));
#pragma unroll
      for (int i = 0; i < 16; ++i) negm[i] = m0;
      asm volatile("" : "+v"(negm));
      SCHED();
    }
    XSEG(32, 64, WAIT_V(1));
  }
  STAMP(7);
  const char* kvsrc = (wr ? kb_g + 65536 : vb_g + 32768) + wc * 8192 - 1024;
  unsigned kvo[2][2];
#pragma unroll
  for (int par = 0; par < 2; ++par) {
    const int xx = wr ? ((lane & 3) ^ ((2 * par + ((lane >> 4) & 1)) & 3)) : (lane & 3);
    kvo[par][0] = (unsigned)(((lane >> 2) & 7) * 256 + (lane >> 5) * 64 + xx * 16 + 1024);
    kvo[par][1] = kvo[par][0] + 128 - 1024;
  }
#pragma unroll 1
  for (int c = 0; c < 3; ++c) {
    if (c == 0) STAMP(8); else if (c == 1) STAMP(12);
    ST128((u32x4){silu_pk(hq[0][0]), silu_pk(hq[0][1]), silu_pk(hq[0][2]), silu_pk(hq[0][3])}, hqo, c == 2 ? 16384 : c * 2048);
    hq[0] = hq[1]; hq[1] = hq[2];
    SCHED();
    if (c + wr < 3) {
      const int slot = wr ? (c == 0 ? SLOT_B : SLOT_C) : (c == 0 ? SLOT_D : (c == 1 ? SLOT_E : SLOT_A));
#pragma unroll
      for (int hf = 0; hf < 2; ++hf)
        asm volatile("s_mov_b32 m0, %5\n\ts_nop 0\n\t"
                     "global_load_lds_dwordx4 %0, %4\n\tglobal_load_lds_dwordx4 %1, %4 offset:1024\n\t"
                     "global_load_lds_dwordx4 %2, %4 offset:2048\n\tglobal_load_lds_dwordx4 %3, %4 offset:3072"
                     ::"v"(kvo[0][0]), "v"(kvo[0][1]), "v"(kvo[1][0]), "v"(kvo[1][1]), "s"(kvsrc + c * 32768 + hf * 4096), "s"(lds0 + slot + wc * 8192 + hf * 4096) : "memory");
    }
    TOUCH(c + 1);
    SOFTMAX();
    const int kslot = (c == 0) ? SLOT_A : ((c == 1) ? SLOT_B : SLOT_C);
    const int vslot = (c == 0) ? SLOT_C : ((c == 1) ? SLOT_D : SLOT_E);
    const lptr k0 = kr[0] + kslot, k1 = kr[1] + kslot, v0 = vr + vslot;
    PREF(0);
    if (c == 0) STAMP(9); else if (c == 1) STAMP(13);
    BAR(); SCHED();
    if (c == 0) STAMP(10); else if (c == 1) STAMP(14);
    XSEG(0, 64, WAIT_V(1); if (c == 2) WAIT_V(0));
    if (c == 0) STAMP(11); else if (c == 2) STAMP(15);
  }
  if (wid == 4 && lane == 0) __builtin_amdgcn_raw_buffer_store_b32(tag, wrs, 0, WS_HFLAG + (rb * 32 + hd) * 128, 16);
  STAMP(16);
  SOFTMAX();
  {
    const lptr v0 = vr + SLOT_A;
#define FRAGD(i) ldtr2(v0 + (((i) & 7) * 8 + ((i) >> 3)) * 512, 2048)
#pragma unroll
    for (int i = 0; i < RD; ++i) fr[i] = FRAGD(i);
    asm volatile("s_waitcnt vmcnt(0)" :: "v"(pft) : "memory");
    SCHED();
    STAMP(17);
    if (wr == 0) BAR();
    SCHED();
    STAMP(18);
    __builtin_amdgcn_s_setprio(1);
    const unsigned oo = (unsigned)(WS_O + 2 * hd * 262144 + (rb * 16 + wr * 8 + wc * 2 + (r32 >> 4)) * 2048 + (hh * 16 + (r32 & 15)) * 16);
    float inv = 0.f;
    u32x4 ow;
#pragma unroll
    for (int i = 0; i < 42; ++i) {
      if (i < 32) o[i >> 3] = MFMA32(fr[i % RD], pf[i & 7], o[i >> 3]);
      if (i < 8) lacc = MFMA16(sel, pf[i], lacc);
      if (i + RD < 32) fr[i % RD] = FRAGD(i + RD);
      if (i == 9) inv = __builtin_amdgcn_rcpf(lacc[0]);
      if (i >= 10) {
        const int d = (i - 10) >> 3, q = (i - 10) & 7;
        ow[q & 3] = pk2(o[d][2 * q] * inv, o[d][2 * q + 1] * inv);
        asm volatile("" : "+v"(ow));
        if ((q & 3) == 3) ST128(ow, oo + (d & 1) * 1024 + (q >> 2) * 512, (d >> 1) * 262144);
      }
      SCHED();
    }
    __builtin_amdgcn_s_setprio(0);
  }
  STAMP(19);
  int lane2 = __builtin_amdgcn_mbcnt_hi(~0u, __builtin_amdgcn_mbcnt_lo(~0u, 0u));
  asm volatile("" : "+v"(lane2));
#define lane lane2
  const int isG = (idx >> 1) & 1, kh = xcd & 1, tm = 4 * (xcd >> 1) + ((idx >> 1) & 2) + (idx & 1), tn = 2 * (idx >> 3) + kh;
#define KDS 1
#define KDG 1
  const int kd = isG ? KDG : KDS, sd2 = kh ? -kd : kd;
  const int npw = (isG ? 48 : 56) + 2 * sd2;
  const int k2c = ((npw + 19) % 6 < 4) ? K2_C : 0;
#define O_FLAG() do { if (wid == 0 && lane == 0) __builtin_amdgcn_raw_buffer_store_b32(tag, wrs, 0, WS_OFLAG + (rb * 32 + hd) * OFS, 16); } while (0)
#define POLL32(off_, slp) do { for (unsigned spins = 0; spins < 2000000u; ++spins) { \
      if (__builtin_amdgcn_ballot_w64(__builtin_amdgcn_raw_buffer_load_b32(wrs, (off_), 0, 16) != tag) == 0ull) break; \
      __builtin_amdgcn_s_sleep(slp); asm volatile("" ::: "memory"); } \
    asm volatile("" ::: "memory"); } while (0)
  const int c8 = lane & 15, r0 = wc * 4 + (lane >> 4);
  const unsigned goff = (unsigned)((tm * 128 + r0) * 2048 + tn * 256 + c8 * 16);
  const lptr cb = lds + k2c + r0 * 512;
#define G_FIN() do { f32x4 a0[4], a1[4]; \
      _Pragma("unroll") for (int i = 0; i < 4; ++i) { a0[i] = *(const LAS f32x4*)(cb + (wr * 4 + i) * 8192 + (((2 * c8) ^ (r0 & 7)) << 4)); a1[i] = *(const LAS f32x4*)(cb + (wr * 4 + i) * 8192 + (((2 * c8 + 1) ^ (r0 & 7)) << 4)); } \
      _Pragma("unroll") for (int i = 0; i < 4; ++i) \
        ST128((u32x4){pk2(silu(a0[i][0]), silu(a0[i][1])), pk2(silu(a0[i][2]), silu(a0[i][3])), pk2(silu(a1[i][0]), silu(a1[i][1])), pk2(silu(a1[i][2]), silu(a1[i][3]))}, goff + i * 32768, WS_G + wr * 131072); \
      WAIT_V(0); BAR(); } while (0)
  const unsigned f1 = isG ? (unsigned)(WS_HFLAG + ((tm >> 1) * 32 + 16 + (lane & 15)) * 128) : (unsigned)(WS_OFLAG + ((tm >> 1) * 32 + 16 + (lane & 15)) * OFS);
  STAMP(20);
  if (wr == 0) {
    const char* ab = ws + (isG ? WS_H : WS_O) + tm * 16384 + wc * 4096;
    const char* wbp = (isG ? w2 : wo) + (tn >> 1) * 512 - 3072 + (isG ? wc : (((wc & 1) << 1) | (wc >> 1))) * 16384;
    const unsigned voffA0 = lane * 16, voffA1 = lane * 16 + 1024;
    unsigned voffW[4];
    {
      const int r = lane >> 3, c = lane & 7;
      const unsigned lp = (unsigned)(r * 2048 + ((c ^ (((r >> 1) & 3) << 1)) << 4) + 3072);
      voffW[0] = lp + kh * 256; voffW[1] = voffW[0] + 128 - 1024; voffW[2] = lp + (kh ^ 1) * 256 - 2048; voffW[3] = voffW[2] + 128 - 1024;
    }
    const char *ap, *wp;
    unsigned dst = lds0 + 4 * K2_STG;
#define K2_RUN(k_) do { ap = ab + (size_t)(k_) * 262144; wp = wbp + (size_t)(k_) * 131072; } while (0)
#define K2_ISSUE(WIDE, ks) do { \
      asm volatile("s_mov_b32 m0, %3\n\ts_nop 0\n\tglobal_load_lds_dwordx4 %0, %2 sc1\n\tglobal_load_lds_dwordx4 %1, %2 offset:1024 sc1" \
                   ::"v"(voffA0), "v"(voffA1), "s"(ap + (ks) * 1024), "s"(dst + wc * 2048) : "memory"); \
      if (WIDE) GLDS4(voffW[0], voffW[1], voffW[2], voffW[3], wp + (ks) * 65536, dst + 8192 + wc * 4096); \
      else GLDS2(voffW[0], voffW[1], wp + (ks) * 65536, dst + 8192 + wc * 4096); \
      dst = (dst == lds0 + 5 * K2_STG) ? lds0 : dst + K2_STG; \
      if (ks) { ap += 262144; wp += 131072; } } while (0)
#define K2_STEP(WIDE, ks, EVENT) do { WAIT_V((WIDE) ? 18 : 12); BAR(); EVENT; K2_ISSUE(WIDE, ks); } while (0)
#define K2_TILES(WIDE, n_) do { _Pragma("unroll 1") for (int i_ = 0; i_ < (n_); ++i_) { K2_STEP(WIDE, 0, ); K2_STEP(WIDE, 1, ); } } while (0)
#define LOOK(f_) asm volatile("buffer_load_dword %0, %1, %2, 0 offen sc1" : "+v"(pol) : "v"(f_), "s"(wrs) : "memory")
    const unsigned f2 = f1 - 16 * (isG ? 128 : OFS);
    const unsigned f3 = (unsigned)(WS_PFLAG + (bid ^ 1) * 128);
    u32x4 xh[8];
#define XH_LD(i) asm volatile("global_load_dwordx4 %0, %1, %2" : "=v"(xh[i]) : "v"(goff), "s"(xn + (i) * 32768) : "memory")
    POLL32(f1, 4);
    WAIT_V(0);
    if (isG) { BAR(); O_FLAG(); }
    K2_RUN(32 + (isG ? 12 : 14) * kh);
    K2_ISSUE(1, 0); K2_ISSUE(1, 1);
    STAMP(21);
    WAIT_V(6); BAR();
    K2_ISSUE(1, 0); K2_ISSUE(1, 1); K2_ISSUE(1, 0);
    STAMP(22);
    unsigned pol = 0;
    K2_STEP(1, 1, );
    K2_TILES(1, 4);
    if (!isG) O_FLAG();
    K2_TILES(1, isG ? 3 : 5);
    LOOK(f2);
    K2_TILES(1, 2);
    K2_RUN(((isG ? 12 : 14) + kd) * kh);
    K2_STEP(1, 0, asm volatile("" : "+v"(pol)); if (__builtin_amdgcn_ballot_w64(pol != tag) != 0ull) POLL32(f2, 4));
    K2_STEP(1, 1, );
#pragma unroll
    for (int i = 0; i < 8; ++i) { K2_STEP(1, 0, XH_LD(i)); K2_STEP(1, 1, ); }
    K2_TILES(1, (isG ? 3 : 5) + sd2);
    asm volatile("" : "+v"(xh[0]), "+v"(xh[1]), "+v"(xh[2]), "+v"(xh[3]), "+v"(xh[4]), "+v"(xh[5]), "+v"(xh[6]), "+v"(xh[7]));
    K2_RUN(isG ? 24 : 60);
    K2_TILES(0, 4);
    K2_RUN(28);
    K2_STEP(0, 0, );
    K2_STEP(0, 1, if (wid == 0 && lane == 0) __builtin_amdgcn_raw_buffer_store_b32(tag, wrs, 0, WS_PFLAG + bid * 128, 16));
    K2_STEP(0, 0, ); K2_STEP(0, 1, );
    const unsigned f4 = (lane & 1) ? (unsigned)(WS_GFLAG + (tm * 8 + tn) * 128) : f3;
    K2_STEP(0, 0, LOOK(f4)); K2_STEP(0, 1, ); K2_STEP(0, 0, );
    WAIT_V(12);
    asm volatile("" : "+v"(pol));
    const unsigned long long miss = __builtin_amdgcn_ballot_w64(pol != tag);
    if (wid == 0 && (miss & 1ull)) POLL32(f3, 2);
    BAR();
    K2_ISSUE(0, 1);
    WAIT_V(12); BAR();
    WAIT_V(8); BAR();
    if (isG) {
      WAIT_V(4); BAR();
      WAIT_V(0); BAR();
      BAR();
      STAMP(23);
      BAR();
    } else {
      u32x4 gv[8];
      const __amdgpu_buffer_rsrc_t ors = __builtin_amdgcn_make_buffer_rsrc(out, 0, 2048 * 2048, 0x00020000);
#define LD8(dst_, rs_, so_, pol_) asm volatile("buffer_load_dwordx4 %0, %8, %10, %11 offen" pol_ "\n\tbuffer_load_dwordx4 %1, %9, %10, %11 offen" pol_ "\n\t" \
        "buffer_load_dwordx4 %2, %8, %10, %12 offen" pol_ "\n\tbuffer_load_dwordx4 %3, %9, %10, %12 offen" pol_ "\n\t" \
        "buffer_load_dwordx4 %4, %8, %10, %13 offen" pol_ "\n\tbuffer_load_dwordx4 %5, %9, %10, %13 offen" pol_ "\n\t" \
        "buffer_load_dwordx4 %6, %8, %10, %14 offen" pol_ "\n\tbuffer_load_dwordx4 %7, %9, %10, %14 offen" pol_ \
        : "=&v"(dst_[0]), "=&v"(dst_[1]), "=&v"(dst_[2]), "=&v"(dst_[3]), "=&v"(dst_[4]), "=&v"(dst_[5]), "=&v"(dst_[6]), "=&v"(dst_[7]) \
        : "v"(goff), "v"(goff + 32768), "s"(rs_), "s"((so_)), "s"((so_) + 65536), "s"((so_) + 131072), "s"((so_) + 196608) : "memory")
      if (miss & 2ull) {
        WAIT_V(4); BAR(); WAIT_V(0); BAR(); BAR();
        POLL32((unsigned)(WS_GFLAG + (tm * 8 + tn) * 128), 1);
      }
      LD8(gv, wrs, WS_G, " sc1");
      if (!(miss & 2ull)) { WAIT_V(12); BAR(); WAIT_V(8); BAR(); BAR(); }
      float xf[8][8];
#pragma unroll
      for (int i = 0; i < 8; ++i) {
#pragma unroll
        for (int e = 0; e < 4; ++e) { xf[i][2 * e] = bf2f(xh[i][e] & 0xffffu); xf[i][2 * e + 1] = U2F(xh[i][e] & 0xffff0000u); }
        asm volatile("" : "+v"(xf[i][0]), "+v"(xf[i][1]), "+v"(xf[i][2]), "+v"(xf[i][3]), "+v"(xf[i][4]), "+v"(xf[i][5]), "+v"(xf[i][6]), "+v"(xf[i][7]));
      }
      asm volatile("s_waitcnt vmcnt(0)" : "+v"(gv[0]), "+v"(gv[1]), "+v"(gv[2]), "+v"(gv[3]), "+v"(gv[4]), "+v"(gv[5]), "+v"(gv[6]), "+v"(gv[7]) :: "memory");
      float gf2[8][8];
#pragma unroll
      for (int i = 0; i < 8; ++i) {
#pragma unroll
        for (int e = 0; e < 4; ++e) { gf2[i][2 * e] = bf2f(gv[i][e] & 0xffffu); gf2[i][2 * e + 1] = U2F(gv[i][e] & 0xffff0000u); }
        asm volatile("" : "+v"(gf2[i][0]), "+v"(gf2[i][1]), "+v"(gf2[i][2]), "+v"(gf2[i][3]), "+v"(gf2[i][4]), "+v"(gf2[i][5]), "+v"(gf2[i][6]), "+v"(gf2[i][7]));
      }
      STAMP(23);
      BAR();
      f32x4 a0[8], a1[8];
#pragma unroll
      for (int i = 0; i < 8; ++i) { a0[i] = *(const LAS f32x4*)(cb + i * 8192 + (((2 * c8) ^ (r0 & 7)) << 4)); a1[i] = *(const LAS f32x4*)(cb + i * 8192 + (((2 * c8 + 1) ^ (r0 & 7)) << 4)); }
      SCHED();
#pragma unroll
      for (int i = 0; i < 8; ++i) {
        const u32x4 w = {pk2(xf[i][0] + a0[i][0] * gf2[i][0], xf[i][1] + a0[i][1] * gf2[i][1]), pk2(xf[i][2] + a0[i][2] * gf2[i][2], xf[i][3] + a0[i][3] * gf2[i][3]),
                         pk2(xf[i][4] + a1[i][0] * gf2[i][4], xf[i][5] + a1[i][1] * gf2[i][5]), pk2(xf[i][6] + a1[i][2] * gf2[i][6], xf[i][7] + a1[i][3] * gf2[i][7])};
        __builtin_amdgcn_raw_buffer_store_b128(w, ors, goff + i * 32768, 0, 16);
      }
    }
  } else {
    if (isG) { WAIT_V(0); BAR(); }
    STAMP(21);
    const int wm = wc >> 1, wn = wc & 1, g = lane >> 4, t = lane & 15, q4 = (lane >> 2) & 3, p4 = lane & 3;
    lptr w2b[4];
#pragma unroll
    for (int ft = 0; ft < 4; ++ft) w2b[ft] = lds + 4 * K2_STG + 8192 + wn * 1024 + (g >> 1) * 4096 + (4 * (g & 1) + q4) * 128 + 32 * (ft ^ (2 * (g & 1) + (q4 >> 1))) + 8 * p4;
    lptr ar = lds + 4 * K2_STG + wm * 4096 + lane * 16;
    f32x4 ac2[8][4];
#pragma unroll
    for (int m = 0; m < 8; ++m)
#pragma unroll
      for (int n = 0; n < 4; ++n) ac2[m][n] = (f32x4){0.f, 0.f, 0.f, 0.f};
    bf16x8 Af[2][4];
    bf16x4 Wr[4][2];
    int slot = 4, dlt = 0;
#define K2_RW(f, hf) Wr[(f) & 3][hf] = __builtin_amdgcn_ds_read_tr16_b64_v4bf16((LAS bf16x4*)(w2b[(f) & 3] + (((f) >> 2) & 1) * 2048 + (hf) * 8192))
#define K2_W(m) __builtin_shufflevector(Wr[(m) & 3][0], Wr[(m) & 3][1], 0, 1, 2, 3, 4, 5, 6, 7)
#define K2_NEXT() do { dlt = (slot == 5) ? -5 * K2_STG : K2_STG; slot = (slot == 5) ? 0 : slot + 1; } while (0)
#define K2_ADV(x) do { x += dlt; asm volatile("" : "+v"(x)); } while (0)
#define K2_WIDE(st) do { _Pragma("unroll") for (int i = 0; i < 32; ++i) { const int m = i >> 2, q = i & 3; \
        const int n_ = (m & 1) ? 3 - q : q; ac2[m][n_] = MFMA16(K2_W(m), Af[st][n_], ac2[m][n_]); SCHED(); \
        if (q < 2) K2_RW((m + 3) & 7, q); \
        if (q == 2 && m >= 3 && m < 7) Af[(st) ^ 1][m - 3] = ldb128(ar + (m - 3) * 1024); \
        if (i == 2) K2_ADV(ar); \
        if (i == 6) K2_ADV(w2b[0]); \
        if (i == 10) K2_ADV(w2b[1]); \
        if (i == 11) BAR(); \
        if (i == 19) K2_ADV(w2b[2]); \
        if (i == 23) K2_ADV(w2b[3]); \
        if (i == 30) K2_NEXT(); \
        SCHED(); } } while (0)
#define K2_NARROW(st) do { _Pragma("unroll") for (int i = 0; i < 16; ++i) { const int m = i >> 2, q = i & 3; \
        const int n_ = (m & 1) ? 3 - q : q; ac2[m][n_] = MFMA16(K2_W(m), Af[st][n_], ac2[m][n_]); SCHED(); \
        if (q < 2) K2_RW((m + 3) & 3, q); \
        if (q >= 2 && (m == 1 || m == 2)) Af[(st) ^ 1][2 * m + q - 4] = ldb128(ar + (2 * m + q - 4) * 1024); \
        if (i == 2) { K2_ADV(ar); K2_ADV(w2b[0]); } \
        if (i == 3) BAR(); \
        if (i == 5) K2_ADV(w2b[1]); \
        if (i == 9) K2_ADV(w2b[2]); \
        if (i == 14) K2_ADV(w2b[3]); \
        if (i == 15) K2_NEXT(); \
        SCHED(); } } while (0)
    K2_NEXT();
    BAR();
    STAMP(22);
#pragma unroll
    for (int i = 0; i < 4; ++i) { if (i < 3) { K2_RW(i, 0); K2_RW(i, 1); } Af[0][i] = ldb128(ar + i * 1024); SCHED(); }
#pragma unroll 1
    for (int p = 0; p < 8; p += 2) { K2_WIDE(0); K2_WIDE(1); }
    WAIT_V(0);
#pragma unroll 1
    for (int p = 8; p < npw; p += 2) { K2_WIDE(0); K2_WIDE(1); }
    const unsigned po = (unsigned)(WS_P + wc * 8192 + lane * 16);
#pragma unroll
    for (int i = 0; i < 8; ++i) {
      const f32x4 a = ac2[4 + (i >> 1)][2 * (i & 1)], b = ac2[4 + (i >> 1)][2 * (i & 1) + 1];
      ST128((u32x4){pk2(a[0], a[1]), pk2(a[2], a[3]), pk2(b[0], b[1]), pk2(b[2], b[3])}, po + i * 1024, bid * 32768);
    }
    SCHED();
#pragma unroll 1
    for (int p = 0; p < 4; p += 2) { K2_NARROW(0); K2_NARROW(1); }
    WAIT_V(0);
#pragma unroll 1
    for (int p = 4; p < 10; p += 2) { K2_NARROW(0); K2_NARROW(1); }
    K2_NARROW(0);
    u32x4 pv[8];
#pragma unroll
    for (int i = 0; i < 8; ++i) pv[i] = __builtin_amdgcn_raw_buffer_load_b128(wrs, po + i * 1024 + (bid ^ 1) * 32768, 0, 16);
    SCHED();
    K2_NARROW(1);
#pragma unroll 1
    for (int p = 12; p < 16; p += 2) { K2_NARROW(0); K2_NARROW(1); }
    STAMP(23);
    const lptr cw = lds + k2c + (wm * 64 + t) * 512;
#pragma unroll
    for (int m = 0; m < 4; ++m)
#pragma unroll
      for (int n = 0; n < 4; ++n) {
        const unsigned lo = pv[2 * m + (n >> 1)][2 * (n & 1)], hi = pv[2 * m + (n >> 1)][2 * (n & 1) + 1];
        *(LAS f32x4*)(cw + n * 8192 + (((wn * 16 + m * 4 + g) ^ (t & 7)) << 4)) = ac2[m][n] + (f32x4){bf2f(lo & 0xffffu), U2F(lo & 0xffff0000u), bf2f(hi & 0xffffu), U2F(hi & 0xffff0000u)};
      }
    WAIT_L(0); BAR();
    if (isG) G_FIN();
  STAMP(24);
  STAMP_OUT(ws + (57 << 20), 30);
    __builtin_amdgcn_endpgm();
  }
  if (isG) {
      G_FIN();
      if (wid == 0 && lane == 0) __builtin_amdgcn_raw_buffer_store_b32(tag, wrs, 0, WS_GFLAG + (tm * 8 + tn) * 128, 16);
      if (bid == 16 && wid == 0) {
        for (unsigned spins = 0; spins < 2000000u; ++spins) {
          bool ok = true;
#pragma unroll
          for (int i = 0; i < 4; ++i) ok = ok && __builtin_amdgcn_raw_buffer_load_b32(wrs, (unsigned)((lane + i * 64) * 128), WS_HFLAG, 16) == tag;
          if (__builtin_amdgcn_ballot_w64(!ok) == 0ull) break;
          __builtin_amdgcn_s_sleep(4);
          asm volatile("" ::: "memory");
        }
        asm volatile("" ::: "memory");
        if (lane == 0) __builtin_amdgcn_raw_buffer_store_b32(tag, wrs, 0, 0, 16);
      }
    }
  STAMP(24);
  STAMP_OUT(ws + (57 << 20), 30);
#undef lane
}

extern "C" void kernel_launch(void* const* d_in, const int* in_sizes, int n_in, void* d_out, int out_size, void* d_ws, size_t ws_size, hipStream_t stream) {
  const char* x = (const char*)d_in[0]; const char* kc = (const char*)d_in[1]; const char* vc = (const char*)d_in[2];
  const char* wq = (const char*)d_in[3]; const char* wo = (const char*)d_in[4]; const char* w1 = (const char*)d_in[6]; const char* w2 = (const char*)d_in[7];
  (void)hipFuncSetAttribute((const void*)mega_kernel, hipFuncAttributeMaxDynamicSharedMemorySize, K1_LDS);
  mega_kernel<<<256, 512, K1_LDS, stream>>>(x, kc, vc, wq, w1, wo, w2, (char*)d_ws, (char*)d_out);
}
```

```cpp
#define PROBE_A 19
#define PROBE_B 22
#define PROBE_S 1
#include <hip/hip_runtime.h>
#include <stdint.h>

typedef unsigned short u16;
typedef __bf16 bf16x2 __attribute__((ext_vector_type(2)));
typedef __bf16 bf16x4 __attribute__((ext_vector_type(4)));
typedef __bf16 bf16x8 __attribute__((ext_vector_type(8)));
typedef float f32x2 __attribute__((ext_vector_type(2)));
typedef float f32x4 __attribute__((ext_vector_type(4)));
typedef float f32x16 __attribute__((ext_vector_type(16)));
typedef unsigned u32x2 __attribute__((ext_vector_type(2)));
typedef unsigned u32x4 __attribute__((ext_vector_type(4)));
#define LAS __attribute__((address_space(3)))
typedef LAS char* lptr;
#if defined(__HIP_DEVICE_COMPILE__) && !defined(USEPK)
#define NOPK __attribute__((target("no-packed-fp32-ops")))
#else
#define NOPK
#endif
#define DI NOPK __device__ __forceinline__
#define F2U(x) __builtin_bit_cast(unsigned, (float)(x))
#define U2F(x) __builtin_bit_cast(float, (unsigned)(x))
#define TID ((int)__builtin_amdgcn_workitem_id_x())
#define BID ((int)__builtin_amdgcn_workgroup_id_x())
#define SCHED() __builtin_amdgcn_sched_barrier(0)
#define BAR() asm volatile("s_barrier" ::: "memory")
#define WAIT_V(n) asm volatile("s_waitcnt vmcnt(%0)" :: "n"(n) : "memory")
#define WAIT_L(n) do { asm volatile("" ::: "memory"); __builtin_amdgcn_s_waitcnt(0xC07F | ((n) << 8)); asm volatile("" ::: "memory"); } while (0)
#define MFMA16(a, b, c) __builtin_amdgcn_mfma_f32_16x16x32_bf16(a, b, c, 0, 0, 0)
#define MFMA32(a, b, c) __builtin_amdgcn_mfma_f32_32x32x16_bf16(a, b, c, 0, 0, 0)

DI void glds(unsigned voff, const char* sbase, unsigned ldsaddr) {
  asm volatile("s_mov_b32 m0, %2\n\ts_nop 0\n\tglobal_load_lds_dwordx4 %0, %1" ::"v"(voff), "s"(sbase), "s"(ldsaddr) : "memory");
}
DI void glds1(unsigned voff, const char* sbase, unsigned ldsaddr) {
  asm volatile("s_mov_b32 m0, %2\n\ts_nop 0\n\tglobal_load_lds_dwordx4 %0, %1 sc1" ::"v"(voff), "s"(sbase), "s"(ldsaddr) : "memory");
}
DI unsigned pk2(float lo, float hi) { f32x2 v = {lo, hi}; return __builtin_bit_cast(unsigned, __builtin_convertvector(v, bf16x2)); }
DI float bf2f(unsigned b) { return U2F(b << 16); }
DI bf16x8 ldb128(lptr p) { return *(const LAS bf16x8*)p; }
DI bf16x8 ldtr2(lptr p, int d) {
  bf16x4 a = __builtin_amdgcn_ds_read_tr16_b64_v4bf16((LAS bf16x4*)p);
  bf16x4 b = __builtin_amdgcn_ds_read_tr16_b64_v4bf16((LAS bf16x4*)(p + d));
  return __builtin_shufflevector(a, b, 0, 1, 2, 3, 4, 5, 6, 7);
}
DI void ST128_(__amdgpu_buffer_rsrc_t r, u32x4 v, unsigned voff, int soff) { __builtin_amdgcn_raw_buffer_store_b128(v, r, voff + (unsigned)soff, 0, 16); }
DI unsigned silu_pk(unsigned w) {
  const float a = U2F(w << 16), b = U2F(w & 0xffff0000u);
  const float ea = __builtin_amdgcn_exp2f(a) * -1.4426950408889634f - 1.4426950408889634f, eb = __builtin_amdgcn_exp2f(b) * -1.4426950408889634f - 1.4426950408889634f;
  return pk2(a * __builtin_amdgcn_rcpf(ea), b * __builtin_amdgcn_rcpf(eb));
}
DI float silu(float v) { return v * __builtin_amdgcn_rcpf(1.0f + __builtin_amdgcn_exp2f(v * -1.4426950408889634f)); }

#define STAMP_DECL unsigned long long pa_ = 0, pb_ = 0; const unsigned long long p0_ = __builtin_amdgcn_s_memrealtime();
#define LSTAMP(i)
#define STAMP(i) do { if ((i) == PROBE_A) asm volatile("s_memtime %0\n\ts_waitcnt lgkmcnt(0)" : "=s"(pa_) :: "memory"); if ((i) == PROBE_B) asm volatile("s_memtime %0\n\ts_waitcnt lgkmcnt(0)" : "=s"(pb_) :: "memory"); } while (0)
#define STAMP_OUT(buf, n) do { if (bid == 7 && wid == 4) { const unsigned long long d_ = 10000 + ((pb_ - pa_) >> PROBE_S); \
    while (__builtin_amdgcn_s_memrealtime() - p0_ < d_ && d_ < 100000ull) __builtin_amdgcn_s_sleep(1); } } while (0)
template <int CTRL> DI float dpp_add(float v) { return v + U2F(__builtin_amdgcn_update_dpp(0u, F2U(v), CTRL, 0xf, 0xf, true)); }
DI float half_sum(float v) {
  v = dpp_add<0xB1>(v); v = dpp_add<0x4E>(v); v = dpp_add<0x141>(v); v = dpp_add<0x140>(v);
  auto rr = __builtin_amdgcn_permlane16_swap(F2U(v), F2U(v), false, false);
  return U2F(rr[0]) + U2F(rr[1]);
}
typedef __attribute__((address_space(1))) unsigned long long gu64;

#define SX(b, h) (98304 + (b) * 32768 + (h) * 16384)
constexpr int SLOT_B = 0, SLOT_E = 32768, SLOT_A = 65536, SLOT_C = 98304, SLOT_D = 131072, K1_LDS = 163840;

#define XSWAP 0
constexpr int WS_HFLAG = 4096;
constexpr int WS_GFLAG = 65536;
constexpr int WS_OFLAG = 131072;
#define OFS 16
constexpr int WS_STATS = 1 << 20;
constexpr int WS_PFLAG = 196608;
constexpr int WS_G = 5 << 20, WS_O = 9 << 20, WS_H = 25 << 20, WS_P = 39 << 20, WS_END = 47 << 20;
constexpr int K2_STG = 24576, K2_C = 98304;

NOPK __global__ __launch_bounds__(512, 2) void mega_kernel(const char* __restrict__ xn, const char* __restrict__ kc, const char* __restrict__ vc,
                                                           const char* __restrict__ wq, const char* __restrict__ w1, const char* __restrict__ wo, const char* __restrict__ w2,
                                                           char* ws, char* __restrict__ out) {
  STAMP_DECL
  STAMP(0);
  extern __shared__ __attribute__((aligned(16))) char shm[];
  const lptr lds = (lptr)shm;
  const unsigned lds0 = (unsigned)(uintptr_t)lds;
  const int tid = TID, wid = __builtin_amdgcn_readfirstlane(tid >> 6), lane = tid & 63, wr = wid >> 2, wc = wid & 3;
  const int bid = BID ^ XSWAP, xcd = bid & 7, idx = bid >> 3, hd = (idx & 2) * 8 + 2 * xcd + (idx & 1), rb = idx >> 2;
  const int g = lane >> 4, t = lane & 15, q4 = (lane >> 2) & 3, p4 = lane & 3;

  const bool light = (idx & 2) != 0;
  unsigned voffX[4], voffQ[2], voffH[2];
  {
    const int r = lane >> 3, c = lane & 7;
#pragma unroll
    for (int i = 0; i < 4; ++i) voffX[i] = (unsigned)((8 * i + r) * 2048 + ((c ^ (4 * (i & 1) + (r >> 1))) << 4) - i * 1024);
    const int k = 32 * (wid >> 2) + 16 * (wid & 1) + 4 * ((wid >> 1) & 1) + 8 * (r >> 2) + (r & 3);
    const int cw = c ^ (((r >> 1) & 3) << 1);
    voffQ[0] = (unsigned)(k * 8192 + cw * 16 + 1024); voffQ[1] = voffQ[0] + 128 - 1024;
    voffH[0] = (unsigned)(k * 7168 + 128 + (light ? (cw & 3) : cw) * 16 + 1024);
    voffH[1] = (unsigned)(k * 7168 + cw * 16);
  }
  const char* xb = xn + (size_t)rb * 256 * 2048 + wid * 65536;
  const char* qb = wq + hd * 256 - 1024;
  const char* hb = w1 + (light ? 4096 + 192 * (hd - 16) : 256 * hd) - 1024;
  const bool full = wr == 1 || !light;
#define GLDS2(v0_, v1_, sb_, ld_) asm volatile("s_mov_b32 m0, %3\n\ts_nop 0\n\tglobal_load_lds_dwordx4 %0, %2\n\tglobal_load_lds_dwordx4 %1, %2 offset:1024" \
    ::"v"(v0_), "v"(v1_), "s"(sb_), "s"(ld_) : "memory")
#define GLDS4(v0_, v1_, v2_, v3_, sb_, ld_) asm volatile("s_mov_b32 m0, %5\n\ts_nop 0\n\tglobal_load_lds_dwordx4 %0, %4\n\tglobal_load_lds_dwordx4 %1, %4 offset:1024\n\t" \
    "global_load_lds_dwordx4 %2, %4 offset:2048\n\tglobal_load_lds_dwordx4 %3, %4 offset:3072" ::"v"(v0_), "v"(v1_), "v"(v2_), "v"(v3_), "s"(sb_), "s"(ld_) : "memory")
#define ST_X(b, kt) GLDS4(voffX[0], voffX[1], voffX[2], voffX[3], xb + (kt) * 128, lds0 + SX(b, 0) + wid * 4096)
#define ST_W0(wo_, kt) GLDS2(voffQ[0], voffQ[1], qb + (size_t)(kt) * 524288, lds0 + (wo_) + wid * 2048)
#define ST_W1(wo_, kt) GLDS2(voffH[0], voffH[1], hb + (size_t)(kt) * 458752, lds0 + (wo_) + 16384 + wid * 2048)

  lptr wb[4], xr[2];
#pragma unroll
  for (int m = 0; m < 4; ++m) wb[m] = lds + wr * 1024 + (g >> 1) * 2048 + (4 * (g & 1) + q4) * 128 + 32 * ((m ^ wc) ^ (2 * (g & 1) + (q4 >> 1))) + 8 * p4;
#pragma unroll
  for (int ks = 0; ks < 2; ++ks) xr[ks] = lds + 98304 + (wc * 32 + t) * 128 + 16 * ((4 * ks + g) ^ (t >> 1));
#define LDW(dst, h) do { _Pragma("unroll") for (int m = 0; m < 4; ++m) _Pragma("unroll") for (int k = 0; k < 2; ++k) dst[m][k] = ldtr2(wb[m] + (h) * 16384 + k * 8192, 4096); } while (0)
#define LDX(dst, b, h) do { _Pragma("unroll") for (int n = 0; n < 2; ++n) _Pragma("unroll") for (int k = 0; k < 2; ++k) dst[n][k] = ldb128(xr[k] + (SX(b, h) - 98304) + n * 2048); } while (0)

  f32x4 acc[2][2][4][2];
#pragma unroll
  for (int a = 0; a < 2; ++a)
#pragma unroll
    for (int b = 0; b < 2; ++b)
#pragma unroll
      for (int m = 0; m < 4; ++m)
#pragma unroll
        for (int n = 0; n < 2; ++n) acc[a][b][m][n] = (f32x4){0.f, 0.f, 0.f, 0.f};
  bf16x8 At[4][2], B0[2][2], B1[2][2];

#define MM1(ai, m, k, j) do { if ((j) < 2) acc[ai][0][m][(j) & 1] = MFMA16(At[m][k], B0[(j) & 1][k], acc[ai][0][m][(j) & 1]); else acc[ai][1][m][(j) & 1] = MFMA16(At[m][k], B1[(j) & 1][k], acc[ai][1][m][(j) & 1]); SCHED(); } while (0)
#define MM4(ai, m, k) do { _Pragma("unroll") for (int q = 0; q < 4; ++q) MM1(ai, m, k, ((m) & 1) ? 3 - q : q); } while (0)
#define MMA2(ai, CLOSE) do { __builtin_amdgcn_s_setprio(1); \
    cs[ai] = MFMA16(At[0][0], ones, cs[ai]); SCHED(); MM4(ai, 0, 0); MM4(ai, 1, 0); \
    if ((ai) == 0) { MM4(ai, 2, 0); MM4(ai, 3, 0); } \
    cs[ai] = MFMA16(At[0][1], ones, cs[ai]); SCHED(); MM4(ai, 0, 1); MM4(ai, 1, 1); \
    asm volatile("" : "+v"(cs[ai])); \
    if ((ai) == 0 || full) { MM4(ai, 2, 1); MM4(ai, 3, 1); if ((ai) == 1) { MM4(ai, 2, 0); MM4(ai, 3, 0); } } \
    SCHED(); CLOSE; __builtin_amdgcn_s_setprio(0); SCHED(); } while (0)
  char* const stats = ws + WS_STATS;
  const __amdgpu_buffer_rsrc_t wrs = __builtin_amdgcn_make_buffer_rsrc(ws, 0, WS_END, 0x00020000);
  f32x4 cs[2] = {{0.f, 0.f, 0.f, 0.f}, {0.f, 0.f, 0.f, 0.f}};
  bf16x8 ones = __builtin_bit_cast(bf16x8, (u32x4){0x3f803f80u, 0x3f803f80u, 0x3f803f80u, 0x3f803f80u});
#define PIN_SETUP() do { asm volatile("" : "+v"(ones), "+v"(cs[0]), "+v"(cs[1]), "+v"(wb[0]), "+v"(wb[1]), "+v"(wb[2]), "+v"(wb[3]), "+v"(xr[0]), "+v"(xr[1])); \
    _Pragma("unroll") for (int a = 0; a < 2; ++a) _Pragma("unroll") for (int b = 0; b < 2; ++b) \
      asm volatile("" : "+v"(acc[a][b][0][0]), "+v"(acc[a][b][0][1]), "+v"(acc[a][b][1][0]), "+v"(acc[a][b][1][1]), "+v"(acc[a][b][2][0]), "+v"(acc[a][b][2][1]), "+v"(acc[a][b][3][0]), "+v"(acc[a][b][3][1])); } while (0)
  unsigned tag;
  {
    if (wr == 0) {
      unsigned ep;
      ST_X(0, 0); ST_W0(0, 0);
      GLDS4(voffX[0], voffX[1], voffX[2], voffX[3], xb + 262144, lds0 + SX(0, 1) + wid * 4096);
      GLDS2(voffQ[0], voffQ[1], qb + 262144, lds0 + 8192 + wid * 2048);
      asm volatile("global_load_dword %0, %1, %2 sc1" : "=&v"(ep) : "v"(0), "s"(ws) : "memory");
      PIN_SETUP();
      asm volatile("s_waitcnt vmcnt(0)" : "+v"(ep) :: "memory");
      tag = __builtin_amdgcn_readfirstlane(ep) + 1u;
      BAR();
    } else {
      const int row = rb * 256 + hd * 8 + wc * 2 + (lane >> 5);
      u32x4 a[4];
      unsigned ep;
      asm volatile("global_load_dword %0, %1, %2 sc1" : "=&v"(ep) : "v"(0), "s"(ws) : "memory");
      asm volatile("global_load_dwordx4 %0, %4, %5\n\tglobal_load_dwordx4 %1, %4, %5 offset:512\n\tglobal_load_dwordx4 %2, %4, %5 offset:1024\n\tglobal_load_dwordx4 %3, %4, %5 offset:1536"
                   : "=&v"(a[0]), "=&v"(a[1]), "=&v"(a[2]), "=&v"(a[3]) : "v"(row * 2048 + (lane & 31) * 16), "s"(xn) : "memory");
      ST_W1(0, 0); GLDS2(voffH[0], voffH[1], hb - 229376, lds0 + 16384 + wc * 2048);
      PIN_SETUP();
      BAR();
      ST_W0(32768, 1); GLDS2(voffQ[0], voffQ[1], qb + 524288 - 262144, lds0 + 32768 + wc * 2048);
      ST_X(1, 1); GLDS4(voffX[0], voffX[1], voffX[2], voffX[3], xb - 262144 + 128, lds0 + SX(1, 0) + wc * 4096);
      asm volatile("s_waitcnt vmcnt(16)" : "+v"(ep), "+v"(a[0]), "+v"(a[1]), "+v"(a[2]), "+v"(a[3]) :: "memory");
      tag = __builtin_amdgcn_readfirstlane(ep) + 1u;
      float v[32];
#pragma unroll
      for (int i = 0; i < 16; ++i) { v[2 * i] = bf2f(a[i >> 2][i & 3] & 0xffffu); v[2 * i + 1] = U2F(a[i >> 2][i & 3] & 0xffff0000u); }
      float s0 = 0.f, s1 = 0.f, s2 = 0.f, s3 = 0.f;
#pragma unroll
      for (int i = 0; i < 8; ++i) { s0 += v[i]; s1 += v[8 + i]; s2 += v[16 + i]; s3 += v[24 + i]; }
      const float mean = half_sum((s0 + s1) + (s2 + s3)) * (1.0f / 1024.0f);
      s0 = s1 = s2 = s3 = 0.f;
#pragma unroll
      for (int i = 0; i < 8; ++i) { const float d0 = v[i] - mean, d1 = v[8 + i] - mean, d2 = v[16 + i] - mean, d3 = v[24 + i] - mean; s0 += d0 * d0; s1 += d1 * d1; s2 += d2 * d2; s3 += d3 * d3; }
      const float sd = __builtin_amdgcn_sqrtf(half_sum((s0 + s1) + (s2 + s3)) * (1.0f / 1024.0f) + 1e-5f);
      if ((lane & 31) < 2) __hip_atomic_store((gu64*)(stats + row * 16 + (lane & 1) * 8), ((unsigned long long)tag << 32) | F2U((lane & 1) ? sd : mean), __ATOMIC_RELAXED, __HIP_MEMORY_SCOPE_AGENT);
    }
  }
  if (wr == 1) BAR();
  SCHED();
  STAMP(1);
  const int kvg = hd >> 3;
  const char* kb_g = kc + ((size_t)kvg * 2048 + 1536) * 256;
  const char* vb_g = vc + ((size_t)kvg * 2048 + 1536) * 256;
#define VOFFK ((unsigned)(((lane >> 2) & 7) * 256 + (4 * (lane >> 5) + ((lane & 3) ^ ((2 * (wid & 1) + ((lane >> 4) & 1)) & 3))) * 16))
#define VOFFV ((unsigned)(((lane >> 2) & 7) * 256 + (lane >> 5) * 64 + (lane & 3) * 16))
#define ST_KV8(voff, gsrc, slot) do { glds(voff, (gsrc) + wid * 2048, lds0 + (slot) + wid * 2048); glds(voff, (gsrc) + wid * 2048 + 128, lds0 + (slot) + wid * 2048 + 1024); } while (0)
  int wprv = 65536, wnxt = 32768;
#define W_ADV() do { const int d_ = (wnxt == 0) ? -65536 : 32768, cur_ = wnxt - d_; wnxt = wprv; wprv = cur_; \
    _Pragma("unroll") for (int f = 0; f < 4; ++f) wb[f] += d_; \
    asm volatile("" : "+v"(wb[0]), "+v"(wb[1]), "+v"(wb[2]), "+v"(wb[3])); } while (0)
#define PH_A(b, STAGE) do { LDX(B0, b, 0); LDX(B1, b, 1); SCHED(); LDW(At, 0); STAGE; WAIT_V(10); WAIT_L(12); BAR(); SCHED(); MMA2(0, WAIT_L(0); BAR()); } while (0)
#define PH_B(STAGE) do { LDW(At, 1); STAGE; W_ADV(); WAIT_V(8); BAR(); SCHED(); MMA2(1, WAIT_L(0); BAR()); } while (0)
#pragma unroll 1
  for (int kt = 0; kt < 14; kt += 2) {
    PH_A(0, ST_W1(wnxt, kt + 1); ST_W0(wprv, kt + 2));
    PH_B(ST_X(0, kt + 2));
    PH_A(1, ST_W1(wnxt, kt + 2); ST_W0(wprv, kt + 3));
    PH_B(ST_X(1, kt + 3));
  }
  STAMP(2);
  const unsigned voffK = VOFFK, voffV = VOFFV;
  PH_A(0, ST_W1(0, 15); ST_KV8(voffK, kb_g, SLOT_E));
  PH_B(ST_KV8(voffV, vb_g, SLOT_C); ST_KV8(voffV, vb_g + 16384, SLOT_C + 16384));
  const unsigned soff = (unsigned)((rb * 256 + (g >> 1) * 128 + wc * 32 + (g & 1) * 16 + t) * 16);
  u32x4 sg;
  LDX(B0, 1, 0); LDX(B1, 1, 1); SCHED(); LDW(At, 0);
  asm volatile("global_load_dwordx4 %0, %1, %2 sc1" : "=&v"(sg) : "v"(soff), "s"(stats) : "memory");
  ST_KV8(voffK, kb_g + 16384, SLOT_E + 16384); ST_KV8(voffK, kb_g + 32768, SLOT_A);
  WAIT_V(11); BAR(); SCHED(); MMA2(0, WAIT_L(0); BAR());
  LDW(At, 1);
  ST_KV8(voffK, kb_g + 49152, SLOT_A + 16384);
  BAR(); SCHED();
  const lptr csw = lds + SLOT_D + (t & 3) * 8192 + wr * 4096 + wc * 64 + g * 16;
  MMA2(1, *(LAS f32x4*)csw = cs[0]; *(LAS f32x4*)(csw + 256) = cs[1]; asm volatile("s_waitcnt vmcnt(4)" : "+v"(sg) :: "memory"); WAIT_L(0); BAR());
  f32x4 cv[2][4];
#pragma unroll
  for (int a = 0; a < 2; ++a)
#pragma unroll
    for (int m = 0; m < 4; ++m) cv[a][m] = *(const LAS f32x4*)(lds + SLOT_D + wc * 8192 + wr * 4096 + a * 256 + (m ^ wc) * 64 + g * 16);
  if (__builtin_expect(__builtin_amdgcn_ballot_w64(sg[1] != tag || sg[3] != tag) != 0ull, 0)) {
    for (unsigned spins = 0; spins < 4000000u; ++spins) {
      __builtin_amdgcn_s_sleep(2);
      asm volatile("" ::: "memory");
      sg = __builtin_amdgcn_raw_buffer_load_b128(wrs, soff, WS_STATS, 16);
      if (__builtin_amdgcn_ballot_w64(sg[1] != tag || sg[3] != tag) == 0ull) break;
    }
  }
  float mu[2][2], rs[2][2], sd[2][2];
  {
    auto m16 = __builtin_amdgcn_permlane16_swap(sg[0], sg[0], false, false);
    auto s16 = __builtin_amdgcn_permlane16_swap(sg[2], sg[2], false, false);
#pragma unroll
    for (int n = 0; n < 2; ++n) {
      auto m32 = __builtin_amdgcn_permlane32_swap(m16[n], m16[n], false, false);
      auto s32 = __builtin_amdgcn_permlane32_swap(s16[n], s16[n], false, false);
#pragma unroll
      for (int bj = 0; bj < 2; ++bj) { mu[bj][n] = -U2F(m32[bj]); sd[bj][n] = U2F(s32[bj]); rs[bj][n] = __builtin_amdgcn_rcpf(sd[bj][n]) * 1.4426950408889634f; }
    }
  }
  WAIT_L(0);
  STAMP(3);
  {
    const lptr qw = lds + wc * 8192 + wr * 4096 + ((g >> 1) * 32 + t) * 16 + (g & 1) * 8;
#pragma unroll
    for (int bj = 0; bj < 2; ++bj) {
      lptr qwb = qw + (bj ? SLOT_D : SLOT_B);
      asm volatile("" : "+v"(qwb));
#pragma unroll
      for (int m = 0; m < 4; ++m)
#pragma unroll
        for (int n = 0; n < 2; ++n) {
          const f32x4 v = (acc[0][bj][m][n] + cv[0][m] * mu[bj][n]) * (rs[bj][n] * 0.08838834764831845f);
          u32x2 w = {pk2(v[0], v[1]), pk2(v[2], v[3])};
          *(LAS u32x2*)(qwb + (m ^ wc) * 1024 + n * 256) = w;
        }
    }
  }
  WAIT_L(0); BAR(); SCHED();
  STAMP(4);
  unsigned pft = 0;
  const int sl = ((idx >> 2) * 16 + (idx & 1) * 8 + xcd) * 8 + wid;
#define TOUCH(q_) do { const int o_ = light ? ((((q_) + 2) & 3) * 916 + (sl < 916 ? sl : 915)) : ((((q_) + 2) & 3) * 1024 + sl); \
    asm volatile("global_load_dword %0, %1, %2" : "+v"(pft) : "v"((unsigned)((__builtin_amdgcn_mbcnt_lo(~0u, 0u) & 15) * 128)), "s"((light ? w2 : wo) + (light && o_ > 3583 ? 3583 : o_) * 2048) : "memory"); } while (0)
  TOUCH(0);
  bf16x8 qf[8];
  const lptr qr = lds + (wr ? SLOT_D : SLOT_B) + wc * 8192 + lane * 16;
  if (wr == 1) {
#pragma unroll
    for (int s = 0; s < 8; ++s) qf[s] = ldb128(qr + s * 1024);
  }
#define ST128(...) ST128_(wrs, __VA_ARGS__)
  u32x4 hq[3];
  unsigned hqo;
  {
    u32x2 hw[2][2][2];
#define SILU16(m, ACT) do { \
      const f32x4 a0 = acc[1][0][m][0] + cv[1][m] * mu[0][0], a1 = acc[1][0][m][1] + cv[1][m] * mu[0][1], a2 = acc[1][1][m][0] + cv[1][m] * mu[1][0], a3 = acc[1][1][m][1] + cv[1][m] * mu[1][1]; \
      f32x4 e0 = a0 * -rs[0][0], e1 = a1 * -rs[0][1], e2 = a2 * -rs[1][0], e3 = a3 * -rs[1][1]; SCHED(); \
      if (ACT) { \
      _Pragma("unroll") for (int i = 0; i < 4; ++i) { e0[i] = __builtin_amdgcn_exp2f(e0[i]); e1[i] = __builtin_amdgcn_exp2f(e1[i]); e2[i] = __builtin_amdgcn_exp2f(e2[i]); e3[i] = __builtin_amdgcn_exp2f(e3[i]); } SCHED(); \
      e0 = e0 * sd[0][0] + sd[0][0]; e1 = e1 * sd[0][1] + sd[0][1]; e2 = e2 * sd[1][0] + sd[1][0]; e3 = e3 * sd[1][1] + sd[1][1]; SCHED(); \
      _Pragma("unroll") for (int i = 0; i < 4; ++i) { e0[i] = __builtin_amdgcn_rcpf(e0[i]); e1[i] = __builtin_amdgcn_rcpf(e1[i]); e2[i] = __builtin_amdgcn_rcpf(e2[i]); e3[i] = __builtin_amdgcn_rcpf(e3[i]); } SCHED(); \
      e0 *= a0; e1 *= a1; e2 *= a2; e3 *= a3; SCHED(); } \
      else { _Pragma("unroll") for (int i = 0; i < 4; ++i) e3[i] = a3[i] * __builtin_amdgcn_rcpf(__builtin_amdgcn_exp2f(e3[i]) * sd[1][1] + sd[1][1]); } \
      hw[0][0][(m) & 1] = (u32x2){pk2(e0[0], e0[1]), pk2(e0[2], e0[3])}; hw[0][1][(m) & 1] = (u32x2){pk2(e1[0], e1[1]), pk2(e1[2], e1[3])}; \
      hw[1][0][(m) & 1] = (u32x2){pk2(e2[0], e2[1]), pk2(e2[2], e2[3])}; hw[1][1][(m) & 1] = (u32x2){pk2(e3[0], e3[1]), pk2(e3[2], e3[3])}; SCHED(); } while (0)
#define HFRAG(bj, n) ((wc & 1) ? (u32x4){hw[bj][n][1][0], hw[bj][n][1][1], hw[bj][n][0][0], hw[bj][n][0][1]} : (u32x4){hw[bj][n][0][0], hw[bj][n][0][1], hw[bj][n][1][0], hw[bj][n][1][1]})
    const unsigned hoff = (unsigned)(WS_H + (rb * 16 + wc * 2) * 2048 + lane * 16);
    const int pb = (light ? 16 + 3 * hd : 4 * hd) + (wr ? 0 : 2), sw = full ? (wc >> 1) : 0, p0 = pb + sw, p1 = pb + (sw ^ 1);
    SILU16(0, false); SILU16(1, false);
    hqo = hoff + (p0 >> 1) * 262144 + (p0 & 1) * 1024;
    hq[0] = HFRAG(0, 0); hq[1] = HFRAG(0, 1); hq[2] = HFRAG(1, 0);
    ST128(HFRAG(1, 1), hqo + 2048, 16384);
    asm volatile("" : "+v"(hq[0]), "+v"(hq[1]), "+v"(hq[2]));
    if (wr == 0) {
      BAR(); SCHED();
#pragma unroll
      for (int s = 0; s < 8; ++s) qf[s] = ldb128(qr + s * 1024);
    }
    if (full) {
      SILU16(2, true); SILU16(3, true);
#pragma unroll
      for (int bj = 0; bj < 2; ++bj)
#pragma unroll
        for (int n = 0; n < 2; ++n) ST128(HFRAG(bj, n), hoff + n * 2048, (p1 >> 1) * 262144 + (p1 & 1) * 1024 + bj * 16384);
    }
  }
  STAMP(5);
  if (wr == 1) { WAIT_L(0); BAR(); }
  __builtin_amdgcn_s_setprio(1);
  SCHED();
  STAMP(6);
  const int r32 = lane & 31, hh = lane >> 5;
  lptr kr[2];
#pragma unroll
  for (int e = 0; e < 2; ++e) kr[e] = lds + 2048 * (r32 >> 3) + 64 * (r32 & 7) + 16 * ((2 * e + hh) ^ ((r32 >> 2) & 3));
  const lptr vr = lds + ((lane & 3) << 3) + (((lane >> 2) & 3) << 6) + (((lane >> 4) & 1) << 5) + (hh << 8);
  f32x16 o[4], s[4];
#pragma unroll
  for (int d = 0; d < 4; ++d)
#pragma unroll
    for (int i = 0; i < 16; ++i) o[d][i] = 0.f;
  bf16x8 pf[8];
  constexpr int RD = 6;
  bf16x8 fr[RD];
  f32x4 lacc = {0.f, 0.f, 0.f, 0.f};
  const unsigned selw = (((lane >> 4) ^ (lane >> 2)) & 1) ? 0u : 0x3f803f80u;
  bf16x8 sel = __builtin_bit_cast(bf16x8, (u32x4){selw, selw, selw, selw});
  asm volatile("" : "+v"(sel));
  f32x16 negm = {0.f, 0.f, 0.f, 0.f, 0.f, 0.f, 0.f, 0.f, 0.f, 0.f, 0.f, 0.f, 0.f, 0.f, 0.f, 0.f};
#define SM_THR 60.0f
#define FRAG(i) ((i) < 32 ? ldtr2(v0 + (((i) >> 2) * 8 + ((i) & 3)) * 512, 2048) : ldb128((((((i) - 32) >> 2) & 1) ? k1 : k0) + ((i) & 3) * 8192 + (((i) - 32) >> 3) * 512))
#define XEARLY 3
#define XPRIO 1
#define YPRIO 0
#define XSEG(lo, hi, CLOSE) do { __builtin_amdgcn_s_setprio(XPRIO); \
    _Pragma("unroll") for (int i = (lo); i < (hi); ++i) { \
      if (i < 32) o[i & 3] = MFMA32(fr[i % RD], pf[(i >> 2) & 7], o[i & 3]); \
      else s[i & 3] = MFMA32(fr[i % RD], qf[((i - 32) >> 2) & 7], i < 36 ? negm : s[i & 3]); \
      if (i < 32 && (i & 3) == 1) lacc = MFMA16(sel, pf[(i >> 2) & 7], lacc); \
      if (i + RD < (hi)) fr[i % RD] = FRAG(i + RD); \
      SCHED(); \
      if (i == 63 - XEARLY) { __builtin_amdgcn_s_setprio(2); CLOSE; WAIT_L(0); BAR(); SCHED(); } } \
    __builtin_amdgcn_s_setprio(YPRIO); } while (0)
#define PREF(lo) do { _Pragma("unroll") for (int i = (lo); i < (lo) + RD; ++i) fr[i % RD] = FRAG(i); SCHED(); } while (0)
#define MX3(a, b, c) __builtin_fmaxf(__builtin_fmaxf((a), (b)), (c))
#define SOFTMAX() do { \
      float m0 = MX3(s[0][0], s[0][1], s[0][2]), m1 = MX3(s[1][0], s[1][1], s[1][2]), m2 = MX3(s[2][0], s[2][1], s[2][2]), m3 = MX3(s[3][0], s[3][1], s[3][2]); \
      _Pragma("unroll") for (int i = 3; i < 15; i += 2) { m0 = MX3(m0, s[0][i], s[0][i + 1]); m1 = MX3(m1, s[1][i], s[1][i + 1]); m2 = MX3(m2, s[2][i], s[2][i + 1]); m3 = MX3(m3, s[3][i], s[3][i + 1]); } \
      m0 = MX3(m0, s[0][15], s[1][15]); m2 = MX3(m2, s[2][15], s[3][15]); \
      float mx = __builtin_fmaxf(MX3(m0, m1, m2), m3); \
        \
      if (__builtin_expect(__builtin_amdgcn_ballot_w64(!(mx <= SM_THR)) != 0ull, 0)) { \
        auto rr = __builtin_amdgcn_permlane32_swap(F2U(mx), F2U(mx), false, false); \
        const float dl = MX3(U2F(rr[0]), U2F(rr[1]), 0.f); \
        _Pragma("unroll") for (int kb = 0; kb < 4; ++kb) s[kb] -= dl; \
        negm -= dl; \
        const float alpha = __builtin_amdgcn_exp2f(-dl); lacc *= alpha; _Pragma("unroll") for (int d = 0; d < 4; ++d) o[d] *= alpha; \
      } \
      SCHED(); \
      _Pragma("unroll") for (int kb = 0; kb < 4; ++kb) { \
        _Pragma("unroll") for (int e = 0; e < 2; ++e) { \
          _Pragma("unroll") for (int i = 0; i < 8; ++i) s[kb][8 * e + i] = __builtin_amdgcn_exp2f(s[kb][8 * e + i]); \
          u32x4 w = {pk2(s[kb][8 * e], s[kb][8 * e + 1]), pk2(s[kb][8 * e + 2], s[kb][8 * e + 3]), pk2(s[kb][8 * e + 4], s[kb][8 * e + 5]), pk2(s[kb][8 * e + 6], s[kb][8 * e + 7])}; \
          pf[2 * kb + e] = __builtin_bit_cast(bf16x8, w); } \
        asm volatile("" : "+v"(pf[2 * kb]), "+v"(pf[2 * kb + 1])); \
        SCHED(); } } while (0)
  {
    const lptr k0 = kr[0] + SLOT_E, k1 = kr[1] + SLOT_E, v0 = vr;
    {
      bf16x8 kf[8];
#pragma unroll
      for (int st = 0; st < 8; ++st) kf[st] = FRAG(32 + 4 * st);
      PREF(32);
#pragma unroll
      for (int st = 0; st < 8; ++st) negm = MFMA32(kf[st], qf[st], negm);
      float m0 = MX3(negm[0], negm[1], negm[2]), m1 = MX3(negm[3], negm[4], negm[5]);
      m0 = MX3(m0, negm[6], negm[7]); m1 = MX3(m1, negm[8], negm[9]); m0 = MX3(m0, negm[10], negm[11]); m1 = MX3(m1, negm[12], negm[13]);
      m0 = __builtin_fmaxf(MX3(m0, negm[14], negm[15]), m1);
      auto rr = __builtin_amdgcn_permlane32_swap(F2U(m0), F2U(m0), false, false);
      m0 = -__builtin_fmaxf(U2F(rr[0]), U2F(rr[1]));
#pragma unroll
      for (int i = 0; i < 16; ++i) negm[i] = m0;
      asm volatile("" : "+v"(negm));
      SCHED();
    }
    XSEG(32, 64, WAIT_V(1));
  }
  STAMP(7);
  const char* kvsrc = (wr ? kb_g + 65536 : vb_g + 32768) + wc * 8192 - 1024;
  unsigned kvo[2][2];
#pragma unroll
  for (int par = 0; par < 2; ++par) {
    const int xx = wr ? ((lane & 3) ^ ((2 * par + ((lane >> 4) & 1)) & 3)) : (lane & 3);
    kvo[par][0] = (unsigned)(((lane >> 2) & 7) * 256 + (lane >> 5) * 64 + xx * 16 + 1024);
    kvo[par][1] = kvo[par][0] + 128 - 1024;
  }
#pragma unroll 1
  for (int c = 0; c < 3; ++c) {
    if (c == 0) STAMP(8); else if (c == 1) STAMP(12);
    ST128((u32x4){silu_pk(hq[0][0]), silu_pk(hq[0][1]), silu_pk(hq[0][2]), silu_pk(hq[0][3])}, hqo, c == 2 ? 16384 : c * 2048);
    hq[0] = hq[1]; hq[1] = hq[2];
    SCHED();
    if (c + wr < 3) {
      const int slot = wr ? (c == 0 ? SLOT_B : SLOT_C) : (c == 0 ? SLOT_D : (c == 1 ? SLOT_E : SLOT_A));
#pragma unroll
      for (int hf = 0; hf < 2; ++hf)
        asm volatile("s_mov_b32 m0, %5\n\ts_nop 0\n\t"
                     "global_load_lds_dwordx4 %0, %4\n\tglobal_load_lds_dwordx4 %1, %4 offset:1024\n\t"
                     "global_load_lds_dwordx4 %2, %4 offset:2048\n\tglobal_load_lds_dwordx4 %3, %4 offset:3072"
                     ::"v"(kvo[0][0]), "v"(kvo[0][1]), "v"(kvo[1][0]), "v"(kvo[1][1]), "s"(kvsrc + c * 32768 + hf * 4096), "s"(lds0 + slot + wc * 8192 + hf * 4096) : "memory");
    }
    TOUCH(c + 1);
    SOFTMAX();
    const int kslot = (c == 0) ? SLOT_A : ((c == 1) ? SLOT_B : SLOT_C);
    const int vslot = (c == 0) ? SLOT_C : ((c == 1) ? SLOT_D : SLOT_E);
    const lptr k0 = kr[0] + kslot, k1 = kr[1] + kslot, v0 = vr + vslot;
    PREF(0);
    if (c == 0) STAMP(9); else if (c == 1) STAMP(13);
    BAR(); SCHED();
    if (c == 0) STAMP(10); else if (c == 1) STAMP(14);
    XSEG(0, 64, WAIT_V(1); if (c == 2) WAIT_V(0));
    if (c == 0) STAMP(11); else if (c == 2) STAMP(15);
  }
  if (wid == 4 && lane == 0) __builtin_amdgcn_raw_buffer_store_b32(tag, wrs, 0, WS_HFLAG + (rb * 32 + hd) * 128, 16);
  STAMP(16);
  SOFTMAX();
  {
    const lptr v0 = vr + SLOT_A;
#define FRAGD(i) ldtr2(v0 + (((i) & 7) * 8 + ((i) >> 3)) * 512, 2048)
#pragma unroll
    for (int i = 0; i < RD; ++i) fr[i] = FRAGD(i);
    asm volatile("s_waitcnt vmcnt(0)" :: "v"(pft) : "memory");
    SCHED();
    STAMP(17);
    if (wr == 0) BAR();
    SCHED();
    STAMP(18);
    __builtin_amdgcn_s_setprio(1);
    const unsigned oo = (unsigned)(WS_O + 2 * hd * 262144 + (rb * 16 + wr * 8 + wc * 2 + (r32 >> 4)) * 2048 + (hh * 16 + (r32 & 15)) * 16);
    float inv = 0.f;
    u32x4 ow;
#pragma unroll
    for (int i = 0; i < 42; ++i) {
      if (i < 32) o[i >> 3] = MFMA32(fr[i % RD], pf[i & 7], o[i >> 3]);
      if (i < 8) lacc = MFMA16(sel, pf[i], lacc);
      if (i + RD < 32) fr[i % RD] = FRAGD(i + RD);
      if (i == 9) inv = __builtin_amdgcn_rcpf(lacc[0]);
      if (i >= 10) {
        const int d = (i - 10) >> 3, q = (i - 10) & 7;
        ow[q & 3] = pk2(o[d][2 * q] * inv, o[d][2 * q + 1] * inv);
        asm volatile("" : "+v"(ow));
        if ((q & 3) == 3) ST128(ow, oo + (d & 1) * 1024 + (q >> 2) * 512, (d >> 1) * 262144);
      }
      SCHED();
    }
    __builtin_amdgcn_s_setprio(0);
  }
  STAMP(19);
  int lane2 = __builtin_amdgcn_mbcnt_hi(~0u, __builtin_amdgcn_mbcnt_lo(~0u, 0u));
  asm volatile("" : "+v"(lane2));
#define lane lane2
  const int isG = (idx >> 1) & 1, kh = xcd & 1, tm = 4 * (xcd >> 1) + ((idx >> 1) & 2) + (idx & 1), tn = 2 * (idx >> 3) + kh;
#define KDS 1
#define KDG 1
  const int kd = isG ? KDG : KDS, sd2 = kh ? -kd : kd;
  const int npw = (isG ? 48 : 56) + 2 * sd2;
  const int k2c = ((npw + 19) % 6 < 4) ? K2_C : 0;
#define O_FLAG() do { if (wid == 0 && lane == 0) __builtin_amdgcn_raw_buffer_store_b32(tag, wrs, 0, WS_OFLAG + (rb * 32 + hd) * OFS, 16); } while (0)
#define POLL32(off_, slp) do { for (unsigned spins = 0; spins < 2000000u; ++spins) { \
      if (__builtin_amdgcn_ballot_w64(__builtin_amdgcn_raw_buffer_load_b32(wrs, (off_), 0, 16) != tag) == 0ull) break; \
      __builtin_amdgcn_s_sleep(slp); asm volatile("" ::: "memory"); } \
    asm volatile("" ::: "memory"); } while (0)
  const int c8 = lane & 15, r0 = wc * 4 + (lane >> 4);
  const unsigned goff = (unsigned)((tm * 128 + r0) * 2048 + tn * 256 + c8 * 16);
  const lptr cb = lds + k2c + r0 * 512;
#define G_FIN() do { f32x4 a0[4], a1[4]; \
      _Pragma("unroll") for (int i = 0; i < 4; ++i) { a0[i] = *(const LAS f32x4*)(cb + (wr * 4 + i) * 8192 + (((2 * c8) ^ (r0 & 7)) << 4)); a1[i] = *(const LAS f32x4*)(cb + (wr * 4 + i) * 8192 + (((2 * c8 + 1) ^ (r0 & 7)) << 4)); } \
      _Pragma("unroll") for (int i = 0; i < 4; ++i) \
        ST128((u32x4){pk2(silu(a0[i][0]), silu(a0[i][1])), pk2(silu(a0[i][2]), silu(a0[i][3])), pk2(silu(a1[i][0]), silu(a1[i][1])), pk2(silu(a1[i][2]), silu(a1[i][3]))}, goff + i * 32768, WS_G + wr * 131072); \
      WAIT_V(0); BAR(); } while (0)
  const unsigned f1 = isG ? (unsigned)(WS_HFLAG + ((tm >> 1) * 32 + 16 + (lane & 15)) * 128) : (unsigned)(WS_OFLAG + ((tm >> 1) * 32 + 16 + (lane & 15)) * OFS);
  STAMP(20);
  if (wr == 0) {
    const char* ab = ws + (isG ? WS_H : WS_O) + tm * 16384 + wc * 4096;
    const char* wbp = (isG ? w2 : wo) + (tn >> 1) * 512 - 3072 + (isG ? wc : (((wc & 1) << 1) | (wc >> 1))) * 16384;
    const unsigned voffA0 = lane * 16, voffA1 = lane * 16 + 1024;
    unsigned voffW[4];
    {
      const int r = lane >> 3, c = lane & 7;
      const unsigned lp = (unsigned)(r * 2048 + ((c ^ (((r >> 1) & 3) << 1)) << 4) + 3072);
      voffW[0] = lp + kh * 256; voffW[1] = voffW[0] + 128 - 1024; voffW[2] = lp + (kh ^ 1) * 256 - 2048; voffW[3] = voffW[2] + 128 - 1024;
    }
    const char *ap, *wp;
    unsigned dst = lds0 + 4 * K2_STG;
#define K2_RUN(k_) do { ap = ab + (size_t)(k_) * 262144; wp = wbp + (size_t)(k_) * 131072; } while (0)
#define K2_ISSUE(WIDE, ks) do { \
      asm volatile("s_mov_b32 m0, %3\n\ts_nop 0\n\tglobal_load_lds_dwordx4 %0, %2 sc1\n\tglobal_load_lds_dwordx4 %1, %2 offset:1024 sc1" \
                   ::"v"(voffA0), "v"(voffA1), "s"(ap + (ks) * 1024), "s"(dst + wc * 2048) : "memory"); \
      if (WIDE) GLDS4(voffW[0], voffW[1], voffW[2], voffW[3], wp + (ks) * 65536, dst + 8192 + wc * 4096); \
      else GLDS2(voffW[0], voffW[1], wp + (ks) * 65536, dst + 8192 + wc * 4096); \
      dst = (dst == lds0 + 5 * K2_STG) ? lds0 : dst + K2_STG; \
      if (ks) { ap += 262144; wp += 131072; } } while (0)
#define K2_STEP(WIDE, ks, EVENT) do { WAIT_V((WIDE) ? 18 : 12); BAR(); EVENT; K2_ISSUE(WIDE, ks); } while (0)
#define K2_TILES(WIDE, n_) do { _Pragma("unroll 1") for (int i_ = 0; i_ < (n_); ++i_) { K2_STEP(WIDE, 0, ); K2_STEP(WIDE, 1, ); } } while (0)
#define LOOK(f_) asm volatile("buffer_load_dword %0, %1, %2, 0 offen sc1" : "+v"(pol) : "v"(f_), "s"(wrs) : "memory")
    const unsigned f2 = f1 - 16 * (isG ? 128 : OFS);
    const unsigned f3 = (unsigned)(WS_PFLAG + (bid ^ 1) * 128);
    u32x4 xh[8];
#define XH_LD(i) asm volatile("global_load_dwordx4 %0, %1, %2" : "=v"(xh[i]) : "v"(goff), "s"(xn + (i) * 32768) : "memory")
    POLL32(f1, 4);
    WAIT_V(0);
    if (isG) { BAR(); O_FLAG(); }
    K2_RUN(32 + (isG ? 12 : 14) * kh);
    K2_ISSUE(1, 0); K2_ISSUE(1, 1);
    STAMP(21);
    WAIT_V(6); BAR();
    K2_ISSUE(1, 0); K2_ISSUE(1, 1); K2_ISSUE(1, 0);
    STAMP(22);
    unsigned pol = 0;
    K2_STEP(1, 1, );
    K2_TILES(1, 4);
    if (!isG) O_FLAG();
    K2_TILES(1, isG ? 3 : 5);
    LOOK(f2);
    K2_TILES(1, 2);
    K2_RUN(((isG ? 12 : 14) + kd) * kh);
    K2_STEP(1, 0, asm volatile("" : "+v"(pol)); if (__builtin_amdgcn_ballot_w64(pol != tag) != 0ull) POLL32(f2, 4));
    K2_STEP(1, 1, );
#pragma unroll
    for (int i = 0; i < 8; ++i) { K2_STEP(1, 0, XH_LD(i)); K2_STEP(1, 1, ); }
    K2_TILES(1, (isG ? 3 : 5) + sd2);
    asm volatile("" : "+v"(xh[0]), "+v"(xh[1]), "+v"(xh[2]), "+v"(xh[3]), "+v"(xh[4]), "+v"(xh[5]), "+v"(xh[6]), "+v"(xh[7]));
    K2_RUN(isG ? 24 : 60);
    K2_TILES(0, 4);
    K2_RUN(28);
    K2_STEP(0, 0, );
    K2_STEP(0, 1, if (wid == 0 && lane == 0) __builtin_amdgcn_raw_buffer_store_b32(tag, wrs, 0, WS_PFLAG + bid * 128, 16));
    K2_STEP(0, 0, ); K2_STEP(0, 1, );
    const unsigned f4 = (lane & 1) ? (unsigned)(WS_GFLAG + (tm * 8 + tn) * 128) : f3;
    K2_STEP(0, 0, LOOK(f4)); K2_STEP(0, 1, ); K2_STEP(0, 0, );
    WAIT_V(12);
    asm volatile("" : "+v"(pol));
    const unsigned long long miss = __builtin_amdgcn_ballot_w64(pol != tag);
    if (wid == 0 && (miss & 1ull)) POLL32(f3, 2);
    BAR();
    K2_ISSUE(0, 1);
    WAIT_V(12); BAR();
    WAIT_V(8); BAR();
    if (isG) {
      WAIT_V(4); BAR();
      WAIT_V(0); BAR();
      BAR();
      STAMP(23);
      BAR();
    } else {
      u32x4 gv[8];
      const __amdgpu_buffer_rsrc_t ors = __builtin_amdgcn_make_buffer_rsrc(out, 0, 2048 * 2048, 0x00020000);
#define LD8(dst_, rs_, so_, pol_) asm volatile("buffer_load_dwordx4 %0, %8, %10, %11 offen" pol_ "\n\tbuffer_load_dwordx4 %1, %9, %10, %11 offen" pol_ "\n\t" \
        "buffer_load_dwordx4 %2, %8, %10, %12 offen" pol_ "\n\tbuffer_load_dwordx4 %3, %9, %10, %12 offen" pol_ "\n\t" \
        "buffer_load_dwordx4 %4, %8, %10, %13 offen" pol_ "\n\tbuffer_load_dwordx4 %5, %9, %10, %13 offen" pol_ "\n\t" \
        "buffer_load_dwordx4 %6, %8, %10, %14 offen" pol_ "\n\tbuffer_load_dwordx4 %7, %9, %10, %14 offen" pol_ \
        : "=&v"(dst_[0]), "=&v"(dst_[1]), "=&v"(dst_[2]), "=&v"(dst_[3]), "=&v"(dst_[4]), "=&v"(dst_[5]), "=&v"(dst_[6]), "=&v"(dst_[7]) \
        : "v"(goff), "v"(goff + 32768), "s"(rs_), "s"((so_)), "s"((so_) + 65536), "s"((so_) + 131072), "s"((so_) + 196608) : "memory")
      if (miss & 2ull) {
        WAIT_V(4); BAR(); WAIT_V(0); BAR(); BAR();
        POLL32((unsigned)(WS_GFLAG + (tm * 8 + tn) * 128), 1);
      }
      LD8(gv, wrs, WS_G, " sc1");
      if (!(miss & 2ull)) { WAIT_V(12); BAR(); WAIT_V(8); BAR(); BAR(); }
      float xf[8][8];
#pragma unroll
      for (int i = 0; i < 8; ++i) {
#pragma unroll
        for (int e = 0; e < 4; ++e) { xf[i][2 * e] = bf2f(xh[i][e] & 0xffffu); xf[i][2 * e + 1] = U2F(xh[i][e] & 0xffff0000u); }
        asm volatile("" : "+v"(xf[i][0]), "+v"(xf[i][1]), "+v"(xf[i][2]), "+v"(xf[i][3]), "+v"(xf[i][4]), "+v"(xf[i][5]), "+v"(xf[i][6]), "+v"(xf[i][7]));
      }
      asm volatile("s_waitcnt vmcnt(0)" : "+v"(gv[0]), "+v"(gv[1]), "+v"(gv[2]), "+v"(gv[3]), "+v"(gv[4]), "+v"(gv[5]), "+v"(gv[6]), "+v"(gv[7]) :: "memory");
      float gf2[8][8];
#pragma unroll
      for (int i = 0; i < 8; ++i) {
#pragma unroll
        for (int e = 0; e < 4; ++e) { gf2[i][2 * e] = bf2f(gv[i][e] & 0xffffu); gf2[i][2 * e + 1] = U2F(gv[i][e] & 0xffff0000u); }
        asm volatile("" : "+v"(gf2[i][0]), "+v"(gf2[i][1]), "+v"(gf2[i][2]), "+v"(gf2[i][3]), "+v"(gf2[i][4]), "+v"(gf2[i][5]), "+v"(gf2[i][6]), "+v"(gf2[i][7]));
      }
      STAMP(23);
      BAR();
      f32x4 a0[8], a1[8];
#pragma unroll
      for (int i = 0; i < 8; ++i) { a0[i] = *(const LAS f32x4*)(cb + i * 8192 + (((2 * c8) ^ (r0 & 7)) << 4)); a1[i] = *(const LAS f32x4*)(cb + i * 8192 + (((2 * c8 + 1) ^ (r0 & 7)) << 4)); }
      SCHED();
#pragma unroll
      for (int i = 0; i < 8; ++i) {
        const u32x4 w = {pk2(xf[i][0] + a0[i][0] * gf2[i][0], xf[i][1] + a0[i][1] * gf2[i][1]), pk2(xf[i][2] + a0[i][2] * gf2[i][2], xf[i][3] + a0[i][3] * gf2[i][3]),
                         pk2(xf[i][4] + a1[i][0] * gf2[i][4], xf[i][5] + a1[i][1] * gf2[i][5]), pk2(xf[i][6] + a1[i][2] * gf2[i][6], xf[i][7] + a1[i][3] * gf2[i][7])};
        __builtin_amdgcn_raw_buffer_store_b128(w, ors, goff + i * 32768, 0, 16);
      }
    }
  } else {
    if (isG) { WAIT_V(0); BAR(); }
    STAMP(21);
    const int wm = wc >> 1, wn = wc & 1, g = lane >> 4, t = lane & 15, q4 = (lane >> 2) & 3, p4 = lane & 3;
    lptr w2b[4];
#pragma unroll
    for (int ft = 0; ft < 4; ++ft) w2b[ft] = lds + 4 * K2_STG + 8192 + wn * 1024 + (g >> 1) * 4096 + (4 * (g & 1) + q4) * 128 + 32 * (ft ^ (2 * (g & 1) + (q4 >> 1))) + 8 * p4;
    lptr ar = lds + 4 * K2_STG + wm * 4096 + lane * 16;
    f32x4 ac2[8][4];
#pragma unroll
    for (int m = 0; m < 8; ++m)
#pragma unroll
      for (int n = 0; n < 4; ++n) ac2[m][n] = (f32x4){0.f, 0.f, 0.f, 0.f};
    bf16x8 Af[2][4];
    bf16x4 Wr[4][2];
    int slot = 4, dlt = 0;
#define K2_RW(f, hf) Wr[(f) & 3][hf] = __builtin_amdgcn_ds_read_tr16_b64_v4bf16((LAS bf16x4*)(w2b[(f) & 3] + (((f) >> 2) & 1) * 2048 + (hf) * 8192))
#define K2_W(m) __builtin_shufflevector(Wr[(m) & 3][0], Wr[(m) & 3][1], 0, 1, 2, 3, 4, 5, 6, 7)
#define K2_NEXT() do { dlt = (slot == 5) ? -5 * K2_STG : K2_STG; slot = (slot == 5) ? 0 : slot + 1; } while (0)
#define K2_ADV(x) do { x += dlt; asm volatile("" : "+v"(x)); } while (0)
#define K2_WIDE(st) do { _Pragma("unroll") for (int i = 0; i < 32; ++i) { const int m = i >> 2, q = i & 3; \
        const int n_ = (m & 1) ? 3 - q : q; ac2[m][n_] = MFMA16(K2_W(m), Af[st][n_], ac2[m][n_]); SCHED(); \
        if (q < 2) K2_RW((m + 3) & 7, q); \
        if (q == 2 && m >= 3 && m < 7) Af[(st) ^ 1][m - 3] = ldb128(ar + (m - 3) * 1024); \
        if (i == 2) K2_ADV(ar); \
        if (i == 6) K2_ADV(w2b[0]); \
        if (i == 10) K2_ADV(w2b[1]); \
        if (i == 11) BAR(); \
        if (i == 19) K2_ADV(w2b[2]); \
        if (i == 23) K2_ADV(w2b[3]); \
        if (i == 30) K2_NEXT(); \
        SCHED(); } } while (0)
#define K2_NARROW(st) do { _Pragma("unroll") for (int i = 0; i < 16; ++i) { const int m = i >> 2, q = i & 3; \
        const int n_ = (m & 1) ? 3 - q : q; ac2[m][n_] = MFMA16(K2_W(m), Af[st][n_], ac2[m][n_]); SCHED(); \
        if (q < 2) K2_RW((m + 3) & 3, q); \
        if (q >= 2 && (m == 1 || m == 2)) Af[(st) ^ 1][2 * m + q - 4] = ldb128(ar + (2 * m + q - 4) * 1024); \
        if (i == 2) { K2_ADV(ar); K2_ADV(w2b[0]); } \
        if (i == 3) BAR(); \
        if (i == 5) K2_ADV(w2b[1]); \
        if (i == 9) K2_ADV(w2b[2]); \
        if (i == 14) K2_ADV(w2b[3]); \
        if (i == 15) K2_NEXT(); \
        SCHED(); } } while (0)
    K2_NEXT();
    BAR();
    STAMP(22);
#pragma unroll
    for (int i = 0; i < 4; ++i) { if (i < 3) { K2_RW(i, 0); K2_RW(i, 1); } Af[0][i] = ldb128(ar + i * 1024); SCHED(); }
#pragma unroll 1
    for (int p = 0; p < 8; p += 2) { K2_WIDE(0); K2_WIDE(1); }
    WAIT_V(0);
#pragma unroll 1
    for (int p = 8; p < npw; p += 2) { K2_WIDE(0); K2_WIDE(1); }
    const unsigned po = (unsigned)(WS_P + wc * 8192 + lane * 16);
#pragma unroll
    for (int i = 0; i < 8; ++i) {
      const f32x4 a = ac2[4 + (i >> 1)][2 * (i & 1)], b = ac2[4 + (i >> 1)][2 * (i & 1) + 1];
      ST128((u32x4){pk2(a[0], a[1]), pk2(a[2], a[3]), pk2(b[0], b[1]), pk2(b[2], b[3])}, po + i * 1024, bid * 32768);
    }
    SCHED();
#pragma unroll 1
    for (int p = 0; p < 4; p += 2) { K2_NARROW(0); K2_NARROW(1); }
    WAIT_V(0);
#pragma unroll 1
    for (int p = 4; p < 10; p += 2) { K2_NARROW(0); K2_NARROW(1); }
    K2_NARROW(0);
    u32x4 pv[8];
#pragma unroll
    for (int i = 0; i < 8; ++i) pv[i] = __builtin_amdgcn_raw_buffer_load_b128(wrs, po + i * 1024 + (bid ^ 1) * 32768, 0, 16);
    SCHED();
    K2_NARROW(1);
#pragma unroll 1
    for (int p = 12; p < 16; p += 2) { K2_NARROW(0); K2_NARROW(1); }
    STAMP(23);
    const lptr cw = lds + k2c + (wm * 64 + t) * 512;
#pragma unroll
    for (int m = 0; m < 4; ++m)
#pragma unroll
      for (int n = 0; n < 4; ++n) {
        const unsigned lo = pv[2 * m + (n >> 1)][2 * (n & 1)], hi = pv[2 * m + (n >> 1)][2 * (n & 1) + 1];
        *(LAS f32x4*)(cw + n * 8192 + (((wn * 16 + m * 4 + g) ^ (t & 7)) << 4)) = ac2[m][n] + (f32x4){bf2f(lo & 0xffffu), U2F(lo & 0xffff0000u), bf2f(hi & 0xffffu), U2F(hi & 0xffff0000u)};
      }
    WAIT_L(0); BAR();
    if (isG) G_FIN();
  STAMP(24);
  STAMP_OUT(ws + (57 << 20), 30);
    __builtin_amdgcn_endpgm();
  }
  if (isG) {
      G_FIN();
      if (wid == 0 && lane == 0) __builtin_amdgcn_raw_buffer_store_b32(tag, wrs, 0, WS_GFLAG + (tm * 8 + tn) * 128, 16);
      if (bid == 16 && wid == 0) {
        for (unsigned spins = 0; spins < 2000000u; ++spins) {
          bool ok = true;
#pragma unroll
          for (int i = 0; i < 4; ++i) ok = ok && __builtin_amdgcn_raw_buffer_load_b32(wrs, (unsigned)((lane + i * 64) * 128), WS_HFLAG, 16) == tag;
          if (__builtin_amdgcn_ballot_w64(!ok) == 0ull) break;
          __builtin_amdgcn_s_sleep(4);
          asm volatile("" ::: "memory");
        }
        asm volatile("" ::: "memory");
        if (lane == 0) __builtin_amdgcn_raw_buffer_store_b32(tag, wrs, 0, 0, 16);
      }
    }
  STAMP(24);
  STAMP_OUT(ws + (57 << 20), 30);
#undef lane
}

extern "C" void kernel_launch(void* const* d_in, const int* in_sizes, int n_in, void* d_out, int out_size, void* d_ws, size_t ws_size, hipStream_t stream) {
  const char* x = (const char*)d_in[0]; const char* kc = (const char*)d_in[1]; const char* vc = (const char*)d_in[2];
  const char* wq = (const char*)d_in[3]; const char* wo = (const char*)d_in[4]; const char* w1 = (const char*)d_in[6]; const char* w2 = (const char*)d_in[7];
  (void)hipFuncSetAttribute((const void*)mega_kernel, hipFuncAttributeMaxDynamicSharedMemorySize, K1_LDS);
  mega_kernel<<<256, 512, K1_LDS, stream>>>(x, kc, vc, wq, w1, wo, w2, (char*)d_ws, (char*)d_out);
}
```

```cpp
#include <hip/hip_runtime.h>
#include <stdint.h>

typedef unsigned short u16;
typedef __bf16 bf16x2 __attribute__((ext_vector_type(2)));
typedef __bf16 bf16x4 __attribute__((ext_vector_type(4)));
typedef __bf16 bf16x8 __attribute__((ext_vector_type(8)));
typedef float f32x2 __attribute__((ext_vector_type(2)));
typedef float f32x4 __attribute__((ext_vector_type(4)));
typedef float f32x16 __attribute__((ext_vector_type(16)));
typedef unsigned u32x2 __attribute__((ext_vector_type(2)));
typedef unsigned u32x4 __attribute__((ext_vector_type(4)));
#define LAS __attribute__((address_space(3)))
typedef LAS char* lptr;
#if defined(__HIP_DEVICE_COMPILE__) && !defined(USEPK)
#define NOPK __attribute__((target("no-packed-fp32-ops")))
#else
#define NOPK
#endif
#define DI NOPK __device__ __forceinline__
#define F2U(x) __builtin_bit_cast(unsigned, (float)(x))
#define U2F(x) __builtin_bit_cast(float, (unsigned)(x))
#define TID ((int)__builtin_amdgcn_workitem_id_x())
#define BID ((int)__builtin_amdgcn_workgroup_id_x())
#define SCHED() __builtin_amdgcn_sched_barrier(0)
#define BAR() asm volatile("s_barrier" ::: "memory")
#define WAIT_V(n) asm volatile("s_waitcnt vmcnt(%0)" :: "n"(n) : "memory")
#define WAIT_L(n) do { asm volatile("" ::: "memory"); __builtin_amdgcn_s_waitcnt(0xC07F | ((n) << 8)); asm volatile("" ::: "memory"); } while (0)
#define MFMA16(a, b, c) __builtin_amdgcn_mfma_f32_16x16x32_bf16(a, b, c, 0, 0, 0)
#define MFMA32(a, b, c) __builtin_amdgcn_mfma_f32_32x32x16_bf16(a, b, c, 0, 0, 0)

DI void glds(unsigned voff, const char* sbase, unsigned ldsaddr) {
  asm volatile("s_mov_b32 m0, %2\n\ts_nop 0\n\tglobal_load_lds_dwordx4 %0, %1" ::"v"(voff), "s"(sbase), "s"(ldsaddr) : "memory");
}
DI void glds1(unsigned voff, const char* sbase, unsigned ldsaddr) {
  asm volatile("s_mov_b32 m0, %2\n\ts_nop 0\n\tglobal_load_lds_dwordx4 %0, %1 sc1" ::"v"(voff), "s"(sbase), "s"(ldsaddr) : "memory");
}
DI unsigned pk2(float lo, float hi) { f32x2 v = {lo, hi}; return __builtin_bit_cast(unsigned, __builtin_convertvector(v, bf16x2)); }
DI float bf2f(unsigned b) { return U2F(b << 16); }
DI bf16x8 ldb128(lptr p) { return *(const LAS bf16x8*)p; }
DI bf16x8 ldtr2(lptr p, int d) {
  bf16x4 a = __builtin_amdgcn_ds_read_tr16_b64_v4bf16((LAS bf16x4*)p);
  bf16x4 b = __builtin_amdgcn_ds_read_tr16_b64_v4bf16((LAS bf16x4*)(p + d));
  return __builtin_shufflevector(a, b, 0, 1, 2, 3, 4, 5, 6, 7);
}
DI void ST128_(__amdgpu_buffer_rsrc_t r, u32x4 v, unsigned voff, int soff) { __builtin_amdgcn_raw_buffer_store_b128(v, r, voff + (unsigned)soff, 0, 16); }
DI unsigned silu_pk(unsigned w) {
  const float a = U2F(w << 16), b = U2F(w & 0xffff0000u);
  const float ea = __builtin_amdgcn_exp2f(a) * -1.4426950408889634f - 1.4426950408889634f, eb = __builtin_amdgcn_exp2f(b) * -1.4426950408889634f - 1.4426950408889634f;
  return pk2(a * __builtin_amdgcn_rcpf(ea), b * __builtin_amdgcn_rcpf(eb));
}
DI float silu(float v) { return v * __builtin_amdgcn_rcpf(1.0f + __builtin_amdgcn_exp2f(v * -1.4426950408889634f)); }

template <int CTRL> DI float dpp_add(float v) { return v + U2F(__builtin_amdgcn_update_dpp(0u, F2U(v), CTRL, 0xf, 0xf, true)); }
DI float half_sum(float v) {
  v = dpp_add<0xB1>(v); v = dpp_add<0x4E>(v); v = dpp_add<0x141>(v); v = dpp_add<0x140>(v);
  auto rr = __builtin_amdgcn_permlane16_swap(F2U(v), F2U(v), false, false);
  return U2F(rr[0]) + U2F(rr[1]);
}
typedef __attribute__((address_space(1))) unsigned long long gu64;

#define SX(b, h) (98304 + (b) * 32768 + (h) * 16384)
constexpr int SLOT_B = 0, SLOT_E = 32768, SLOT_A = 65536, SLOT_C = 98304, SLOT_D = 131072, K1_LDS = 163840;

#define XSWAP 0
constexpr int WS_HFLAG = 4096;
constexpr int WS_GFLAG = 65536;
constexpr int WS_OFLAG = 131072;
#define OFS 16
constexpr int WS_EFLAG = 163840;
constexpr int WS_STATS = 1 << 20;
constexpr int WS_PFLAG = 196608;
constexpr int WS_G = 5 << 20, WS_O = 9 << 20, WS_H = 25 << 20, WS_P = 39 << 20, WS_END = 47 << 20;
constexpr int K2_STG = 24576, K2_C = 98304;

NOPK __global__ __launch_bounds__(512, 2) void mega_kernel(const char* __restrict__ xn, const char* __restrict__ kc, const char* __restrict__ vc,
                                                           const char* __restrict__ wq, const char* __restrict__ w1, const char* __restrict__ wo, const char* __restrict__ w2,
                                                           char* ws, char* __restrict__ out) {
  extern __shared__ __attribute__((aligned(16))) char shm[];
  const lptr lds = (lptr)shm;
  const unsigned lds0 = (unsigned)(uintptr_t)lds;
  const int tid = TID, wid = __builtin_amdgcn_readfirstlane(tid >> 6), lane = tid & 63, wr = wid >> 2, wc = wid & 3;
  const int bid = BID ^ XSWAP, xcd = bid & 7, idx = bid >> 3, hd = (idx & 2) * 8 + 2 * xcd + (idx & 1), rb = idx >> 2;
  const int g = lane >> 4, t = lane & 15, q4 = (lane >> 2) & 3, p4 = lane & 3;

  const bool light = (idx & 2) != 0;
  unsigned voffX[4], voffQ[2], voffH[2];
  {
    const int r = lane >> 3, c = lane & 7;
#pragma unroll
    for (int i = 0; i < 4; ++i) voffX[i] = (unsigned)((8 * i + r) * 2048 + ((c ^ (4 * (i & 1) + (r >> 1))) << 4) - i * 1024);
    const int k = 32 * (wid >> 2) + 16 * (wid & 1) + 4 * ((wid >> 1) & 1) + 8 * (r >> 2) + (r & 3);
    const int cw = c ^ (((r >> 1) & 3) << 1);
    voffQ[0] = (unsigned)(k * 8192 + cw * 16 + 1024); voffQ[1] = voffQ[0] + 128 - 1024;
    voffH[0] = (unsigned)(k * 7168 + 128 + (light ? (cw & 3) : cw) * 16 + 1024);
    voffH[1] = (unsigned)(k * 7168 + cw * 16);
  }
  const char* xb = xn + (size_t)rb * 256 * 2048 + wid * 65536;
  const char* qb = wq + hd * 256 - 1024;
  const char* hb = w1 + (light ? 4096 + 192 * (hd - 16) : 256 * hd) - 1024;
  const bool full = wr == 1 || !light;
  const int sb = hd >= 24;
#define GLDS2(v0_, v1_, sb_, ld_) asm volatile("s_mov_b32 m0, %3\n\ts_nop 0\n\tglobal_load_lds_dwordx4 %0, %2\n\tglobal_load_lds_dwordx4 %1, %2 offset:1024" \
    ::"v"(v0_), "v"(v1_), "s"(sb_), "s"(ld_) : "memory")
#define GLDS4(v0_, v1_, v2_, v3_, sb_, ld_) asm volatile("s_mov_b32 m0, %5\n\ts_nop 0\n\tglobal_load_lds_dwordx4 %0, %4\n\tglobal_load_lds_dwordx4 %1, %4 offset:1024\n\t" \
    "global_load_lds_dwordx4 %2, %4 offset:2048\n\tglobal_load_lds_dwordx4 %3, %4 offset:3072" ::"v"(v0_), "v"(v1_), "v"(v2_), "v"(v3_), "s"(sb_), "s"(ld_) : "memory")
#define ST_X(b, kt) GLDS4(voffX[0], voffX[1], voffX[2], voffX[3], xb + (kt) * 128, lds0 + SX(b, 0) + wid * 4096)
#define ST_W0(wo_, kt) GLDS2(voffQ[0], voffQ[1], qb + (size_t)(kt) * 524288, lds0 + (wo_) + wid * 2048)
#define ST_W1(wo_, kt) GLDS2(voffH[0], voffH[1], hb + (size_t)(kt) * 458752, lds0 + (wo_) + 16384 + wid * 2048)

  lptr wb[4], xr[2];
#pragma unroll
  for (int m = 0; m < 4; ++m) wb[m] = lds + wr * 1024 + (g >> 1) * 2048 + (4 * (g & 1) + q4) * 128 + 32 * ((m ^ wc) ^ (2 * (g & 1) + (q4 >> 1))) + 8 * p4;
#pragma unroll
  for (int ks = 0; ks < 2; ++ks) xr[ks] = lds + 98304 + (wc * 32 + t) * 128 + 16 * ((4 * ks + g) ^ (t >> 1));
#define LDW(dst, h) do { _Pragma("unroll") for (int m = 0; m < 4; ++m) _Pragma("unroll") for (int k = 0; k < 2; ++k) dst[m][k] = ldtr2(wb[m] + (h) * 16384 + k * 8192, 4096); } while (0)
#define LDX(dst, b, h) do { _Pragma("unroll") for (int n = 0; n < 2; ++n) _Pragma("unroll") for (int k = 0; k < 2; ++k) dst[n][k] = ldb128(xr[k] + (SX(b, h) - 98304) + n * 2048); } while (0)

  f32x4 acc[2][2][4][2];
#pragma unroll
  for (int a = 0; a < 2; ++a)
#pragma unroll
    for (int b = 0; b < 2; ++b)
#pragma unroll
      for (int m = 0; m < 4; ++m)
#pragma unroll
        for (int n = 0; n < 2; ++n) acc[a][b][m][n] = (f32x4){0.f, 0.f, 0.f, 0.f};
  bf16x8 At[4][2], B0[2][2], B1[2][2];

#define MM1(ai, m, k, j) do { if ((j) < 2) acc[ai][0][m][(j) & 1] = MFMA16(At[m][k], B0[(j) & 1][k], acc[ai][0][m][(j) & 1]); else acc[ai][1][m][(j) & 1] = MFMA16(At[m][k], B1[(j) & 1][k], acc[ai][1][m][(j) & 1]); SCHED(); } while (0)
#define MM4(ai, m, k) do { _Pragma("unroll") for (int q = 0; q < 4; ++q) MM1(ai, m, k, ((m) & 1) ? 3 - q : q); } while (0)
#define MMA2(ai, CLOSE) do { __builtin_amdgcn_s_setprio(1); \
    cs[ai] = MFMA16(At[0][0], ones, cs[ai]); SCHED(); MM4(ai, 0, 0); MM4(ai, 1, 0); \
    if ((ai) == 0) { MM4(ai, 2, 0); MM4(ai, 3, 0); } \
    cs[ai] = MFMA16(At[0][1], ones, cs[ai]); SCHED(); MM4(ai, 0, 1); MM4(ai, 1, 1); \
    asm volatile("" : "+v"(cs[ai])); \
    if ((ai) == 0 || full) { MM4(ai, 2, 1); MM4(ai, 3, 1); if ((ai) == 1) { MM4(ai, 2, 0); MM4(ai, 3, 0); } } \
    SCHED(); CLOSE; __builtin_amdgcn_s_setprio(0); SCHED(); } while (0)
  char* const stats = ws + WS_STATS;
  const __amdgpu_buffer_rsrc_t wrs = __builtin_amdgcn_make_buffer_rsrc(ws, 0, WS_END, 0x00020000);
  f32x4 cs[2] = {{0.f, 0.f, 0.f, 0.f}, {0.f, 0.f, 0.f, 0.f}};
  bf16x8 ones = __builtin_bit_cast(bf16x8, (u32x4){0x3f803f80u, 0x3f803f80u, 0x3f803f80u, 0x3f803f80u});
#define PIN_SETUP() do { asm volatile("" : "+v"(ones), "+v"(cs[0]), "+v"(cs[1]), "+v"(wb[0]), "+v"(wb[1]), "+v"(wb[2]), "+v"(wb[3]), "+v"(xr[0]), "+v"(xr[1])); \
    _Pragma("unroll") for (int a = 0; a < 2; ++a) _Pragma("unroll") for (int b = 0; b < 2; ++b) \
      asm volatile("" : "+v"(acc[a][b][0][0]), "+v"(acc[a][b][0][1]), "+v"(acc[a][b][1][0]), "+v"(acc[a][b][1][1]), "+v"(acc[a][b][2][0]), "+v"(acc[a][b][2][1]), "+v"(acc[a][b][3][0]), "+v"(acc[a][b][3][1])); } while (0)
  unsigned tag;
  {
    if (wr == 0) {
      unsigned ep;
      ST_X(0, 0); ST_W0(0, 0);
      GLDS4(voffX[0], voffX[1], voffX[2], voffX[3], xb + 262144, lds0 + SX(0, 1) + wid * 4096);
      GLDS2(voffQ[0], voffQ[1], qb + 262144, lds0 + 8192 + wid * 2048);
      asm volatile("global_load_dword %0, %1, %2 sc1" : "=&v"(ep) : "v"(0), "s"(ws) : "memory");
      PIN_SETUP();
      asm volatile("s_waitcnt vmcnt(0)" : "+v"(ep) :: "memory");
      tag = __builtin_amdgcn_readfirstlane(ep) + 1u;
      BAR();
    } else {
      const int row = rb * 256 + hd * 8 + wc * 2 + (lane >> 5);
      u32x4 a[4];
      unsigned ep;
      asm volatile("global_load_dword %0, %1, %2 sc1" : "=&v"(ep) : "v"(0), "s"(ws) : "memory");
      asm volatile("global_load_dwordx4 %0, %4, %5\n\tglobal_load_dwordx4 %1, %4, %5 offset:512\n\tglobal_load_dwordx4 %2, %4, %5 offset:1024\n\tglobal_load_dwordx4 %3, %4, %5 offset:1536"
                   : "=&v"(a[0]), "=&v"(a[1]), "=&v"(a[2]), "=&v"(a[3]) : "v"(row * 2048 + (lane & 31) * 16), "s"(xn) : "memory");
      ST_W1(0, 0); GLDS2(voffH[0], voffH[1], hb - 229376, lds0 + 16384 + wc * 2048);
      PIN_SETUP();
      BAR();
      ST_W0(32768, 1); GLDS2(voffQ[0], voffQ[1], qb + 524288 - 262144, lds0 + 32768 + wc * 2048);
      ST_X(1, 1); GLDS4(voffX[0], voffX[1], voffX[2], voffX[3], xb - 262144 + 128, lds0 + SX(1, 0) + wc * 4096);
      asm volatile("s_waitcnt vmcnt(16)" : "+v"(ep), "+v"(a[0]), "+v"(a[1]), "+v"(a[2]), "+v"(a[3]) :: "memory");
      tag = __builtin_amdgcn_readfirstlane(ep) + 1u;
      float v[32];
#pragma unroll
      for (int i = 0; i < 16; ++i) { v[2 * i] = bf2f(a[i >> 2][i & 3] & 0xffffu); v[2 * i + 1] = U2F(a[i >> 2][i & 3] & 0xffff0000u); }
      float s0 = 0.f, s1 = 0.f, s2 = 0.f, s3 = 0.f;
#pragma unroll
      for (int i = 0; i < 8; ++i) { s0 += v[i]; s1 += v[8 + i]; s2 += v[16 + i]; s3 += v[24 + i]; }
      const float mean = half_sum((s0 + s1) + (s2 + s3)) * (1.0f / 1024.0f);
      s0 = s1 = s2 = s3 = 0.f;
#pragma unroll
      for (int i = 0; i < 8; ++i) { const float d0 = v[i] - mean, d1 = v[8 + i] - mean, d2 = v[16 + i] - mean, d3 = v[24 + i] - mean; s0 += d0 * d0; s1 += d1 * d1; s2 += d2 * d2; s3 += d3 * d3; }
      const float sd = __builtin_amdgcn_sqrtf(half_sum((s0 + s1) + (s2 + s3)) * (1.0f / 1024.0f) + 1e-5f);
      if ((lane & 31) < 2) __hip_atomic_store((gu64*)(stats + row * 16 + (lane & 1) * 8), ((unsigned long long)tag << 32) | F2U((lane & 1) ? sd : mean), __ATOMIC_RELAXED, __HIP_MEMORY_SCOPE_AGENT);
    }
  }
  if (wr == 1) BAR();
  SCHED();
  const int kvg = hd >> 3;
  const char* kb_g = kc + ((size_t)kvg * 2048 + 1536) * 256;
  const char* vb_g = vc + ((size_t)kvg * 2048 + 1536) * 256;
#define VOFFK ((unsigned)(((lane >> 2) & 7) * 256 + (4 * (lane >> 5) + ((lane & 3) ^ ((2 * (wid & 1) + ((lane >> 4) & 1)) & 3))) * 16))
#define VOFFV ((unsigned)(((lane >> 2) & 7) * 256 + (lane >> 5) * 64 + (lane & 3) * 16))
#define ST_KV8(voff, gsrc, slot) do { glds(voff, (gsrc) + wid * 2048, lds0 + (slot) + wid * 2048); glds(voff, (gsrc) + wid * 2048 + 128, lds0 + (slot) + wid * 2048 + 1024); } while (0)
  int wprv = 65536, wnxt = 32768;
#define W_ADV() do { const int d_ = (wnxt == 0) ? -65536 : 32768, cur_ = wnxt - d_; wnxt = wprv; wprv = cur_; \
    _Pragma("unroll") for (int f = 0; f < 4; ++f) wb[f] += d_; \
    asm volatile("" : "+v"(wb[0]), "+v"(wb[1]), "+v"(wb[2]), "+v"(wb[3])); } while (0)
#define PH_A(b, STAGE) do { LDX(B0, b, 0); LDX(B1, b, 1); SCHED(); LDW(At, 0); STAGE; WAIT_V(10); WAIT_L(12); BAR(); SCHED(); MMA2(0, WAIT_L(0); BAR()); } while (0)
#define PH_B(STAGE) do { LDW(At, 1); STAGE; W_ADV(); WAIT_V(8); BAR(); SCHED(); MMA2(1, WAIT_L(0); BAR()); } while (0)
#pragma unroll 1
  for (int kt = 0; kt < 14; kt += 2) {
    PH_A(0, ST_W1(wnxt, kt + 1); ST_W0(wprv, kt + 2));
    PH_B(ST_X(0, kt + 2));
    PH_A(1, ST_W1(wnxt, kt + 2); ST_W0(wprv, kt + 3));
    PH_B(ST_X(1, kt + 3));
  }
  const unsigned voffK = VOFFK, voffV = VOFFV;
  PH_A(0, ST_W1(0, 15); ST_KV8(voffK, kb_g, SLOT_E));
  PH_B(ST_KV8(voffV, vb_g, SLOT_C); ST_KV8(voffV, vb_g + 16384, SLOT_C + 16384));
  const unsigned soff = (unsigned)((rb * 256 + (g >> 1) * 128 + wc * 32 + (g & 1) * 16 + t) * 16);
  u32x4 sg;
  LDX(B0, 1, 0); LDX(B1, 1, 1); SCHED(); LDW(At, 0);
  asm volatile("global_load_dwordx4 %0, %1, %2 sc1" : "=&v"(sg) : "v"(soff), "s"(stats) : "memory");
  ST_KV8(voffK, kb_g + 16384, SLOT_E + 16384); ST_KV8(voffK, kb_g + 32768, SLOT_A);
  WAIT_V(11); BAR(); SCHED(); MMA2(0, WAIT_L(0); BAR());
  LDW(At, 1);
  ST_KV8(voffK, kb_g + 49152, SLOT_A + 16384);
  BAR(); SCHED();
  const lptr csw = lds + SLOT_D + (t & 3) * 8192 + wr * 4096 + wc * 64 + g * 16;
  MMA2(1, *(LAS f32x4*)csw = cs[0]; *(LAS f32x4*)(csw + 256) = cs[1]; asm volatile("s_waitcnt vmcnt(4)" : "+v"(sg) :: "memory"); WAIT_L(0); BAR());
  f32x4 cv[2][4];
#pragma unroll
  for (int a = 0; a < 2; ++a)
#pragma unroll
    for (int m = 0; m < 4; ++m) cv[a][m] = *(const LAS f32x4*)(lds + SLOT_D + wc * 8192 + wr * 4096 + a * 256 + (m ^ wc) * 64 + g * 16);
  if (__builtin_expect(__builtin_amdgcn_ballot_w64(sg[1] != tag || sg[3] != tag) != 0ull, 0)) {
    for (unsigned spins = 0; spins < 4000000u; ++spins) {
      __builtin_amdgcn_s_sleep(2);
      asm volatile("" ::: "memory");
      sg = __builtin_amdgcn_raw_buffer_load_b128(wrs, soff, WS_STATS, 16);
      if (__builtin_amdgcn_ballot_w64(sg[1] != tag || sg[3] != tag) == 0ull) break;
    }
  }
  float mu[2][2], rs[2][2], sd[2][2];
  {
    auto m16 = __builtin_amdgcn_permlane16_swap(sg[0], sg[0], false, false);
    auto s16 = __builtin_amdgcn_permlane16_swap(sg[2], sg[2], false, false);
#pragma unroll
    for (int n = 0; n < 2; ++n) {
      auto m32 = __builtin_amdgcn_permlane32_swap(m16[n], m16[n], false, false);
      auto s32 = __builtin_amdgcn_permlane32_swap(s16[n], s16[n], false, false);
#pragma unroll
      for (int bj = 0; bj < 2; ++bj) { mu[bj][n] = -U2F(m32[bj]); sd[bj][n] = U2F(s32[bj]); rs[bj][n] = __builtin_amdgcn_rcpf(sd[bj][n]) * 1.4426950408889634f; }
    }
  }
  WAIT_L(0);
  {
    const lptr qw = lds + wc * 8192 + wr * 4096 + ((g >> 1) * 32 + t) * 16 + (g & 1) * 8;
#pragma unroll
    for (int bj = 0; bj < 2; ++bj) {
      lptr qwb = qw + ((bj ^ sb) ? SLOT_D : SLOT_B);
      asm volatile("" : "+v"(qwb));
#pragma unroll
      for (int m = 0; m < 4; ++m)
#pragma unroll
        for (int n = 0; n < 2; ++n) {
          const f32x4 v = (acc[0][bj][m][n] + cv[0][m] * mu[bj][n]) * (rs[bj][n] * 0.08838834764831845f);
          u32x2 w = {pk2(v[0], v[1]), pk2(v[2], v[3])};
          *(LAS u32x2*)(qwb + (m ^ wc) * 1024 + n * 256) = w;
        }
    }
  }
  WAIT_L(0); BAR(); SCHED();
  unsigned pft = 0;
  const int sl = ((idx >> 2) * 16 + (idx & 1) * 8 + xcd) * 8 + wid;
#define TOUCH(q_) do { const int o_ = light ? ((((q_) + 2) & 3) * 916 + (sl < 916 ? sl : 915)) : ((((q_) + 2) & 3) * 1024 + sl); \
    asm volatile("global_load_dword %0, %1, %2" : "+v"(pft) : "v"((unsigned)((__builtin_amdgcn_mbcnt_lo(~0u, 0u) & 15) * 128)), "s"((light ? w2 : wo) + (light && o_ > 3583 ? 3583 : o_) * 2048) : "memory"); } while (0)
  TOUCH(0);
  bf16x8 qf[8];
  const lptr qr = lds + (wr ? SLOT_D : SLOT_B) + wc * 8192 + lane * 16;
  if (wr == 1) {
#pragma unroll
    for (int s = 0; s < 8; ++s) qf[s] = ldb128(qr + s * 1024);
  }
#define ST128(...) ST128_(wrs, __VA_ARGS__)
  u32x4 hq[3];
  unsigned hqo;
  {
    u32x2 hw[2][2][2];
#define SILU16(m, ACT) do { \
      const f32x4 a0 = acc[1][0][m][0] + cv[1][m] * mu[0][0], a1 = acc[1][0][m][1] + cv[1][m] * mu[0][1], a2 = acc[1][1][m][0] + cv[1][m] * mu[1][0], a3 = acc[1][1][m][1] + cv[1][m] * mu[1][1]; \
      f32x4 e0 = a0 * -rs[0][0], e1 = a1 * -rs[0][1], e2 = a2 * -rs[1][0], e3 = a3 * -rs[1][1]; SCHED(); \
      if (ACT) { \
      _Pragma("unroll") for (int i = 0; i < 4; ++i) { e0[i] = __builtin_amdgcn_exp2f(e0[i]); e1[i] = __builtin_amdgcn_exp2f(e1[i]); e2[i] = __builtin_amdgcn_exp2f(e2[i]); e3[i] = __builtin_amdgcn_exp2f(e3[i]); } SCHED(); \
      e0 = e0 * sd[0][0] + sd[0][0]; e1 = e1 * sd[0][1] + sd[0][1]; e2 = e2 * sd[1][0] + sd[1][0]; e3 = e3 * sd[1][1] + sd[1][1]; SCHED(); \
      _Pragma("unroll") for (int i = 0; i < 4; ++i) { e0[i] = __builtin_amdgcn_rcpf(e0[i]); e1[i] = __builtin_amdgcn_rcpf(e1[i]); e2[i] = __builtin_amdgcn_rcpf(e2[i]); e3[i] = __builtin_amdgcn_rcpf(e3[i]); } SCHED(); \
      e0 *= a0; e1 *= a1; e2 *= a2; e3 *= a3; SCHED(); } \
      else { _Pragma("unroll") for (int i = 0; i < 4; ++i) e3[i] = a3[i] * __builtin_amdgcn_rcpf(__builtin_amdgcn_exp2f(e3[i]) * sd[1][1] + sd[1][1]); } \
      hw[0][0][(m) & 1] = (u32x2){pk2(e0[0], e0[1]), pk2(e0[2], e0[3])}; hw[0][1][(m) & 1] = (u32x2){pk2(e1[0], e1[1]), pk2(e1[2], e1[3])}; \
      hw[1][0][(m) & 1] = (u32x2){pk2(e2[0], e2[1]), pk2(e2[2], e2[3])}; hw[1][1][(m) & 1] = (u32x2){pk2(e3[0], e3[1]), pk2(e3[2], e3[3])}; SCHED(); } while (0)
#define HFRAG(bj, n) ((wc & 1) ? (u32x4){hw[bj][n][1][0], hw[bj][n][1][1], hw[bj][n][0][0], hw[bj][n][0][1]} : (u32x4){hw[bj][n][0][0], hw[bj][n][0][1], hw[bj][n][1][0], hw[bj][n][1][1]})
    const unsigned hoff = (unsigned)(WS_H + (rb * 16 + wc * 2) * 2048 + lane * 16);
    const int pb = (light ? 16 + 3 * hd : 4 * hd) + (wr ? 0 : 2), sw = full ? (wc >> 1) : 0, p0 = pb + sw, p1 = pb + (sw ^ 1);
    SILU16(0, false); SILU16(1, false);
    hqo = hoff + (p0 >> 1) * 262144 + (p0 & 1) * 1024;
    hq[0] = HFRAG(0, 0); hq[1] = HFRAG(0, 1); hq[2] = HFRAG(1, 0);
    ST128(HFRAG(1, 1), hqo + 2048, 16384);
    asm volatile("" : "+v"(hq[0]), "+v"(hq[1]), "+v"(hq[2]));
    if (wr == 0) {
      BAR(); SCHED();
#pragma unroll
      for (int s = 0; s < 8; ++s) qf[s] = ldb128(qr + s * 1024);
    }
    if (full) {
      SILU16(2, true); SILU16(3, true);
#pragma unroll
      for (int bj = 0; bj < 2; ++bj)
#pragma unroll
        for (int n = 0; n < 2; ++n) ST128(HFRAG(bj, n), hoff + n * 2048, (p1 >> 1) * 262144 + (p1 & 1) * 1024 + bj * 16384);
    }
  }
  if (wr == 1) { WAIT_L(0); BAR(); }
  __builtin_amdgcn_s_setprio(1);
  SCHED();
  const int r32 = lane & 31, hh = lane >> 5;
  lptr kr[2];
#pragma unroll
  for (int e = 0; e < 2; ++e) kr[e] = lds + 2048 * (r32 >> 3) + 64 * (r32 & 7) + 16 * ((2 * e + hh) ^ ((r32 >> 2) & 3));
  const lptr vr = lds + ((lane & 3) << 3) + (((lane >> 2) & 3) << 6) + (((lane >> 4) & 1) << 5) + (hh << 8);
  f32x16 o[4], s[4];
#pragma unroll
  for (int d = 0; d < 4; ++d)
#pragma unroll
    for (int i = 0; i < 16; ++i) o[d][i] = 0.f;
  bf16x8 pf[8];
  constexpr int RD = 6;
  bf16x8 fr[RD];
  f32x4 lacc = {0.f, 0.f, 0.f, 0.f};
  const unsigned selw = (((lane >> 4) ^ (lane >> 2)) & 1) ? 0u : 0x3f803f80u;
  bf16x8 sel = __builtin_bit_cast(bf16x8, (u32x4){selw, selw, selw, selw});
  asm volatile("" : "+v"(sel));
  f32x16 negm = {0.f, 0.f, 0.f, 0.f, 0.f, 0.f, 0.f, 0.f, 0.f, 0.f, 0.f, 0.f, 0.f, 0.f, 0.f, 0.f};
#define SM_THR 60.0f
#define FRAG(i) ((i) < 32 ? ldtr2(v0 + (((i) >> 2) * 8 + ((i) & 3)) * 512, 2048) : ldb128((((((i) - 32) >> 2) & 1) ? k1 : k0) + ((i) & 3) * 8192 + (((i) - 32) >> 3) * 512))
#define XEARLY 3
#define XPRIO 1
#define YPRIO 0
#define XSEG(lo, hi, CLOSE) do { __builtin_amdgcn_s_setprio(XPRIO); \
    _Pragma("unroll") for (int i = (lo); i < (hi); ++i) { \
      if (i < 32) o[i & 3] = MFMA32(fr[i % RD], pf[(i >> 2) & 7], o[i & 3]); \
      else s[i & 3] = MFMA32(fr[i % RD], qf[((i - 32) >> 2) & 7], i < 36 ? negm : s[i & 3]); \
      if (i < 32 && (i & 3) == 1) lacc = MFMA16(sel, pf[(i >> 2) & 7], lacc); \
      if (i + RD < (hi)) fr[i % RD] = FRAG(i + RD); \
      SCHED(); \
      if (i == 63 - XEARLY) { __builtin_amdgcn_s_setprio(2); CLOSE; WAIT_L(0); BAR(); SCHED(); } } \
    __builtin_amdgcn_s_setprio(YPRIO); } while (0)
#define PREF(lo) do { _Pragma("unroll") for (int i = (lo); i < (lo) + RD; ++i) fr[i % RD] = FRAG(i); SCHED(); } while (0)
#define MX3(a, b, c) __builtin_fmaxf(__builtin_fmaxf((a), (b)), (c))
#define SOFTMAX() do { \
      float m0 = MX3(s[0][0], s[0][1], s[0][2]), m1 = MX3(s[1][0], s[1][1], s[1][2]), m2 = MX3(s[2][0], s[2][1], s[2][2]), m3 = MX3(s[3][0], s[3][1], s[3][2]); \
      _Pragma("unroll") for (int i = 3; i < 15; i += 2) { m0 = MX3(m0, s[0][i], s[0][i + 1]); m1 = MX3(m1, s[1][i], s[1][i + 1]); m2 = MX3(m2, s[2][i], s[2][i + 1]); m3 = MX3(m3, s[3][i], s[3][i + 1]); } \
      m0 = MX3(m0, s[0][15], s[1][15]); m2 = MX3(m2, s[2][15], s[3][15]); \
      float mx = __builtin_fmaxf(MX3(m0, m1, m2), m3); \
        \
      if (__builtin_expect(__builtin_amdgcn_ballot_w64(!(mx <= SM_THR)) != 0ull, 0)) { \
        auto rr = __builtin_amdgcn_permlane32_swap(F2U(mx), F2U(mx), false, false); \
        const float dl = MX3(U2F(rr[0]), U2F(rr[1]), 0.f); \
        _Pragma("unroll") for (int kb = 0; kb < 4; ++kb) s[kb] -= dl; \
        negm -= dl; \
        const float alpha = __builtin_amdgcn_exp2f(-dl); lacc *= alpha; _Pragma("unroll") for (int d = 0; d < 4; ++d) o[d] *= alpha; \
      } \
      SCHED(); \
      _Pragma("unroll") for (int kb = 0; kb < 4; ++kb) { \
        _Pragma("unroll") for (int e = 0; e < 2; ++e) { \
          _Pragma("unroll") for (int i = 0; i < 8; ++i) s[kb][8 * e + i] = __builtin_amdgcn_exp2f(s[kb][8 * e + i]); \
          u32x4 w = {pk2(s[kb][8 * e], s[kb][8 * e + 1]), pk2(s[kb][8 * e + 2], s[kb][8 * e + 3]), pk2(s[kb][8 * e + 4], s[kb][8 * e + 5]), pk2(s[kb][8 * e + 6], s[kb][8 * e + 7])}; \
          pf[2 * kb + e] = __builtin_bit_cast(bf16x8, w); } \
        asm volatile("" : "+v"(pf[2 * kb]), "+v"(pf[2 * kb + 1])); \
        SCHED(); } } while (0)
  {
    const lptr k0 = kr[0] + SLOT_E, k1 = kr[1] + SLOT_E, v0 = vr;
    {
      bf16x8 kf[8];
#pragma unroll
      for (int st = 0; st < 8; ++st) kf[st] = FRAG(32 + 4 * st);
      PREF(32);
#pragma unroll
      for (int st = 0; st < 8; ++st) negm = MFMA32(kf[st], qf[st], negm);
      float m0 = MX3(negm[0], negm[1], negm[2]), m1 = MX3(negm[3], negm[4], negm[5]);
      m0 = MX3(m0, negm[6], negm[7]); m1 = MX3(m1, negm[8], negm[9]); m0 = MX3(m0, negm[10], negm[11]); m1 = MX3(m1, negm[12], negm[13]);
      m0 = __builtin_fmaxf(MX3(m0, negm[14], negm[15]), m1);
      auto rr = __builtin_amdgcn_permlane32_swap(F2U(m0), F2U(m0), false, false);
      m0 = -__builtin_fmaxf(U2F(rr[0]), U2F(rr[1]));
#pragma unroll
      for (int i = 0; i < 16; ++i) negm[i] = m0;
      asm volatile("" : "+v"(negm));
      SCHED();
    }
    XSEG(32, 64, WAIT_V(1));
  }
  const char* kvsrc = (wr ? kb_g + 65536 : vb_g + 32768) + wc * 8192 - 1024;
  unsigned kvo[2][2];
#pragma unroll
  for (int par = 0; par < 2; ++par) {
    const int xx = wr ? ((lane & 3) ^ ((2 * par + ((lane >> 4) & 1)) & 3)) : (lane & 3);
    kvo[par][0] = (unsigned)(((lane >> 2) & 7) * 256 + (lane >> 5) * 64 + xx * 16 + 1024);
    kvo[par][1] = kvo[par][0] + 128 - 1024;
  }
#pragma unroll 1
  for (int c = 0; c < 3; ++c) {
    ST128((u32x4){silu_pk(hq[0][0]), silu_pk(hq[0][1]), silu_pk(hq[0][2]), silu_pk(hq[0][3])}, hqo, c == 2 ? 16384 : c * 2048);
    hq[0] = hq[1]; hq[1] = hq[2];
    SCHED();
    if (c + wr < 3) {
      const int slot = wr ? (c == 0 ? SLOT_B : SLOT_C) : (c == 0 ? SLOT_D : (c == 1 ? SLOT_E : SLOT_A));
#pragma unroll
      for (int hf = 0; hf < 2; ++hf)
        asm volatile("s_mov_b32 m0, %5\n\ts_nop 0\n\t"
                     "global_load_lds_dwordx4 %0, %4\n\tglobal_load_lds_dwordx4 %1, %4 offset:1024\n\t"
                     "global_load_lds_dwordx4 %2, %4 offset:2048\n\tglobal_load_lds_dwordx4 %3, %4 offset:3072"
                     ::"v"(kvo[0][0]), "v"(kvo[0][1]), "v"(kvo[1][0]), "v"(kvo[1][1]), "s"(kvsrc + c * 32768 + hf * 4096), "s"(lds0 + slot + wc * 8192 + hf * 4096) : "memory");
    }
    TOUCH(c + 1);
    SOFTMAX();
    const int kslot = (c == 0) ? SLOT_A : ((c == 1) ? SLOT_B : SLOT_C);
    const int vslot = (c == 0) ? SLOT_C : ((c == 1) ? SLOT_D : SLOT_E);
    const lptr k0 = kr[0] + kslot, k1 = kr[1] + kslot, v0 = vr + vslot;
    PREF(0);
    BAR(); SCHED();
    XSEG(0, 64, WAIT_V(1); if (c == 2) WAIT_V(0));
  }
  if (wid == 4 && lane == 0) __builtin_amdgcn_raw_buffer_store_b32(tag, wrs, 0, WS_HFLAG + (rb * 32 + hd) * 128, 16);
  SOFTMAX();
  {
    const lptr v0 = vr + SLOT_A;
#define FRAGD(i) ldtr2(v0 + (((i) & 7) * 8 + ((i) >> 3)) * 512, 2048)
#pragma unroll
    for (int i = 0; i < RD; ++i) fr[i] = FRAGD(i);
    asm volatile("s_waitcnt vmcnt(0)" :: "v"(pft) : "memory");
    SCHED();
    if (wr == 0) BAR();
    SCHED();
    __builtin_amdgcn_s_setprio(1);
    const unsigned oo = (unsigned)(WS_O + 2 * hd * 262144 + (rb * 16 + (wr ^ sb) * 8 + wc * 2 + (r32 >> 4)) * 2048 + (hh * 16 + (r32 & 15)) * 16);
    float inv = 0.f;
    u32x4 ow;
#pragma unroll
    for (int i = 0; i < 42; ++i) {
      if (i < 32) o[i >> 3] = MFMA32(fr[i % RD], pf[i & 7], o[i >> 3]);
      if (i < 8) lacc = MFMA16(sel, pf[i], lacc);
      if (i + RD < 32) fr[i % RD] = FRAGD(i + RD);
      if (i == 9) inv = __builtin_amdgcn_rcpf(lacc[0]);
      if (i >= 10) {
        const int d = (i - 10) >> 3, q = (i - 10) & 7;
        ow[q & 3] = pk2(o[d][2 * q] * inv, o[d][2 * q + 1] * inv);
        asm volatile("" : "+v"(ow));
        if ((q & 3) == 3) ST128(ow, oo + (d & 1) * 1024 + (q >> 2) * 512, (d >> 1) * 262144);
      }
      SCHED();
    }
    __builtin_amdgcn_s_setprio(0);
  }
  int lane2 = __builtin_amdgcn_mbcnt_hi(~0u, __builtin_amdgcn_mbcnt_lo(~0u, 0u));
  asm volatile("" : "+v"(lane2));
#define lane lane2
  const int isG = (idx >> 1) & 1, kh = xcd & 1, tm = 4 * (xcd >> 1) + ((idx >> 1) & 2) + (idx & 1), tn = 2 * (idx >> 3) + kh;
#define KDS 1
#define KDG 1
  const int kd = isG ? KDG : KDS, sd2 = kh ? -kd : kd;
  const int npw = (isG ? 48 : 56) + 2 * sd2;
  const int k2c = ((npw + 19) % 6 < 4) ? K2_C : 0;
#define O_FLAG() do { if (wid == 0 && lane == 0) __builtin_amdgcn_raw_buffer_store_b32(tag, wrs, 0, WS_OFLAG + (rb * 32 + hd) * OFS, 16); } while (0)
#define POLL32(off_, slp) do { for (unsigned spins = 0; spins < 2000000u; ++spins) { \
      if (__builtin_amdgcn_ballot_w64(__builtin_amdgcn_raw_buffer_load_b32(wrs, (off_), 0, 16) != tag) == 0ull) break; \
      __builtin_amdgcn_s_sleep(slp); asm volatile("" ::: "memory"); } \
    asm volatile("" ::: "memory"); } while (0)
  const int c8 = lane & 15, r0 = wc * 4 + (lane >> 4);
  const unsigned goff = (unsigned)((tm * 128 + r0) * 2048 + tn * 256 + c8 * 16);
  const lptr cb = lds + k2c + r0 * 512;
#define G_FIN() do { f32x4 a0[4], a1[4]; \
      _Pragma("unroll") for (int i = 0; i < 4; ++i) { a0[i] = *(const LAS f32x4*)(cb + (wr * 4 + i) * 8192 + (((2 * c8) ^ (r0 & 7)) << 4)); a1[i] = *(const LAS f32x4*)(cb + (wr * 4 + i) * 8192 + (((2 * c8 + 1) ^ (r0 & 7)) << 4)); } \
      _Pragma("unroll") for (int i = 0; i < 4; ++i) \
        ST128((u32x4){pk2(silu(a0[i][0]), silu(a0[i][1])), pk2(silu(a0[i][2]), silu(a0[i][3])), pk2(silu(a1[i][0]), silu(a1[i][1])), pk2(silu(a1[i][2]), silu(a1[i][3]))}, goff + i * 32768, WS_G + wr * 131072); \
      WAIT_V(0); BAR(); } while (0)
  const unsigned f1b = isG ? (unsigned)(WS_HFLAG + ((tm >> 1) * 32 + 16 + (lane & 15)) * 128) : (unsigned)(WS_OFLAG + ((tm >> 1) * 32 + 16 + (lane & 15)) * OFS);
  const unsigned f1 = isG ? f1b : (unsigned)(WS_EFLAG + ((tm >> 1) * 16 + (tm & 1) * 8 + kh * 4 + ((lane >> 2) & 3)) * 64 + (lane & 3) * 4);
  if (wr == 0) {
    const char* ab = ws + (isG ? WS_H : WS_O) + tm * 16384 + wc * 4096;
    const char* wbp = (isG ? w2 : wo) + (tn >> 1) * 512 - 3072 + (isG ? wc : (((wc & 1) << 1) | (wc >> 1))) * 16384;
    const unsigned voffA0 = lane * 16, voffA1 = lane * 16 + 1024;
    unsigned voffW[4];
    {
      const int r = lane >> 3, c = lane & 7;
      const unsigned lp = (unsigned)(r * 2048 + ((c ^ (((r >> 1) & 3) << 1)) << 4) + 3072);
      voffW[0] = lp + kh * 256; voffW[1] = voffW[0] + 128 - 1024; voffW[2] = lp + (kh ^ 1) * 256 - 2048; voffW[3] = voffW[2] + 128 - 1024;
    }
    const char *ap, *wp;
    unsigned dst = lds0 + 4 * K2_STG;
#define K2_RUN(k_) do { ap = ab + (size_t)(k_) * 262144; wp = wbp + (size_t)(k_) * 131072; } while (0)
#define K2_ISSUE(WIDE, ks) do { \
      asm volatile("s_mov_b32 m0, %3\n\ts_nop 0\n\tglobal_load_lds_dwordx4 %0, %2 sc1\n\tglobal_load_lds_dwordx4 %1, %2 offset:1024 sc1" \
                   ::"v"(voffA0), "v"(voffA1), "s"(ap + (ks) * 1024), "s"(dst + wc * 2048) : "memory"); \
      if (WIDE) GLDS4(voffW[0], voffW[1], voffW[2], voffW[3], wp + (ks) * 65536, dst + 8192 + wc * 4096); \
      else GLDS2(voffW[0], voffW[1], wp + (ks) * 65536, dst + 8192 + wc * 4096); \
      dst = (dst == lds0 + 5 * K2_STG) ? lds0 : dst + K2_STG; \
      if (ks) { ap += 262144; wp += 131072; } } while (0)
#define K2_STEP(WIDE, ks, EVENT) do { WAIT_V((WIDE) ? 18 : 12); BAR(); EVENT; K2_ISSUE(WIDE, ks); } while (0)
#define K2_TILES(WIDE, n_) do { _Pragma("unroll 1") for (int i_ = 0; i_ < (n_); ++i_) { K2_STEP(WIDE, 0, ); K2_STEP(WIDE, 1, ); } } while (0)
#define LOOK(f_) asm volatile("buffer_load_dword %0, %1, %2, 0 offen sc1" : "+v"(pol) : "v"(f_), "s"(wrs) : "memory")
    const unsigned f2 = f1b - 16 * (isG ? 128 : OFS);
    const unsigned f3 = (unsigned)(WS_PFLAG + (bid ^ 1) * 128);
    u32x4 xh[8];
#define XH_LD(i) asm volatile("global_load_dwordx4 %0, %1, %2" : "=v"(xh[i]) : "v"(goff), "s"(xn + (i) * 32768) : "memory")
    WAIT_V(0);
    if (isG && lane == 0) __builtin_amdgcn_raw_buffer_store_b32(tag, wrs, 0, WS_EFLAG + (rb * 16 + hd - 16) * 64 + wc * 4, 16);
    POLL32(f1, 4);
    K2_RUN(isG ? 32 + 12 * kh : 32 + 16 * (tm & 1) + 8 * kh);
    K2_ISSUE(1, 0); K2_ISSUE(1, 1);
    WAIT_V(6); BAR();
    K2_ISSUE(1, 0); K2_ISSUE(1, 1); K2_ISSUE(1, 0);
    unsigned pol = 0;
#define CHECK(f_) do { asm volatile("" : "+v"(pol)); if (__builtin_amdgcn_ballot_w64(pol != tag) != 0ull) POLL32(f_, 4); } while (0)
    K2_STEP(1, 1, );
    K2_TILES(1, 2);
    O_FLAG();
    K2_TILES(1, 1);
    LOOK(f1b);
    K2_TILES(1, 2);
    if (!isG) K2_RUN(48 - 16 * (tm & 1) + 6 * kh);
    K2_STEP(1, 0, CHECK(f1b)); K2_STEP(1, 1, );
    K2_TILES(1, isG ? 1 : 3);
    LOOK(f2);
    K2_TILES(1, 2);
    K2_RUN(((isG ? 12 : 14) + kd) * kh);
    K2_STEP(1, 0, CHECK(f2));
    K2_STEP(1, 1, );
#pragma unroll
    for (int i = 0; i < 8; ++i) { K2_STEP(1, 0, XH_LD(i)); K2_STEP(1, 1, ); }
    K2_TILES(1, (isG ? 3 : 5) + sd2);
    asm volatile("" : "+v"(xh[0]), "+v"(xh[1]), "+v"(xh[2]), "+v"(xh[3]), "+v"(xh[4]), "+v"(xh[5]), "+v"(xh[6]), "+v"(xh[7]));
    K2_RUN(isG ? 24 : 60 - 16 * (tm & 1));
    K2_TILES(0, 4);
    K2_RUN(28);
    K2_STEP(0, 0, );
    K2_STEP(0, 1, if (wid == 0 && lane == 0) __builtin_amdgcn_raw_buffer_store_b32(tag, wrs, 0, WS_PFLAG + bid * 128, 16));
    K2_STEP(0, 0, ); K2_STEP(0, 1, );
    const unsigned f4 = (lane & 1) ? (unsigned)(WS_GFLAG + (tm * 8 + tn) * 128) : f3;
    K2_STEP(0, 0, LOOK(f4)); K2_STEP(0, 1, ); K2_STEP(0, 0, );
    WAIT_V(12);
    asm volatile("" : "+v"(pol));
    const unsigned long long miss = __builtin_amdgcn_ballot_w64(pol != tag);
    if (wid == 0 && (miss & 1ull)) POLL32(f3, 2);
    BAR();
    K2_ISSUE(0, 1);
    WAIT_V(12); BAR();
    WAIT_V(8); BAR();
    if (isG) {
      WAIT_V(4); BAR();
      WAIT_V(0); BAR();
      BAR();
      BAR();
    } else {
      u32x4 gv[8];
      const __amdgpu_buffer_rsrc_t ors = __builtin_amdgcn_make_buffer_rsrc(out, 0, 2048 * 2048, 0x00020000);
#define LD8(dst_, rs_, so_, pol_) asm volatile("buffer_load_dwordx4 %0, %8, %10, %11 offen" pol_ "\n\tbuffer_load_dwordx4 %1, %9, %10, %11 offen" pol_ "\n\t" \
        "buffer_load_dwordx4 %2, %8, %10, %12 offen" pol_ "\n\tbuffer_load_dwordx4 %3, %9, %10, %12 offen" pol_ "\n\t" \
        "buffer_load_dwordx4 %4, %8, %10, %13 offen" pol_ "\n\tbuffer_load_dwordx4 %5, %9, %10, %13 offen" pol_ "\n\t" \
        "buffer_load_dwordx4 %6, %8, %10, %14 offen" pol_ "\n\tbuffer_load_dwordx4 %7, %9, %10, %14 offen" pol_ \
        : "=&v"(dst_[0]), "=&v"(dst_[1]), "=&v"(dst_[2]), "=&v"(dst_[3]), "=&v"(dst_[4]), "=&v"(dst_[5]), "=&v"(dst_[6]), "=&v"(dst_[7]) \
        : "v"(goff), "v"(goff + 32768), "s"(rs_), "s"((so_)), "s"((so_) + 65536), "s"((so_) + 131072), "s"((so_) + 196608) : "memory")
      if (miss & 2ull) {
        WAIT_V(4); BAR(); WAIT_V(0); BAR(); BAR();
        POLL32((unsigned)(WS_GFLAG + (tm * 8 + tn) * 128), 1);
      }
      LD8(gv, wrs, WS_G, " sc1");
      if (!(miss & 2ull)) { WAIT_V(12); BAR(); WAIT_V(8); BAR(); BAR(); }
      float xf[8][8];
#pragma unroll
      for (int i = 0; i < 8; ++i) {
#pragma unroll
        for (int e = 0; e < 4; ++e) { xf[i][2 * e] = bf2f(xh[i][e] & 0xffffu); xf[i][2 * e + 1] = U2F(xh[i][e] & 0xffff0000u); }
        asm volatile("" : "+v"(xf[i][0]), "+v"(xf[i][1]), "+v"(xf[i][2]), "+v"(xf[i][3]), "+v"(xf[i][4]), "+v"(xf[i][5]), "+v"(xf[i][6]), "+v"(xf[i][7]));
      }
      asm volatile("s_waitcnt vmcnt(0)" : "+v"(gv[0]), "+v"(gv[1]), "+v"(gv[2]), "+v"(gv[3]), "+v"(gv[4]), "+v"(gv[5]), "+v"(gv[6]), "+v"(gv[7]) :: "memory");
      float gf2[8][8];
#pragma unroll
      for (int i = 0; i < 8; ++i) {
#pragma unroll
        for (int e = 0; e < 4; ++e) { gf2[i][2 * e] = bf2f(gv[i][e] & 0xffffu); gf2[i][2 * e + 1] = U2F(gv[i][e] & 0xffff0000u); }
        asm volatile("" : "+v"(gf2[i][0]), "+v"(gf2[i][1]), "+v"(gf2[i][2]), "+v"(gf2[i][3]), "+v"(gf2[i][4]), "+v"(gf2[i][5]), "+v"(gf2[i][6]), "+v"(gf2[i][7]));
      }
      BAR();
      f32x4 a0[8], a1[8];
#pragma unroll
      for (int i = 0; i < 8; ++i) { a0[i] = *(const LAS f32x4*)(cb + i * 8192 + (((2 * c8) ^ (r0 & 7)) << 4)); a1[i] = *(const LAS f32x4*)(cb + i * 8192 + (((2 * c8 + 1) ^ (r0 & 7)) << 4)); }
      SCHED();
#pragma unroll
      for (int i = 0; i < 8; ++i) {
        const u32x4 w = {pk2(xf[i][0] + a0[i][0] * gf2[i][0], xf[i][1] + a0[i][1] * gf2[i][1]), pk2(xf[i][2] + a0[i][2] * gf2[i][2], xf[i][3] + a0[i][3] * gf2[i][3]),
                         pk2(xf[i][4] + a1[i][0] * gf2[i][4], xf[i][5] + a1[i][1] * gf2[i][5]), pk2(xf[i][6] + a1[i][2] * gf2[i][6], xf[i][7] + a1[i][3] * gf2[i][7])};
        __builtin_amdgcn_raw_buffer_store_b128(w, ors, goff + i * 32768, 0, 16);
      }
    }
  } else {
    const int wm = wc >> 1, wn = wc & 1, g = lane >> 4, t = lane & 15, q4 = (lane >> 2) & 3, p4 = lane & 3;
    lptr w2b[4];
#pragma unroll
    for (int ft = 0; ft < 4; ++ft) w2b[ft] = lds + 4 * K2_STG + 8192 + wn * 1024 + (g >> 1) * 4096 + (4 * (g & 1) + q4) * 128 + 32 * (ft ^ (2 * (g & 1) + (q4 >> 1))) + 8 * p4;
    lptr ar = lds + 4 * K2_STG + wm * 4096 + lane * 16;
    f32x4 ac2[8][4];
#pragma unroll
    for (int m = 0; m < 8; ++m)
#pragma unroll
      for (int n = 0; n < 4; ++n) ac2[m][n] = (f32x4){0.f, 0.f, 0.f, 0.f};
    bf16x8 Af[2][4];
    bf16x4 Wr[4][2];
    int slot = 4, dlt = 0;
#define K2_RW(f, hf) Wr[(f) & 3][hf] = __builtin_amdgcn_ds_read_tr16_b64_v4bf16((LAS bf16x4*)(w2b[(f) & 3] + (((f) >> 2) & 1) * 2048 + (hf) * 8192))
#define K2_W(m) __builtin_shufflevector(Wr[(m) & 3][0], Wr[(m) & 3][1], 0, 1, 2, 3, 4, 5, 6, 7)
#define K2_NEXT() do { dlt = (slot == 5) ? -5 * K2_STG : K2_STG; slot = (slot == 5) ? 0 : slot + 1; } while (0)
#define K2_ADV(x) do { x += dlt; asm volatile("" : "+v"(x)); } while (0)
#define K2_WIDE(st) do { _Pragma("unroll") for (int i = 0; i < 32; ++i) { const int m = i >> 2, q = i & 3; \
        const int n_ = (m & 1) ? 3 - q : q; ac2[m][n_] = MFMA16(K2_W(m), Af[st][n_], ac2[m][n_]); SCHED(); \
        if (q < 2) K2_RW((m + 3) & 7, q); \
        if (q == 2 && m >= 3 && m < 7) Af[(st) ^ 1][m - 3] = ldb128(ar + (m - 3) * 1024); \
        if (i == 2) K2_ADV(ar); \
        if (i == 6) K2_ADV(w2b[0]); \
        if (i == 10) K2_ADV(w2b[1]); \
        if (i == 11) BAR(); \
        if (i == 19) K2_ADV(w2b[2]); \
        if (i == 23) K2_ADV(w2b[3]); \
        if (i == 30) K2_NEXT(); \
        SCHED(); } } while (0)
#define K2_NARROW(st) do { _Pragma("unroll") for (int i = 0; i < 16; ++i) { const int m = i >> 2, q = i & 3; \
        const int n_ = (m & 1) ? 3 - q : q; ac2[m][n_] = MFMA16(K2_W(m), Af[st][n_], ac2[m][n_]); SCHED(); \
        if (q < 2) K2_RW((m + 3) & 3, q); \
        if (q >= 2 && (m == 1 || m == 2)) Af[(st) ^ 1][2 * m + q - 4] = ldb128(ar + (2 * m + q - 4) * 1024); \
        if (i == 2) { K2_ADV(ar); K2_ADV(w2b[0]); } \
        if (i == 3) BAR(); \
        if (i == 5) K2_ADV(w2b[1]); \
        if (i == 9) K2_ADV(w2b[2]); \
        if (i == 14) K2_ADV(w2b[3]); \
        if (i == 15) K2_NEXT(); \
        SCHED(); } } while (0)
    K2_NEXT();
    BAR();
#pragma unroll
    for (int i = 0; i < 4; ++i) { if (i < 3) { K2_RW(i, 0); K2_RW(i, 1); } Af[0][i] = ldb128(ar + i * 1024); SCHED(); }
#pragma unroll 1
    for (int p = 0; p < 4; p += 2) { K2_WIDE(0); K2_WIDE(1); }
    WAIT_V(0);
#pragma unroll 1
    for (int p = 4; p < npw; p += 2) { K2_WIDE(0); K2_WIDE(1); }
    const unsigned po = (unsigned)(WS_P + wc * 8192 + lane * 16);
#pragma unroll
    for (int i = 0; i < 8; ++i) {
      const f32x4 a = ac2[4 + (i >> 1)][2 * (i & 1)], b = ac2[4 + (i >> 1)][2 * (i & 1) + 1];
      ST128((u32x4){pk2(a[0], a[1]), pk2(a[2], a[3]), pk2(b[0], b[1]), pk2(b[2], b[3])}, po + i * 1024, bid * 32768);
    }
    SCHED();
#pragma unroll 1
    for (int p = 0; p < 4; p += 2) { K2_NARROW(0); K2_NARROW(1); }
    WAIT_V(0);
#pragma unroll 1
    for (int p = 4; p < 10; p += 2) { K2_NARROW(0); K2_NARROW(1); }
    K2_NARROW(0);
    u32x4 pv[8];
#pragma unroll
    for (int i = 0; i < 8; ++i) pv[i] = __builtin_amdgcn_raw_buffer_load_b128(wrs, po + i * 1024 + (bid ^ 1) * 32768, 0, 16);
    SCHED();
    K2_NARROW(1);
#pragma unroll 1
    for (int p = 12; p < 16; p += 2) { K2_NARROW(0); K2_NARROW(1); }
    const lptr cw = lds + k2c + (wm * 64 + t) * 512;
#pragma unroll
    for (int m = 0; m < 4; ++m)
#pragma unroll
      for (int n = 0; n < 4; ++n) {
        const unsigned lo = pv[2 * m + (n >> 1)][2 * (n & 1)], hi = pv[2 * m + (n >> 1)][2 * (n & 1) + 1];
        *(LAS f32x4*)(cw + n * 8192 + (((wn * 16 + m * 4 + g) ^ (t & 7)) << 4)) = ac2[m][n] + (f32x4){bf2f(lo & 0xffffu), U2F(lo & 0xffff0000u), bf2f(hi & 0xffffu), U2F(hi & 0xffff0000u)};
      }
    WAIT_L(0); BAR();
    if (isG) G_FIN();
    __builtin_amdgcn_endpgm();
  }
  if (isG) {
      G_FIN();
      if (wid == 0 && lane == 0) __builtin_amdgcn_raw_buffer_store_b32(tag, wrs, 0, WS_GFLAG + (tm * 8 + tn) * 128, 16);
      if (bid == 16 && wid == 0) {
        for (unsigned spins = 0; spins < 2000000u; ++spins) {
          bool ok = true;
#pragma unroll
          for (int i = 0; i < 4; ++i) ok = ok && __builtin_amdgcn_raw_buffer_load_b32(wrs, (unsigned)((lane + i * 64) * 128), WS_HFLAG, 16) == tag;
          if (__builtin_amdgcn_ballot_w64(!ok) == 0ull) break;
          __builtin_amdgcn_s_sleep(4);
          asm volatile("" ::: "memory");
        }
        asm volatile("" ::: "memory");
        if (lane == 0) __builtin_amdgcn_raw_buffer_store_b32(tag, wrs, 0, 0, 16);
      }
    }
#undef lane
}

extern "C" void kernel_launch(void* const* d_in, const int* in_sizes, int n_in, void* d_out, int out_size, void* d_ws, size_t ws_size, hipStream_t stream) {
  const char* x = (const char*)d_in[0]; const char* kc = (const char*)d_in[1]; const char* vc = (const char*)d_in[2];
  const char* wq = (const char*)d_in[3]; const char* wo = (const char*)d_in[4]; const char* w1 = (const char*)d_in[6]; const char* w2 = (const char*)d_in[7];
  (void)hipFuncSetAttribute((const void*)mega_kernel, hipFuncAttributeMaxDynamicSharedMemorySize, K1_LDS);
  mega_kernel<<<256, 512, K1_LDS, stream>>>(x, kc, vc, wq, w1, wo, w2, (char*)d_ws, (char*)d_out);
}
```

```cpp
#include <hip/hip_runtime.h>
#include <stdint.h>

typedef unsigned short u16;
typedef __bf16 bf16x2 __attribute__((ext_vector_type(2)));
typedef __bf16 bf16x4 __attribute__((ext_vector_type(4)));
typedef __bf16 bf16x8 __attribute__((ext_vector_type(8)));
typedef float f32x2 __attribute__((ext_vector_type(2)));
typedef float f32x4 __attribute__((ext_vector_type(4)));
typedef float f32x16 __attribute__((ext_vector_type(16)));
typedef unsigned u32x2 __attribute__((ext_vector_type(2)));
typedef unsigned u32x4 __attribute__((ext_vector_type(4)));
#define LAS __attribute__((address_space(3)))
typedef LAS char* lptr;
#if defined(__HIP_DEVICE_COMPILE__) && !defined(USEPK)
#define NOPK __attribute__((target("no-packed-fp32-ops")))
#else
#define NOPK
#endif
#define DI NOPK __device__ __forceinline__
#define F2U(x) __builtin_bit_cast(unsigned, (float)(x))
#define U2F(x) __builtin_bit_cast(float, (unsigned)(x))
#define TID ((int)__builtin_amdgcn_workitem_id_x())
#define BID ((int)__builtin_amdgcn_workgroup_id_x())
#define SCHED() __builtin_amdgcn_sched_barrier(0)
#define BAR() asm volatile("s_barrier" ::: "memory")
#define WAIT_V(n) asm volatile("s_waitcnt vmcnt(%0)" :: "n"(n) : "memory")
#define WAIT_L(n) do { asm volatile("" ::: "memory"); __builtin_amdgcn_s_waitcnt(0xC07F | ((n) << 8)); asm volatile("" ::: "memory"); } while (0)
#define MFMA16(a, b, c) __builtin_amdgcn_mfma_f32_16x16x32_bf16(a, b, c, 0, 0, 0)
#define MFMA32(a, b, c) __builtin_amdgcn_mfma_f32_32x32x16_bf16(a, b, c, 0, 0, 0)

DI void glds(unsigned voff, const char* sbase, unsigned ldsaddr) {
  asm volatile("s_mov_b32 m0, %2\n\ts_nop 0\n\tglobal_load_lds_dwordx4 %0, %1" ::"v"(voff), "s"(sbase), "s"(ldsaddr) : "memory");
}
DI void glds1(unsigned voff, const char* sbase, unsigned ldsaddr) {
  asm volatile("s_mov_b32 m0, %2\n\ts_nop 0\n\tglobal_load_lds_dwordx4 %0, %1 sc1" ::"v"(voff), "s"(sbase), "s"(ldsaddr) : "memory");
}
DI unsigned pk2(float lo, float hi) { f32x2 v = {lo, hi}; return __builtin_bit_cast(unsigned, __builtin_convertvector(v, bf16x2)); }
DI float bf2f(unsigned b) { return U2F(b << 16); }
DI bf16x8 ldb128(lptr p) { return *(const LAS bf16x8*)p; }
DI bf16x8 ldtr2(lptr p, int d) {
  bf16x4 a = __builtin_amdgcn_ds_read_tr16_b64_v4bf16((LAS bf16x4*)p);
  bf16x4 b = __builtin_amdgcn_ds_read_tr16_b64_v4bf16((LAS bf16x4*)(p + d));
  return __builtin_shufflevector(a, b, 0, 1, 2, 3, 4, 5, 6, 7);
}
DI void ST128_(__amdgpu_buffer_rsrc_t r, u32x4 v, unsigned voff, int soff) { __builtin_amdgcn_raw_buffer_store_b128(v, r, voff + (unsigned)soff, 0, 16); }
DI float silu(float v) { return v * __builtin_amdgcn_rcpf(1.0f + __builtin_amdgcn_exp2f(v * -1.4426950408889634f)); }

template <int CTRL> DI float dpp_add(float v) { return v + U2F(__builtin_amdgcn_update_dpp(0u, F2U(v), CTRL, 0xf, 0xf, true)); }
DI float half_sum(float v) {
  v = dpp_add<0xB1>(v); v = dpp_add<0x4E>(v); v = dpp_add<0x141>(v); v = dpp_add<0x140>(v);
  auto rr = __builtin_amdgcn_permlane16_swap(F2U(v), F2U(v), false, false);
  return U2F(rr[0]) + U2F(rr[1]);
}
DI float col_max(float v) {
  auto a = __builtin_amdgcn_permlane16_swap(F2U(v), F2U(v), false, false);
  v = __builtin_fmaxf(U2F(a[0]), U2F(a[1]));
  auto b = __builtin_amdgcn_permlane32_swap(F2U(v), F2U(v), false, false);
  return __builtin_fmaxf(U2F(b[0]), U2F(b[1]));
}
typedef __attribute__((address_space(1))) unsigned long long gu64;

#define SX(b, h) (98304 + (b) * 32768 + (h) * 16384)
constexpr int SLOT_B = 0, SLOT_E = 32768, SLOT_A = 65536, SLOT_C = 98304, SLOT_D = 131072, K1_LDS = 163840;

#define XSWAP 0
constexpr int WS_HFLAG = 4096;
constexpr int WS_GFLAG = 65536;
constexpr int WS_OFLAG = 131072;
#define OFS 16
constexpr int WS_EFLAG = 163840;
constexpr int WS_STATS = 1 << 20;
constexpr int WS_PFLAG = 196608;
constexpr int WS_G = 5 << 20, WS_O = 9 << 20, WS_H = 25 << 20, WS_P = 39 << 20, WS_END = 47 << 20;
constexpr int K2_STG = 24576, K2_C = 98304;

NOPK __global__ __launch_bounds__(512, 2) void mega_kernel(const char* __restrict__ xn, const char* __restrict__ kc, const char* __restrict__ vc,
                                                           const char* __restrict__ wq, const char* __restrict__ w1, const char* __restrict__ wo, const char* __restrict__ w2,
                                                           char* ws, char* __restrict__ out) {
  extern __shared__ __attribute__((aligned(16))) char shm[];
  const lptr lds = (lptr)shm;
  const unsigned lds0 = (unsigned)(uintptr_t)lds;
  const int tid = TID, wid = __builtin_amdgcn_readfirstlane(tid >> 6), lane = tid & 63, wr = wid >> 2, wc = wid & 3;
  const int bid = BID ^ XSWAP, xcd = bid & 7, idx = bid >> 3, hd = (idx & 2) * 8 + 2 * xcd + (idx & 1), rb = idx >> 2;
  const int g = lane >> 4, t = lane & 15, q4 = (lane >> 2) & 3, p4 = lane & 3;

  const bool light = (idx & 2) != 0;
  unsigned voffX[4], voffQ[2], voffH[2];
  {
    const int r = lane >> 3, c = lane & 7;
#pragma unroll
    for (int i = 0; i < 4; ++i) voffX[i] = (unsigned)((8 * i + r) * 2048 + ((c ^ (4 * (i & 1) + (r >> 1))) << 4) - i * 1024);
    const int k = 32 * (wid >> 2) + 16 * (wid & 1) + 4 * ((wid >> 1) & 1) + 8 * (r >> 2) + (r & 3);
    const int cw = c ^ (((r >> 1) & 3) << 1);
    voffQ[0] = (unsigned)(k * 8192 + cw * 16 + 1024); voffQ[1] = voffQ[0] + 128 - 1024;
    voffH[0] = (unsigned)(k * 7168 + 128 + (light ? (cw & 3) : cw) * 16 + 1024);
    voffH[1] = (unsigned)(k * 7168 + cw * 16);
  }
  const char* xb = xn + (size_t)rb * 256 * 2048 + wid * 65536;
  const char* qb = wq + hd * 256 - 1024;
  const char* hb = w1 + (light ? 4096 + 192 * (hd - 16) : 256 * hd) - 1024;
  const bool full = wr == 1 || !light;
  const int sb = hd >= 24;
#define GLDS2(v0_, v1_, sb_, ld_) asm volatile("s_mov_b32 m0, %3\n\ts_nop 0\n\tglobal_load_lds_dwordx4 %0, %2\n\tglobal_load_lds_dwordx4 %1, %2 offset:1024" \
    ::"v"(v0_), "v"(v1_), "s"(sb_), "s"(ld_) : "memory")
#define GLDS4(v0_, v1_, v2_, v3_, sb_, ld_) asm volatile("s_mov_b32 m0, %5\n\ts_nop 0\n\tglobal_load_lds_dwordx4 %0, %4\n\tglobal_load_lds_dwordx4 %1, %4 offset:1024\n\t" \
    "global_load_lds_dwordx4 %2, %4 offset:2048\n\tglobal_load_lds_dwordx4 %3, %4 offset:3072" ::"v"(v0_), "v"(v1_), "v"(v2_), "v"(v3_), "s"(sb_), "s"(ld_) : "memory")
#define ST_X(b, kt) GLDS4(voffX[0], voffX[1], voffX[2], voffX[3], xb + (kt) * 128, lds0 + SX(b, 0) + wid * 4096)
#define ST_W0(wo_, kt) GLDS2(voffQ[0], voffQ[1], qb + (size_t)(kt) * 524288, lds0 + (wo_) + wid * 2048)
#define ST_W1(wo_, kt) GLDS2(voffH[0], voffH[1], hb + (size_t)(kt) * 458752, lds0 + (wo_) + 16384 + wid * 2048)

  lptr wb[4], xr[2];
#pragma unroll
  for (int m = 0; m < 4; ++m) wb[m] = lds + wr * 1024 + (g >> 1) * 2048 + (4 * (g & 1) + q4) * 128 + 32 * ((m ^ wc) ^ (2 * (g & 1) + (q4 >> 1))) + 8 * p4;
#pragma unroll
  for (int ks = 0; ks < 2; ++ks) xr[ks] = lds + 98304 + (wc * 32 + t) * 128 + 16 * ((4 * ks + g) ^ (t >> 1));
#define LDW(dst, h) do { _Pragma("unroll") for (int m = 0; m < 4; ++m) _Pragma("unroll") for (int k = 0; k < 2; ++k) dst[m][k] = ldtr2(wb[m] + (h) * 16384 + k * 8192, 4096); } while (0)
#define LDX(dst, b, h) do { _Pragma("unroll") for (int n = 0; n < 2; ++n) _Pragma("unroll") for (int k = 0; k < 2; ++k) dst[n][k] = ldb128(xr[k] + (SX(b, h) - 98304) + n * 2048); } while (0)

  f32x4 acc[2][2][4][2];
#pragma unroll
  for (int a = 0; a < 2; ++a)
#pragma unroll
    for (int b = 0; b < 2; ++b)
#pragma unroll
      for (int m = 0; m < 4; ++m)
#pragma unroll
        for (int n = 0; n < 2; ++n) acc[a][b][m][n] = (f32x4){0.f, 0.f, 0.f, 0.f};
  bf16x8 At[4][2], B0[2][2], B1[2][2];

#define MM1(ai, m, k, j) do { if ((j) < 2) acc[ai][0][m][(j) & 1] = MFMA16(At[m][k], B0[(j) & 1][k], acc[ai][0][m][(j) & 1]); else acc[ai][1][m][(j) & 1] = MFMA16(At[m][k], B1[(j) & 1][k], acc[ai][1][m][(j) & 1]); SCHED(); } while (0)
#define MM8(ai, m) do { _Pragma("unroll") for (int q = 0; q < 4; ++q) { MM1(ai, m, q & 1, ((m) & 1) ? 3 - q : q); MM1(ai, m, (q & 1) ^ 1, ((m) & 1) ? 3 - q : q); } } while (0)
#define MMA2(ai, CLOSE) do { __builtin_amdgcn_s_setprio(1); \
    cs[ai] = MFMA16(At[0][1], ones, cs[ai]); SCHED(); cs[ai] = MFMA16(At[0][0], ones, cs[ai]); SCHED(); MM8(ai, 0); asm volatile("" : "+v"(cs[ai])); MM8(ai, 1); \
    if ((ai) == 0 || full) { MM8(ai, 2); MM8(ai, 3); } \
    SCHED(); CLOSE; __builtin_amdgcn_s_setprio(0); SCHED(); } while (0)
  char* const stats = ws + WS_STATS;
  const __amdgpu_buffer_rsrc_t wrs = __builtin_amdgcn_make_buffer_rsrc(ws, 0, WS_END, 0x00020000);
  f32x4 cs[2] = {{0.f, 0.f, 0.f, 0.f}, {0.f, 0.f, 0.f, 0.f}};
  bf16x8 ones = __builtin_bit_cast(bf16x8, (u32x4){0x3f803f80u, 0x3f803f80u, 0x3f803f80u, 0x3f803f80u});
#define PIN_SETUP() do { asm volatile("" : "+v"(ones), "+v"(cs[0]), "+v"(cs[1]), "+v"(wb[0]), "+v"(wb[1]), "+v"(wb[2]), "+v"(wb[3]), "+v"(xr[0]), "+v"(xr[1])); \
    _Pragma("unroll") for (int a = 0; a < 2; ++a) _Pragma("unroll") for (int b = 0; b < 2; ++b) \
      asm volatile("" : "+v"(acc[a][b][0][0]), "+v"(acc[a][b][0][1]), "+v"(acc[a][b][1][0]), "+v"(acc[a][b][1][1]), "+v"(acc[a][b][2][0]), "+v"(acc[a][b][2][1]), "+v"(acc[a][b][3][0]), "+v"(acc[a][b][3][1])); } while (0)
  unsigned tag;
  {
    if (wr == 0) {
      unsigned ep;
      ST_X(0, 0); ST_W0(0, 0);
      GLDS4(voffX[0], voffX[1], voffX[2], voffX[3], xb + 262144, lds0 + SX(0, 1) + wid * 4096);
      GLDS2(voffQ[0], voffQ[1], qb + 262144, lds0 + 8192 + wid * 2048);
      asm volatile("global_load_dword %0, %1, %2 sc1" : "=&v"(ep) : "v"(0), "s"(ws) : "memory");
      PIN_SETUP();
      asm volatile("s_waitcnt vmcnt(0)" : "+v"(ep) :: "memory");
      tag = __builtin_amdgcn_readfirstlane(ep) + 1u;
      BAR();
    } else {
      const int row = rb * 256 + hd * 8 + wc * 2 + (lane >> 5);
      u32x4 a[4];
      unsigned ep;
      asm volatile("global_load_dword %0, %1, %2 sc1" : "=&v"(ep) : "v"(0), "s"(ws) : "memory");
      asm volatile("global_load_dwordx4 %0, %4, %5\n\tglobal_load_dwordx4 %1, %4, %5 offset:512\n\tglobal_load_dwordx4 %2, %4, %5 offset:1024\n\tglobal_load_dwordx4 %3, %4, %5 offset:1536"
                   : "=&v"(a[0]), "=&v"(a[1]), "=&v"(a[2]), "=&v"(a[3]) : "v"(row * 2048 + (lane & 31) * 16), "s"(xn) : "memory");
      ST_W1(0, 0); GLDS2(voffH[0], voffH[1], hb - 229376, lds0 + 16384 + wc * 2048);
      PIN_SETUP();
      BAR();
      ST_W0(32768, 1); GLDS2(voffQ[0], voffQ[1], qb + 524288 - 262144, lds0 + 32768 + wc * 2048);
      ST_X(1, 1); GLDS4(voffX[0], voffX[1], voffX[2], voffX[3], xb - 262144 + 128, lds0 + SX(1, 0) + wc * 4096);
      asm volatile("s_waitcnt vmcnt(16)" : "+v"(ep), "+v"(a[0]), "+v"(a[1]), "+v"(a[2]), "+v"(a[3]) :: "memory");
      tag = __builtin_amdgcn_readfirstlane(ep) + 1u;
      float v[32];
#pragma unroll
      for (int i = 0; i < 16; ++i) { v[2 * i] = bf2f(a[i >> 2][i & 3] & 0xffffu); v[2 * i + 1] = U2F(a[i >> 2][i & 3] & 0xffff0000u); }
      float s0 = 0.f, s1 = 0.f, s2 = 0.f, s3 = 0.f;
#pragma unroll
      for (int i = 0; i < 8; ++i) { s0 += v[i]; s1 += v[8 + i]; s2 += v[16 + i]; s3 += v[24 + i]; }
      const float mean = half_sum((s0 + s1) + (s2 + s3)) * (1.0f / 1024.0f);
      s0 = s1 = s2 = s3 = 0.f;
#pragma unroll
      for (int i = 0; i < 8; ++i) { const float d0 = v[i] - mean, d1 = v[8 + i] - mean, d2 = v[16 + i] - mean, d3 = v[24 + i] - mean; s0 += d0 * d0; s1 += d1 * d1; s2 += d2 * d2; s3 += d3 * d3; }
      const float sd = __builtin_amdgcn_sqrtf(half_sum((s0 + s1) + (s2 + s3)) * (1.0f / 1024.0f) + 1e-5f);
      if ((lane & 31) < 2) __hip_atomic_store((gu64*)(stats + row * 16 + (lane & 1) * 8), ((unsigned long long)tag << 32) | F2U((lane & 1) ? sd : mean), __ATOMIC_RELAXED, __HIP_MEMORY_SCOPE_AGENT);
    }
  }
  if (wr == 1) BAR();
  SCHED();
  const int kvg = hd >> 3;
  const char* kb_g = kc + ((size_t)kvg * 2048 + 1536) * 256;
  const char* vb_g = vc + ((size_t)kvg * 2048 + 1536) * 256;
#define VOFFK ((unsigned)((lane >> 3) * 256 + (((lane & 7) ^ (4 * (wid & 1) + (lane >> 4))) << 4)))
#define VOFFV ((unsigned)((lane >> 3) * 256 + (((lane & 7) ^ ((lane >> 4) << 1)) << 4)))
#define ST_KV8(voff, gsrc, slot) do { glds(voff, (gsrc) + wid * 2048, lds0 + (slot) + wid * 2048); glds(voff, (gsrc) + wid * 2048 + 128, lds0 + (slot) + wid * 2048 + 1024); } while (0)
  int wprv = 65536, wnxt = 32768;
#define W_ADV() do { const int d_ = (wnxt == 0) ? -65536 : 32768, cur_ = wnxt - d_; wnxt = wprv; wprv = cur_; \
    _Pragma("unroll") for (int f = 0; f < 4; ++f) wb[f] += d_; \
    asm volatile("" : "+v"(wb[0]), "+v"(wb[1]), "+v"(wb[2]), "+v"(wb[3])); } while (0)
#define PH_A(b, STAGE) do { LDX(B0, b, 0); LDX(B1, b, 1); SCHED(); LDW(At, 0); STAGE; WAIT_V(10); WAIT_L(12); BAR(); SCHED(); MMA2(0, WAIT_L(0); BAR()); } while (0)
#define PH_B(STAGE) do { LDW(At, 1); STAGE; W_ADV(); WAIT_V(8); BAR(); SCHED(); MMA2(1, WAIT_L(0); BAR()); } while (0)
#pragma unroll 1
  for (int kt = 0; kt < 14; kt += 2) {
    PH_A(0, ST_W1(wnxt, kt + 1); ST_W0(wprv, kt + 2));
    PH_B(ST_X(0, kt + 2));
    PH_A(1, ST_W1(wnxt, kt + 2); ST_W0(wprv, kt + 3));
    PH_B(ST_X(1, kt + 3));
  }
  const unsigned voffK = VOFFK, voffV = VOFFV;
  PH_A(0, ST_W1(0, 15); ST_KV8(voffK, kb_g, SLOT_E));
  PH_B(ST_KV8(voffV, vb_g, SLOT_C); ST_KV8(voffV, vb_g + 16384, SLOT_C + 16384));
  const unsigned soff = (unsigned)((rb * 256 + (g >> 1) * 128 + wc * 32 + (g & 1) * 16 + t) * 16);
  u32x4 sg;
  LDX(B0, 1, 0); LDX(B1, 1, 1); SCHED(); LDW(At, 0);
  asm volatile("global_load_dwordx4 %0, %1, %2 sc1" : "=&v"(sg) : "v"(soff), "s"(stats) : "memory");
  ST_KV8(voffK, kb_g + 16384, SLOT_E + 16384); ST_KV8(voffK, kb_g + 32768, SLOT_A);
  WAIT_V(11); BAR(); SCHED(); MMA2(0, WAIT_L(0); BAR());
  LDW(At, 1);
  ST_KV8(voffK, kb_g + 49152, SLOT_A + 16384);
  BAR(); SCHED();
  const lptr csw = lds + SLOT_D + (t & 3) * 8192 + wr * 4096 + wc * 64 + g * 16;
  MMA2(1, *(LAS f32x4*)csw = cs[0]; *(LAS f32x4*)(csw + 256) = cs[1]; asm volatile("s_waitcnt vmcnt(4)" : "+v"(sg) :: "memory"); WAIT_L(0); BAR());
  f32x4 cv[2][4];
#pragma unroll
  for (int a = 0; a < 2; ++a)
#pragma unroll
    for (int m = 0; m < 4; ++m) cv[a][m] = *(const LAS f32x4*)(lds + SLOT_D + wc * 8192 + wr * 4096 + a * 256 + (m ^ wc) * 64 + g * 16);
  if (__builtin_expect(__builtin_amdgcn_ballot_w64(sg[1] != tag || sg[3] != tag) != 0ull, 0)) {
    for (unsigned spins = 0; spins < 4000000u; ++spins) {
      __builtin_amdgcn_s_sleep(2);
      asm volatile("" ::: "memory");
      sg = __builtin_amdgcn_raw_buffer_load_b128(wrs, soff, WS_STATS, 16);
      if (__builtin_amdgcn_ballot_w64(sg[1] != tag || sg[3] != tag) == 0ull) break;
    }
  }
  float mu[2][2], rs[2][2], sd[2][2];
  {
    auto m16 = __builtin_amdgcn_permlane16_swap(sg[0], sg[0], false, false);
    auto s16 = __builtin_amdgcn_permlane16_swap(sg[2], sg[2], false, false);
#pragma unroll
    for (int n = 0; n < 2; ++n) {
      auto m32 = __builtin_amdgcn_permlane32_swap(m16[n], m16[n], false, false);
      auto s32 = __builtin_amdgcn_permlane32_swap(s16[n], s16[n], false, false);
#pragma unroll
      for (int bj = 0; bj < 2; ++bj) { mu[bj][n] = -U2F(m32[bj]); sd[bj][n] = U2F(s32[bj]); rs[bj][n] = __builtin_amdgcn_rcpf(sd[bj][n]) * 1.4426950408889634f; }
    }
  }
  WAIT_L(0);
  {
    const lptr qw = lds + wc * 8192 + wr * 4096 + ((g >> 1) * 16 + t) * 16 + (g & 1) * 8;
#pragma unroll
    for (int bj = 0; bj < 2; ++bj) {
      lptr qwb = qw + ((bj ^ sb) ? SLOT_D : SLOT_B);
      asm volatile("" : "+v"(qwb));
#pragma unroll
      for (int m = 0; m < 4; ++m)
#pragma unroll
        for (int n = 0; n < 2; ++n) {
          const f32x4 v = (acc[0][bj][m][n] + cv[0][m] * mu[bj][n]) * (rs[bj][n] * 0.08838834764831845f);
          u32x2 w = {pk2(v[0], v[1]), pk2(v[2], v[3])};
          *(LAS u32x2*)(qwb + ((m ^ wc) >> 1) * 2048 + ((m ^ wc) & 1) * 512 + n * 1024) = w;
        }
    }
  }
  WAIT_L(0); BAR(); SCHED();
  unsigned pft = 0;
  const int sl = ((idx >> 2) * 16 + (idx & 1) * 8 + xcd) * 8 + wid;
  const int tstr = light ? 896 : 1024;
  const char* tbase = (light ? w2 : wo) + (sl < tstr ? sl : tstr - 1) * 2048;
#define TOUCH(q_) asm volatile("global_load_dword %0, %1, %2" : "+v"(pft) : "v"((unsigned)((__builtin_amdgcn_mbcnt_lo(~0u, 0u) & 15) * 128)), "s"(tbase + (((q_) + 2) & 3) * tstr * 2048) : "memory")
  TOUCH(0);
  bf16x8 qf[8];
  const lptr qr = lds + (wr ? SLOT_D : SLOT_B) + wc * 8192 + lane * 16;
  if (wr == 1) {
#pragma unroll
    for (int s = 0; s < 8; ++s) qf[s] = ldb128(qr + s * 1024);
  }
#define ST128(...) ST128_(wrs, __VA_ARGS__)
  u32x4 hq[3];
  unsigned hqo;
  {
    u32x2 hw[2][2][2];
#define SILU16(m, ACT) do { \
      const f32x4 a0 = acc[1][0][m][0] + cv[1][m] * mu[0][0], a1 = acc[1][0][m][1] + cv[1][m] * mu[0][1], a2 = acc[1][1][m][0] + cv[1][m] * mu[1][0], a3 = acc[1][1][m][1] + cv[1][m] * mu[1][1]; \
      f32x4 e0 = a0 * -rs[0][0], e1 = a1 * -rs[0][1], e2 = a2 * -rs[1][0], e3 = a3 * -rs[1][1]; SCHED(); \
      if (ACT) { \
      _Pragma("unroll") for (int i = 0; i < 4; ++i) { e0[i] = __builtin_amdgcn_exp2f(e0[i]); e1[i] = __builtin_amdgcn_exp2f(e1[i]); e2[i] = __builtin_amdgcn_exp2f(e2[i]); e3[i] = __builtin_amdgcn_exp2f(e3[i]); } SCHED(); \
      e0 = e0 * sd[0][0] + sd[0][0]; e1 = e1 * sd[0][1] + sd[0][1]; e2 = e2 * sd[1][0] + sd[1][0]; e3 = e3 * sd[1][1] + sd[1][1]; SCHED(); \
      _Pragma("unroll") for (int i = 0; i < 4; ++i) { e0[i] = __builtin_amdgcn_rcpf(e0[i]); e1[i] = __builtin_amdgcn_rcpf(e1[i]); e2[i] = __builtin_amdgcn_rcpf(e2[i]); e3[i] = __builtin_amdgcn_rcpf(e3[i]); } SCHED(); \
      e0 *= a0; e1 *= a1; e2 *= a2; e3 *= a3; SCHED(); } \
      else { _Pragma("unroll") for (int i = 0; i < 4; ++i) e3[i] = a3[i] * __builtin_amdgcn_rcpf(__builtin_amdgcn_exp2f(e3[i]) * sd[1][1] + sd[1][1]); } \
      hw[0][0][(m) & 1] = (u32x2){pk2(e0[0], e0[1]), pk2(e0[2], e0[3])}; hw[0][1][(m) & 1] = (u32x2){pk2(e1[0], e1[1]), pk2(e1[2], e1[3])}; \
      hw[1][0][(m) & 1] = (u32x2){pk2(e2[0], e2[1]), pk2(e2[2], e2[3])}; hw[1][1][(m) & 1] = (u32x2){pk2(e3[0], e3[1]), pk2(e3[2], e3[3])}; SCHED(); } while (0)
#define HFRAG(bj, n) ((wc & 1) ? (u32x4){hw[bj][n][1][0], hw[bj][n][1][1], hw[bj][n][0][0], hw[bj][n][0][1]} : (u32x4){hw[bj][n][0][0], hw[bj][n][0][1], hw[bj][n][1][0], hw[bj][n][1][1]})
    const unsigned hoff = (unsigned)(WS_H + (rb * 16 + wc * 2) * 2048 + lane * 16);
    const int pb = (light ? 16 + 3 * hd : 4 * hd) + (wr ? 0 : 2), sw = full ? (wc >> 1) : 0, p0 = pb + sw, p1 = pb + (sw ^ 1);
    SILU16(0, false); SILU16(1, false);
    hqo = hoff + (p0 >> 1) * 262144 + (p0 & 1) * 1024;
    hq[0] = HFRAG(0, 0); hq[1] = HFRAG(0, 1); hq[2] = HFRAG(1, 0);
    ST128(HFRAG(1, 1), hqo + 2048, 16384);
    asm volatile("" : "+v"(hq[0]), "+v"(hq[1]), "+v"(hq[2]));
    if (wr == 0) {
      BAR(); SCHED();
#pragma unroll
      for (int s = 0; s < 8; ++s) qf[s] = ldb128(qr + s * 1024);
    }
    if (full) {
      SILU16(2, true); SILU16(3, true);
#pragma unroll
      for (int bj = 0; bj < 2; ++bj)
#pragma unroll
        for (int n = 0; n < 2; ++n) ST128(HFRAG(bj, n), hoff + n * 2048, (p1 >> 1) * 262144 + (p1 & 1) * 1024 + bj * 16384);
    }
  }
  if (wr == 1) { WAIT_L(0); BAR(); }
  __builtin_amdgcn_s_setprio(1);
  SCHED();
  lptr kb[2], vb[4];
#pragma unroll
  for (int e = 0; e < 2; ++e) kb[e] = lds + SLOT_E + (t >> 3) * 2048 + (t & 7) * 128 + (((4 * e + g) ^ (t >> 1)) << 4);
#pragma unroll
  for (int f = 0; f < 4; ++f) vb[f] = lds + SLOT_C + (g >> 1) * 2048 + (4 * (g & 1) + q4) * 128 + 32 * (f ^ (2 * (g & 1) + (q4 >> 1))) + 8 * p4;
  f32x4 o[8][2], s[8][2], lacc[2], negm[2];
#pragma unroll
  for (int n = 0; n < 2; ++n) {
    lacc[n] = (f32x4){0.f, 0.f, 0.f, 0.f};
#pragma unroll
    for (int d = 0; d < 8; ++d) o[d][n] = (f32x4){0.f, 0.f, 0.f, 0.f};
  }
  bf16x8 pf[8], Kf[32];
  bf16x4 Vh[32][2];
#define SM_THR 60.0f
#define LA 6
#define GW 0
#define MAXX 1
#define MX3(a, b, c) __builtin_fmaxf(__builtin_fmaxf((a), (b)), (c))
  float mxp = 0.f;
#define RD_V(f, hf) Vh[f][hf] = __builtin_amdgcn_ds_read_tr16_b64_v4bf16((LAS bf16x4*)(vb[((f) >> 2) & 3] + ((f) >> 4) * 1024 + ((f) & 3) * 8192 + (hf) * 4096))
#define RD_K(f) Kf[f] = ldb128(kb[(f) & 1] + ((f) >> 2) * 4096 + (((f) >> 1) & 1) * 1024)
#define VF(f) __builtin_shufflevector(Vh[f][0], Vh[f][1], 0, 1, 2, 3, 4, 5, 6, 7)
#define PREF_V() do { _Pragma("unroll") for (int f = 0; f < LA; ++f) { RD_V(f, 0); RD_V(f, 1); } SCHED(); } while (0)
#define PV_SEG(NEXTK, FILL) do { \
    _Pragma("unroll") for (int i = 0; i < 8; ++i) { lacc[i >> 2] = MFMA16(ones, pf[2 * (i & 3) + (i >> 2)], lacc[i >> 2]); SCHED(); } \
    _Pragma("unroll") for (int i = 0; i < 64; ++i) { const int d = i >> 3, n = (i >> 2) & 1, k = n ? 3 - (i & 3) : (i & 3), f = (i >> 1) + LA; \
      if (GW && (i & 7) == 0 && i < 48) WAIT_L(2 * LA - 8); \
      o[d][n] = MFMA16(VF(4 * d + k), pf[2 * k + n], o[d][n]); SCHED(); \
      if (f < 32) RD_V(f < 32 ? f : 0, i & 1); else if ((NEXTK) && (i & 1)) RD_K(f >= 32 ? f - 32 : 0); \
      FILL; SCHED(); } } while (0)
#define QK_SEG(HAVE, CLOSE) do { \
    _Pragma("unroll") for (int i = 0; i < 64; ++i) { const int r = i >> 3, n = (i >> 2) & 1, k = n ? 3 - (i & 3) : (i & 3), f = (i >> 1) + LA; \
      if (GW && (i & 7) == 0 && i < 48) WAIT_L(LA - 4); \
      s[r][n] = MFMA16(Kf[4 * r + k], qf[2 * k + n], (i & 3) == 0 ? negm[n] : s[r][n]); SCHED(); \
      if ((i & 1) && f < 32 && f >= (HAVE)) RD_K(f < 32 ? f : 0); \
      if (MAXX && !(i & 1) && i >= 8) { const int e_ = (i - 8) >> 1, r_ = e_ >> 2, n_ = (e_ >> 1) & 1, h_ = (e_ & 1) * 2; \
        mxp = MX3(e_ == 0 ? s[0][0][0] : mxp, s[r_][n_][h_], s[r_][n_][h_ + 1]); asm volatile("" : "+v"(mxp)); } \
      SCHED(); \
      if (i == 59) { __builtin_amdgcn_s_setprio(2); CLOSE; WAIT_L(0); BAR(); SCHED(); } } \
    __builtin_amdgcn_s_setprio(0); } while (0)
#define MX4(m_, v_) MX3(MX3(m_, v_[0], v_[1]), v_[2], v_[3])
#define ROWMAX(n) MX4(MX4(MX4(MX4(MX4(MX4(MX4(MX4(s[0][n][0], s[0][n]), s[1][n]), s[2][n]), s[3][n]), s[4][n]), s[5][n]), s[6][n]), s[7][n])
#define SOFTMAX() do { \
      const float mx = MAXX ? MX4(MX4(mxp, s[7][0]), s[7][1]) : __builtin_fmaxf(ROWMAX(0), ROWMAX(1)); \
        \
      if (__builtin_expect(__builtin_amdgcn_ballot_w64(!(mx <= SM_THR)) != 0ull, 0)) { \
        const float m0 = ROWMAX(0), m1 = ROWMAX(1); \
        _Pragma("unroll") for (int n = 0; n < 2; ++n) { \
          const float dl = __builtin_fmaxf(col_max(n ? m1 : m0), 0.f), alpha = __builtin_amdgcn_exp2f(-dl); \
          negm[n] -= dl; lacc[n] *= alpha; \
          _Pragma("unroll") for (int r = 0; r < 8; ++r) { s[r][n] -= dl; o[r][n] *= alpha; } } \
      } \
      SCHED(); \
      _Pragma("unroll") for (int k = 0; k < 4; ++k) { \
        _Pragma("unroll") for (int n = 0; n < 2; ++n) { \
          _Pragma("unroll") for (int i = 0; i < 4; ++i) { s[2 * k][n][i] = __builtin_amdgcn_exp2f(s[2 * k][n][i]); s[2 * k + 1][n][i] = __builtin_amdgcn_exp2f(s[2 * k + 1][n][i]); } \
          u32x4 w = {pk2(s[2 * k][n][0], s[2 * k][n][1]), pk2(s[2 * k][n][2], s[2 * k][n][3]), pk2(s[2 * k + 1][n][0], s[2 * k + 1][n][1]), pk2(s[2 * k + 1][n][2], s[2 * k + 1][n][3])}; \
          pf[2 * k + n] = __builtin_bit_cast(bf16x8, w); } \
        asm volatile("" : "+v"(pf[2 * k]), "+v"(pf[2 * k + 1])); \
        SCHED(); } } while (0)
  {
#pragma unroll
    for (int f = 0; f < 8; ++f) RD_K(f);
#pragma unroll
    for (int d = 0; d < 8; d += 2) asm volatile("" : "+v"(o[d][0]), "+v"(o[d][1]), "+v"(o[d + 1][0]), "+v"(o[d + 1][1]));
    f32x4 u[2][2];
    const f32x4 z4 = {0.f, 0.f, 0.f, 0.f};
#pragma unroll
    for (int i = 0; i < 16; ++i) { const int r = i >> 3, n = (i >> 2) & 1, k = i & 3; u[r][n] = MFMA16(Kf[4 * r + k], qf[2 * k + n], k == 0 ? z4 : u[r][n]); }
#pragma unroll
    for (int n = 0; n < 2; ++n) { const float m = -col_max(MX4(MX3(u[0][n][0], u[0][n][1], __builtin_fmaxf(u[0][n][2], u[0][n][3])), u[1][n])); negm[n] = (f32x4){m, m, m, m}; }
    asm volatile("" : "+v"(negm[0]), "+v"(negm[1]));
    SCHED();
    QK_SEG(8, WAIT_V(1));
  }
  const char* kvsrc = (wr ? kb_g + 65536 : vb_g + 32768) + wc * 8192 - 1024;
  unsigned kvo[2][2];
#pragma unroll
  for (int par = 0; par < 2; ++par) {
    kvo[par][0] = (unsigned)((lane >> 3) * 256 + (((lane & 7) ^ (wr ? 4 * par + (lane >> 4) : (lane >> 4) << 1)) << 4) + 1024);
    kvo[par][1] = kvo[par][0] + 128 - 1024;
  }
#pragma unroll 1
  for (int c = 0; c < 3; ++c) {
    {
      float tv[8], ev[8];
#pragma unroll
      for (int e = 0; e < 4; ++e) { tv[2 * e] = bf2f(hq[0][e] & 0xffffu); tv[2 * e + 1] = U2F(hq[0][e] & 0xffff0000u); }
      SCHED();
#pragma unroll
      for (int e = 0; e < 8; ++e) ev[e] = __builtin_amdgcn_exp2f(tv[e]);
      SCHED();
#pragma unroll
      for (int e = 0; e < 8; ++e) ev[e] = ev[e] * -1.4426950408889634f - 1.4426950408889634f;
      SCHED();
#pragma unroll
      for (int e = 0; e < 8; ++e) ev[e] = __builtin_amdgcn_rcpf(ev[e]);
      SCHED();
      ST128((u32x4){pk2(ev[0] * tv[0], ev[1] * tv[1]), pk2(ev[2] * tv[2], ev[3] * tv[3]), pk2(ev[4] * tv[4], ev[5] * tv[5]), pk2(ev[6] * tv[6], ev[7] * tv[7])}, hqo, c == 2 ? 16384 : c * 2048);
    }
    hq[0] = hq[1]; hq[1] = hq[2];
    SCHED();
    if (__builtin_expect(c + wr < 3, 1)) {
      const int slot = wr ? (c == 0 ? SLOT_B : SLOT_C) : (c == 0 ? SLOT_D : (c == 1 ? SLOT_E : SLOT_A));
#pragma unroll
      for (int hf = 0; hf < 2; ++hf)
        asm volatile("s_mov_b32 m0, %5\n\ts_nop 0\n\t"
                     "global_load_lds_dwordx4 %0, %4\n\tglobal_load_lds_dwordx4 %1, %4 offset:1024\n\t"
                     "global_load_lds_dwordx4 %2, %4 offset:2048\n\tglobal_load_lds_dwordx4 %3, %4 offset:3072"
                     ::"v"(kvo[0][0]), "v"(kvo[0][1]), "v"(kvo[1][0]), "v"(kvo[1][1]), "s"(kvsrc + c * 32768 + hf * 4096), "s"(lds0 + slot + wc * 8192 + hf * 4096) : "memory");
    }
    TOUCH(c + 1);
    SOFTMAX();
    { const int dk = (c == 0) ? SLOT_A - SLOT_E : ((c == 1) ? SLOT_B - SLOT_A : SLOT_C - SLOT_B); kb[0] += dk; kb[1] += dk; asm volatile("" : "+v"(kb[0]), "+v"(kb[1])); }
    PREF_V();
    BAR(); SCHED();
    __builtin_amdgcn_s_setprio(1);
    PV_SEG(true, );
    QK_SEG(LA, WAIT_V(1); if (c == 2) WAIT_V(0));
    { const int dv = (c == 1) ? SLOT_E - SLOT_D : 32768;
#pragma unroll
      for (int f = 0; f < 4; ++f) vb[f] += dv; }
  }
  if (wid == 4 && lane == 0) __builtin_amdgcn_raw_buffer_store_b32(tag, wrs, 0, WS_HFLAG + (rb * 32 + hd) * 128, 16);
  SOFTMAX();
  {
    PREF_V();
    asm volatile("s_waitcnt vmcnt(0)" :: "v"(pft) : "memory");
    SCHED();
    if (wr == 0) BAR();
    SCHED();
    __builtin_amdgcn_s_setprio(1);
    const unsigned oo = (unsigned)(WS_O + 2 * hd * 262144 + (rb * 16 + (wr ^ sb) * 8 + wc * 2) * 2048 + lane * 16);
    float inv[2] = {0.f, 0.f};
    u32x4 ow;
#define O_PIECE(j_) do { const int dp = (j_) >> 3, n_ = ((j_) >> 2) & 1, q_ = (j_) & 3; \
      ow[q_] = pk2(o[2 * dp + (q_ >> 1)][n_][2 * (q_ & 1)] * inv[n_], o[2 * dp + (q_ >> 1)][n_][2 * (q_ & 1) + 1] * inv[n_]); asm volatile("" : "+v"(ow)); \
      if (q_ == 3) ST128(ow, oo + n_ * 2048 + (dp & 1) * 1024, (dp >> 1) * 262144); } while (0)
    PV_SEG(false, if (i == 4) { inv[0] = __builtin_amdgcn_rcpf(lacc[0][0]); inv[1] = __builtin_amdgcn_rcpf(lacc[1][0]); } if (i >= 16 && (i & 1)) O_PIECE(i >= 16 ? (i - 16) >> 1 : 0));
#pragma unroll
    for (int j = 24; j < 32; ++j) O_PIECE(j);
    __builtin_amdgcn_s_setprio(0);
  }
  int lane2 = __builtin_amdgcn_mbcnt_hi(~0u, __builtin_amdgcn_mbcnt_lo(~0u, 0u));
  asm volatile("" : "+v"(lane2));
#define lane lane2
  const int isG = (idx >> 1) & 1, kh = xcd & 1, tm = 4 * (xcd >> 1) + ((idx >> 1) & 2) + (idx & 1), tn = 2 * (idx >> 3) + kh;
#define KDS 1
#define KDG 1
  const int kd = isG ? KDG : KDS, sd2 = kh ? -kd : kd;
  const int npw = (isG ? 48 : 56) + 2 * sd2;
  const int k2c = ((npw + 19) % 6 < 4) ? K2_C : 0;
#define O_FLAG() do { if (wid == 0 && lane == 0) __builtin_amdgcn_raw_buffer_store_b32(tag, wrs, 0, WS_OFLAG + (rb * 32 + hd) * OFS, 16); } while (0)
#define POLL32(off_, slp) do { for (unsigned spins = 0; spins < 2000000u; ++spins) { \
      if (__builtin_amdgcn_ballot_w64(__builtin_amdgcn_raw_buffer_load_b32(wrs, (off_), 0, 16) != tag) == 0ull) break; \
      __builtin_amdgcn_s_sleep(slp); asm volatile("" ::: "memory"); } \
    asm volatile("" ::: "memory"); } while (0)
  const int c8 = lane & 15, r0 = wc * 4 + (lane >> 4);
  const unsigned goff = (unsigned)((tm * 128 + r0) * 2048 + tn * 256 + c8 * 16);
  const lptr cb = lds + k2c + r0 * 512;
#define G_FIN() do { f32x4 a0[4], a1[4]; \
      _Pragma("unroll") for (int i = 0; i < 4; ++i) { a0[i] = *(const LAS f32x4*)(cb + (wr * 4 + i) * 8192 + (((2 * c8) ^ (r0 & 7)) << 4)); a1[i] = *(const LAS f32x4*)(cb + (wr * 4 + i) * 8192 + (((2 * c8 + 1) ^ (r0 & 7)) << 4)); } \
      _Pragma("unroll") for (int i = 0; i < 4; ++i) \
        ST128((u32x4){pk2(silu(a0[i][0]), silu(a0[i][1])), pk2(silu(a0[i][2]), silu(a0[i][3])), pk2(silu(a1[i][0]), silu(a1[i][1])), pk2(silu(a1[i][2]), silu(a1[i][3]))}, goff + i * 32768, WS_G + wr * 131072); \
      WAIT_V(0); BAR(); } while (0)
  const unsigned f1b = isG ? (unsigned)(WS_HFLAG + ((tm >> 1) * 32 + 16 + (lane & 15)) * 128) : (unsigned)(WS_OFLAG + ((tm >> 1) * 32 + 16 + (lane & 15)) * OFS);
  const unsigned f1 = isG ? f1b : (unsigned)(WS_EFLAG + ((tm >> 1) * 16 + (tm & 1) * 8 + kh * 4 + ((lane >> 2) & 3)) * 64 + (lane & 3) * 4);
  if (wr == 0) {
    const char* ab = ws + (isG ? WS_H : WS_O) + tm * 16384 + wc * 4096;
    const char* wbp = (isG ? w2 : wo) + (tn >> 1) * 512 - 3072 + wc * 16384;
    const unsigned voffA0 = lane * 16, voffA1 = lane * 16 + 1024;
    unsigned voffW[4];
    {
      const int r = lane >> 3, c = lane & 7;
      const unsigned lp = (unsigned)(r * 2048 + ((c ^ (((r >> 1) & 3) << 1)) << 4) + 3072);
      voffW[0] = lp + kh * 256; voffW[1] = voffW[0] + 128 - 1024; voffW[2] = lp + (kh ^ 1) * 256 - 2048; voffW[3] = voffW[2] + 128 - 1024;
    }
    const char *ap, *wp;
    unsigned dst = lds0 + 4 * K2_STG;
#define K2_RUN(k_) do { ap = ab + (size_t)(k_) * 262144; wp = wbp + (size_t)(k_) * 131072; } while (0)
#define K2_ISSUE(WIDE, ks) do { \
      asm volatile("s_mov_b32 m0, %3\n\ts_nop 0\n\tglobal_load_lds_dwordx4 %0, %2 sc1\n\tglobal_load_lds_dwordx4 %1, %2 offset:1024 sc1" \
                   ::"v"(voffA0), "v"(voffA1), "s"(ap + (ks) * 1024), "s"(dst + wc * 2048) : "memory"); \
      if (WIDE) GLDS4(voffW[0], voffW[1], voffW[2], voffW[3], wp + (ks) * 65536, dst + 8192 + wc * 4096); \
      else GLDS2(voffW[0], voffW[1], wp + (ks) * 65536, dst + 8192 + wc * 4096); \
      dst = (dst == lds0 + 5 * K2_STG) ? lds0 : dst + K2_STG; \
      if (ks) { ap += 262144; wp += 131072; } } while (0)
#define K2_STEP(WIDE, ks, EVENT) do { WAIT_V((WIDE) ? 18 : 12); BAR(); EVENT; K2_ISSUE(WIDE, ks); } while (0)
#define K2_TILES(WIDE, n_) do { _Pragma("unroll 1") for (int i_ = 0; i_ < (n_); ++i_) { K2_STEP(WIDE, 0, ); K2_STEP(WIDE, 1, ); } } while (0)
#define LOOK(f_) asm volatile("buffer_load_dword %0, %1, %2, 0 offen sc1" : "+v"(pol) : "v"(f_), "s"(wrs) : "memory")
    const unsigned f2 = f1b - 16 * (isG ? 128 : OFS);
    const unsigned f3 = (unsigned)(WS_PFLAG + (bid ^ 1) * 128);
    u32x4 xh[8];
#define XH_LD(i) asm volatile("global_load_dwordx4 %0, %1, %2" : "=v"(xh[i]) : "v"(goff), "s"(xn + (i) * 32768) : "memory")
    WAIT_V(0);
    if (isG && lane == 0) __builtin_amdgcn_raw_buffer_store_b32(tag, wrs, 0, WS_EFLAG + (rb * 16 + hd - 16) * 64 + wc * 4, 16);
    POLL32(f1, 4);
    K2_RUN(isG ? 32 + 12 * kh : 32 + 16 * (tm & 1) + 8 * kh);
    K2_ISSUE(1, 0); K2_ISSUE(1, 1);
    WAIT_V(6); BAR();
    K2_ISSUE(1, 0); K2_ISSUE(1, 1); K2_ISSUE(1, 0);
    unsigned pol = 0;
#define CHECK(f_) do { asm volatile("" : "+v"(pol)); if (__builtin_amdgcn_ballot_w64(pol != tag) != 0ull) POLL32(f_, 4); } while (0)
    K2_STEP(1, 1, );
    K2_TILES(1, 2);
    O_FLAG();
    K2_TILES(1, 1);
    LOOK(f1b);
    K2_TILES(1, 2);
    if (!isG) K2_RUN(48 - 16 * (tm & 1) + 6 * kh);
    K2_STEP(1, 0, CHECK(f1b)); K2_STEP(1, 1, );
    K2_TILES(1, isG ? 1 : 3);
    LOOK(f2);
    K2_TILES(1, 2);
    K2_RUN(((isG ? 12 : 14) + kd) * kh);
    K2_STEP(1, 0, CHECK(f2));
    K2_STEP(1, 1, );
#pragma unroll
    for (int i = 0; i < 8; ++i) { K2_STEP(1, 0, XH_LD(i)); K2_STEP(1, 1, ); }
    K2_TILES(1, (isG ? 3 : 5) + sd2);
    asm volatile("" : "+v"(xh[0]), "+v"(xh[1]), "+v"(xh[2]), "+v"(xh[3]), "+v"(xh[4]), "+v"(xh[5]), "+v"(xh[6]), "+v"(xh[7]));
    K2_RUN(isG ? 24 : 60 - 16 * (tm & 1));
    K2_TILES(0, 4);
    K2_RUN(28);
    K2_STEP(0, 0, );
    K2_STEP(0, 1, if (wid == 0 && lane == 0) __builtin_amdgcn_raw_buffer_store_b32(tag, wrs, 0, WS_PFLAG + bid * 128, 16));
    K2_STEP(0, 0, ); K2_STEP(0, 1, );
    const unsigned f4 = (lane & 1) ? (unsigned)(WS_GFLAG + (tm * 8 + tn) * 128) : f3;
    K2_STEP(0, 0, LOOK(f4)); K2_STEP(0, 1, ); K2_STEP(0, 0, );
    WAIT_V(12);
    asm volatile("" : "+v"(pol));
    const unsigned long long miss = __builtin_amdgcn_ballot_w64(pol != tag);
    if (wid == 0 && (miss & 1ull)) POLL32(f3, 2);
    BAR();
    K2_ISSUE(0, 1);
    WAIT_V(12); BAR();
    WAIT_V(8); BAR();
    if (isG) {
      WAIT_V(4); BAR();
      WAIT_V(0); BAR();
      BAR();
      BAR();
    } else {
      u32x4 gv[8];
      const __amdgpu_buffer_rsrc_t ors = __builtin_amdgcn_make_buffer_rsrc(out, 0, 2048 * 2048, 0x00020000);
#define LD8(dst_, rs_, so_, pol_) asm volatile("buffer_load_dwordx4 %0, %8, %10, %11 offen" pol_ "\n\tbuffer_load_dwordx4 %1, %9, %10, %11 offen" pol_ "\n\t" \
        "buffer_load_dwordx4 %2, %8, %10, %12 offen" pol_ "\n\tbuffer_load_dwordx4 %3, %9, %10, %12 offen" pol_ "\n\t" \
        "buffer_load_dwordx4 %4, %8, %10, %13 offen" pol_ "\n\tbuffer_load_dwordx4 %5, %9, %10, %13 offen" pol_ "\n\t" \
        "buffer_load_dwordx4 %6, %8, %10, %14 offen" pol_ "\n\tbuffer_load_dwordx4 %7, %9, %10, %14 offen" pol_ \
        : "=&v"(dst_[0]), "=&v"(dst_[1]), "=&v"(dst_[2]), "=&v"(dst_[3]), "=&v"(dst_[4]), "=&v"(dst_[5]), "=&v"(dst_[6]), "=&v"(dst_[7]) \
        : "v"(goff), "v"(goff + 32768), "s"(rs_), "s"((so_)), "s"((so_) + 65536), "s"((so_) + 131072), "s"((so_) + 196608) : "memory")
      if (miss & 2ull) {
        WAIT_V(4); BAR(); WAIT_V(0); BAR(); BAR();
        POLL32((unsigned)(WS_GFLAG + (tm * 8 + tn) * 128), 1);
      }
      LD8(gv, wrs, WS_G, " sc1");
      if (!(miss & 2ull)) { WAIT_V(12); BAR(); WAIT_V(8); BAR(); BAR(); }
      float xf[8][8];
#pragma unroll
      for (int i = 0; i < 8; ++i) {
#pragma unroll
        for (int e = 0; e < 4; ++e) { xf[i][2 * e] = bf2f(xh[i][e] & 0xffffu); xf[i][2 * e + 1] = U2F(xh[i][e] & 0xffff0000u); }
        asm volatile("" : "+v"(xf[i][0]), "+v"(xf[i][1]), "+v"(xf[i][2]), "+v"(xf[i][3]), "+v"(xf[i][4]), "+v"(xf[i][5]), "+v"(xf[i][6]), "+v"(xf[i][7]));
      }
      asm volatile("s_waitcnt vmcnt(0)" : "+v"(gv[0]), "+v"(gv[1]), "+v"(gv[2]), "+v"(gv[3]), "+v"(gv[4]), "+v"(gv[5]), "+v"(gv[6]), "+v"(gv[7]) :: "memory");
      float gf2[8][8];
#pragma unroll
      for (int i = 0; i < 8; ++i) {
#pragma unroll
        for (int e = 0; e < 4; ++e) { gf2[i][2 * e] = bf2f(gv[i][e] & 0xffffu); gf2[i][2 * e + 1] = U2F(gv[i][e] & 0xffff0000u); }
        asm volatile("" : "+v"(gf2[i][0]), "+v"(gf2[i][1]), "+v"(gf2[i][2]), "+v"(gf2[i][3]), "+v"(gf2[i][4]), "+v"(gf2[i][5]), "+v"(gf2[i][6]), "+v"(gf2[i][7]));
      }
      BAR();
      f32x4 a0[8], a1[8];
#pragma unroll
      for (int i = 0; i < 8; ++i) { a0[i] = *(const LAS f32x4*)(cb + i * 8192 + (((2 * c8) ^ (r0 & 7)) << 4)); a1[i] = *(const LAS f32x4*)(cb + i * 8192 + (((2 * c8 + 1) ^ (r0 & 7)) << 4)); }
      SCHED();
#pragma unroll
      for (int i = 0; i < 8; ++i) {
        const u32x4 w = {pk2(xf[i][0] + a0[i][0] * gf2[i][0], xf[i][1] + a0[i][1] * gf2[i][1]), pk2(xf[i][2] + a0[i][2] * gf2[i][2], xf[i][3] + a0[i][3] * gf2[i][3]),
                         pk2(xf[i][4] + a1[i][0] * gf2[i][4], xf[i][5] + a1[i][1] * gf2[i][5]), pk2(xf[i][6] + a1[i][2] * gf2[i][6], xf[i][7] + a1[i][3] * gf2[i][7])};
        __builtin_amdgcn_raw_buffer_store_b128(w, ors, goff + i * 32768, 0, 16);
      }
    }
  } else {
    const int wm = wc >> 1, wn = wc & 1, g = lane >> 4, t = lane & 15, q4 = (lane >> 2) & 3, p4 = lane & 3;
    lptr w2b[4];
#pragma unroll
    for (int ft = 0; ft < 4; ++ft) w2b[ft] = lds + 4 * K2_STG + 8192 + wn * 1024 + (g >> 1) * 4096 + (4 * (g & 1) + q4) * 128 + 32 * (ft ^ (2 * (g & 1) + (q4 >> 1))) + 8 * p4;
    lptr ar = lds + 4 * K2_STG + wm * 4096 + lane * 16;
    f32x4 ac2[8][4];
#pragma unroll
    for (int m = 0; m < 8; ++m)
#pragma unroll
      for (int n = 0; n < 4; ++n) ac2[m][n] = (f32x4){0.f, 0.f, 0.f, 0.f};
    bf16x8 Af[2][4];
    bf16x4 Wr[4][2];
    int slot = 4, dlt = 0;
#define K2_RW(f, hf) Wr[(f) & 3][hf] = __builtin_amdgcn_ds_read_tr16_b64_v4bf16((LAS bf16x4*)(w2b[(f) & 3] + (((f) >> 2) & 1) * 2048 + (hf) * 8192))
#define K2_W(m) __builtin_shufflevector(Wr[(m) & 3][0], Wr[(m) & 3][1], 0, 1, 2, 3, 4, 5, 6, 7)
#define K2_NEXT() do { dlt = (slot == 5) ? -5 * K2_STG : K2_STG; slot = (slot == 5) ? 0 : slot + 1; } while (0)
#define K2_ADV(x) do { x += dlt; asm volatile("" : "+v"(x)); } while (0)
#define K2_WIDE(st) do { _Pragma("unroll") for (int i = 0; i < 32; ++i) { const int m = i >> 2, q = i & 3; \
        const int n_ = (m & 1) ? 3 - q : q; ac2[m][n_] = MFMA16(K2_W(m), Af[st][n_], ac2[m][n_]); SCHED(); \
        if (q < 2) K2_RW((m + 3) & 7, q); \
        if (q == 2 && m >= 3 && m < 7) Af[(st) ^ 1][m - 3] = ldb128(ar + (m - 3) * 1024); \
        if (i == 2) K2_ADV(ar); \
        if (i == 6) K2_ADV(w2b[0]); \
        if (i == 10) K2_ADV(w2b[1]); \
        if (i == 11) BAR(); \
        if (i == 19) K2_ADV(w2b[2]); \
        if (i == 23) K2_ADV(w2b[3]); \
        if (i == 30) K2_NEXT(); \
        SCHED(); } } while (0)
#define K2_NARROW(st) do { _Pragma("unroll") for (int i = 0; i < 16; ++i) { const int m = i >> 2, q = i & 3; \
        const int n_ = (m & 1) ? 3 - q : q; ac2[m][n_] = MFMA16(K2_W(m), Af[st][n_], ac2[m][n_]); SCHED(); \
        if (q < 2) K2_RW((m + 3) & 3, q); \
        if (q >= 2 && (m == 1 || m == 2)) Af[(st) ^ 1][2 * m + q - 4] = ldb128(ar + (2 * m + q - 4) * 1024); \
        if (i == 2) { K2_ADV(ar); K2_ADV(w2b[0]); } \
        if (i == 3) BAR(); \
        if (i == 5) K2_ADV(w2b[1]); \
        if (i == 9) K2_ADV(w2b[2]); \
        if (i == 14) K2_ADV(w2b[3]); \
        if (i == 15) K2_NEXT(); \
        SCHED(); } } while (0)
    K2_NEXT();
    BAR();
#pragma unroll
    for (int i = 0; i < 4; ++i) { if (i < 3) { K2_RW(i, 0); K2_RW(i, 1); } Af[0][i] = ldb128(ar + i * 1024); SCHED(); }
#pragma unroll 1
    for (int p = 0; p < 4; p += 2) { K2_WIDE(0); K2_WIDE(1); }
    WAIT_V(0);
#pragma unroll 1
    for (int p = 4; p < npw; p += 2) { K2_WIDE(0); K2_WIDE(1); }
    const unsigned po = (unsigned)(WS_P + wc * 8192 + lane * 16);
#pragma unroll
    for (int i = 0; i < 8; ++i) {
      const f32x4 a = ac2[4 + (i >> 1)][2 * (i & 1)], b = ac2[4 + (i >> 1)][2 * (i & 1) + 1];
      ST128((u32x4){pk2(a[0], a[1]), pk2(a[2], a[3]), pk2(b[0], b[1]), pk2(b[2], b[3])}, po + i * 1024, bid * 32768);
    }
    SCHED();
#pragma unroll 1
    for (int p = 0; p < 4; p += 2) { K2_NARROW(0); K2_NARROW(1); }
    WAIT_V(0);
#pragma unroll 1
    for (int p = 4; p < 10; p += 2) { K2_NARROW(0); K2_NARROW(1); }
    K2_NARROW(0);
    u32x4 pv[8];
#pragma unroll
    for (int i = 0; i < 8; ++i) pv[i] = __builtin_amdgcn_raw_buffer_load_b128(wrs, po + i * 1024 + (bid ^ 1) * 32768, 0, 16);
    SCHED();
    K2_NARROW(1);
#pragma unroll 1
    for (int p = 12; p < 16; p += 2) { K2_NARROW(0); K2_NARROW(1); }
    const lptr cw = lds + k2c + (wm * 64 + t) * 512;
#pragma unroll
    for (int m = 0; m < 4; ++m)
#pragma unroll
      for (int n = 0; n < 4; ++n) {
        const unsigned lo = pv[2 * m + (n >> 1)][2 * (n & 1)], hi = pv[2 * m + (n >> 1)][2 * (n & 1) + 1];
        *(LAS f32x4*)(cw + n * 8192 + (((wn * 16 + m * 4 + g) ^ (t & 7)) << 4)) = ac2[m][n] + (f32x4){bf2f(lo & 0xffffu), U2F(lo & 0xffff0000u), bf2f(hi & 0xffffu), U2F(hi & 0xffff0000u)};
      }
    WAIT_L(0); BAR();
    if (isG) G_FIN();
    __builtin_amdgcn_endpgm();
  }
  if (isG) {
      G_FIN();
      if (wid == 0 && lane == 0) __builtin_amdgcn_raw_buffer_store_b32(tag, wrs, 0, WS_GFLAG + (tm * 8 + tn) * 128, 16);
      if (bid == 16 && wid == 0) {
        for (unsigned spins = 0; spins < 2000000u; ++spins) {
          bool ok = true;
#pragma unroll
          for (int i = 0; i < 4; ++i) ok = ok && __builtin_amdgcn_raw_buffer_load_b32(wrs, (unsigned)((lane + i * 64) * 128), WS_HFLAG, 16) == tag;
          if (__builtin_amdgcn_ballot_w64(!ok) == 0ull) break;
          __builtin_amdgcn_s_sleep(4);
          asm volatile("" ::: "memory");
        }
        asm volatile("" ::: "memory");
        if (lane == 0) __builtin_amdgcn_raw_buffer_store_b32(tag, wrs, 0, 0, 16);
      }
    }
#undef lane
}

extern "C" void kernel_launch(void* const* d_in, const int* in_sizes, int n_in, void* d_out, int out_size, void* d_ws, size_t ws_size, hipStream_t stream) {
  const char* x = (const char*)d_in[0]; const char* kc = (const char*)d_in[1]; const char* vc = (const char*)d_in[2];
  const char* wq = (const char*)d_in[3]; const char* wo = (const char*)d_in[4]; const char* w1 = (const char*)d_in[6]; const char* w2 = (const char*)d_in[7];
  (void)hipFuncSetAttribute((const void*)mega_kernel, hipFuncAttributeMaxDynamicSharedMemorySize, K1_LDS);
  mega_kernel<<<256, 512, K1_LDS, stream>>>(x, kc, vc, wq, w1, wo, w2, (char*)d_ws, (char*)d_out);
}
```

```cpp
#define PROBE_A 3
#define PROBE_B 22
#define PROBE_S 3
#define PROBE_I "s_memrealtime"
#define PROBE_W 7
#include <hip/hip_runtime.h>
#include <stdint.h>

typedef unsigned short u16;
typedef __bf16 bf16x2 __attribute__((ext_vector_type(2)));
typedef __bf16 bf16x4 __attribute__((ext_vector_type(4)));
typedef __bf16 bf16x8 __attribute__((ext_vector_type(8)));
typedef float f32x2 __attribute__((ext_vector_type(2)));
typedef float f32x4 __attribute__((ext_vector_type(4)));
typedef float f32x16 __attribute__((ext_vector_type(16)));
typedef unsigned u32x2 __attribute__((ext_vector_type(2)));
typedef unsigned u32x4 __attribute__((ext_vector_type(4)));
#define LAS __attribute__((address_space(3)))
typedef LAS char* lptr;
#if defined(__HIP_DEVICE_COMPILE__) && !defined(USEPK)
#define NOPK __attribute__((target("no-packed-fp32-ops")))
#else
#define NOPK
#endif
#define DI NOPK __device__ __forceinline__
#define F2U(x) __builtin_bit_cast(unsigned, (float)(x))
#define U2F(x) __builtin_bit_cast(float, (unsigned)(x))
#define TID ((int)__builtin_amdgcn_workitem_id_x())
#define BID ((int)__builtin_amdgcn_workgroup_id_x())
#define SCHED() __builtin_amdgcn_sched_barrier(0)
#define BAR() asm volatile("s_barrier" ::: "memory")
#define WAIT_V(n) asm volatile("s_waitcnt vmcnt(%0)" :: "n"(n) : "memory")
#define WAIT_L(n) do { asm volatile("" ::: "memory"); __builtin_amdgcn_s_waitcnt(0xC07F | ((n) << 8)); asm volatile("" ::: "memory"); } while (0)
#define MFMA16(a, b, c) __builtin_amdgcn_mfma_f32_16x16x32_bf16(a, b, c, 0, 0, 0)
#define MFMA32(a, b, c) __builtin_amdgcn_mfma_f32_32x32x16_bf16(a, b, c, 0, 0, 0)

DI void glds(unsigned voff, const char* sbase, unsigned ldsaddr) {
  asm volatile("s_mov_b32 m0, %2\n\ts_nop 0\n\tglobal_load_lds_dwordx4 %0, %1" ::"v"(voff), "s"(sbase), "s"(ldsaddr) : "memory");
}
DI void glds1(unsigned voff, const char* sbase, unsigned ldsaddr) {
  asm volatile("s_mov_b32 m0, %2\n\ts_nop 0\n\tglobal_load_lds_dwordx4 %0, %1 sc1" ::"v"(voff), "s"(sbase), "s"(ldsaddr) : "memory");
}
DI unsigned pk2(float lo, float hi) { f32x2 v = {lo, hi}; return __builtin_bit_cast(unsigned, __builtin_convertvector(v, bf16x2)); }
DI float bf2f(unsigned b) { return U2F(b << 16); }
DI bf16x8 ldb128(lptr p) { return *(const LAS bf16x8*)p; }
DI bf16x8 ldtr2(lptr p, int d) {
  bf16x4 a = __builtin_amdgcn_ds_read_tr16_b64_v4bf16((LAS bf16x4*)p);
  bf16x4 b = __builtin_amdgcn_ds_read_tr16_b64_v4bf16((LAS bf16x4*)(p + d));
  return __builtin_shufflevector(a, b, 0, 1, 2, 3, 4, 5, 6, 7);
}
DI void ST128_(__amdgpu_buffer_rsrc_t r, u32x4 v, unsigned voff, int soff) { __builtin_amdgcn_raw_buffer_store_b128(v, r, voff + (unsigned)soff, 0, 16); }
DI float silu(float v) { return v * __builtin_amdgcn_rcpf(1.0f + __builtin_amdgcn_exp2f(v * -1.4426950408889634f)); }

#define STAMP_DECL unsigned long long pa_ = 0, pb_ = 0; const unsigned long long p0_ = __builtin_amdgcn_s_memrealtime();
#define LSTAMP(i)
#define STAMP(i) do { if ((i) == PROBE_A) asm volatile(PROBE_I " %0\n\ts_waitcnt lgkmcnt(0)" : "=s"(pa_) :: "memory"); if ((i) == PROBE_B) asm volatile(PROBE_I " %0\n\ts_waitcnt lgkmcnt(0)" : "=s"(pb_) :: "memory"); } while (0)
#define STAMP_OUT(buf, n) do { if (bid == PROBE_W && wid == 4) { const unsigned long long d_ = 10000 + (((pb_ - pa_) * 16) >> PROBE_S); \
    while (__builtin_amdgcn_s_memrealtime() - p0_ < d_ && d_ < 100000ull) __builtin_amdgcn_s_sleep(1); } } while (0)
template <int CTRL> DI float dpp_add(float v) { return v + U2F(__builtin_amdgcn_update_dpp(0u, F2U(v), CTRL, 0xf, 0xf, true)); }
DI float half_sum(float v) {
  v = dpp_add<0xB1>(v); v = dpp_add<0x4E>(v); v = dpp_add<0x141>(v); v = dpp_add<0x140>(v);
  auto rr = __builtin_amdgcn_permlane16_swap(F2U(v), F2U(v), false, false);
  return U2F(rr[0]) + U2F(rr[1]);
}
DI float col_max(float v) {
  auto a = __builtin_amdgcn_permlane16_swap(F2U(v), F2U(v), false, false);
  v = __builtin_fmaxf(U2F(a[0]), U2F(a[1]));
  auto b = __builtin_amdgcn_permlane32_swap(F2U(v), F2U(v), false, false);
  return __builtin_fmaxf(U2F(b[0]), U2F(b[1]));
}
typedef __attribute__((address_space(1))) unsigned long long gu64;

#define SX(b, h) (98304 + (b) * 32768 + (h) * 16384)
constexpr int SLOT_B = 0, SLOT_E = 32768, SLOT_A = 65536, SLOT_C = 98304, SLOT_D = 131072, K1_LDS = 163840;

#define XSWAP 0
constexpr int WS_HFLAG = 4096;
constexpr int WS_GFLAG = 65536;
constexpr int WS_OFLAG = 131072;
#define OFS 16
constexpr int WS_EFLAG = 163840;
constexpr int WS_STATS = 1 << 20;
constexpr int WS_PFLAG = 196608;
constexpr int WS_G = 5 << 20, WS_O = 9 << 20, WS_H = 25 << 20, WS_P = 39 << 20, WS_END = 47 << 20;
constexpr int K2_STG = 24576, K2_C = 98304;

NOPK __global__ __launch_bounds__(512, 2) void mega_kernel(const char* __restrict__ xn, const char* __restrict__ kc, const char* __restrict__ vc,
                                                           const char* __restrict__ wq, const char* __restrict__ w1, const char* __restrict__ wo, const char* __restrict__ w2,
                                                           char* ws, char* __restrict__ out) {
  STAMP_DECL
  STAMP(0);
  extern __shared__ __attribute__((aligned(16))) char shm[];
  const lptr lds = (lptr)shm;
  const unsigned lds0 = (unsigned)(uintptr_t)lds;
  const int tid = TID, wid = __builtin_amdgcn_readfirstlane(tid >> 6), lane = tid & 63, wr = wid >> 2, wc = wid & 3;
  const int bid = BID ^ XSWAP, xcd = bid & 7, idx = bid >> 3, hd = (idx & 2) * 8 + 2 * xcd + (idx & 1), rb = idx >> 2;
  const int g = lane >> 4, t = lane & 15, q4 = (lane >> 2) & 3, p4 = lane & 3;

  const bool light = (idx & 2) != 0;
  unsigned voffX[4], voffQ[2], voffH[2];
  {
    const int r = lane >> 3, c = lane & 7;
#pragma unroll
    for (int i = 0; i < 4; ++i) voffX[i] = (unsigned)((8 * i + r) * 2048 + ((c ^ (4 * (i & 1) + (r >> 1))) << 4) - i * 1024);
    const int k = 32 * (wid >> 2) + 16 * (wid & 1) + 4 * ((wid >> 1) & 1) + 8 * (r >> 2) + (r & 3);
    const int cw = c ^ (((r >> 1) & 3) << 1);
    voffQ[0] = (unsigned)(k * 8192 + cw * 16 + 1024); voffQ[1] = voffQ[0] + 128 - 1024;
    voffH[0] = (unsigned)(k * 7168 + 128 + (light ? (cw & 3) : cw) * 16 + 1024);
    voffH[1] = (unsigned)(k * 7168 + cw * 16);
  }
  const char* xb = xn + (size_t)rb * 256 * 2048 + wid * 65536;
  const char* qb = wq + hd * 256 - 1024;
  const char* hb = w1 + (light ? 4096 + 192 * (hd - 16) : 256 * hd) - 1024;
  const bool full = wr == 1 || !light;
  const int sb = hd >= 24;
#define GLDS2(v0_, v1_, sb_, ld_) asm volatile("s_mov_b32 m0, %3\n\ts_nop 0\n\tglobal_load_lds_dwordx4 %0, %2\n\tglobal_load_lds_dwordx4 %1, %2 offset:1024" \
    ::"v"(v0_), "v"(v1_), "s"(sb_), "s"(ld_) : "memory")
#define GLDS4(v0_, v1_, v2_, v3_, sb_, ld_) asm volatile("s_mov_b32 m0, %5\n\ts_nop 0\n\tglobal_load_lds_dwordx4 %0, %4\n\tglobal_load_lds_dwordx4 %1, %4 offset:1024\n\t" \
    "global_load_lds_dwordx4 %2, %4 offset:2048\n\tglobal_load_lds_dwordx4 %3, %4 offset:3072" ::"v"(v0_), "v"(v1_), "v"(v2_), "v"(v3_), "s"(sb_), "s"(ld_) : "memory")
#define ST_X(b, kt) GLDS4(voffX[0], voffX[1], voffX[2], voffX[3], xb + (kt) * 128, lds0 + SX(b, 0) + wid * 4096)
#define ST_W0(wo_, kt) GLDS2(voffQ[0], voffQ[1], qb + (size_t)(kt) * 524288, lds0 + (wo_) + wid * 2048)
#define ST_W1(wo_, kt) GLDS2(voffH[0], voffH[1], hb + (size_t)(kt) * 458752, lds0 + (wo_) + 16384 + wid * 2048)

  lptr wb[4], xr[2];
#pragma unroll
  for (int m = 0; m < 4; ++m) wb[m] = lds + wr * 1024 + (g >> 1) * 2048 + (4 * (g & 1) + q4) * 128 + 32 * ((m ^ wc) ^ (2 * (g & 1) + (q4 >> 1))) + 8 * p4;
#pragma unroll
  for (int ks = 0; ks < 2; ++ks) xr[ks] = lds + 98304 + (wc * 32 + t) * 128 + 16 * ((4 * ks + g) ^ (t >> 1));
#define LDW(dst, h) do { _Pragma("unroll") for (int m = 0; m < 4; ++m) _Pragma("unroll") for (int k = 0; k < 2; ++k) dst[m][k] = ldtr2(wb[m] + (h) * 16384 + k * 8192, 4096); } while (0)
#define LDX(dst, b, h) do { _Pragma("unroll") for (int n = 0; n < 2; ++n) _Pragma("unroll") for (int k = 0; k < 2; ++k) dst[n][k] = ldb128(xr[k] + (SX(b, h) - 98304) + n * 2048); } while (0)

  f32x4 acc[2][2][4][2];
#pragma unroll
  for (int a = 0; a < 2; ++a)
#pragma unroll
    for (int b = 0; b < 2; ++b)
#pragma unroll
      for (int m = 0; m < 4; ++m)
#pragma unroll
        for (int n = 0; n < 2; ++n) acc[a][b][m][n] = (f32x4){0.f, 0.f, 0.f, 0.f};
  bf16x8 At[4][2], B0[2][2], B1[2][2];

#define MM1(ai, m, k, j) do { if ((j) < 2) acc[ai][0][m][(j) & 1] = MFMA16(At[m][k], B0[(j) & 1][k], acc[ai][0][m][(j) & 1]); else acc[ai][1][m][(j) & 1] = MFMA16(At[m][k], B1[(j) & 1][k], acc[ai][1][m][(j) & 1]); SCHED(); } while (0)
#define MM8(ai, m) do { _Pragma("unroll") for (int q = 0; q < 4; ++q) { MM1(ai, m, q & 1, ((m) & 1) ? 3 - q : q); MM1(ai, m, (q & 1) ^ 1, ((m) & 1) ? 3 - q : q); } } while (0)
#define MMA2(ai, CLOSE) do { __builtin_amdgcn_s_setprio(1); \
    cs[ai] = MFMA16(At[0][1], ones, cs[ai]); SCHED(); cs[ai] = MFMA16(At[0][0], ones, cs[ai]); SCHED(); MM8(ai, 0); asm volatile("" : "+v"(cs[ai])); MM8(ai, 1); \
    if ((ai) == 0 || full) { MM8(ai, 2); MM8(ai, 3); } \
    SCHED(); CLOSE; __builtin_amdgcn_s_setprio(0); SCHED(); } while (0)
  char* const stats = ws + WS_STATS;
  const __amdgpu_buffer_rsrc_t wrs = __builtin_amdgcn_make_buffer_rsrc(ws, 0, WS_END, 0x00020000);
  f32x4 cs[2] = {{0.f, 0.f, 0.f, 0.f}, {0.f, 0.f, 0.f, 0.f}};
  bf16x8 ones = __builtin_bit_cast(bf16x8, (u32x4){0x3f803f80u, 0x3f803f80u, 0x3f803f80u, 0x3f803f80u});
#define PIN_SETUP() do { asm volatile("" : "+v"(ones), "+v"(cs[0]), "+v"(cs[1]), "+v"(wb[0]), "+v"(wb[1]), "+v"(wb[2]), "+v"(wb[3]), "+v"(xr[0]), "+v"(xr[1])); \
    _Pragma("unroll") for (int a = 0; a < 2; ++a) _Pragma("unroll") for (int b = 0; b < 2; ++b) \
      asm volatile("" : "+v"(acc[a][b][0][0]), "+v"(acc[a][b][0][1]), "+v"(acc[a][b][1][0]), "+v"(acc[a][b][1][1]), "+v"(acc[a][b][2][0]), "+v"(acc[a][b][2][1]), "+v"(acc[a][b][3][0]), "+v"(acc[a][b][3][1])); } while (0)
  unsigned tag;
  {
    if (wr == 0) {
      unsigned ep;
      ST_X(0, 0); ST_W0(0, 0);
      GLDS4(voffX[0], voffX[1], voffX[2], voffX[3], xb + 262144, lds0 + SX(0, 1) + wid * 4096);
      GLDS2(voffQ[0], voffQ[1], qb + 262144, lds0 + 8192 + wid * 2048);
      asm volatile("global_load_dword %0, %1, %2 sc1" : "=&v"(ep) : "v"(0), "s"(ws) : "memory");
      PIN_SETUP();
      asm volatile("s_waitcnt vmcnt(0)" : "+v"(ep) :: "memory");
      tag = __builtin_amdgcn_readfirstlane(ep) + 1u;
      BAR();
    } else {
      const int row = rb * 256 + hd * 8 + wc * 2 + (lane >> 5);
      u32x4 a[4];
      unsigned ep;
      asm volatile("global_load_dword %0, %1, %2 sc1" : "=&v"(ep) : "v"(0), "s"(ws) : "memory");
      asm volatile("global_load_dwordx4 %0, %4, %5\n\tglobal_load_dwordx4 %1, %4, %5 offset:512\n\tglobal_load_dwordx4 %2, %4, %5 offset:1024\n\tglobal_load_dwordx4 %3, %4, %5 offset:1536"
                   : "=&v"(a[0]), "=&v"(a[1]), "=&v"(a[2]), "=&v"(a[3]) : "v"(row * 2048 + (lane & 31) * 16), "s"(xn) : "memory");
      ST_W1(0, 0); GLDS2(voffH[0], voffH[1], hb - 229376, lds0 + 16384 + wc * 2048);
      PIN_SETUP();
      BAR();
      ST_W0(32768, 1); GLDS2(voffQ[0], voffQ[1], qb + 524288 - 262144, lds0 + 32768 + wc * 2048);
      ST_X(1, 1); GLDS4(voffX[0], voffX[1], voffX[2], voffX[3], xb - 262144 + 128, lds0 + SX(1, 0) + wc * 4096);
      asm volatile("s_waitcnt vmcnt(16)" : "+v"(ep), "+v"(a[0]), "+v"(a[1]), "+v"(a[2]), "+v"(a[3]) :: "memory");
      tag = __builtin_amdgcn_readfirstlane(ep) + 1u;
      float v[32];
#pragma unroll
      for (int i = 0; i < 16; ++i) { v[2 * i] = bf2f(a[i >> 2][i & 3] & 0xffffu); v[2 * i + 1] = U2F(a[i >> 2][i & 3] & 0xffff0000u); }
      float s0 = 0.f, s1 = 0.f, s2 = 0.f, s3 = 0.f;
#pragma unroll
      for (int i = 0; i < 8; ++i) { s0 += v[i]; s1 += v[8 + i]; s2 += v[16 + i]; s3 += v[24 + i]; }
      const float mean = half_sum((s0 + s1) + (s2 + s3)) * (1.0f / 1024.0f);
      s0 = s1 = s2 = s3 = 0.f;
#pragma unroll
      for (int i = 0; i < 8; ++i) { const float d0 = v[i] - mean, d1 = v[8 + i] - mean, d2 = v[16 + i] - mean, d3 = v[24 + i] - mean; s0 += d0 * d0; s1 += d1 * d1; s2 += d2 * d2; s3 += d3 * d3; }
      const float sd = __builtin_amdgcn_sqrtf(half_sum((s0 + s1) + (s2 + s3)) * (1.0f / 1024.0f) + 1e-5f);
      if ((lane & 31) < 2) __hip_atomic_store((gu64*)(stats + row * 16 + (lane & 1) * 8), ((unsigned long long)tag << 32) | F2U((lane & 1) ? sd : mean), __ATOMIC_RELAXED, __HIP_MEMORY_SCOPE_AGENT);
    }
  }
  if (wr == 1) BAR();
  SCHED();
  STAMP(1);
  const int kvg = hd >> 3;
  const char* kb_g = kc + ((size_t)kvg * 2048 + 1536) * 256;
  const char* vb_g = vc + ((size_t)kvg * 2048 + 1536) * 256;
#define VOFFK ((unsigned)((lane >> 3) * 256 + (((lane & 7) ^ (4 * (wid & 1) + (lane >> 4))) << 4)))
#define VOFFV ((unsigned)((lane >> 3) * 256 + (((lane & 7) ^ ((lane >> 4) << 1)) << 4)))
#define ST_KV8(voff, gsrc, slot) do { glds(voff, (gsrc) + wid * 2048, lds0 + (slot) + wid * 2048); glds(voff, (gsrc) + wid * 2048 + 128, lds0 + (slot) + wid * 2048 + 1024); } while (0)
  int wprv = 65536, wnxt = 32768;
#define W_ADV() do { const int d_ = (wnxt == 0) ? -65536 : 32768, cur_ = wnxt - d_; wnxt = wprv; wprv = cur_; \
    _Pragma("unroll") for (int f = 0; f < 4; ++f) wb[f] += d_; \
    asm volatile("" : "+v"(wb[0]), "+v"(wb[1]), "+v"(wb[2]), "+v"(wb[3])); } while (0)
#define PH_A(b, STAGE) do { LDX(B0, b, 0); LDX(B1, b, 1); SCHED(); LDW(At, 0); STAGE; WAIT_V(10); WAIT_L(12); BAR(); SCHED(); MMA2(0, WAIT_L(0); BAR()); } while (0)
#define PH_B(STAGE) do { LDW(At, 1); STAGE; W_ADV(); WAIT_V(8); BAR(); SCHED(); MMA2(1, WAIT_L(0); BAR()); } while (0)
#pragma unroll 1
  for (int kt = 0; kt < 14; kt += 2) {
    PH_A(0, ST_W1(wnxt, kt + 1); ST_W0(wprv, kt + 2));
    PH_B(ST_X(0, kt + 2));
    PH_A(1, ST_W1(wnxt, kt + 2); ST_W0(wprv, kt + 3));
    PH_B(ST_X(1, kt + 3));
  }
  STAMP(2);
  const unsigned voffK = VOFFK, voffV = VOFFV;
  PH_A(0, ST_W1(0, 15); ST_KV8(voffK, kb_g, SLOT_E));
  PH_B(ST_KV8(voffV, vb_g, SLOT_C); ST_KV8(voffV, vb_g + 16384, SLOT_C + 16384));
  const unsigned soff = (unsigned)((rb * 256 + (g >> 1) * 128 + wc * 32 + (g & 1) * 16 + t) * 16);
  u32x4 sg;
  LDX(B0, 1, 0); LDX(B1, 1, 1); SCHED(); LDW(At, 0);
  asm volatile("global_load_dwordx4 %0, %1, %2 sc1" : "=&v"(sg) : "v"(soff), "s"(stats) : "memory");
  ST_KV8(voffK, kb_g + 16384, SLOT_E + 16384); ST_KV8(voffK, kb_g + 32768, SLOT_A);
  WAIT_V(11); BAR(); SCHED(); MMA2(0, WAIT_L(0); BAR());
  LDW(At, 1);
  ST_KV8(voffK, kb_g + 49152, SLOT_A + 16384);
  BAR(); SCHED();
  const lptr csw = lds + SLOT_D + (t & 3) * 8192 + wr * 4096 + wc * 64 + g * 16;
  MMA2(1, *(LAS f32x4*)csw = cs[0]; *(LAS f32x4*)(csw + 256) = cs[1]; asm volatile("s_waitcnt vmcnt(4)" : "+v"(sg) :: "memory"); WAIT_L(0); BAR());
  f32x4 cv[2][4];
#pragma unroll
  for (int a = 0; a < 2; ++a)
#pragma unroll
    for (int m = 0; m < 4; ++m) cv[a][m] = *(const LAS f32x4*)(lds + SLOT_D + wc * 8192 + wr * 4096 + a * 256 + (m ^ wc) * 64 + g * 16);
  if (__builtin_expect(__builtin_amdgcn_ballot_w64(sg[1] != tag || sg[3] != tag) != 0ull, 0)) {
    for (unsigned spins = 0; spins < 4000000u; ++spins) {
      __builtin_amdgcn_s_sleep(2);
      asm volatile("" ::: "memory");
      sg = __builtin_amdgcn_raw_buffer_load_b128(wrs, soff, WS_STATS, 16);
      if (__builtin_amdgcn_ballot_w64(sg[1] != tag || sg[3] != tag) == 0ull) break;
    }
  }
  float mu[2][2], rs[2][2], sd[2][2];
  {
    auto m16 = __builtin_amdgcn_permlane16_swap(sg[0], sg[0], false, false);
    auto s16 = __builtin_amdgcn_permlane16_swap(sg[2], sg[2], false, false);
#pragma unroll
    for (int n = 0; n < 2; ++n) {
      auto m32 = __builtin_amdgcn_permlane32_swap(m16[n], m16[n], false, false);
      auto s32 = __builtin_amdgcn_permlane32_swap(s16[n], s16[n], false, false);
#pragma unroll
      for (int bj = 0; bj < 2; ++bj) { mu[bj][n] = -U2F(m32[bj]); sd[bj][n] = U2F(s32[bj]); rs[bj][n] = __builtin_amdgcn_rcpf(sd[bj][n]) * 1.4426950408889634f; }
    }
  }
  WAIT_L(0);
  STAMP(3);
  {
    const lptr qw = lds + wc * 8192 + wr * 4096 + ((g >> 1) * 16 + t) * 16 + (g & 1) * 8;
#pragma unroll
    for (int bj = 0; bj < 2; ++bj) {
      lptr qwb = qw + ((bj ^ sb) ? SLOT_D : SLOT_B);
      asm volatile("" : "+v"(qwb));
#pragma unroll
      for (int m = 0; m < 4; ++m)
#pragma unroll
        for (int n = 0; n < 2; ++n) {
          const f32x4 v = (acc[0][bj][m][n] + cv[0][m] * mu[bj][n]) * (rs[bj][n] * 0.08838834764831845f);
          u32x2 w = {pk2(v[0], v[1]), pk2(v[2], v[3])};
          *(LAS u32x2*)(qwb + ((m ^ wc) >> 1) * 2048 + ((m ^ wc) & 1) * 512 + n * 1024) = w;
        }
    }
  }
  WAIT_L(0); BAR(); SCHED();
  STAMP(4);
  unsigned pft = 0;
  const int sl = ((idx >> 2) * 16 + (idx & 1) * 8 + xcd) * 8 + wid;
  const int tstr = light ? 896 : 1024;
  const char* tbase = (light ? w2 : wo) + (sl < tstr ? sl : tstr - 1) * 2048;
#define TOUCH(q_) asm volatile("global_load_dword %0, %1, %2" : "+v"(pft) : "v"((unsigned)((__builtin_amdgcn_mbcnt_lo(~0u, 0u) & 15) * 128)), "s"(tbase + (((q_) + 2) & 3) * tstr * 2048) : "memory")
  TOUCH(0);
  bf16x8 qf[8];
  const lptr qr = lds + (wr ? SLOT_D : SLOT_B) + wc * 8192 + lane * 16;
  if (wr == 1) {
#pragma unroll
    for (int s = 0; s < 8; ++s) qf[s] = ldb128(qr + s * 1024);
  }
#define ST128(...) ST128_(wrs, __VA_ARGS__)
  u32x4 hq[3];
  unsigned hqo;
  {
    u32x2 hw[2][2][2];
#define SILU16(m, ACT) do { \
      const f32x4 a0 = acc[1][0][m][0] + cv[1][m] * mu[0][0], a1 = acc[1][0][m][1] + cv[1][m] * mu[0][1], a2 = acc[1][1][m][0] + cv[1][m] * mu[1][0], a3 = acc[1][1][m][1] + cv[1][m] * mu[1][1]; \
      f32x4 e0 = a0 * -rs[0][0], e1 = a1 * -rs[0][1], e2 = a2 * -rs[1][0], e3 = a3 * -rs[1][1]; SCHED(); \
      if (ACT) { \
      _Pragma("unroll") for (int i = 0; i < 4; ++i) { e0[i] = __builtin_amdgcn_exp2f(e0[i]); e1[i] = __builtin_amdgcn_exp2f(e1[i]); e2[i] = __builtin_amdgcn_exp2f(e2[i]); e3[i] = __builtin_amdgcn_exp2f(e3[i]); } SCHED(); \
      e0 = e0 * sd[0][0] + sd[0][0]; e1 = e1 * sd[0][1] + sd[0][1]; e2 = e2 * sd[1][0] + sd[1][0]; e3 = e3 * sd[1][1] + sd[1][1]; SCHED(); \
      _Pragma("unroll") for (int i = 0; i < 4; ++i) { e0[i] = __builtin_amdgcn_rcpf(e0[i]); e1[i] = __builtin_amdgcn_rcpf(e1[i]); e2[i] = __builtin_amdgcn_rcpf(e2[i]); e3[i] = __builtin_amdgcn_rcpf(e3[i]); } SCHED(); \
      e0 *= a0; e1 *= a1; e2 *= a2; e3 *= a3; SCHED(); } \
      else { _Pragma("unroll") for (int i = 0; i < 4; ++i) e3[i] = a3[i] * __builtin_amdgcn_rcpf(__builtin_amdgcn_exp2f(e3[i]) * sd[1][1] + sd[1][1]); } \
      hw[0][0][(m) & 1] = (u32x2){pk2(e0[0], e0[1]), pk2(e0[2], e0[3])}; hw[0][1][(m) & 1] = (u32x2){pk2(e1[0], e1[1]), pk2(e1[2], e1[3])}; \
      hw[1][0][(m) & 1] = (u32x2){pk2(e2[0], e2[1]), pk2(e2[2], e2[3])}; hw[1][1][(m) & 1] = (u32x2){pk2(e3[0], e3[1]), pk2(e3[2], e3[3])}; SCHED(); } while (0)
#define HFRAG(bj, n) ((wc & 1) ? (u32x4){hw[bj][n][1][0], hw[bj][n][1][1], hw[bj][n][0][0], hw[bj][n][0][1]} : (u32x4){hw[bj][n][0][0], hw[bj][n][0][1], hw[bj][n][1][0], hw[bj][n][1][1]})
    const unsigned hoff = (unsigned)(WS_H + (rb * 16 + wc * 2) * 2048 + lane * 16);
    const int pb = (light ? 16 + 3 * hd : 4 * hd) + (wr ? 0 : 2), sw = full ? (wc >> 1) : 0, p0 = pb + sw, p1 = pb + (sw ^ 1);
    SILU16(0, false); SILU16(1, false);
    hqo = hoff + (p0 >> 1) * 262144 + (p0 & 1) * 1024;
    hq[0] = HFRAG(0, 0); hq[1] = HFRAG(0, 1); hq[2] = HFRAG(1, 0);
    ST128(HFRAG(1, 1), hqo + 2048, 16384);
    asm volatile("" : "+v"(hq[0]), "+v"(hq[1]), "+v"(hq[2]));
    if (wr == 0) {
      BAR(); SCHED();
#pragma unroll
      for (int s = 0; s < 8; ++s) qf[s] = ldb128(qr + s * 1024);
    }
    if (full) {
      SILU16(2, true); SILU16(3, true);
#pragma unroll
      for (int bj = 0; bj < 2; ++bj)
#pragma unroll
        for (int n = 0; n < 2; ++n) ST128(HFRAG(bj, n), hoff + n * 2048, (p1 >> 1) * 262144 + (p1 & 1) * 1024 + bj * 16384);
    }
  }
  STAMP(5);
  if (wr == 1) { WAIT_L(0); BAR(); }
  __builtin_amdgcn_s_setprio(1);
  SCHED();
  STAMP(6);
  lptr kb[2], vb[4];
#pragma unroll
  for (int e = 0; e < 2; ++e) kb[e] = lds + SLOT_E + (t >> 3) * 2048 + (t & 7) * 128 + (((4 * e + g) ^ (t >> 1)) << 4);
#pragma unroll
  for (int f = 0; f < 4; ++f) vb[f] = lds + SLOT_C + (g >> 1) * 2048 + (4 * (g & 1) + q4) * 128 + 32 * (f ^ (2 * (g & 1) + (q4 >> 1))) + 8 * p4;
  f32x4 o[8][2], s[8][2], lacc[2], negm[2];
#pragma unroll
  for (int n = 0; n < 2; ++n) {
    lacc[n] = (f32x4){0.f, 0.f, 0.f, 0.f};
#pragma unroll
    for (int d = 0; d < 8; ++d) o[d][n] = (f32x4){0.f, 0.f, 0.f, 0.f};
  }
  bf16x8 pf[8], Kf[32];
  bf16x4 Vh[32][2];
#define SM_THR 60.0f
#define LA 6
#define GW 0
#define MAXX 1
#define MX3(a, b, c) __builtin_fmaxf(__builtin_fmaxf((a), (b)), (c))
  float mxp = 0.f;
#define RD_V(f, hf) Vh[f][hf] = __builtin_amdgcn_ds_read_tr16_b64_v4bf16((LAS bf16x4*)(vb[((f) >> 2) & 3] + ((f) >> 4) * 1024 + ((f) & 3) * 8192 + (hf) * 4096))
#define RD_K(f) Kf[f] = ldb128(kb[(f) & 1] + ((f) >> 2) * 4096 + (((f) >> 1) & 1) * 1024)
#define VF(f) __builtin_shufflevector(Vh[f][0], Vh[f][1], 0, 1, 2, 3, 4, 5, 6, 7)
#define PREF_V() do { _Pragma("unroll") for (int f = 0; f < LA; ++f) { RD_V(f, 0); RD_V(f, 1); } SCHED(); } while (0)
#define PV_SEG(NEXTK, FILL) do { \
    _Pragma("unroll") for (int i = 0; i < 8; ++i) { lacc[i >> 2] = MFMA16(ones, pf[2 * (i & 3) + (i >> 2)], lacc[i >> 2]); SCHED(); } \
    _Pragma("unroll") for (int i = 0; i < 64; ++i) { const int d = i >> 3, n = (i >> 2) & 1, k = n ? 3 - (i & 3) : (i & 3), f = (i >> 1) + LA; \
      if (GW && (i & 7) == 0 && i < 48) WAIT_L(2 * LA - 8); \
      o[d][n] = MFMA16(VF(4 * d + k), pf[2 * k + n], o[d][n]); SCHED(); \
      if (f < 32) RD_V(f < 32 ? f : 0, i & 1); else if ((NEXTK) && (i & 1)) RD_K(f >= 32 ? f - 32 : 0); \
      FILL; SCHED(); } } while (0)
#define QK_SEG(HAVE, CLOSE) do { \
    _Pragma("unroll") for (int i = 0; i < 64; ++i) { const int r = i >> 3, n = (i >> 2) & 1, k = n ? 3 - (i & 3) : (i & 3), f = (i >> 1) + LA; \
      if (GW && (i & 7) == 0 && i < 48) WAIT_L(LA - 4); \
      s[r][n] = MFMA16(Kf[4 * r + k], qf[2 * k + n], (i & 3) == 0 ? negm[n] : s[r][n]); SCHED(); \
      if ((i & 1) && f < 32 && f >= (HAVE)) RD_K(f < 32 ? f : 0); \
      if (MAXX && !(i & 1) && i >= 8) { const int e_ = (i - 8) >> 1, r_ = e_ >> 2, n_ = (e_ >> 1) & 1, h_ = (e_ & 1) * 2; \
        mxp = MX3(e_ == 0 ? s[0][0][0] : mxp, s[r_][n_][h_], s[r_][n_][h_ + 1]); asm volatile("" : "+v"(mxp)); } \
      SCHED(); \
      if (i == 59) { __builtin_amdgcn_s_setprio(2); CLOSE; WAIT_L(0); BAR(); SCHED(); } } \
    __builtin_amdgcn_s_setprio(0); } while (0)
#define MX4(m_, v_) MX3(MX3(m_, v_[0], v_[1]), v_[2], v_[3])
#define ROWMAX(n) MX4(MX4(MX4(MX4(MX4(MX4(MX4(MX4(s[0][n][0], s[0][n]), s[1][n]), s[2][n]), s[3][n]), s[4][n]), s[5][n]), s[6][n]), s[7][n])
#define SOFTMAX() do { \
      const float mx = MAXX ? MX4(MX4(mxp, s[7][0]), s[7][1]) : __builtin_fmaxf(ROWMAX(0), ROWMAX(1)); \
        \
      if (__builtin_expect(__builtin_amdgcn_ballot_w64(!(mx <= SM_THR)) != 0ull, 0)) { \
        const float m0 = ROWMAX(0), m1 = ROWMAX(1); \
        _Pragma("unroll") for (int n = 0; n < 2; ++n) { \
          const float dl = __builtin_fmaxf(col_max(n ? m1 : m0), 0.f), alpha = __builtin_amdgcn_exp2f(-dl); \
          negm[n] -= dl; lacc[n] *= alpha; \
          _Pragma("unroll") for (int r = 0; r < 8; ++r) { s[r][n] -= dl; o[r][n] *= alpha; } } \
      } \
      SCHED(); \
      _Pragma("unroll") for (int k = 0; k < 4; ++k) { \
        _Pragma("unroll") for (int n = 0; n < 2; ++n) { \
          _Pragma("unroll") for (int i = 0; i < 4; ++i) { s[2 * k][n][i] = __builtin_amdgcn_exp2f(s[2 * k][n][i]); s[2 * k + 1][n][i] = __builtin_amdgcn_exp2f(s[2 * k + 1][n][i]); } \
          u32x4 w = {pk2(s[2 * k][n][0], s[2 * k][n][1]), pk2(s[2 * k][n][2], s[2 * k][n][3]), pk2(s[2 * k + 1][n][0], s[2 * k + 1][n][1]), pk2(s[2 * k + 1][n][2], s[2 * k + 1][n][3])}; \
          pf[2 * k + n] = __builtin_bit_cast(bf16x8, w); } \
        asm volatile("" : "+v"(pf[2 * k]), "+v"(pf[2 * k + 1])); \
        SCHED(); } } while (0)
  {
#pragma unroll
    for (int f = 0; f < 8; ++f) RD_K(f);
#pragma unroll
    for (int d = 0; d < 8; d += 2) asm volatile("" : "+v"(o[d][0]), "+v"(o[d][1]), "+v"(o[d + 1][0]), "+v"(o[d + 1][1]));
    f32x4 u[2][2];
    const f32x4 z4 = {0.f, 0.f, 0.f, 0.f};
#pragma unroll
    for (int i = 0; i < 16; ++i) { const int r = i >> 3, n = (i >> 2) & 1, k = i & 3; u[r][n] = MFMA16(Kf[4 * r + k], qf[2 * k + n], k == 0 ? z4 : u[r][n]); }
#pragma unroll
    for (int n = 0; n < 2; ++n) { const float m = -col_max(MX4(MX3(u[0][n][0], u[0][n][1], __builtin_fmaxf(u[0][n][2], u[0][n][3])), u[1][n])); negm[n] = (f32x4){m, m, m, m}; }
    asm volatile("" : "+v"(negm[0]), "+v"(negm[1]));
    SCHED();
    QK_SEG(8, WAIT_V(1));
  }
  STAMP(7);
  const char* kvsrc = (wr ? kb_g + 65536 : vb_g + 32768) + wc * 8192 - 1024;
  unsigned kvo[2][2];
#pragma unroll
  for (int par = 0; par < 2; ++par) {
    kvo[par][0] = (unsigned)((lane >> 3) * 256 + (((lane & 7) ^ (wr ? 4 * par + (lane >> 4) : (lane >> 4) << 1)) << 4) + 1024);
    kvo[par][1] = kvo[par][0] + 128 - 1024;
  }
#pragma unroll 1
  for (int c = 0; c < 3; ++c) {
    if (c == 0) STAMP(8); else if (c == 1) STAMP(12);
    {
      float tv[8], ev[8];
#pragma unroll
      for (int e = 0; e < 4; ++e) { tv[2 * e] = bf2f(hq[0][e] & 0xffffu); tv[2 * e + 1] = U2F(hq[0][e] & 0xffff0000u); }
      SCHED();
#pragma unroll
      for (int e = 0; e < 8; ++e) ev[e] = __builtin_amdgcn_exp2f(tv[e]);
      SCHED();
#pragma unroll
      for (int e = 0; e < 8; ++e) ev[e] = ev[e] * -1.4426950408889634f - 1.4426950408889634f;
      SCHED();
#pragma unroll
      for (int e = 0; e < 8; ++e) ev[e] = __builtin_amdgcn_rcpf(ev[e]);
      SCHED();
      ST128((u32x4){pk2(ev[0] * tv[0], ev[1] * tv[1]), pk2(ev[2] * tv[2], ev[3] * tv[3]), pk2(ev[4] * tv[4], ev[5] * tv[5]), pk2(ev[6] * tv[6], ev[7] * tv[7])}, hqo, c == 2 ? 16384 : c * 2048);
    }
    hq[0] = hq[1]; hq[1] = hq[2];
    SCHED();
    if (__builtin_expect(c + wr < 3, 1)) {
      const int slot = wr ? (c == 0 ? SLOT_B : SLOT_C) : (c == 0 ? SLOT_D : (c == 1 ? SLOT_E : SLOT_A));
#pragma unroll
      for (int hf = 0; hf < 2; ++hf)
        asm volatile("s_mov_b32 m0, %5\n\ts_nop 0\n\t"
                     "global_load_lds_dwordx4 %0, %4\n\tglobal_load_lds_dwordx4 %1, %4 offset:1024\n\t"
                     "global_load_lds_dwordx4 %2, %4 offset:2048\n\tglobal_load_lds_dwordx4 %3, %4 offset:3072"
                     ::"v"(kvo[0][0]), "v"(kvo[0][1]), "v"(kvo[1][0]), "v"(kvo[1][1]), "s"(kvsrc + c * 32768 + hf * 4096), "s"(lds0 + slot + wc * 8192 + hf * 4096) : "memory");
    }
    TOUCH(c + 1);
    SOFTMAX();
    { const int dk = (c == 0) ? SLOT_A - SLOT_E : ((c == 1) ? SLOT_B - SLOT_A : SLOT_C - SLOT_B); kb[0] += dk; kb[1] += dk; asm volatile("" : "+v"(kb[0]), "+v"(kb[1])); }
    PREF_V();
    if (c == 0) STAMP(9); else if (c == 1) STAMP(13);
    BAR(); SCHED();
    if (c == 0) STAMP(10); else if (c == 1) STAMP(14);
    __builtin_amdgcn_s_setprio(1);
    PV_SEG(true, );
    QK_SEG(LA, WAIT_V(1); if (c == 2) WAIT_V(0));
    { const int dv = (c == 1) ? SLOT_E - SLOT_D : 32768;
#pragma unroll
      for (int f = 0; f < 4; ++f) vb[f] += dv; }
    if (c == 0) STAMP(11); else if (c == 2) STAMP(15);
  }
  if (wid == 4 && lane == 0) __builtin_amdgcn_raw_buffer_store_b32(tag, wrs, 0, WS_HFLAG + (rb * 32 + hd) * 128, 16);
  STAMP(16);
  SOFTMAX();
  {
    PREF_V();
    asm volatile("s_waitcnt vmcnt(0)" :: "v"(pft) : "memory");
    SCHED();
    STAMP(17);
    if (wr == 0) BAR();
    SCHED();
    STAMP(18);
    __builtin_amdgcn_s_setprio(1);
    const unsigned oo = (unsigned)(WS_O + 2 * hd * 262144 + (rb * 16 + (wr ^ sb) * 8 + wc * 2) * 2048 + lane * 16);
    float inv[2] = {0.f, 0.f};
    u32x4 ow;
#define O_PIECE(j_) do { const int dp = (j_) >> 3, n_ = ((j_) >> 2) & 1, q_ = (j_) & 3; \
      ow[q_] = pk2(o[2 * dp + (q_ >> 1)][n_][2 * (q_ & 1)] * inv[n_], o[2 * dp + (q_ >> 1)][n_][2 * (q_ & 1) + 1] * inv[n_]); asm volatile("" : "+v"(ow)); \
      if (q_ == 3) ST128(ow, oo + n_ * 2048 + (dp & 1) * 1024, (dp >> 1) * 262144); } while (0)
    PV_SEG(false, if (i == 4) { inv[0] = __builtin_amdgcn_rcpf(lacc[0][0]); inv[1] = __builtin_amdgcn_rcpf(lacc[1][0]); } if (i >= 16 && (i & 1)) O_PIECE(i >= 16 ? (i - 16) >> 1 : 0));
#pragma unroll
    for (int j = 24; j < 32; ++j) O_PIECE(j);
    __builtin_amdgcn_s_setprio(0);
  }
  STAMP(19);
  int lane2 = __builtin_amdgcn_mbcnt_hi(~0u, __builtin_amdgcn_mbcnt_lo(~0u, 0u));
  asm volatile("" : "+v"(lane2));
#define lane lane2
  const int isG = (idx >> 1) & 1, kh = xcd & 1, tm = 4 * (xcd >> 1) + ((idx >> 1) & 2) + (idx & 1), tn = 2 * (idx >> 3) + kh;
#define KDS 1
#define KDG 1
  const int kd = isG ? KDG : KDS, sd2 = kh ? -kd : kd;
  const int npw = (isG ? 48 : 56) + 2 * sd2;
  const int k2c = ((npw + 19) % 6 < 4) ? K2_C : 0;
#define O_FLAG() do { if (wid == 0 && lane == 0) __builtin_amdgcn_raw_buffer_store_b32(tag, wrs, 0, WS_OFLAG + (rb * 32 + hd) * OFS, 16); } while (0)
#define POLL32(off_, slp) do { for (unsigned spins = 0; spins < 2000000u; ++spins) { \
      if (__builtin_amdgcn_ballot_w64(__builtin_amdgcn_raw_buffer_load_b32(wrs, (off_), 0, 16) != tag) == 0ull) break; \
      __builtin_amdgcn_s_sleep(slp); asm volatile("" ::: "memory"); } \
    asm volatile("" ::: "memory"); } while (0)
  const int c8 = lane & 15, r0 = wc * 4 + (lane >> 4);
  const unsigned goff = (unsigned)((tm * 128 + r0) * 2048 + tn * 256 + c8 * 16);
  const lptr cb = lds + k2c + r0 * 512;
#define G_FIN() do { f32x4 a0[4], a1[4]; \
      _Pragma("unroll") for (int i = 0; i < 4; ++i) { a0[i] = *(const LAS f32x4*)(cb + (wr * 4 + i) * 8192 + (((2 * c8) ^ (r0 & 7)) << 4)); a1[i] = *(const LAS f32x4*)(cb + (wr * 4 + i) * 8192 + (((2 * c8 + 1) ^ (r0 & 7)) << 4)); } \
      _Pragma("unroll") for (int i = 0; i < 4; ++i) \
        ST128((u32x4){pk2(silu(a0[i][0]), silu(a0[i][1])), pk2(silu(a0[i][2]), silu(a0[i][3])), pk2(silu(a1[i][0]), silu(a1[i][1])), pk2(silu(a1[i][2]), silu(a1[i][3]))}, goff + i * 32768, WS_G + wr * 131072); \
      WAIT_V(0); BAR(); } while (0)
  const unsigned f1b = isG ? (unsigned)(WS_HFLAG + ((tm >> 1) * 32 + 16 + (lane & 15)) * 128) : (unsigned)(WS_OFLAG + ((tm >> 1) * 32 + 16 + (lane & 15)) * OFS);
  const unsigned f1 = isG ? f1b : (unsigned)(WS_EFLAG + ((tm >> 1) * 16 + (tm & 1) * 8 + kh * 4 + ((lane >> 2) & 3)) * 64 + (lane & 3) * 4);
  STAMP(20);
  if (wr == 0) {
    const char* ab = ws + (isG ? WS_H : WS_O) + tm * 16384 + wc * 4096;
    const char* wbp = (isG ? w2 : wo) + (tn >> 1) * 512 - 3072 + wc * 16384;
    const unsigned voffA0 = lane * 16, voffA1 = lane * 16 + 1024;
    unsigned voffW[4];
    {
      const int r = lane >> 3, c = lane & 7;
      const unsigned lp = (unsigned)(r * 2048 + ((c ^ (((r >> 1) & 3) << 1)) << 4) + 3072);
      voffW[0] = lp + kh * 256; voffW[1] = voffW[0] + 128 - 1024; voffW[2] = lp + (kh ^ 1) * 256 - 2048; voffW[3] = voffW[2] + 128 - 1024;
    }
    const char *ap, *wp;
    unsigned dst = lds0 + 4 * K2_STG;
#define K2_RUN(k_) do { ap = ab + (size_t)(k_) * 262144; wp = wbp + (size_t)(k_) * 131072; } while (0)
#define K2_ISSUE(WIDE, ks) do { \
      asm volatile("s_mov_b32 m0, %3\n\ts_nop 0\n\tglobal_load_lds_dwordx4 %0, %2 sc1\n\tglobal_load_lds_dwordx4 %1, %2 offset:1024 sc1" \
                   ::"v"(voffA0), "v"(voffA1), "s"(ap + (ks) * 1024), "s"(dst + wc * 2048) : "memory"); \
      if (WIDE) GLDS4(voffW[0], voffW[1], voffW[2], voffW[3], wp + (ks) * 65536, dst + 8192 + wc * 4096); \
      else GLDS2(voffW[0], voffW[1], wp + (ks) * 65536, dst + 8192 + wc * 4096); \
      dst = (dst == lds0 + 5 * K2_STG) ? lds0 : dst + K2_STG; \
      if (ks) { ap += 262144; wp += 131072; } } while (0)
#define K2_STEP(WIDE, ks, EVENT) do { WAIT_V((WIDE) ? 18 : 12); BAR(); EVENT; K2_ISSUE(WIDE, ks); } while (0)
#define K2_TILES(WIDE, n_) do { _Pragma("unroll 1") for (int i_ = 0; i_ < (n_); ++i_) { K2_STEP(WIDE, 0, ); K2_STEP(WIDE, 1, ); } } while (0)
#define LOOK(f_) asm volatile("buffer_load_dword %0, %1, %2, 0 offen sc1" : "+v"(pol) : "v"(f_), "s"(wrs) : "memory")
    const unsigned f2 = f1b - 16 * (isG ? 128 : OFS);
    const unsigned f3 = (unsigned)(WS_PFLAG + (bid ^ 1) * 128);
    u32x4 xh[8];
#define XH_LD(i) asm volatile("global_load_dwordx4 %0, %1, %2" : "=v"(xh[i]) : "v"(goff), "s"(xn + (i) * 32768) : "memory")
    WAIT_V(0);
    if (isG && lane == 0) __builtin_amdgcn_raw_buffer_store_b32(tag, wrs, 0, WS_EFLAG + (rb * 16 + hd - 16) * 64 + wc * 4, 16);
    POLL32(f1, 4);
    K2_RUN(isG ? 32 + 12 * kh : 32 + 16 * (tm & 1) + 8 * kh);
    K2_ISSUE(1, 0); K2_ISSUE(1, 1);
    STAMP(21);
    WAIT_V(6); BAR();
    K2_ISSUE(1, 0); K2_ISSUE(1, 1); K2_ISSUE(1, 0);
    STAMP(22);
    unsigned pol = 0;
#define CHECK(f_) do { asm volatile("" : "+v"(pol)); if (__builtin_amdgcn_ballot_w64(pol != tag) != 0ull) POLL32(f_, 4); } while (0)
    K2_STEP(1, 1, );
    K2_TILES(1, 2);
    O_FLAG();
    K2_TILES(1, 1);
    LOOK(f1b);
    K2_TILES(1, 2);
    if (!isG) K2_RUN(48 - 16 * (tm & 1) + 6 * kh);
    K2_STEP(1, 0, CHECK(f1b)); K2_STEP(1, 1, );
    K2_TILES(1, isG ? 1 : 3);
    LOOK(f2);
    K2_TILES(1, 2);
    K2_RUN(((isG ? 12 : 14) + kd) * kh);
    K2_STEP(1, 0, CHECK(f2));
    K2_STEP(1, 1, );
#pragma unroll
    for (int i = 0; i < 8; ++i) { K2_STEP(1, 0, XH_LD(i)); K2_STEP(1, 1, ); }
    K2_TILES(1, (isG ? 3 : 5) + sd2);
    asm volatile("" : "+v"(xh[0]), "+v"(xh[1]), "+v"(xh[2]), "+v"(xh[3]), "+v"(xh[4]), "+v"(xh[5]), "+v"(xh[6]), "+v"(xh[7]));
    K2_RUN(isG ? 24 : 60 - 16 * (tm & 1));
    K2_TILES(0, 4);
    K2_RUN(28);
    K2_STEP(0, 0, );
    K2_STEP(0, 1, if (wid == 0 && lane == 0) __builtin_amdgcn_raw_buffer_store_b32(tag, wrs, 0, WS_PFLAG + bid * 128, 16));
    K2_STEP(0, 0, ); K2_STEP(0, 1, );
    const unsigned f4 = (lane & 1) ? (unsigned)(WS_GFLAG + (tm * 8 + tn) * 128) : f3;
    K2_STEP(0, 0, LOOK(f4)); K2_STEP(0, 1, ); K2_STEP(0, 0, );
    WAIT_V(12);
    asm volatile("" : "+v"(pol));
    const unsigned long long miss = __builtin_amdgcn_ballot_w64(pol != tag);
    if (wid == 0 && (miss & 1ull)) POLL32(f3, 2);
    BAR();
    K2_ISSUE(0, 1);
    WAIT_V(12); BAR();
    WAIT_V(8); BAR();
    if (isG) {
      WAIT_V(4); BAR();
      WAIT_V(0); BAR();
      BAR();
      STAMP(23);
      BAR();
    } else {
      u32x4 gv[8];
      const __amdgpu_buffer_rsrc_t ors = __builtin_amdgcn_make_buffer_rsrc(out, 0, 2048 * 2048, 0x00020000);
#define LD8(dst_, rs_, so_, pol_) asm volatile("buffer_load_dwordx4 %0, %8, %10, %11 offen" pol_ "\n\tbuffer_load_dwordx4 %1, %9, %10, %11 offen" pol_ "\n\t" \
        "buffer_load_dwordx4 %2, %8, %10, %12 offen" pol_ "\n\tbuffer_load_dwordx4 %3, %9, %10, %12 offen" pol_ "\n\t" \
        "buffer_load_dwordx4 %4, %8, %10, %13 offen" pol_ "\n\tbuffer_load_dwordx4 %5, %9, %10, %13 offen" pol_ "\n\t" \
        "buffer_load_dwordx4 %6, %8, %10, %14 offen" pol_ "\n\tbuffer_load_dwordx4 %7, %9, %10, %14 offen" pol_ \
        : "=&v"(dst_[0]), "=&v"(dst_[1]), "=&v"(dst_[2]), "=&v"(dst_[3]), "=&v"(dst_[4]), "=&v"(dst_[5]), "=&v"(dst_[6]), "=&v"(dst_[7]) \
        : "v"(goff), "v"(goff + 32768), "s"(rs_), "s"((so_)), "s"((so_) + 65536), "s"((so_) + 131072), "s"((so_) + 196608) : "memory")
      if (miss & 2ull) {
        WAIT_V(4); BAR(); WAIT_V(0); BAR(); BAR();
        POLL32((unsigned)(WS_GFLAG + (tm * 8 + tn) * 128), 1);
      }
      LD8(gv, wrs, WS_G, " sc1");
      if (!(miss & 2ull)) { WAIT_V(12); BAR(); WAIT_V(8); BAR(); BAR(); }
      float xf[8][8];
#pragma unroll
      for (int i = 0; i < 8; ++i) {
#pragma unroll
        for (int e = 0; e < 4; ++e) { xf[i][2 * e] = bf2f(xh[i][e] & 0xffffu); xf[i][2 * e + 1] = U2F(xh[i][e] & 0xffff0000u); }
        asm volatile("" : "+v"(xf[i][0]), "+v"(xf[i][1]), "+v"(xf[i][2]), "+v"(xf[i][3]), "+v"(xf[i][4]), "+v"(xf[i][5]), "+v"(xf[i][6]), "+v"(xf[i][7]));
      }
      asm volatile("s_waitcnt vmcnt(0)" : "+v"(gv[0]), "+v"(gv[1]), "+v"(gv[2]), "+v"(gv[3]), "+v"(gv[4]), "+v"(gv[5]), "+v"(gv[6]), "+v"(gv[7]) :: "memory");
      float gf2[8][8];
#pragma unroll
      for (int i = 0; i < 8; ++i) {
#pragma unroll
        for (int e = 0; e < 4; ++e) { gf2[i][2 * e] = bf2f(gv[i][e] & 0xffffu); gf2[i][2 * e + 1] = U2F(gv[i][e] & 0xffff0000u); }
        asm volatile("" : "+v"(gf2[i][0]), "+v"(gf2[i][1]), "+v"(gf2[i][2]), "+v"(gf2[i][3]), "+v"(gf2[i][4]), "+v"(gf2[i][5]), "+v"(gf2[i][6]), "+v"(gf2[i][7]));
      }
      STAMP(23);
      BAR();
      f32x4 a0[8], a1[8];
#pragma unroll
      for (int i = 0; i < 8; ++i) { a0[i] = *(const LAS f32x4*)(cb + i * 8192 + (((2 * c8) ^ (r0 & 7)) << 4)); a1[i] = *(const LAS f32x4*)(cb + i * 8192 + (((2 * c8 + 1) ^ (r0 & 7)) << 4)); }
      SCHED();
#pragma unroll
      for (int i = 0; i < 8; ++i) {
        const u32x4 w = {pk2(xf[i][0] + a0[i][0] * gf2[i][0], xf[i][1] + a0[i][1] * gf2[i][1]), pk2(xf[i][2] + a0[i][2] * gf2[i][2], xf[i][3] + a0[i][3] * gf2[i][3]),
                         pk2(xf[i][4] + a1[i][0] * gf2[i][4], xf[i][5] + a1[i][1] * gf2[i][5]), pk2(xf[i][6] + a1[i][2] * gf2[i][6], xf[i][7] + a1[i][3] * gf2[i][7])};
        __builtin_amdgcn_raw_buffer_store_b128(w, ors, goff + i * 32768, 0, 16);
      }
    }
  } else {
    STAMP(21);
    const int wm = wc >> 1, wn = wc & 1, g = lane >> 4, t = lane & 15, q4 = (lane >> 2) & 3, p4 = lane & 3;
    lptr w2b[4];
#pragma unroll
    for (int ft = 0; ft < 4; ++ft) w2b[ft] = lds + 4 * K2_STG + 8192 + wn * 1024 + (g >> 1) * 4096 + (4 * (g & 1) + q4) * 128 + 32 * (ft ^ (2 * (g & 1) + (q4 >> 1))) + 8 * p4;
    lptr ar = lds + 4 * K2_STG + wm * 4096 + lane * 16;
    f32x4 ac2[8][4];
#pragma unroll
    for (int m = 0; m < 8; ++m)
#pragma unroll
      for (int n = 0; n < 4; ++n) ac2[m][n] = (f32x4){0.f, 0.f, 0.f, 0.f};
    bf16x8 Af[2][4];
    bf16x4 Wr[4][2];
    int slot = 4, dlt = 0;
#define K2_RW(f, hf) Wr[(f) & 3][hf] = __builtin_amdgcn_ds_read_tr16_b64_v4bf16((LAS bf16x4*)(w2b[(f) & 3] + (((f) >> 2) & 1) * 2048 + (hf) * 8192))
#define K2_W(m) __builtin_shufflevector(Wr[(m) & 3][0], Wr[(m) & 3][1], 0, 1, 2, 3, 4, 5, 6, 7)
#define K2_NEXT() do { dlt = (slot == 5) ? -5 * K2_STG : K2_STG; slot = (slot == 5) ? 0 : slot + 1; } while (0)
#define K2_ADV(x) do { x += dlt; asm volatile("" : "+v"(x)); } while (0)
#define K2_WIDE(st) do { _Pragma("unroll") for (int i = 0; i < 32; ++i) { const int m = i >> 2, q = i & 3; \
        const int n_ = (m & 1) ? 3 - q : q; ac2[m][n_] = MFMA16(K2_W(m), Af[st][n_], ac2[m][n_]); SCHED(); \
        if (q < 2) K2_RW((m + 3) & 7, q); \
        if (q == 2 && m >= 3 && m < 7) Af[(st) ^ 1][m - 3] = ldb128(ar + (m - 3) * 1024); \
        if (i == 2) K2_ADV(ar); \
        if (i == 6) K2_ADV(w2b[0]); \
        if (i == 10) K2_ADV(w2b[1]); \
        if (i == 11) BAR(); \
        if (i == 19) K2_ADV(w2b[2]); \
        if (i == 23) K2_ADV(w2b[3]); \
        if (i == 30) K2_NEXT(); \
        SCHED(); } } while (0)
#define K2_NARROW(st) do { _Pragma("unroll") for (int i = 0; i < 16; ++i) { const int m = i >> 2, q = i & 3; \
        const int n_ = (m & 1) ? 3 - q : q; ac2[m][n_] = MFMA16(K2_W(m), Af[st][n_], ac2[m][n_]); SCHED(); \
        if (q < 2) K2_RW((m + 3) & 3, q); \
        if (q >= 2 && (m == 1 || m == 2)) Af[(st) ^ 1][2 * m + q - 4] = ldb128(ar + (2 * m + q - 4) * 1024); \
        if (i == 2) { K2_ADV(ar); K2_ADV(w2b[0]); } \
        if (i == 3) BAR(); \
        if (i == 5) K2_ADV(w2b[1]); \
        if (i == 9) K2_ADV(w2b[2]); \
        if (i == 14) K2_ADV(w2b[3]); \
        if (i == 15) K2_NEXT(); \
        SCHED(); } } while (0)
    K2_NEXT();
    BAR();
    STAMP(22);
#pragma unroll
    for (int i = 0; i < 4; ++i) { if (i < 3) { K2_RW(i, 0); K2_RW(i, 1); } Af[0][i] = ldb128(ar + i * 1024); SCHED(); }
#pragma unroll 1
    for (int p = 0; p < 4; p += 2) { K2_WIDE(0); K2_WIDE(1); }
    WAIT_V(0);
#pragma unroll 1
    for (int p = 4; p < npw; p += 2) { K2_WIDE(0); K2_WIDE(1); }
    const unsigned po = (unsigned)(WS_P + wc * 8192 + lane * 16);
#pragma unroll
    for (int i = 0; i < 8; ++i) {
      const f32x4 a = ac2[4 + (i >> 1)][2 * (i & 1)], b = ac2[4 + (i >> 1)][2 * (i & 1) + 1];
      ST128((u32x4){pk2(a[0], a[1]), pk2(a[2], a[3]), pk2(b[0], b[1]), pk2(b[2], b[3])}, po + i * 1024, bid * 32768);
    }
    SCHED();
#pragma unroll 1
    for (int p = 0; p < 4; p += 2) { K2_NARROW(0); K2_NARROW(1); }
    WAIT_V(0);
#pragma unroll 1
    for (int p = 4; p < 10; p += 2) { K2_NARROW(0); K2_NARROW(1); }
    K2_NARROW(0);
    u32x4 pv[8];
#pragma unroll
    for (int i = 0; i < 8; ++i) pv[i] = __builtin_amdgcn_raw_buffer_load_b128(wrs, po + i * 1024 + (bid ^ 1) * 32768, 0, 16);
    SCHED();
    K2_NARROW(1);
#pragma unroll 1
    for (int p = 12; p < 16; p += 2) { K2_NARROW(0); K2_NARROW(1); }
    STAMP(23);
    const lptr cw = lds + k2c + (wm * 64 + t) * 512;
#pragma unroll
    for (int m = 0; m < 4; ++m)
#pragma unroll
      for (int n = 0; n < 4; ++n) {
        const unsigned lo = pv[2 * m + (n >> 1)][2 * (n & 1)], hi = pv[2 * m + (n >> 1)][2 * (n & 1) + 1];
        *(LAS f32x4*)(cw + n * 8192 + (((wn * 16 + m * 4 + g) ^ (t & 7)) << 4)) = ac2[m][n] + (f32x4){bf2f(lo & 0xffffu), U2F(lo & 0xffff0000u), bf2f(hi & 0xffffu), U2F(hi & 0xffff0000u)};
      }
    WAIT_L(0); BAR();
    if (isG) G_FIN();
  STAMP(24);
  STAMP_OUT(ws + (57 << 20), 30);
    __builtin_amdgcn_endpgm();
  }
  if (isG) {
      G_FIN();
      if (wid == 0 && lane == 0) __builtin_amdgcn_raw_buffer_store_b32(tag, wrs, 0, WS_GFLAG + (tm * 8 + tn) * 128, 16);
      if (bid == 16 && wid == 0) {
        for (unsigned spins = 0; spins < 2000000u; ++spins) {
          bool ok = true;
#pragma unroll
          for (int i = 0; i < 4; ++i) ok = ok && __builtin_amdgcn_raw_buffer_load_b32(wrs, (unsigned)((lane + i * 64) * 128), WS_HFLAG, 16) == tag;
          if (__builtin_amdgcn_ballot_w64(!ok) == 0ull) break;
          __builtin_amdgcn_s_sleep(4);
          asm volatile("" ::: "memory");
        }
        asm volatile("" ::: "memory");
        if (lane == 0) __builtin_amdgcn_raw_buffer_store_b32(tag, wrs, 0, 0, 16);
      }
    }
  STAMP(24);
  STAMP_OUT(ws + (57 << 20), 30);
#undef lane
}

extern "C" void kernel_launch(void* const* d_in, const int* in_sizes, int n_in, void* d_out, int out_size, void* d_ws, size_t ws_size, hipStream_t stream) {
  const char* x = (const char*)d_in[0]; const char* kc = (const char*)d_in[1]; const char* vc = (const char*)d_in[2];
  const char* wq = (const char*)d_in[3]; const char* wo = (const char*)d_in[4]; const char* w1 = (const char*)d_in[6]; const char* w2 = (const char*)d_in[7];
  (void)hipFuncSetAttribute((const void*)mega_kernel, hipFuncAttributeMaxDynamicSharedMemorySize, K1_LDS);
  mega_kernel<<<256, 512, K1_LDS, stream>>>(x, kc, vc, wq, w1, wo, w2, (char*)d_ws, (char*)d_out);
}
```

```cpp
#include <hip/hip_runtime.h>
#include <stdint.h>

typedef unsigned short u16;
typedef __bf16 bf16x2 __attribute__((ext_vector_type(2)));
typedef __bf16 bf16x4 __attribute__((ext_vector_type(4)));
typedef __bf16 bf16x8 __attribute__((ext_vector_type(8)));
typedef float f32x2 __attribute__((ext_vector_type(2)));
typedef float f32x4 __attribute__((ext_vector_type(4)));
typedef float f32x16 __attribute__((ext_vector_type(16)));
typedef unsigned u32x2 __attribute__((ext_vector_type(2)));
typedef unsigned u32x4 __attribute__((ext_vector_type(4)));
#define LAS __attribute__((address_space(3)))
typedef LAS char* lptr;
#if defined(__HIP_DEVICE_COMPILE__) && !defined(USEPK)
#define NOPK __attribute__((target("no-packed-fp32-ops")))
#else
#define NOPK
#endif
#define DI NOPK __device__ __forceinline__
#define F2U(x) __builtin_bit_cast(unsigned, (float)(x))
#define U2F(x) __builtin_bit_cast(float, (unsigned)(x))
#define TID ((int)__builtin_amdgcn_workitem_id_x())
#define BID ((int)__builtin_amdgcn_workgroup_id_x())
#define SCHED() __builtin_amdgcn_sched_barrier(0)
#define BAR() asm volatile("s_barrier" ::: "memory")
#define WAIT_V(n) asm volatile("s_waitcnt vmcnt(%0)" :: "n"(n) : "memory")
#define WAIT_L(n) do { asm volatile("" ::: "memory"); __builtin_amdgcn_s_waitcnt(0xC07F | ((n) << 8)); asm volatile("" ::: "memory"); } while (0)
#define MFMA16(a, b, c) __builtin_amdgcn_mfma_f32_16x16x32_bf16(a, b, c, 0, 0, 0)
#define MFMA32(a, b, c) __builtin_amdgcn_mfma_f32_32x32x16_bf16(a, b, c, 0, 0, 0)

DI void glds(unsigned voff, const char* sbase, unsigned ldsaddr) {
  asm volatile("s_mov_b32 m0, %2\n\ts_nop 0\n\tglobal_load_lds_dwordx4 %0, %1" ::"v"(voff), "s"(sbase), "s"(ldsaddr) : "memory");
}
DI void glds1(unsigned voff, const char* sbase, unsigned ldsaddr) {
  asm volatile("s_mov_b32 m0, %2\n\ts_nop 0\n\tglobal_load_lds_dwordx4 %0, %1 sc1" ::"v"(voff), "s"(sbase), "s"(ldsaddr) : "memory");
}
DI unsigned pk2(float lo, float hi) { f32x2 v = {lo, hi}; return __builtin_bit_cast(unsigned, __builtin_convertvector(v, bf16x2)); }
DI float bf2f(unsigned b) { return U2F(b << 16); }
DI bf16x8 ldb128(lptr p) { return *(const LAS bf16x8*)p; }
DI bf16x8 ldtr2(lptr p, int d) {
  bf16x4 a = __builtin_amdgcn_ds_read_tr16_b64_v4bf16((LAS bf16x4*)p);
  bf16x4 b = __builtin_amdgcn_ds_read_tr16_b64_v4bf16((LAS bf16x4*)(p + d));
  return __builtin_shufflevector(a, b, 0, 1, 2, 3, 4, 5, 6, 7);
}
DI void ST128_(__amdgpu_buffer_rsrc_t r, u32x4 v, unsigned voff, int soff) { __builtin_amdgcn_raw_buffer_store_b128(v, r, voff + (unsigned)soff, 0, 16); }
DI float silu(float v) { return v * __builtin_amdgcn_rcpf(1.0f + __builtin_amdgcn_exp2f(v * -1.4426950408889634f)); }

template <int CTRL> DI float dpp_add(float v) { return v + U2F(__builtin_amdgcn_update_dpp(0u, F2U(v), CTRL, 0xf, 0xf, true)); }
DI float half_sum(float v) {
  v = dpp_add<0xB1>(v); v = dpp_add<0x4E>(v); v = dpp_add<0x141>(v); v = dpp_add<0x140>(v);
  auto rr = __builtin_amdgcn_permlane16_swap(F2U(v), F2U(v), false, false);
  return U2F(rr[0]) + U2F(rr[1]);
}
DI float col_max(float v) {
  auto a = __builtin_amdgcn_permlane16_swap(F2U(v), F2U(v), false, false);
  v = __builtin_fmaxf(U2F(a[0]), U2F(a[1]));
  auto b = __builtin_amdgcn_permlane32_swap(F2U(v), F2U(v), false, false);
  return __builtin_fmaxf(U2F(b[0]), U2F(b[1]));
}
typedef __attribute__((address_space(1))) unsigned long long gu64;

#define SX(b, h) (98304 + (b) * 32768 + (h) * 16384)
constexpr int SLOT_B = 0, SLOT_E = 32768, SLOT_A = 65536, SLOT_C = 98304, SLOT_D = 131072, K1_LDS = 163840;

#define XSWAP 0
#define NDEF 1
constexpr int WS_HFLAG = 4096;
constexpr int WS_GFLAG = 65536;
constexpr int WS_OFLAG = 131072;
#define OFS 16
constexpr int WS_EFLAG = 163840;
constexpr int WS_STATS = 1 << 20;
constexpr int WS_PFLAG = 196608;
constexpr int WS_G = 5 << 20, WS_O = 9 << 20, WS_H = 25 << 20, WS_P = 39 << 20, WS_END = 47 << 20;
constexpr int K2_STG = 24576, K2_C = 98304;

NOPK __global__ __launch_bounds__(512, 2) void mega_kernel(const char* __restrict__ xn, const char* __restrict__ kc, const char* __restrict__ vc,
                                                           const char* __restrict__ wq, const char* __restrict__ w1, const char* __restrict__ wo, const char* __restrict__ w2,
                                                           char* ws, char* __restrict__ out) {
  extern __shared__ __attribute__((aligned(16))) char shm[];
  const lptr lds = (lptr)shm;
  const unsigned lds0 = (unsigned)(uintptr_t)lds;
  const int wid = __builtin_amdgcn_readfirstlane(TID >> 6), wr = wid >> 2, wc = wid & 3;
  const int bid = BID ^ XSWAP, xcd = bid & 7, idx = bid >> 3, hd = (idx & 2) * 8 + 2 * xcd + (idx & 1), rb = idx >> 2;
  int lane, g, t, q4, p4;
#define RELANE() do { lane = __builtin_amdgcn_mbcnt_hi(~0u, __builtin_amdgcn_mbcnt_lo(~0u, 0u)); asm volatile("" : "+v"(lane)); g = lane >> 4; t = lane & 15; q4 = (lane >> 2) & 3; p4 = lane & 3; } while (0)
  RELANE();

  const bool light = (idx & 2) != 0;
  unsigned voffX[4], voffQ[2], voffH[2];
  {
    const int r = lane >> 3, c = lane & 7;
#pragma unroll
    for (int i = 0; i < 4; ++i) voffX[i] = (unsigned)((8 * i + r) * 2048 + ((c ^ (4 * (i & 1) + (r >> 1))) << 4) - i * 1024);
    const int k = 32 * (wid >> 2) + 16 * (wid & 1) + 4 * ((wid >> 1) & 1) + 8 * (r >> 2) + (r & 3);
    const int cw = c ^ (((r >> 1) & 3) << 1);
    voffQ[0] = (unsigned)(k * 8192 + cw * 16 + 1024); voffQ[1] = voffQ[0] + 128 - 1024;
    voffH[0] = (unsigned)(k * 7168 + 128 + (light ? (cw & 3) : cw) * 16 + 1024);
    voffH[1] = (unsigned)(k * 7168 + cw * 16);
  }
  const char* xb = xn + (size_t)rb * 256 * 2048 + wid * 65536;
  const char* qb = wq + hd * 256 - 1024;
  const char* hb = w1 + (light ? 4096 + 192 * (hd - 16) : 256 * hd) - 1024;
  const bool full = wr == 1 || !light;
  const int sb = hd >= 24;
#define GLDS2(v0_, v1_, sb_, ld_) asm volatile("s_mov_b32 m0, %3\n\ts_nop 0\n\tglobal_load_lds_dwordx4 %0, %2\n\tglobal_load_lds_dwordx4 %1, %2 offset:1024" \
    ::"v"(v0_), "v"(v1_), "s"(sb_), "s"(ld_) : "memory")
#define GLDS4(v0_, v1_, v2_, v3_, sb_, ld_) asm volatile("s_mov_b32 m0, %5\n\ts_nop 0\n\tglobal_load_lds_dwordx4 %0, %4\n\tglobal_load_lds_dwordx4 %1, %4 offset:1024\n\t" \
    "global_load_lds_dwordx4 %2, %4 offset:2048\n\tglobal_load_lds_dwordx4 %3, %4 offset:3072" ::"v"(v0_), "v"(v1_), "v"(v2_), "v"(v3_), "s"(sb_), "s"(ld_) : "memory")
#define ST_X(b, kt) GLDS4(voffX[0], voffX[1], voffX[2], voffX[3], xb + (kt) * 128, lds0 + SX(b, 0) + wid * 4096)
#define ST_W0(wo_, kt) GLDS2(voffQ[0], voffQ[1], qb + (size_t)(kt) * 524288, lds0 + (wo_) + wid * 2048)
#define ST_W1(wo_, kt) GLDS2(voffH[0], voffH[1], hb + (size_t)(kt) * 458752, lds0 + (wo_) + 16384 + wid * 2048)

  lptr wb[4], xr[2];
#pragma unroll
  for (int m = 0; m < 4; ++m) wb[m] = lds + wr * 1024 + (g >> 1) * 2048 + (4 * (g & 1) + q4) * 128 + 32 * ((m ^ wc) ^ (2 * (g & 1) + (q4 >> 1))) + 8 * p4;
#pragma unroll
  for (int ks = 0; ks < 2; ++ks) xr[ks] = lds + 98304 + (wc * 32 + t) * 128 + 16 * ((4 * ks + g) ^ (t >> 1));
#define LDW(dst, h) do { _Pragma("unroll") for (int m = 0; m < 4; ++m) _Pragma("unroll") for (int k = 0; k < 2; ++k) dst[m][k] = ldtr2(wb[m] + (h) * 16384 + k * 8192, 4096); } while (0)
#define LDX(dst, b, h) do { _Pragma("unroll") for (int n = 0; n < 2; ++n) _Pragma("unroll") for (int k = 0; k < 2; ++k) dst[n][k] = ldb128(xr[k] + (SX(b, h) - 98304) + n * 2048); } while (0)

  f32x4 acc[2][2][4][2];
#pragma unroll
  for (int a = 0; a < 2; ++a)
#pragma unroll
    for (int b = 0; b < 2; ++b)
#pragma unroll
      for (int m = 0; m < 4; ++m)
#pragma unroll
        for (int n = 0; n < 2; ++n) acc[a][b][m][n] = (f32x4){0.f, 0.f, 0.f, 0.f};
  bf16x8 At[4][2], B0[2][2], B1[2][2];

#define MM1(ai, m, k, j) do { if ((j) < 2) acc[ai][0][m][(j) & 1] = MFMA16(At[m][k], B0[(j) & 1][k], acc[ai][0][m][(j) & 1]); else acc[ai][1][m][(j) & 1] = MFMA16(At[m][k], B1[(j) & 1][k], acc[ai][1][m][(j) & 1]); SCHED(); } while (0)
#define MM8(ai, m) do { _Pragma("unroll") for (int q = 0; q < 4; ++q) { MM1(ai, m, q & 1, ((m) & 1) ? 3 - q : q); MM1(ai, m, (q & 1) ^ 1, ((m) & 1) ? 3 - q : q); } } while (0)
#define MMA2(ai, CLOSE) do { __builtin_amdgcn_s_setprio(1); \
    cs[ai] = MFMA16(At[0][1], ones, cs[ai]); SCHED(); cs[ai] = MFMA16(At[0][0], ones, cs[ai]); SCHED(); MM8(ai, 0); asm volatile("" : "+v"(cs[ai])); MM8(ai, 1); \
    if ((ai) == 0 || full) { MM8(ai, 2); MM8(ai, 3); } \
    SCHED(); CLOSE; __builtin_amdgcn_s_setprio(0); SCHED(); } while (0)
  char* const stats = ws + WS_STATS;
  const __amdgpu_buffer_rsrc_t wrs = __builtin_amdgcn_make_buffer_rsrc(ws, 0, WS_END, 0x00020000);
  f32x4 cs[2] = {{0.f, 0.f, 0.f, 0.f}, {0.f, 0.f, 0.f, 0.f}};
  bf16x8 ones = __builtin_bit_cast(bf16x8, (u32x4){0x3f803f80u, 0x3f803f80u, 0x3f803f80u, 0x3f803f80u});
#define PIN_SETUP() do { asm volatile("" : "+v"(ones), "+v"(cs[0]), "+v"(cs[1]), "+v"(wb[0]), "+v"(wb[1]), "+v"(wb[2]), "+v"(wb[3]), "+v"(xr[0]), "+v"(xr[1])); \
    _Pragma("unroll") for (int a = 0; a < 2; ++a) _Pragma("unroll") for (int b = 0; b < 2; ++b) \
      asm volatile("" : "+v"(acc[a][b][0][0]), "+v"(acc[a][b][0][1]), "+v"(acc[a][b][1][0]), "+v"(acc[a][b][1][1]), "+v"(acc[a][b][2][0]), "+v"(acc[a][b][2][1]), "+v"(acc[a][b][3][0]), "+v"(acc[a][b][3][1])); } while (0)
  unsigned tag;
  {
    if (wr == 0) {
      unsigned ep;
      ST_X(0, 0); ST_W0(0, 0);
      GLDS4(voffX[0], voffX[1], voffX[2], voffX[3], xb + 262144, lds0 + SX(0, 1) + wid * 4096);
      GLDS2(voffQ[0], voffQ[1], qb + 262144, lds0 + 8192 + wid * 2048);
      asm volatile("global_load_dword %0, %1, %2 sc1" : "=&v"(ep) : "v"(0), "s"(ws) : "memory");
      PIN_SETUP();
      asm volatile("s_waitcnt vmcnt(0)" : "+v"(ep) :: "memory");
      tag = __builtin_amdgcn_readfirstlane(ep) + 1u;
      BAR();
    } else {
      const int row = rb * 256 + hd * 8 + wc * 2 + (lane >> 5);
      u32x4 a[4];
      unsigned ep;
      asm volatile("global_load_dword %0, %1, %2 sc1" : "=&v"(ep) : "v"(0), "s"(ws) : "memory");
      asm volatile("global_load_dwordx4 %0, %4, %5\n\tglobal_load_dwordx4 %1, %4, %5 offset:512\n\tglobal_load_dwordx4 %2, %4, %5 offset:1024\n\tglobal_load_dwordx4 %3, %4, %5 offset:1536"
                   : "=&v"(a[0]), "=&v"(a[1]), "=&v"(a[2]), "=&v"(a[3]) : "v"(row * 2048 + (lane & 31) * 16), "s"(xn) : "memory");
      ST_W1(0, 0); GLDS2(voffH[0], voffH[1], hb - 229376, lds0 + 16384 + wc * 2048);
      PIN_SETUP();
      BAR();
      ST_W0(32768, 1); GLDS2(voffQ[0], voffQ[1], qb + 524288 - 262144, lds0 + 32768 + wc * 2048);
      ST_X(1, 1); GLDS4(voffX[0], voffX[1], voffX[2], voffX[3], xb - 262144 + 128, lds0 + SX(1, 0) + wc * 4096);
      asm volatile("s_waitcnt vmcnt(16)" : "+v"(ep), "+v"(a[0]), "+v"(a[1]), "+v"(a[2]), "+v"(a[3]) :: "memory");
      tag = __builtin_amdgcn_readfirstlane(ep) + 1u;
      float v[32];
#pragma unroll
      for (int i = 0; i < 16; ++i) { v[2 * i] = bf2f(a[i >> 2][i & 3] & 0xffffu); v[2 * i + 1] = U2F(a[i >> 2][i & 3] & 0xffff0000u); }
      float s0 = 0.f, s1 = 0.f, s2 = 0.f, s3 = 0.f;
#pragma unroll
      for (int i = 0; i < 8; ++i) { s0 += v[i]; s1 += v[8 + i]; s2 += v[16 + i]; s3 += v[24 + i]; }
      const float mean = half_sum((s0 + s1) + (s2 + s3)) * (1.0f / 1024.0f);
      s0 = s1 = s2 = s3 = 0.f;
#pragma unroll
      for (int i = 0; i < 8; ++i) { const float d0 = v[i] - mean, d1 = v[8 + i] - mean, d2 = v[16 + i] - mean, d3 = v[24 + i] - mean; s0 += d0 * d0; s1 += d1 * d1; s2 += d2 * d2; s3 += d3 * d3; }
      const float sd = __builtin_amdgcn_sqrtf(half_sum((s0 + s1) + (s2 + s3)) * (1.0f / 1024.0f) + 1e-5f);
      if ((lane & 31) < 2) __hip_atomic_store((gu64*)(stats + row * 16 + (lane & 1) * 8), ((unsigned long long)tag << 32) | F2U((lane & 1) ? sd : mean), __ATOMIC_RELAXED, __HIP_MEMORY_SCOPE_AGENT);
    }
  }
  if (wr == 1) BAR();
  SCHED();
  const int kvg = hd >> 3;
  const char* kb_g = kc + ((size_t)kvg * 2048 + 1536) * 256;
  const char* vb_g = vc + ((size_t)kvg * 2048 + 1536) * 256;
#define VOFFK ((unsigned)((lane >> 3) * 256 + (((lane & 7) ^ (4 * (wid & 1) + (lane >> 4))) << 4)))
#define VOFFV ((unsigned)((lane >> 3) * 256 + (((lane & 7) ^ ((lane >> 4) << 1)) << 4)))
#define ST_KV8(voff, gsrc, slot) do { glds(voff, (gsrc) + wid * 2048, lds0 + (slot) + wid * 2048); glds(voff, (gsrc) + wid * 2048 + 128, lds0 + (slot) + wid * 2048 + 1024); } while (0)
  int wprv = 65536, wnxt = 32768;
#define W_ADV() do { const int d_ = (wnxt == 0) ? -65536 : 32768, cur_ = wnxt - d_; wnxt = wprv; wprv = cur_; \
    _Pragma("unroll") for (int f = 0; f < 4; ++f) wb[f] += d_; \
    asm volatile("" : "+v"(wb[0]), "+v"(wb[1]), "+v"(wb[2]), "+v"(wb[3])); } while (0)
#define PH_A(b, STAGE) do { LDX(B0, b, 0); LDX(B1, b, 1); SCHED(); LDW(At, 0); STAGE; WAIT_V(10); WAIT_L(12); BAR(); SCHED(); MMA2(0, WAIT_L(0); BAR()); } while (0)
#define PH_B(STAGE) do { LDW(At, 1); STAGE; W_ADV(); WAIT_V(8); BAR(); SCHED(); MMA2(1, WAIT_L(0); BAR()); } while (0)
#pragma unroll 1
  for (int kt = 0; kt < 14; kt += 2) {
    PH_A(0, ST_W1(wnxt, kt + 1); ST_W0(wprv, kt + 2));
    PH_B(ST_X(0, kt + 2));
    PH_A(1, ST_W1(wnxt, kt + 2); ST_W0(wprv, kt + 3));
    PH_B(ST_X(1, kt + 3));
  }
  RELANE();
  const unsigned voffK = VOFFK, voffV = VOFFV;
  PH_A(0, ST_W1(0, 15); ST_KV8(voffK, kb_g, SLOT_E));
  PH_B(ST_KV8(voffV, vb_g, SLOT_C); ST_KV8(voffV, vb_g + 16384, SLOT_C + 16384));
  const unsigned soff = (unsigned)((rb * 256 + (g >> 1) * 128 + wc * 32 + (g & 1) * 16 + t) * 16);
  u32x4 sg;
  LDX(B0, 1, 0); LDX(B1, 1, 1); SCHED(); LDW(At, 0);
  asm volatile("global_load_dwordx4 %0, %1, %2 sc1" : "=&v"(sg) : "v"(soff), "s"(stats) : "memory");
  ST_KV8(voffK, kb_g + 16384, SLOT_E + 16384); ST_KV8(voffK, kb_g + 32768, SLOT_A);
  WAIT_V(11); BAR(); SCHED(); MMA2(0, WAIT_L(0); BAR());
  LDW(At, 1);
  ST_KV8(voffK, kb_g + 49152, SLOT_A + 16384);
  BAR(); SCHED();
  const lptr csw = lds + SLOT_D + (t & 3) * 8192 + wr * 4096 + wc * 64 + g * 16;
  MMA2(1, *(LAS f32x4*)csw = cs[0]; *(LAS f32x4*)(csw + 256) = cs[1]; asm volatile("s_waitcnt vmcnt(4)" : "+v"(sg) :: "memory"); WAIT_L(0); BAR());
  f32x4 cv[2][4];
#pragma unroll
  for (int a = 0; a < 2; ++a)
#pragma unroll
    for (int m = 0; m < 4; ++m) cv[a][m] = *(const LAS f32x4*)(lds + SLOT_D + wc * 8192 + wr * 4096 + a * 256 + (m ^ wc) * 64 + g * 16);
  if (__builtin_expect(__builtin_amdgcn_ballot_w64(sg[1] != tag || sg[3] != tag) != 0ull, 0)) {
    for (unsigned spins = 0; spins < 4000000u; ++spins) {
      __builtin_amdgcn_s_sleep(2);
      asm volatile("" ::: "memory");
      sg = __builtin_amdgcn_raw_buffer_load_b128(wrs, soff, WS_STATS, 16);
      if (__builtin_amdgcn_ballot_w64(sg[1] != tag || sg[3] != tag) == 0ull) break;
    }
  }
  float mu[2][2], rs[2][2], sd[2][2];
  {
    auto m16 = __builtin_amdgcn_permlane16_swap(sg[0], sg[0], false, false);
    auto s16 = __builtin_amdgcn_permlane16_swap(sg[2], sg[2], false, false);
#pragma unroll
    for (int n = 0; n < 2; ++n) {
      auto m32 = __builtin_amdgcn_permlane32_swap(m16[n], m16[n], false, false);
      auto s32 = __builtin_amdgcn_permlane32_swap(s16[n], s16[n], false, false);
#pragma unroll
      for (int bj = 0; bj < 2; ++bj) { mu[bj][n] = -U2F(m32[bj]); sd[bj][n] = U2F(s32[bj]); rs[bj][n] = __builtin_amdgcn_rcpf(sd[bj][n]) * 1.4426950408889634f; }
    }
  }
  WAIT_L(0);
  {
    const lptr qw = lds + wc * 8192 + wr * 4096 + ((g >> 1) * 16 + t) * 16 + (g & 1) * 8;
#pragma unroll
    for (int bj = 0; bj < 2; ++bj) {
      lptr qwb = qw + ((bj ^ sb) ? SLOT_D : SLOT_B);
      asm volatile("" : "+v"(qwb));
#pragma unroll
      for (int m = 0; m < 4; ++m)
#pragma unroll
        for (int n = 0; n < 2; ++n) {
          const f32x4 v = (acc[0][bj][m][n] + cv[0][m] * mu[bj][n]) * (rs[bj][n] * 0.08838834764831845f);
          u32x2 w = {pk2(v[0], v[1]), pk2(v[2], v[3])};
          *(LAS u32x2*)(qwb + ((m ^ wc) >> 1) * 2048 + ((m ^ wc) & 1) * 512 + n * 1024) = w;
        }
    }
  }
  WAIT_L(0); BAR(); SCHED();
  unsigned pft = 0;
  const int sl = ((idx >> 2) * 16 + (idx & 1) * 8 + xcd) * 8 + wid;
  const int tstr = light ? 896 : 1024;
  const char* tbase = (light ? w2 : wo) + (sl < tstr ? sl : tstr - 1) * 2048;
#define TOUCH(q_) asm volatile("global_load_dword %0, %1, %2" : "+v"(pft) : "v"((unsigned)((__builtin_amdgcn_mbcnt_lo(~0u, 0u) & 15) * 128)), "s"(tbase + (((q_) + 2) & 3) * tstr * 2048) : "memory")
  TOUCH(0);
  bf16x8 qf[8];
  const lptr qr = lds + (wr ? SLOT_D : SLOT_B) + wc * 8192 + lane * 16;
  if (wr == 1) {
#pragma unroll
    for (int s = 0; s < 8; ++s) qf[s] = ldb128(qr + s * 1024);
  }
#define ST128(...) ST128_(wrs, __VA_ARGS__)
  u32x4 hq[3];
  unsigned hqo;
  {
    u32x2 hw[2][2][2];
#define SILU16(m, ACT) do { \
      const f32x4 a0 = acc[1][0][m][0] + cv[1][m] * mu[0][0], a1 = acc[1][0][m][1] + cv[1][m] * mu[0][1], a2 = acc[1][1][m][0] + cv[1][m] * mu[1][0], a3 = acc[1][1][m][1] + cv[1][m] * mu[1][1]; \
      f32x4 e0 = a0 * -rs[0][0], e1 = a1 * -rs[0][1], e2 = a2 * -rs[1][0], e3 = a3 * -rs[1][1]; SCHED(); \
      if (ACT) { \
      _Pragma("unroll") for (int i = 0; i < 4; ++i) { e0[i] = __builtin_amdgcn_exp2f(e0[i]); e1[i] = __builtin_amdgcn_exp2f(e1[i]); e2[i] = __builtin_amdgcn_exp2f(e2[i]); e3[i] = __builtin_amdgcn_exp2f(e3[i]); } SCHED(); \
      e0 = e0 * sd[0][0] + sd[0][0]; e1 = e1 * sd[0][1] + sd[0][1]; e2 = e2 * sd[1][0] + sd[1][0]; e3 = e3 * sd[1][1] + sd[1][1]; SCHED(); \
      _Pragma("unroll") for (int i = 0; i < 4; ++i) { e0[i] = __builtin_amdgcn_rcpf(e0[i]); e1[i] = __builtin_amdgcn_rcpf(e1[i]); e2[i] = __builtin_amdgcn_rcpf(e2[i]); e3[i] = __builtin_amdgcn_rcpf(e3[i]); } SCHED(); \
      e0 *= a0; e1 *= a1; e2 *= a2; e3 *= a3; SCHED(); } \
      else { _Pragma("unroll") for (int i = 0; i < 4; ++i) e3[i] = a3[i] * __builtin_amdgcn_rcpf(__builtin_amdgcn_exp2f(e3[i]) * sd[1][1] + sd[1][1]); } \
      hw[0][0][(m) & 1] = (u32x2){pk2(e0[0], e0[1]), pk2(e0[2], e0[3])}; hw[0][1][(m) & 1] = (u32x2){pk2(e1[0], e1[1]), pk2(e1[2], e1[3])}; \
      hw[1][0][(m) & 1] = (u32x2){pk2(e2[0], e2[1]), pk2(e2[2], e2[3])}; hw[1][1][(m) & 1] = (u32x2){pk2(e3[0], e3[1]), pk2(e3[2], e3[3])}; SCHED(); } while (0)
#define HFRAG(bj, n) ((wc & 1) ? (u32x4){hw[bj][n][1][0], hw[bj][n][1][1], hw[bj][n][0][0], hw[bj][n][0][1]} : (u32x4){hw[bj][n][0][0], hw[bj][n][0][1], hw[bj][n][1][0], hw[bj][n][1][1]})
    const unsigned hoff = (unsigned)(WS_H + (rb * 16 + wc * 2) * 2048 + lane * 16);
    const int pb = (light ? 16 + 3 * hd : 4 * hd) + (wr ? 0 : 2), sw = full ? (wc >> 1) : 0, p0 = pb + sw, p1 = pb + (sw ^ 1);
    SILU16(0, false); SILU16(1, false);
    hqo = hoff + (p0 >> 1) * 262144 + (p0 & 1) * 1024;
    hq[0] = HFRAG(0, 0); hq[1] = HFRAG(0, 1); hq[2] = HFRAG(1, 0);
    ST128(HFRAG(1, 1), hqo + 2048, 16384);
    asm volatile("" : "+v"(hq[0]), "+v"(hq[1]), "+v"(hq[2]));
    if (wr == 0) {
      BAR(); SCHED();
#pragma unroll
      for (int s = 0; s < 8; ++s) qf[s] = ldb128(qr + s * 1024);
    }
    if (full) {
      SILU16(2, true); SILU16(3, true);
#pragma unroll
      for (int bj = 0; bj < 2; ++bj)
#pragma unroll
        for (int n = 0; n < 2; ++n) ST128(HFRAG(bj, n), hoff + n * 2048, (p1 >> 1) * 262144 + (p1 & 1) * 1024 + bj * 16384);
    }
  }
  if (wr == 1) { WAIT_L(0); BAR(); }
  __builtin_amdgcn_s_setprio(1);
  SCHED();
  RELANE();
  lptr kb[2], vb[4];
#pragma unroll
  for (int e = 0; e < 2; ++e) kb[e] = lds + SLOT_E + (t >> 3) * 2048 + (t & 7) * 128 + (((4 * e + g) ^ (t >> 1)) << 4);
#pragma unroll
  for (int f = 0; f < 4; ++f) vb[f] = lds + SLOT_C + (g >> 1) * 2048 + (4 * (g & 1) + q4) * 128 + 32 * (f ^ (2 * (g & 1) + (q4 >> 1))) + 8 * p4;
  f32x4 o[8][2], s[8][2], lacc[2], negm[2];
#pragma unroll
  for (int n = 0; n < 2; ++n) {
    lacc[n] = (f32x4){0.f, 0.f, 0.f, 0.f};
#pragma unroll
    for (int d = 0; d < 8; ++d) o[d][n] = (f32x4){0.f, 0.f, 0.f, 0.f};
  }
  bf16x8 pf[8], Kf[32];
  bf16x4 Vh[32][2];
#define SM_LIM 1.152921504606847e18f
#define LA 6
#define GW 0
#define MAXX 0
#define MX3(a, b, c) __builtin_fmaxf(__builtin_fmaxf((a), (b)), (c))
  float mxp = 0.f;
#define RD_V(f, hf) Vh[f][hf] = __builtin_amdgcn_ds_read_tr16_b64_v4bf16((LAS bf16x4*)(vb[((f) >> 2) & 3] + ((f) >> 4) * 1024 + ((f) & 3) * 8192 + (hf) * 4096))
#define RD_K(f) Kf[f] = ldb128(kb[(f) & 1] + ((f) >> 2) * 4096 + (((f) >> 1) & 1) * 1024)
#define VF(f) __builtin_shufflevector(Vh[f][0], Vh[f][1], 0, 1, 2, 3, 4, 5, 6, 7)
#define PREF_V() do { _Pragma("unroll") for (int f = 0; f < LA; ++f) { RD_V(f, 0); RD_V(f, 1); } SCHED(); } while (0)
#define PV_SEG(NEXTK, FILL) do { \
    _Pragma("unroll") for (int i = 0; i < 64; ++i) { const int d = i >> 3, n = (i >> 2) & 1, k = n ? 3 - (i & 3) : (i & 3), f = (i >> 1) + LA; \
      if (GW && (i & 7) == 0 && i < 48) WAIT_L(2 * LA - 8); \
      o[d][n] = MFMA16(VF(4 * d + k), pf[2 * k + n], o[d][n]); SCHED(); \
      if (f < 32) RD_V(f < 32 ? f : 0, i & 1); else if ((NEXTK) && (i & 1)) RD_K(f >= 32 ? f - 32 : 0); \
      FILL; SCHED(); } } while (0)
#define QK_SEG(HAVE, CLOSE) do { \
    _Pragma("unroll") for (int i = 0; i < 64; ++i) { const int r = i >> 3, n = (i >> 2) & 1, k = n ? 3 - (i & 3) : (i & 3), f = (i >> 1) + LA; \
      if (GW && (i & 7) == 0 && i < 48) WAIT_L(LA - 4); \
      s[r][n] = MFMA16(Kf[4 * r + k], qf[2 * k + n], (i & 3) == 0 ? negm[n] : s[r][n]); SCHED(); \
      if ((i & 1) && f < 32 && f >= (HAVE)) RD_K(f < 32 ? f : 0); \
      if (MAXX && !(i & 1) && i >= 8) { const int e_ = (i - 8) >> 1, r_ = e_ >> 2, n_ = (e_ >> 1) & 1, h_ = (e_ & 1) * 2; \
        mxp = MX3(e_ == 0 ? s[0][0][0] : mxp, s[r_][n_][h_], s[r_][n_][h_ + 1]); asm volatile("" : "+v"(mxp)); } \
      SCHED(); \
      if (i == 59) { __builtin_amdgcn_s_setprio(2); CLOSE; WAIT_L(0); BAR(); SCHED(); } } \
    __builtin_amdgcn_s_setprio(0); } while (0)
#define MX4(m_, v_) MX3(MX3(m_, v_[0], v_[1]), v_[2], v_[3])
#define ROWMAX(n) MX4(MX4(MX4(MX4(MX4(MX4(MX4(MX4(s[0][n][0], s[0][n]), s[1][n]), s[2][n]), s[3][n]), s[4][n]), s[5][n]), s[6][n]), s[7][n])
#define EXP_PACK() do { \
      _Pragma("unroll") for (int k = 0; k < 4; ++k) { \
        _Pragma("unroll") for (int n = 0; n < 2; ++n) { \
          _Pragma("unroll") for (int i = 0; i < 4; ++i) { s[2 * k][n][i] = __builtin_amdgcn_exp2f(s[2 * k][n][i]); s[2 * k + 1][n][i] = __builtin_amdgcn_exp2f(s[2 * k + 1][n][i]); } \
          u32x4 w = {pk2(s[2 * k][n][0], s[2 * k][n][1]), pk2(s[2 * k][n][2], s[2 * k][n][3]), pk2(s[2 * k + 1][n][0], s[2 * k + 1][n][1]), pk2(s[2 * k + 1][n][2], s[2 * k + 1][n][3])}; \
          pf[2 * k + n] = __builtin_bit_cast(bf16x8, w); } \
        asm volatile("" : "+v"(pf[2 * k]), "+v"(pf[2 * k + 1])); \
        SCHED(); } \
      __builtin_amdgcn_s_setprio(2); \
      _Pragma("unroll") for (int i = 0; i < 8; ++i) { ln[i >> 2] = MFMA16(ones, pf[2 * (i & 3) + (i >> 2)], (i & 3) == 0 ? lacc[i >> 2] : ln[i >> 2]); SCHED(); } \
      __builtin_amdgcn_s_setprio(0); } while (0)
#define SOFTMAX(PRE) do { f32x4 ln[2]; \
      EXP_PACK(); \
      PRE; \
      if (__builtin_expect(__builtin_amdgcn_ballot_w64(!(ln[0][0] <= SM_LIM && ln[1][0] <= SM_LIM)) != 0ull, 0)) { \
        _Pragma("unroll") for (int r = 0; r < 8; ++r) _Pragma("unroll") for (int k = 0; k < 4; ++k) { \
          const bf16x8 kf_ = ldb128(kb[k & 1] + r * 4096 + (k >> 1) * 1024); \
          s[r][0] = MFMA16(kf_, qf[2 * k], k == 0 ? negm[0] : s[r][0]); s[r][1] = MFMA16(kf_, qf[2 * k + 1], k == 0 ? negm[1] : s[r][1]); if (k == 3) SCHED(); } \
        const float m0 = ROWMAX(0), m1 = ROWMAX(1); \
        _Pragma("unroll") for (int n = 0; n < 2; ++n) { \
          const float dl = __builtin_fmaxf(col_max(n ? m1 : m0), 0.f), alpha = __builtin_amdgcn_exp2f(-dl); \
          negm[n] -= dl; lacc[n] *= alpha; \
          _Pragma("unroll") for (int r = 0; r < 8; ++r) { s[r][n] -= dl; o[r][n] *= alpha; } } \
        EXP_PACK(); \
        PRE; \
      } \
      lacc[0] = ln[0]; lacc[1] = ln[1]; SCHED(); } while (0)
  {
#pragma unroll
    for (int f = 0; f < 8; ++f) RD_K(f);
#pragma unroll
    for (int d = 0; d < 8; d += 2) asm volatile("" : "+v"(o[d][0]), "+v"(o[d][1]), "+v"(o[d + 1][0]), "+v"(o[d + 1][1]));
    f32x4 u[2][2];
    const f32x4 z4 = {0.f, 0.f, 0.f, 0.f};
#pragma unroll
    for (int i = 0; i < 16; ++i) { const int r = i >> 3, n = (i >> 2) & 1, k = i & 3; u[r][n] = MFMA16(Kf[4 * r + k], qf[2 * k + n], k == 0 ? z4 : u[r][n]); }
#pragma unroll
    for (int n = 0; n < 2; ++n) { const float m = -col_max(MX4(MX3(u[0][n][0], u[0][n][1], __builtin_fmaxf(u[0][n][2], u[0][n][3])), u[1][n])); negm[n] = (f32x4){m, m, m, m}; }
    asm volatile("" : "+v"(negm[0]), "+v"(negm[1]));
    SCHED();
    QK_SEG(8, WAIT_V(1));
  }
  const char* kvsrc = (wr ? kb_g + 65536 : vb_g + 32768) + wc * 8192 - 1024;
  unsigned kvo[2][2];
#pragma unroll
  for (int par = 0; par < 2; ++par) {
    kvo[par][0] = (unsigned)((lane >> 3) * 256 + (((lane & 7) ^ (wr ? 4 * par + (lane >> 4) : (lane >> 4) << 1)) << 4) + 1024);
    kvo[par][1] = kvo[par][0] + 128 - 1024;
  }
#pragma unroll 1
  for (int c = 0; c < 3; ++c) {
    {
      float tv[8], ev[8];
#pragma unroll
      for (int e = 0; e < 4; ++e) { tv[2 * e] = bf2f(hq[0][e] & 0xffffu); tv[2 * e + 1] = U2F(hq[0][e] & 0xffff0000u); }
      SCHED();
#pragma unroll
      for (int e = 0; e < 8; ++e) ev[e] = __builtin_amdgcn_exp2f(tv[e]);
      SCHED();
#pragma unroll
      for (int e = 0; e < 8; ++e) ev[e] = ev[e] * -1.4426950408889634f - 1.4426950408889634f;
      SCHED();
#pragma unroll
      for (int e = 0; e < 8; ++e) ev[e] = __builtin_amdgcn_rcpf(ev[e]);
      SCHED();
      ST128((u32x4){pk2(ev[0] * tv[0], ev[1] * tv[1]), pk2(ev[2] * tv[2], ev[3] * tv[3]), pk2(ev[4] * tv[4], ev[5] * tv[5]), pk2(ev[6] * tv[6], ev[7] * tv[7])}, hqo, c == 2 ? 16384 : c * 2048);
    }
    hq[0] = hq[1]; hq[1] = hq[2];
    SCHED();
    if (__builtin_expect(c + wr < 3, 1)) {
      const int slot = wr ? (c == 0 ? SLOT_B : SLOT_C) : (c == 0 ? SLOT_D : (c == 1 ? SLOT_E : SLOT_A));
#pragma unroll
      for (int hf = 0; hf < 2; ++hf)
        asm volatile("s_mov_b32 m0, %5\n\ts_nop 0\n\t"
                     "global_load_lds_dwordx4 %0, %4\n\tglobal_load_lds_dwordx4 %1, %4 offset:1024\n\t"
                     "global_load_lds_dwordx4 %2, %4 offset:2048\n\tglobal_load_lds_dwordx4 %3, %4 offset:3072"
                     ::"v"(kvo[0][0]), "v"(kvo[0][1]), "v"(kvo[1][0]), "v"(kvo[1][1]), "s"(kvsrc + c * 32768 + hf * 4096), "s"(lds0 + slot + wc * 8192 + hf * 4096) : "memory");
    }
    TOUCH(c + 1);
    SOFTMAX(PREF_V());
    { const int dk = (c == 0) ? SLOT_A - SLOT_E : ((c == 1) ? SLOT_B - SLOT_A : SLOT_C - SLOT_B); kb[0] += dk; kb[1] += dk; asm volatile("" : "+v"(kb[0]), "+v"(kb[1])); }
    BAR(); SCHED();
    __builtin_amdgcn_s_setprio(1);
    PV_SEG(true, );
    QK_SEG(LA, WAIT_V(1); if (c == 2) WAIT_V(0));
    { const int dv = (c == 1) ? SLOT_E - SLOT_D : 32768;
#pragma unroll
      for (int f = 0; f < 4; ++f) vb[f] += dv; }
  }
  RELANE();
  if (wid == 4 && lane == 0) __builtin_amdgcn_raw_buffer_store_b32(tag, wrs, 0, WS_HFLAG + (rb * 32 + hd) * 128, 16);
  SOFTMAX(PREF_V());
  {
    asm volatile("s_waitcnt vmcnt(0)" :: "v"(pft) : "memory");
    SCHED();
    if (wr == 0) BAR();
    SCHED();
    __builtin_amdgcn_s_setprio(1);
    const unsigned oo = (unsigned)(WS_O + 2 * hd * 262144 + (rb * 16 + (wr ^ sb) * 8 + wc * 2) * 2048 + lane * 16);
    float inv[2] = {0.f, 0.f};
    u32x4 ow;
#define O_PIECE(j_) do { const int dp = (j_) >> 3, n_ = ((j_) >> 2) & 1, q_ = (j_) & 3; \
      ow[q_] = pk2(o[2 * dp + (q_ >> 1)][n_][2 * (q_ & 1)] * inv[n_], o[2 * dp + (q_ >> 1)][n_][2 * (q_ & 1) + 1] * inv[n_]); asm volatile("" : "+v"(ow)); \
      if (q_ == 3) ST128(ow, oo + n_ * 2048 + (dp & 1) * 1024, (dp >> 1) * 262144); } while (0)
    PV_SEG(false, if (i == 4) { inv[0] = __builtin_amdgcn_rcpf(lacc[0][0]); inv[1] = __builtin_amdgcn_rcpf(lacc[1][0]); } if (i >= 16 && (i & 1)) O_PIECE(i >= 16 ? (i - 16) >> 1 : 0));
#pragma unroll
    for (int j = 24; j < 32; ++j) O_PIECE(j);
    __builtin_amdgcn_s_setprio(0);
  }
  RELANE();
  const int isG = (idx >> 1) & 1, kh = xcd & 1, tm = 4 * (xcd >> 1) + ((idx >> 1) & 2) + (idx & 1), tn = 2 * (idx >> 3) + kh;
#define KDS 1
#define KDG 1
  const int kd = isG ? KDG : KDS, sd2 = kh ? -kd : kd;
  const int npw = (isG ? 48 : 56) + 2 * sd2;
  const int k2c = ((npw + 19) % 6 < 4) ? K2_C : 0;
#define O_FLAG() do { if (wid == 0 && lane == 0) __builtin_amdgcn_raw_buffer_store_b32(tag, wrs, 0, WS_OFLAG + (rb * 32 + hd) * OFS, 16); } while (0)
#define POLL32(off_, slp) do { for (unsigned spins = 0; spins < 2000000u; ++spins) { \
      if (__builtin_amdgcn_ballot_w64(__builtin_amdgcn_raw_buffer_load_b32(wrs, (off_), 0, 16) != tag) == 0ull) break; \
      __builtin_amdgcn_s_sleep(slp); asm volatile("" ::: "memory"); } \
    asm volatile("" ::: "memory"); } while (0)
  const int c8 = lane & 15, r0 = wc * 4 + (lane >> 4);
  const unsigned goff = (unsigned)((tm * 128 + r0) * 2048 + tn * 256 + c8 * 16);
  const lptr cb = lds + k2c + r0 * 512;
#define G_FIN() do { f32x4 a0[4], a1[4]; \
      _Pragma("unroll") for (int i = 0; i < 4; ++i) { a0[i] = *(const LAS f32x4*)(cb + (wr * 4 + i) * 8192 + (((2 * c8) ^ (r0 & 7)) << 4)); a1[i] = *(const LAS f32x4*)(cb + (wr * 4 + i) * 8192 + (((2 * c8 + 1) ^ (r0 & 7)) << 4)); } \
      _Pragma("unroll") for (int i = 0; i < 4; ++i) \
        ST128((u32x4){pk2(silu(a0[i][0]), silu(a0[i][1])), pk2(silu(a0[i][2]), silu(a0[i][3])), pk2(silu(a1[i][0]), silu(a1[i][1])), pk2(silu(a1[i][2]), silu(a1[i][3]))}, goff + i * 32768, WS_G + wr * 131072); \
      WAIT_V(0); BAR(); } while (0)
  const unsigned f1b = isG ? (unsigned)(WS_HFLAG + ((tm >> 1) * 32 + 16 + (lane & 15)) * 128) : (unsigned)(WS_OFLAG + ((tm >> 1) * 32 + 16 + (lane & 15)) * OFS);
  const unsigned f1 = isG ? f1b : (unsigned)(WS_EFLAG + ((tm >> 1) * 16 + (tm & 1) * 8 + kh * 4 + ((lane >> 2) & 3)) * 64 + (lane & 3) * 4);
  if (wr == 0) {
    const char* ab = ws + (isG ? WS_H : WS_O) + tm * 16384 + wc * 4096;
    const char* wbp = (isG ? w2 : wo) + (tn >> 1) * 512 - 3072 + wc * 16384;
    const unsigned voffA0 = lane * 16, voffA1 = lane * 16 + 1024;
    unsigned voffW[4];
    {
      const int r = lane >> 3, c = lane & 7;
      const unsigned lp = (unsigned)(r * 2048 + ((c ^ (((r >> 1) & 3) << 1)) << 4) + 3072);
      voffW[0] = lp + kh * 256; voffW[1] = voffW[0] + 128 - 1024; voffW[2] = lp + (kh ^ 1) * 256 - 2048; voffW[3] = voffW[2] + 128 - 1024;
    }
    const char *ap, *wp;
    unsigned dst = lds0 + 4 * K2_STG;
#define K2_RUN(k_) do { ap = ab + (size_t)(k_) * 262144; wp = wbp + (size_t)(k_) * 131072; } while (0)
#define K2_ISSUE(WIDE, ks) do { \
      asm volatile("s_mov_b32 m0, %3\n\ts_nop 0\n\tglobal_load_lds_dwordx4 %0, %2 sc1\n\tglobal_load_lds_dwordx4 %1, %2 offset:1024 sc1" \
                   ::"v"(voffA0), "v"(voffA1), "s"(ap + (ks) * 1024), "s"(dst + wc * 2048) : "memory"); \
      if (WIDE) GLDS4(voffW[0], voffW[1], voffW[2], voffW[3], wp + (ks) * 65536, dst + 8192 + wc * 4096); \
      else GLDS2(voffW[0], voffW[1], wp + (ks) * 65536, dst + 8192 + wc * 4096); \
      dst = (dst == lds0 + 5 * K2_STG) ? lds0 : dst + K2_STG; \
      if (ks) { ap += 262144; wp += 131072; } } while (0)
#define K2_STEP(WIDE, ks, EVENT) do { WAIT_V((WIDE) ? 18 : 12); BAR(); EVENT; K2_ISSUE(WIDE, ks); } while (0)
#define K2_TILES(WIDE, n_) do { _Pragma("unroll 1") for (int i_ = 0; i_ < (n_); ++i_) { K2_STEP(WIDE, 0, ); K2_STEP(WIDE, 1, ); } } while (0)
#define LOOK(f_) asm volatile("buffer_load_dword %0, %1, %2, 0 offen sc1" : "+v"(pol) : "v"(f_), "s"(wrs) : "memory")
    const unsigned f2 = f1b - 16 * (isG ? 128 : OFS);
    const unsigned f3 = (unsigned)(WS_PFLAG + (bid ^ 1) * 128);
    u32x4 xh[8];
#define XH_LD(i) asm volatile("global_load_dwordx4 %0, %1, %2" : "=v"(xh[i]) : "v"(goff), "s"(xn + (i) * 32768) : "memory")
    WAIT_V(0);
    if (isG && lane == 0) __builtin_amdgcn_raw_buffer_store_b32(tag, wrs, 0, WS_EFLAG + (rb * 16 + hd - 16) * 64 + wc * 4, 16);
    POLL32(f1, 4);
    K2_RUN(isG ? 32 + 12 * kh : 32 + 16 * (tm & 1) + 8 * kh);
    K2_ISSUE(1, 0); K2_ISSUE(1, 1);
    WAIT_V(6); BAR();
    K2_ISSUE(1, 0); K2_ISSUE(1, 1); K2_ISSUE(1, 0);
    unsigned pol = 0;
#define CHECK(f_) do { asm volatile("" : "+v"(pol)); if (__builtin_amdgcn_ballot_w64(pol != tag) != 0ull) POLL32(f_, 4); } while (0)
    K2_STEP(1, 1, );
    K2_TILES(1, 2);
    O_FLAG();
    K2_TILES(1, 1);
    LOOK(f1b);
    K2_TILES(1, 2);
    if (!isG) K2_RUN(48 - 16 * (tm & 1) + 6 * kh);
    K2_STEP(1, 0, CHECK(f1b)); K2_STEP(1, 1, );
    K2_TILES(1, isG ? 1 : 3);
    LOOK(f2);
    K2_TILES(1, 2);
    K2_RUN(((isG ? 12 : 14) + kd) * kh);
    K2_STEP(1, 0, CHECK(f2));
    K2_STEP(1, 1, );
#pragma unroll
    for (int i = 0; i < 8; ++i) { K2_STEP(1, 0, XH_LD(i)); K2_STEP(1, 1, ); }
    K2_TILES(1, (isG ? 3 : 5) + sd2);
    asm volatile("" : "+v"(xh[0]), "+v"(xh[1]), "+v"(xh[2]), "+v"(xh[3]), "+v"(xh[4]), "+v"(xh[5]), "+v"(xh[6]), "+v"(xh[7]));
    K2_RUN(isG ? 24 : 60 - 16 * (tm & 1));
    K2_TILES(0, 4);
    K2_RUN(28);
    K2_STEP(0, 0, );
    K2_STEP(0, 1, if (wid == 0 && lane == 0) __builtin_amdgcn_raw_buffer_store_b32(tag, wrs, 0, WS_PFLAG + bid * 128, 16));
    K2_STEP(0, 0, ); K2_STEP(0, 1, );
    const unsigned f4 = (lane & 1) ? (unsigned)(WS_GFLAG + (tm * 8 + tn) * 128) : f3;
    K2_STEP(0, 0, LOOK(f4)); K2_STEP(0, 1, ); K2_STEP(0, 0, );
    WAIT_V(12);
    asm volatile("" : "+v"(pol));
    const unsigned long long miss = __builtin_amdgcn_ballot_w64(pol != tag);
    if (wid == 0 && (miss & 1ull)) POLL32(f3, 2);
    BAR();
    K2_ISSUE(0, 1);
    WAIT_V(12); BAR();
    WAIT_V(8); BAR();
    if (isG) {
      WAIT_V(4); BAR();
      WAIT_V(0); BAR();
      BAR();
      BAR();
    } else {
      u32x4 gv[8];
      const __amdgpu_buffer_rsrc_t ors = __builtin_amdgcn_make_buffer_rsrc(out, 0, 2048 * 2048, 0x00020000);
#define LD8(dst_, rs_, so_, pol_) asm volatile("buffer_load_dwordx4 %0, %8, %10, %11 offen" pol_ "\n\tbuffer_load_dwordx4 %1, %9, %10, %11 offen" pol_ "\n\t" \
        "buffer_load_dwordx4 %2, %8, %10, %12 offen" pol_ "\n\tbuffer_load_dwordx4 %3, %9, %10, %12 offen" pol_ "\n\t" \
        "buffer_load_dwordx4 %4, %8, %10, %13 offen" pol_ "\n\tbuffer_load_dwordx4 %5, %9, %10, %13 offen" pol_ "\n\t" \
        "buffer_load_dwordx4 %6, %8, %10, %14 offen" pol_ "\n\tbuffer_load_dwordx4 %7, %9, %10, %14 offen" pol_ \
        : "=&v"(dst_[0]), "=&v"(dst_[1]), "=&v"(dst_[2]), "=&v"(dst_[3]), "=&v"(dst_[4]), "=&v"(dst_[5]), "=&v"(dst_[6]), "=&v"(dst_[7]) \
        : "v"(goff), "v"(goff + 32768), "s"(rs_), "s"((so_)), "s"((so_) + 65536), "s"((so_) + 131072), "s"((so_) + 196608) : "memory")
      if (miss & 2ull) {
        WAIT_V(4); BAR(); WAIT_V(0); BAR(); BAR();
        POLL32((unsigned)(WS_GFLAG + (tm * 8 + tn) * 128), 1);
      }
      LD8(gv, wrs, WS_G, " sc1");
      if (!(miss & 2ull)) { WAIT_V(12); BAR(); WAIT_V(8); BAR(); BAR(); }
      float xf[8][8];
#pragma unroll
      for (int i = 0; i < 8; ++i) {
#pragma unroll
        for (int e = 0; e < 4; ++e) { xf[i][2 * e] = bf2f(xh[i][e] & 0xffffu); xf[i][2 * e + 1] = U2F(xh[i][e] & 0xffff0000u); }
        asm volatile("" : "+v"(xf[i][0]), "+v"(xf[i][1]), "+v"(xf[i][2]), "+v"(xf[i][3]), "+v"(xf[i][4]), "+v"(xf[i][5]), "+v"(xf[i][6]), "+v"(xf[i][7]));
      }
      asm volatile("s_waitcnt vmcnt(0)" : "+v"(gv[0]), "+v"(gv[1]), "+v"(gv[2]), "+v"(gv[3]), "+v"(gv[4]), "+v"(gv[5]), "+v"(gv[6]), "+v"(gv[7]) :: "memory");
      float gf2[8][8];
#pragma unroll
      for (int i = 0; i < 8; ++i) {
#pragma unroll
        for (int e = 0; e < 4; ++e) { gf2[i][2 * e] = bf2f(gv[i][e] & 0xffffu); gf2[i][2 * e + 1] = U2F(gv[i][e] & 0xffff0000u); }
        asm volatile("" : "+v"(gf2[i][0]), "+v"(gf2[i][1]), "+v"(gf2[i][2]), "+v"(gf2[i][3]), "+v"(gf2[i][4]), "+v"(gf2[i][5]), "+v"(gf2[i][6]), "+v"(gf2[i][7]));
      }
      BAR();
      f32x4 a0[8], a1[8];
#pragma unroll
      for (int i = 0; i < 8; ++i) { a0[i] = *(const LAS f32x4*)(cb + i * 8192 + (((2 * c8) ^ (r0 & 7)) << 4)); a1[i] = *(const LAS f32x4*)(cb + i * 8192 + (((2 * c8 + 1) ^ (r0 & 7)) << 4)); }
      SCHED();
#pragma unroll
      for (int i = 0; i < 8; ++i) {
        const u32x4 w = {pk2(xf[i][0] + a0[i][0] * gf2[i][0], xf[i][1] + a0[i][1] * gf2[i][1]), pk2(xf[i][2] + a0[i][2] * gf2[i][2], xf[i][3] + a0[i][3] * gf2[i][3]),
                         pk2(xf[i][4] + a1[i][0] * gf2[i][4], xf[i][5] + a1[i][1] * gf2[i][5]), pk2(xf[i][6] + a1[i][2] * gf2[i][6], xf[i][7] + a1[i][3] * gf2[i][7])};
        __builtin_amdgcn_raw_buffer_store_b128(w, ors, goff + i * 32768, 0, 16);
      }
    }
  } else {
    const int wm = wc >> 1, wn = wc & 1;
    lptr w2b[4];
#pragma unroll
    for (int ft = 0; ft < 4; ++ft) w2b[ft] = lds + 4 * K2_STG + 8192 + wn * 1024 + (g >> 1) * 4096 + (4 * (g & 1) + q4) * 128 + 32 * (ft ^ (2 * (g & 1) + (q4 >> 1))) + 8 * p4;
    lptr ar = lds + 4 * K2_STG + wm * 4096 + lane * 16;
    f32x4 ac2[8][4];
#pragma unroll
    for (int m = 0; m < 8; ++m)
#pragma unroll
      for (int n = 0; n < 4; ++n) ac2[m][n] = (f32x4){0.f, 0.f, 0.f, 0.f};
    bf16x8 Af[2][4];
    bf16x4 Wr[4][2];
    int slot = 4, dlt = 0;
#define K2_RW(f, hf) Wr[(f) & 3][hf] = __builtin_amdgcn_ds_read_tr16_b64_v4bf16((LAS bf16x4*)(w2b[(f) & 3] + (((f) >> 2) & 1) * 2048 + (hf) * 8192))
#define K2_W(m) __builtin_shufflevector(Wr[(m) & 3][0], Wr[(m) & 3][1], 0, 1, 2, 3, 4, 5, 6, 7)
#define K2_NEXT() do { dlt = (slot == 5) ? -5 * K2_STG : K2_STG; slot = (slot == 5) ? 0 : slot + 1; } while (0)
#define K2_ADV(x) do { x += dlt; asm volatile("" : "+v"(x)); } while (0)
#define K2_WIDE(st) do { _Pragma("unroll") for (int i = 0; i < 32; ++i) { const int m = i >> 2, q = i & 3; \
        const int n_ = (m & 1) ? 3 - q : q; ac2[m][n_] = MFMA16(K2_W(m), Af[st][n_], ac2[m][n_]); SCHED(); \
        if (q < 2) K2_RW((m + 3) & 7, q); \
        if (q == 2 && m >= 3 && m < 7) Af[(st) ^ 1][m - 3] = ldb128(ar + (m - 3) * 1024); \
        if (i == 2) K2_ADV(ar); \
        if (i == 6) K2_ADV(w2b[0]); \
        if (i == 10) K2_ADV(w2b[1]); \
        if (i == 11) BAR(); \
        if (i == 19) K2_ADV(w2b[2]); \
        if (i == 23) K2_ADV(w2b[3]); \
        if (i == 30) K2_NEXT(); \
        SCHED(); } } while (0)
#define K2_NARROW(st) do { _Pragma("unroll") for (int i = 0; i < 16; ++i) { const int m = i >> 2, q = i & 3; \
        const int n_ = (m & 1) ? 3 - q : q; ac2[m][n_] = MFMA16(K2_W(m), Af[st][n_], ac2[m][n_]); SCHED(); \
        if (q < 2) K2_RW((m + 3) & 3, q); \
        if (q >= 2 && (m == 1 || m == 2)) Af[(st) ^ 1][2 * m + q - 4] = ldb128(ar + (2 * m + q - 4) * 1024); \
        if (i == 2) { K2_ADV(ar); K2_ADV(w2b[0]); } \
        if (i == 3) BAR(); \
        if (i == 5) K2_ADV(w2b[1]); \
        if (i == 9) K2_ADV(w2b[2]); \
        if (i == 14) K2_ADV(w2b[3]); \
        if (i == 15) K2_NEXT(); \
        SCHED(); } } while (0)
    K2_NEXT();
    BAR();
#pragma unroll
    for (int i = 0; i < 4; ++i) { if (i < 3) { K2_RW(i, 0); K2_RW(i, 1); } Af[0][i] = ldb128(ar + i * 1024); SCHED(); }
#pragma unroll 1
    for (int p = 0; p < 4; p += 2) { K2_WIDE(0); K2_WIDE(1); }
    WAIT_V(0);
#pragma unroll 1
    for (int p = 4; p < npw; p += 2) { K2_WIDE(0); K2_WIDE(1); }
    const unsigned po = (unsigned)(WS_P + wc * 8192 + lane * 16);
#pragma unroll
    for (int i = 0; i < 8; ++i) {
      const f32x4 a = ac2[4 + (i >> 1)][2 * (i & 1)], b = ac2[4 + (i >> 1)][2 * (i & 1) + 1];
      ST128((u32x4){pk2(a[0], a[1]), pk2(a[2], a[3]), pk2(b[0], b[1]), pk2(b[2], b[3])}, po + i * 1024, bid * 32768);
    }
    SCHED();
#pragma unroll 1
    for (int p = 0; p < 4; p += 2) { K2_NARROW(0); K2_NARROW(1); }
    WAIT_V(0);
#pragma unroll 1
    for (int p = 4; p < 10; p += 2) { K2_NARROW(0); K2_NARROW(1); }
    K2_NARROW(0);
    u32x4 pv[8];
#pragma unroll
    for (int i = 0; i < 8; ++i) pv[i] = __builtin_amdgcn_raw_buffer_load_b128(wrs, po + i * 1024 + (bid ^ 1) * 32768, 0, 16);
    SCHED();
    K2_NARROW(1);
#pragma unroll 1
    for (int p = 12; p < 16; p += 2) { K2_NARROW(0); K2_NARROW(1); }
    const lptr cw = lds + k2c + (wm * 64 + t) * 512;
#pragma unroll
    for (int m = 0; m < 4; ++m)
#pragma unroll
      for (int n = 0; n < 4; ++n) {
        const unsigned lo = pv[2 * m + (n >> 1)][2 * (n & 1)], hi = pv[2 * m + (n >> 1)][2 * (n & 1) + 1];
        *(LAS f32x4*)(cw + n * 8192 + (((wn * 16 + m * 4 + g) ^ (t & 7)) << 4)) = ac2[m][n] + (f32x4){bf2f(lo & 0xffffu), U2F(lo & 0xffff0000u), bf2f(hi & 0xffffu), U2F(hi & 0xffff0000u)};
      }
    WAIT_L(0); BAR();
    if (isG) G_FIN();
    __builtin_amdgcn_endpgm();
  }
  if (isG) {
      G_FIN();
      if (wid == 0 && lane == 0) __builtin_amdgcn_raw_buffer_store_b32(tag, wrs, 0, WS_GFLAG + (tm * 8 + tn) * 128, 16);
      if (bid == 16 && wid == 0) {
        for (unsigned spins = 0; spins < 2000000u; ++spins) {
          bool ok = true;
#pragma unroll
          for (int i = 0; i < 4; ++i) ok = ok && __builtin_amdgcn_raw_buffer_load_b32(wrs, (unsigned)((lane + i * 64) * 128), WS_HFLAG, 16) == tag;
          if (__builtin_amdgcn_ballot_w64(!ok) == 0ull) break;
          __builtin_amdgcn_s_sleep(4);
          asm volatile("" ::: "memory");
        }
        asm volatile("" ::: "memory");
        if (lane == 0) __builtin_amdgcn_raw_buffer_store_b32(tag, wrs, 0, 0, 16);
      }
    }
}

extern "C" void kernel_launch(void* const* d_in, const int* in_sizes, int n_in, void* d_out, int out_size, void* d_ws, size_t ws_size, hipStream_t stream) {
  const char* x = (const char*)d_in[0]; const char* kc = (const char*)d_in[1]; const char* vc = (const char*)d_in[2];
  const char* wq = (const char*)d_in[3]; const char* wo = (const char*)d_in[4]; const char* w1 = (const char*)d_in[6]; const char* w2 = (const char*)d_in[7];
  (void)hipFuncSetAttribute((const void*)mega_kernel, hipFuncAttributeMaxDynamicSharedMemorySize, K1_LDS);
  mega_kernel<<<256, 512, K1_LDS, stream>>>(x, kc, vc, wq, w1, wo, w2, (char*)d_ws, (char*)d_out);
}
```

```cpp
#include <hip/hip_runtime.h>
#include <stdint.h>

typedef __bf16 bf16x2 __attribute__((ext_vector_type(2)));
typedef __bf16 bf16x4 __attribute__((ext_vector_type(4)));
typedef __bf16 bf16x8 __attribute__((ext_vector_type(8)));
typedef float f32x2 __attribute__((ext_vector_type(2)));
typedef float f32x4 __attribute__((ext_vector_type(4)));
typedef unsigned u32x2 __attribute__((ext_vector_type(2)));
typedef unsigned u32x4 __attribute__((ext_vector_type(4)));
#define LAS __attribute__((address_space(3)))
typedef LAS char* lptr;
#if defined(__HIP_DEVICE_COMPILE__)
#define NOPK __attribute__((target("no-packed-fp32-ops")))
#else
#define NOPK
#endif
#define DI NOPK __device__ __forceinline__
#define F2U(x) __builtin_bit_cast(unsigned, (float)(x))
#define U2F(x) __builtin_bit_cast(float, (unsigned)(x))
#define TID ((int)__builtin_amdgcn_workitem_id_x())
#define BID ((int)__builtin_amdgcn_workgroup_id_x())
#define SCHED() __builtin_amdgcn_sched_barrier(0)
#define BAR() asm volatile("s_barrier" ::: "memory")
#define WAIT_V(n) asm volatile("s_waitcnt vmcnt(%0)" :: "n"(n) : "memory")
#define WAIT_L(n) do { asm volatile("" ::: "memory"); __builtin_amdgcn_s_waitcnt(0xC07F | ((n) << 8)); asm volatile("" ::: "memory"); } while (0)
#define MFMA16(a, b, c) __builtin_amdgcn_mfma_f32_16x16x32_bf16(a, b, c, 0, 0, 0)

DI void glds(unsigned voff, const char* sbase, unsigned ldsaddr) {
  asm volatile("s_mov_b32 m0, %2\n\ts_nop 0\n\tglobal_load_lds_dwordx4 %0, %1" ::"v"(voff), "s"(sbase), "s"(ldsaddr) : "memory");
}
DI unsigned pk2(float lo, float hi) { f32x2 v = {lo, hi}; return __builtin_bit_cast(unsigned, __builtin_convertvector(v, bf16x2)); }
DI float bf2f(unsigned b) { return U2F(b << 16); }
DI bf16x8 ldb128(lptr p) { return *(const LAS bf16x8*)p; }
DI bf16x8 ldtr2(lptr p, int d) {
  bf16x4 a = __builtin_amdgcn_ds_read_tr16_b64_v4bf16((LAS bf16x4*)p);
  bf16x4 b = __builtin_amdgcn_ds_read_tr16_b64_v4bf16((LAS bf16x4*)(p + d));
  return __builtin_shufflevector(a, b, 0, 1, 2, 3, 4, 5, 6, 7);
}
DI void ST128_(__amdgpu_buffer_rsrc_t r, u32x4 v, unsigned voff, int soff) { __builtin_amdgcn_raw_buffer_store_b128(v, r, voff + (unsigned)soff, 0, 16); }
DI float silu(float v) { return v * __builtin_amdgcn_rcpf(1.0f + __builtin_amdgcn_exp2f(v * -1.4426950408889634f)); }

template <int CTRL> DI float dpp_add(float v) { return v + U2F(__builtin_amdgcn_update_dpp(0u, F2U(v), CTRL, 0xf, 0xf, true)); }
DI float half_sum(float v) {
  v = dpp_add<0xB1>(v); v = dpp_add<0x4E>(v); v = dpp_add<0x141>(v); v = dpp_add<0x140>(v);
  auto rr = __builtin_amdgcn_permlane16_swap(F2U(v), F2U(v), false, false);
  return U2F(rr[0]) + U2F(rr[1]);
}
DI float col_max(float v) {
  auto a = __builtin_amdgcn_permlane16_swap(F2U(v), F2U(v), false, false);
  v = __builtin_fmaxf(U2F(a[0]), U2F(a[1]));
  auto b = __builtin_amdgcn_permlane32_swap(F2U(v), F2U(v), false, false);
  return __builtin_fmaxf(U2F(b[0]), U2F(b[1]));
}
typedef __attribute__((address_space(1))) unsigned long long gu64;

#define SX(b, h) (98304 + (b) * 32768 + (h) * 16384)
constexpr int SLOT_B = 0, SLOT_E = 32768, SLOT_A = 65536, SLOT_C = 98304, SLOT_D = 131072, K1_LDS = 163840;

constexpr int WS_HFLAG = 4096;
constexpr int WS_GFLAG = 65536;
constexpr int WS_OFLAG = 131072;
constexpr int OFS = 16;
constexpr int WS_EFLAG = 163840;
constexpr int WS_STATS = 1 << 20;
constexpr int WS_PFLAG = 196608;
constexpr int WS_G = 5 << 20, WS_O = 9 << 20, WS_H = 25 << 20, WS_P = 39 << 20, WS_END = 47 << 20;
constexpr int K2_STG = 24576, K2_C = 98304;

NOPK __global__ __launch_bounds__(512, 2) void mega_kernel(const char* __restrict__ xn, const char* __restrict__ kc, const char* __restrict__ vc,
                                                           const char* __restrict__ wq, const char* __restrict__ w1, const char* __restrict__ wo, const char* __restrict__ w2,
                                                           char* ws, char* __restrict__ out) {
  extern __shared__ __attribute__((aligned(16))) char shm[];
  const lptr lds = (lptr)shm;
  const unsigned lds0 = (unsigned)(uintptr_t)lds;
  const int wid = __builtin_amdgcn_readfirstlane(TID >> 6), wr = wid >> 2, wc = wid & 3;
  const int bid = BID, xcd = bid & 7, idx = bid >> 3, hd = (idx & 2) * 8 + 2 * xcd + (idx & 1), rb = idx >> 2;
  int lane, g, t, q4, p4;
#define RELANE() do { lane = __builtin_amdgcn_mbcnt_hi(~0u, __builtin_amdgcn_mbcnt_lo(~0u, 0u)); asm volatile("" : "+v"(lane)); g = lane >> 4; t = lane & 15; q4 = (lane >> 2) & 3; p4 = lane & 3; } while (0)
  RELANE();

  const bool light = (idx & 2) != 0;
  unsigned voffX[4], voffQ[2], voffH[2];
  {
    const int r = lane >> 3, c = lane & 7;
#pragma unroll
    for (int i = 0; i < 4; ++i) voffX[i] = (unsigned)((8 * i + r) * 2048 + ((c ^ (4 * (i & 1) + (r >> 1))) << 4) - i * 1024);
    const int k = 32 * (wid >> 2) + 16 * (wid & 1) + 4 * ((wid >> 1) & 1) + 8 * (r >> 2) + (r & 3);
    const int cw = c ^ (((r >> 1) & 3) << 1);
    voffQ[0] = (unsigned)(k * 8192 + cw * 16 + 1024); voffQ[1] = voffQ[0] + 128 - 1024;
    voffH[0] = (unsigned)(k * 7168 + 128 + (light ? (cw & 3) : cw) * 16 + 1024);
    voffH[1] = (unsigned)(k * 7168 + cw * 16);
  }
  const char* xb = xn + (size_t)rb * 256 * 2048 + wid * 65536;
  const char* qb = wq + hd * 256 - 1024;
  const char* hb = w1 + (light ? 4096 + 192 * (hd - 16) : 256 * hd) - 1024;
  const bool full = wr == 1 || !light;
  const int sb = hd >= 24;
#define GLDS2(v0_, v1_, sb_, ld_) asm volatile("s_mov_b32 m0, %3\n\ts_nop 0\n\tglobal_load_lds_dwordx4 %0, %2\n\tglobal_load_lds_dwordx4 %1, %2 offset:1024" \
    ::"v"(v0_), "v"(v1_), "s"(sb_), "s"(ld_) : "memory")
#define GLDS4(v0_, v1_, v2_, v3_, sb_, ld_) asm volatile("s_mov_b32 m0, %5\n\ts_nop 0\n\tglobal_load_lds_dwordx4 %0, %4\n\tglobal_load_lds_dwordx4 %1, %4 offset:1024\n\t" \
    "global_load_lds_dwordx4 %2, %4 offset:2048\n\tglobal_load_lds_dwordx4 %3, %4 offset:3072" ::"v"(v0_), "v"(v1_), "v"(v2_), "v"(v3_), "s"(sb_), "s"(ld_) : "memory")
#define ST_X(b, kt) GLDS4(voffX[0], voffX[1], voffX[2], voffX[3], xb + (kt) * 128, lds0 + SX(b, 0) + wid * 4096)
#define ST_W0(wo_, kt) GLDS2(voffQ[0], voffQ[1], qb + (size_t)(kt) * 524288, lds0 + (wo_) + wid * 2048)
#define ST_W1(wo_, kt) GLDS2(voffH[0], voffH[1], hb + (size_t)(kt) * 458752, lds0 + (wo_) + 16384 + wid * 2048)

  lptr wb[4], xr[2];
#pragma unroll
  for (int m = 0; m < 4; ++m) wb[m] = lds + wr * 1024 + (g >> 1) * 2048 + (4 * (g & 1) + q4) * 128 + 32 * ((m ^ wc) ^ (2 * (g & 1) + (q4 >> 1))) + 8 * p4;
#pragma unroll
  for (int ks = 0; ks < 2; ++ks) xr[ks] = lds + 98304 + (wc * 32 + t) * 128 + 16 * ((4 * ks + g) ^ (t >> 1));
#define LDW(dst, h) do { _Pragma("unroll") for (int m = 0; m < 4; ++m) _Pragma("unroll") for (int k = 0; k < 2; ++k) dst[m][k] = ldtr2(wb[m] + (h) * 16384 + k * 8192, 4096); } while (0)
#define LDX(dst, b, h) do { _Pragma("unroll") for (int n = 0; n < 2; ++n) _Pragma("unroll") for (int k = 0; k < 2; ++k) dst[n][k] = ldb128(xr[k] + (SX(b, h) - 98304) + n * 2048); } while (0)

  f32x4 acc[2][2][4][2];
#pragma unroll
  for (int a = 0; a < 2; ++a)
#pragma unroll
    for (int b = 0; b < 2; ++b)
#pragma unroll
      for (int m = 0; m < 4; ++m)
#pragma unroll
        for (int n = 0; n < 2; ++n) acc[a][b][m][n] = (f32x4){0.f, 0.f, 0.f, 0.f};
  bf16x8 At[4][2], B0[2][2], B1[2][2];

#define MM1(ai, m, k, j) do { if ((j) < 2) acc[ai][0][m][(j) & 1] = MFMA16(At[m][k], B0[(j) & 1][k], acc[ai][0][m][(j) & 1]); else acc[ai][1][m][(j) & 1] = MFMA16(At[m][k], B1[(j) & 1][k], acc[ai][1][m][(j) & 1]); SCHED(); } while (0)
#define MM8(ai, m) do { _Pragma("unroll") for (int q = 0; q < 4; ++q) { MM1(ai, m, q & 1, ((m) & 1) ? 3 - q : q); MM1(ai, m, (q & 1) ^ 1, ((m) & 1) ? 3 - q : q); } } while (0)
#define MMA2(ai, CLOSE) do { __builtin_amdgcn_s_setprio(1); \
    cs[ai] = MFMA16(At[0][1], ones, cs[ai]); SCHED(); cs[ai] = MFMA16(At[0][0], ones, cs[ai]); SCHED(); MM8(ai, 0); asm volatile("" : "+v"(cs[ai])); MM8(ai, 1); \
    if ((ai) == 0 || full) { MM8(ai, 2); MM8(ai, 3); } \
    SCHED(); CLOSE; __builtin_amdgcn_s_setprio(0); SCHED(); } while (0)
  char* const stats = ws + WS_STATS;
  const __amdgpu_buffer_rsrc_t wrs = __builtin_amdgcn_make_buffer_rsrc(ws, 0, WS_END, 0x00020000);
  f32x4 cs[2] = {{0.f, 0.f, 0.f, 0.f}, {0.f, 0.f, 0.f, 0.f}};
  bf16x8 ones = __builtin_bit_cast(bf16x8, (u32x4){0x3f803f80u, 0x3f803f80u, 0x3f803f80u, 0x3f803f80u});
#define PIN_SETUP() do { asm volatile("" : "+v"(ones), "+v"(cs[0]), "+v"(cs[1]), "+v"(wb[0]), "+v"(wb[1]), "+v"(wb[2]), "+v"(wb[3]), "+v"(xr[0]), "+v"(xr[1])); \
    _Pragma("unroll") for (int a = 0; a < 2; ++a) _Pragma("unroll") for (int b = 0; b < 2; ++b) \
      asm volatile("" : "+v"(acc[a][b][0][0]), "+v"(acc[a][b][0][1]), "+v"(acc[a][b][1][0]), "+v"(acc[a][b][1][1]), "+v"(acc[a][b][2][0]), "+v"(acc[a][b][2][1]), "+v"(acc[a][b][3][0]), "+v"(acc[a][b][3][1])); } while (0)
  unsigned tag;
  {
    if (wr == 0) {
      unsigned ep;
      ST_X(0, 0); ST_W0(0, 0);
      GLDS4(voffX[0], voffX[1], voffX[2], voffX[3], xb + 262144, lds0 + SX(0, 1) + wid * 4096);
      GLDS2(voffQ[0], voffQ[1], qb + 262144, lds0 + 8192 + wid * 2048);
      asm volatile("global_load_dword %0, %1, %2 sc1" : "=&v"(ep) : "v"(0), "s"(ws) : "memory");
      PIN_SETUP();
      asm volatile("s_waitcnt vmcnt(0)" : "+v"(ep) :: "memory");
      tag = __builtin_amdgcn_readfirstlane(ep) + 1u;
      BAR();
    } else {
      const int row = rb * 256 + hd * 8 + wc * 2 + (lane >> 5);
      u32x4 a[4];
      unsigned ep;
      asm volatile("global_load_dword %0, %1, %2 sc1" : "=&v"(ep) : "v"(0), "s"(ws) : "memory");
      asm volatile("global_load_dwordx4 %0, %4, %5\n\tglobal_load_dwordx4 %1, %4, %5 offset:512\n\tglobal_load_dwordx4 %2, %4, %5 offset:1024\n\tglobal_load_dwordx4 %3, %4, %5 offset:1536"
                   : "=&v"(a[0]), "=&v"(a[1]), "=&v"(a[2]), "=&v"(a[3]) : "v"(row * 2048 + (lane & 31) * 16), "s"(xn) : "memory");
      ST_W1(0, 0); GLDS2(voffH[0], voffH[1], hb - 229376, lds0 + 16384 + wc * 2048);
      PIN_SETUP();
      BAR();
      ST_W0(32768, 1); GLDS2(voffQ[0], voffQ[1], qb + 524288 - 262144, lds0 + 32768 + wc * 2048);
      ST_X(1, 1); GLDS4(voffX[0], voffX[1], voffX[2], voffX[3], xb - 262144 + 128, lds0 + SX(1, 0) + wc * 4096);
      asm volatile("s_waitcnt vmcnt(16)" : "+v"(ep), "+v"(a[0]), "+v"(a[1]), "+v"(a[2]), "+v"(a[3]) :: "memory");
      tag = __builtin_amdgcn_readfirstlane(ep) + 1u;
      float v[32];
#pragma unroll
      for (int i = 0; i < 16; ++i) { v[2 * i] = bf2f(a[i >> 2][i & 3] & 0xffffu); v[2 * i + 1] = U2F(a[i >> 2][i & 3] & 0xffff0000u); }
      float s0 = 0.f, s1 = 0.f, s2 = 0.f, s3 = 0.f;
#pragma unroll
      for (int i = 0; i < 8; ++i) { s0 += v[i]; s1 += v[8 + i]; s2 += v[16 + i]; s3 += v[24 + i]; }
      const float mean = half_sum((s0 + s1) + (s2 + s3)) * (1.0f / 1024.0f);
      s0 = s1 = s2 = s3 = 0.f;
#pragma unroll
      for (int i = 0; i < 8; ++i) { const float d0 = v[i] - mean, d1 = v[8 + i] - mean, d2 = v[16 + i] - mean, d3 = v[24 + i] - mean; s0 += d0 * d0; s1 += d1 * d1; s2 += d2 * d2; s3 += d3 * d3; }
      const float sd = __builtin_amdgcn_sqrtf(half_sum((s0 + s1) + (s2 + s3)) * (1.0f / 1024.0f) + 1e-5f);
      if ((lane & 31) < 2) __hip_atomic_store((gu64*)(stats + row * 16 + (lane & 1) * 8), ((unsigned long long)tag << 32) | F2U((lane & 1) ? sd : mean), __ATOMIC_RELAXED, __HIP_MEMORY_SCOPE_AGENT);
    }
  }
  if (wr == 1) BAR();
  SCHED();
  const int kvg = hd >> 3;
  const char* kb_g = kc + ((size_t)kvg * 2048 + 1536) * 256;
  const char* vb_g = vc + ((size_t)kvg * 2048 + 1536) * 256;
#define VOFFK ((unsigned)((lane >> 3) * 256 + (((lane & 7) ^ (4 * (wid & 1) + (lane >> 4))) << 4)))
#define VOFFV ((unsigned)((lane >> 3) * 256 + (((lane & 7) ^ ((lane >> 4) << 1)) << 4)))
#define ST_KV8(voff, gsrc, slot) do { glds(voff, (gsrc) + wid * 2048, lds0 + (slot) + wid * 2048); glds(voff, (gsrc) + wid * 2048 + 128, lds0 + (slot) + wid * 2048 + 1024); } while (0)
  int wprv = 65536, wnxt = 32768;
#define W_ADV() do { const int d_ = (wnxt == 0) ? -65536 : 32768, cur_ = wnxt - d_; wnxt = wprv; wprv = cur_; \
    _Pragma("unroll") for (int f = 0; f < 4; ++f) wb[f] += d_; \
    asm volatile("" : "+v"(wb[0]), "+v"(wb[1]), "+v"(wb[2]), "+v"(wb[3])); } while (0)
#define PH_A(b, STAGE) do { LDX(B0, b, 0); LDX(B1, b, 1); SCHED(); LDW(At, 0); STAGE; WAIT_V(10); WAIT_L(12); BAR(); SCHED(); MMA2(0, WAIT_L(0); BAR()); } while (0)
#define PH_B(STAGE) do { LDW(At, 1); STAGE; W_ADV(); WAIT_V(8); BAR(); SCHED(); MMA2(1, WAIT_L(0); BAR()); } while (0)
#pragma unroll 1
  for (int kt = 0; kt < 14; kt += 2) {
    PH_A(0, ST_W1(wnxt, kt + 1); ST_W0(wprv, kt + 2));
    PH_B(ST_X(0, kt + 2));
    PH_A(1, ST_W1(wnxt, kt + 2); ST_W0(wprv, kt + 3));
    PH_B(ST_X(1, kt + 3));
  }
  RELANE();
  const unsigned voffK = VOFFK, voffV = VOFFV;
  PH_A(0, ST_W1(0, 15); ST_KV8(voffK, kb_g, SLOT_E));
  PH_B(ST_KV8(voffV, vb_g, SLOT_C); ST_KV8(voffV, vb_g + 16384, SLOT_C + 16384));
  const unsigned soff = (unsigned)((rb * 256 + (g >> 1) * 128 + wc * 32 + (g & 1) * 16 + t) * 16);
  u32x4 sg;
  LDX(B0, 1, 0); LDX(B1, 1, 1); SCHED(); LDW(At, 0);
  asm volatile("global_load_dwordx4 %0, %1, %2 sc1" : "=&v"(sg) : "v"(soff), "s"(stats) : "memory");
  ST_KV8(voffK, kb_g + 16384, SLOT_E + 16384); ST_KV8(voffK, kb_g + 32768, SLOT_A);
  WAIT_V(11); BAR(); SCHED(); MMA2(0, WAIT_L(0); BAR());
  LDW(At, 1);
  ST_KV8(voffK, kb_g + 49152, SLOT_A + 16384);
  WAIT_L(0); BAR(); SCHED();
  const lptr csw = lds + SLOT_D + (t & 3) * 8192 + wr * 4096 + wc * 64 + g * 16;
  MMA2(1, *(LAS f32x4*)csw = cs[0]; *(LAS f32x4*)(csw + 256) = cs[1]; asm volatile("s_waitcnt vmcnt(4)" : "+v"(sg) :: "memory"); WAIT_L(0); BAR());
  f32x4 cv[2][4];
#pragma unroll
  for (int a = 0; a < 2; ++a)
#pragma unroll
    for (int m = 0; m < 4; ++m) cv[a][m] = *(const LAS f32x4*)(lds + SLOT_D + wc * 8192 + wr * 4096 + a * 256 + (m ^ wc) * 64 + g * 16);
  if (__builtin_expect(__builtin_amdgcn_ballot_w64(sg[1] != tag || sg[3] != tag) != 0ull, 0)) {
    for (unsigned spins = 0; spins < 4000000u; ++spins) {
      __builtin_amdgcn_s_sleep(2);
      asm volatile("" ::: "memory");
      sg = __builtin_amdgcn_raw_buffer_load_b128(wrs, soff, WS_STATS, 16);
      if (__builtin_amdgcn_ballot_w64(sg[1] != tag || sg[3] != tag) == 0ull) break;
    }
  }
  float mu[2][2], rs[2][2], sd[2][2];
  {
    auto m16 = __builtin_amdgcn_permlane16_swap(sg[0], sg[0], false, false);
    auto s16 = __builtin_amdgcn_permlane16_swap(sg[2], sg[2], false, false);
#pragma unroll
    for (int n = 0; n < 2; ++n) {
      auto m32 = __builtin_amdgcn_permlane32_swap(m16[n], m16[n], false, false);
      auto s32 = __builtin_amdgcn_permlane32_swap(s16[n], s16[n], false, false);
#pragma unroll
      for (int bj = 0; bj < 2; ++bj) { mu[bj][n] = -U2F(m32[bj]); sd[bj][n] = U2F(s32[bj]); rs[bj][n] = __builtin_amdgcn_rcpf(sd[bj][n]) * 1.4426950408889634f; }
    }
  }
  WAIT_L(0);
  {
    const lptr qw = lds + wc * 8192 + wr * 4096 + ((g >> 1) * 16 + t) * 16 + (g & 1) * 8;
#pragma unroll
    for (int bj = 0; bj < 2; ++bj) {
      lptr qwb = qw + ((bj ^ sb) ? SLOT_D : SLOT_B);
      asm volatile("" : "+v"(qwb));
#pragma unroll
      for (int m = 0; m < 4; ++m)
#pragma unroll
        for (int n = 0; n < 2; ++n) {
          const f32x4 v = (acc[0][bj][m][n] + cv[0][m] * mu[bj][n]) * (rs[bj][n] * 0.08838834764831845f);
          u32x2 w = {pk2(v[0], v[1]), pk2(v[2], v[3])};
          *(LAS u32x2*)(qwb + ((m ^ wc) >> 1) * 2048 + ((m ^ wc) & 1) * 512 + n * 1024) = w;
        }
    }
  }
  WAIT_L(0); BAR(); SCHED();
  unsigned pft = 0;
  const int sl = ((idx >> 2) * 16 + (idx & 1) * 8 + xcd) * 8 + wid;
  const int tstr = light ? 896 : 1024;
  const char* tbase = (light ? w2 : wo) + (sl < tstr ? sl : tstr - 1) * 2048;
#define TOUCH(q_) asm volatile("global_load_dword %0, %1, %2" : "+v"(pft) : "v"((unsigned)((__builtin_amdgcn_mbcnt_lo(~0u, 0u) & 15) * 128)), "s"(tbase + (((q_) + 2) & 3) * tstr * 2048) : "memory")
  TOUCH(0);
  bf16x8 qf[8];
  const lptr qr = lds + (wr ? SLOT_D : SLOT_B) + wc * 8192 + lane * 16;
  if (wr == 1) {
#pragma unroll
    for (int s = 0; s < 8; ++s) qf[s] = ldb128(qr + s * 1024);
  }
#define ST128(...) ST128_(wrs, __VA_ARGS__)
  u32x4 hq[3];
  unsigned hqo;
  {
    u32x2 hw[2][2][2];
#define SILU16(m, ACT) do { \
      const f32x4 a0 = acc[1][0][m][0] + cv[1][m] * mu[0][0], a1 = acc[1][0][m][1] + cv[1][m] * mu[0][1], a2 = acc[1][1][m][0] + cv[1][m] * mu[1][0], a3 = acc[1][1][m][1] + cv[1][m] * mu[1][1]; \
      f32x4 e0 = a0 * -rs[0][0], e1 = a1 * -rs[0][1], e2 = a2 * -rs[1][0], e3 = a3 * -rs[1][1]; SCHED(); \
      if (ACT) { \
      _Pragma("unroll") for (int i = 0; i < 4; ++i) { e0[i] = __builtin_amdgcn_exp2f(e0[i]); e1[i] = __builtin_amdgcn_exp2f(e1[i]); e2[i] = __builtin_amdgcn_exp2f(e2[i]); e3[i] = __builtin_amdgcn_exp2f(e3[i]); } SCHED(); \
      e0 = e0 * sd[0][0] + sd[0][0]; e1 = e1 * sd[0][1] + sd[0][1]; e2 = e2 * sd[1][0] + sd[1][0]; e3 = e3 * sd[1][1] + sd[1][1]; SCHED(); \
      _Pragma("unroll") for (int i = 0; i < 4; ++i) { e0[i] = __builtin_amdgcn_rcpf(e0[i]); e1[i] = __builtin_amdgcn_rcpf(e1[i]); e2[i] = __builtin_amdgcn_rcpf(e2[i]); e3[i] = __builtin_amdgcn_rcpf(e3[i]); } SCHED(); \
      e0 *= a0; e1 *= a1; e2 *= a2; e3 *= a3; SCHED(); } \
      else { _Pragma("unroll") for (int i = 0; i < 4; ++i) e3[i] = a3[i] * __builtin_amdgcn_rcpf(__builtin_amdgcn_exp2f(e3[i]) * sd[1][1] + sd[1][1]); } \
      hw[0][0][(m) & 1] = (u32x2){pk2(e0[0], e0[1]), pk2(e0[2], e0[3])}; hw[0][1][(m) & 1] = (u32x2){pk2(e1[0], e1[1]), pk2(e1[2], e1[3])}; \
      hw[1][0][(m) & 1] = (u32x2){pk2(e2[0], e2[1]), pk2(e2[2], e2[3])}; hw[1][1][(m) & 1] = (u32x2){pk2(e3[0], e3[1]), pk2(e3[2], e3[3])}; SCHED(); } while (0)
#define HFRAG(bj, n) ((wc & 1) ? (u32x4){hw[bj][n][1][0], hw[bj][n][1][1], hw[bj][n][0][0], hw[bj][n][0][1]} : (u32x4){hw[bj][n][0][0], hw[bj][n][0][1], hw[bj][n][1][0], hw[bj][n][1][1]})
    const unsigned hoff = (unsigned)(WS_H + (rb * 16 + wc * 2) * 2048 + lane * 16);
    const int pb = (light ? 16 + 3 * hd : 4 * hd) + (wr ? 0 : 2), sw = full ? (wc >> 1) : 0, p0 = pb + sw, p1 = pb + (sw ^ 1);
    SILU16(0, false); SILU16(1, false);
    hqo = hoff + (p0 >> 1) * 262144 + (p0 & 1) * 1024;
    hq[0] = HFRAG(0, 0); hq[1] = HFRAG(0, 1); hq[2] = HFRAG(1, 0);
    ST128(HFRAG(1, 1), hqo + 2048, 16384);
    asm volatile("" : "+v"(hq[0]), "+v"(hq[1]), "+v"(hq[2]));
    if (wr == 0) {
      BAR(); SCHED();
#pragma unroll
      for (int s = 0; s < 8; ++s) qf[s] = ldb128(qr + s * 1024);
    }
    if (full) {
      SILU16(2, true); SILU16(3, true);
#pragma unroll
      for (int bj = 0; bj < 2; ++bj)
#pragma unroll
        for (int n = 0; n < 2; ++n) ST128(HFRAG(bj, n), hoff + n * 2048, (p1 >> 1) * 262144 + (p1 & 1) * 1024 + bj * 16384);
    }
  }
  if (wr == 1) { WAIT_L(0); BAR(); }
  __builtin_amdgcn_s_setprio(1);
  SCHED();
  RELANE();
  lptr kb[2], vb[4];
#pragma unroll
  for (int e = 0; e < 2; ++e) kb[e] = lds + SLOT_E + (t >> 3) * 2048 + (t & 7) * 128 + (((4 * e + g) ^ (t >> 1)) << 4);
#pragma unroll
  for (int f = 0; f < 4; ++f) vb[f] = lds + SLOT_C + (g >> 1) * 2048 + (4 * (g & 1) + q4) * 128 + 32 * (f ^ (2 * (g & 1) + (q4 >> 1))) + 8 * p4;
  f32x4 o[8][2], s[8][2], lacc[2], negm[2];
#pragma unroll
  for (int n = 0; n < 2; ++n) {
    lacc[n] = (f32x4){0.f, 0.f, 0.f, 0.f};
#pragma unroll
    for (int d = 0; d < 8; ++d) o[d][n] = (f32x4){0.f, 0.f, 0.f, 0.f};
  }
  bf16x8 pf[8], Kf[32];
  bf16x4 Vh[32][2];
#define SM_LIM 1.152921504606847e18f
  constexpr int LA = 6;
#define MX3(a, b, c) __builtin_fmaxf(__builtin_fmaxf((a), (b)), (c))
#define RD_V(f, hf) Vh[f][hf] = __builtin_amdgcn_ds_read_tr16_b64_v4bf16((LAS bf16x4*)(vb[((f) >> 2) & 3] + ((f) >> 4) * 1024 + ((f) & 3) * 8192 + (hf) * 4096))
#define RD_K(f) Kf[f] = ldb128(kb[(f) & 1] + ((f) >> 2) * 4096 + (((f) >> 1) & 1) * 1024)
#define VF(f) __builtin_shufflevector(Vh[f][0], Vh[f][1], 0, 1, 2, 3, 4, 5, 6, 7)
#define PREF_V() do { _Pragma("unroll") for (int f = 0; f < LA; ++f) { RD_V(f, 0); RD_V(f, 1); } SCHED(); } while (0)
#define PV_SEG(NEXTK, FILL) do { \
    _Pragma("unroll") for (int i = 0; i < 64; ++i) { const int d = i >> 3, n = (i >> 2) & 1, k = n ? 3 - (i & 3) : (i & 3), f = (i >> 1) + LA; \
      o[d][n] = MFMA16(VF(4 * d + k), pf[2 * k + n], o[d][n]); SCHED(); \
      if (f < 32) RD_V(f < 32 ? f : 0, i & 1); else if ((NEXTK) && (i & 1)) RD_K(f >= 32 ? f - 32 : 0); \
      FILL; SCHED(); } } while (0)
#define QK_SEG(HAVE, CLOSE) do { \
    _Pragma("unroll") for (int i = 0; i < 64; ++i) { const int r = i >> 3, n = (i >> 2) & 1, k = n ? 3 - (i & 3) : (i & 3), f = (i >> 1) + LA; \
      s[r][n] = MFMA16(Kf[4 * r + k], qf[2 * k + n], (i & 3) == 0 ? negm[n] : s[r][n]); SCHED(); \
      if ((i & 1) && f < 32 && f >= (HAVE)) RD_K(f < 32 ? f : 0); \
      SCHED(); \
      if (i == 59) { __builtin_amdgcn_s_setprio(2); CLOSE; WAIT_L(0); BAR(); SCHED(); } } \
    __builtin_amdgcn_s_setprio(0); } while (0)
#define MX4(m_, v_) MX3(MX3(m_, v_[0], v_[1]), v_[2], v_[3])
#define ROWMAX(n) MX4(MX4(MX4(MX4(MX4(MX4(MX4(MX4(s[0][n][0], s[0][n]), s[1][n]), s[2][n]), s[3][n]), s[4][n]), s[5][n]), s[6][n]), s[7][n])
#define EXP_PACK() do { \
      _Pragma("unroll") for (int k = 0; k < 4; ++k) { \
        _Pragma("unroll") for (int n = 0; n < 2; ++n) { \
          _Pragma("unroll") for (int i = 0; i < 4; ++i) { s[2 * k][n][i] = __builtin_amdgcn_exp2f(s[2 * k][n][i]); s[2 * k + 1][n][i] = __builtin_amdgcn_exp2f(s[2 * k + 1][n][i]); } \
          u32x4 w = {pk2(s[2 * k][n][0], s[2 * k][n][1]), pk2(s[2 * k][n][2], s[2 * k][n][3]), pk2(s[2 * k + 1][n][0], s[2 * k + 1][n][1]), pk2(s[2 * k + 1][n][2], s[2 * k + 1][n][3])}; \
          pf[2 * k + n] = __builtin_bit_cast(bf16x8, w); } \
        asm volatile("" : "+v"(pf[2 * k]), "+v"(pf[2 * k + 1])); \
        SCHED(); } \
      __builtin_amdgcn_s_setprio(2); \
      _Pragma("unroll") for (int i = 0; i < 8; ++i) { ln[i >> 2] = MFMA16(ones, pf[2 * (i & 3) + (i >> 2)], (i & 3) == 0 ? lacc[i >> 2] : ln[i >> 2]); SCHED(); } \
      __builtin_amdgcn_s_setprio(0); } while (0)
#define SOFTMAX(PRE) do { f32x4 ln[2]; \
      EXP_PACK(); \
      PRE; \
      if (__builtin_expect(__builtin_amdgcn_ballot_w64(!(ln[0][0] <= SM_LIM && ln[1][0] <= SM_LIM)) != 0ull, 0)) { \
        _Pragma("unroll") for (int r = 0; r < 8; ++r) _Pragma("unroll") for (int k = 0; k < 4; ++k) { \
          const bf16x8 kf_ = ldb128(kb[k & 1] + r * 4096 + (k >> 1) * 1024); \
          s[r][0] = MFMA16(kf_, qf[2 * k], k == 0 ? negm[0] : s[r][0]); s[r][1] = MFMA16(kf_, qf[2 * k + 1], k == 0 ? negm[1] : s[r][1]); if (k == 3) SCHED(); } \
        const float m0 = ROWMAX(0), m1 = ROWMAX(1); \
        _Pragma("unroll") for (int n = 0; n < 2; ++n) { \
          const float dl = __builtin_fmaxf(col_max(n ? m1 : m0), 0.f), alpha = __builtin_amdgcn_exp2f(-dl); \
          negm[n] -= dl; lacc[n] *= alpha; \
          _Pragma("unroll") for (int r = 0; r < 8; ++r) { s[r][n] -= dl; o[r][n] *= alpha; } } \
        EXP_PACK(); \
        PRE; \
      } \
      lacc[0] = ln[0]; lacc[1] = ln[1]; SCHED(); } while (0)
  {
#pragma unroll
    for (int f = 0; f < 8; ++f) RD_K(f);
#pragma unroll
    for (int d = 0; d < 8; d += 2) asm volatile("" : "+v"(o[d][0]), "+v"(o[d][1]), "+v"(o[d + 1][0]), "+v"(o[d + 1][1]));
    f32x4 u[2][2];
    const f32x4 z4 = {0.f, 0.f, 0.f, 0.f};
#pragma unroll
    for (int i = 0; i < 16; ++i) { const int r = i >> 3, n = (i >> 2) & 1, k = i & 3; u[r][n] = MFMA16(Kf[4 * r + k], qf[2 * k + n], k == 0 ? z4 : u[r][n]); }
#pragma unroll
    for (int n = 0; n < 2; ++n) { const float m = -col_max(MX4(MX3(u[0][n][0], u[0][n][1], __builtin_fmaxf(u[0][n][2], u[0][n][3])), u[1][n])); negm[n] = (f32x4){m, m, m, m}; }
    asm volatile("" : "+v"(negm[0]), "+v"(negm[1]));
    SCHED();
    QK_SEG(8, WAIT_V(1));
  }
  const char* kvsrc = (wr ? kb_g + 65536 : vb_g + 32768) + wc * 8192 - 1024;
  unsigned kvo[2][2];
#pragma unroll
  for (int par = 0; par < 2; ++par) {
    kvo[par][0] = (unsigned)((lane >> 3) * 256 + (((lane & 7) ^ (wr ? 4 * par + (lane >> 4) : (lane >> 4) << 1)) << 4) + 1024);
    kvo[par][1] = kvo[par][0] + 128 - 1024;
  }
#pragma unroll 1
  for (int c = 0; c < 3; ++c) {
    {
      float tv[8], ev[8];
#pragma unroll
      for (int e = 0; e < 4; ++e) { tv[2 * e] = bf2f(hq[0][e] & 0xffffu); tv[2 * e + 1] = U2F(hq[0][e] & 0xffff0000u); }
      SCHED();
#pragma unroll
      for (int e = 0; e < 8; ++e) ev[e] = __builtin_amdgcn_exp2f(tv[e]);
      SCHED();
#pragma unroll
      for (int e = 0; e < 8; ++e) ev[e] = ev[e] * -1.4426950408889634f - 1.4426950408889634f;
      SCHED();
#pragma unroll
      for (int e = 0; e < 8; ++e) ev[e] = __builtin_amdgcn_rcpf(ev[e]);
      SCHED();
      ST128((u32x4){pk2(ev[0] * tv[0], ev[1] * tv[1]), pk2(ev[2] * tv[2], ev[3] * tv[3]), pk2(ev[4] * tv[4], ev[5] * tv[5]), pk2(ev[6] * tv[6], ev[7] * tv[7])}, hqo, c == 2 ? 16384 : c * 2048);
    }
    hq[0] = hq[1]; hq[1] = hq[2];
    SCHED();
    if (__builtin_expect(c + wr < 3, 1)) {
      const int slot = wr ? (c == 0 ? SLOT_B : SLOT_C) : (c == 0 ? SLOT_D : (c == 1 ? SLOT_E : SLOT_A));
#pragma unroll
      for (int hf = 0; hf < 2; ++hf)
        asm volatile("s_mov_b32 m0, %5\n\ts_nop 0\n\t"
                     "global_load_lds_dwordx4 %0, %4\n\tglobal_load_lds_dwordx4 %1, %4 offset:1024\n\t"
                     "global_load_lds_dwordx4 %2, %4 offset:2048\n\tglobal_load_lds_dwordx4 %3, %4 offset:3072"
                     ::"v"(kvo[0][0]), "v"(kvo[0][1]), "v"(kvo[1][0]), "v"(kvo[1][1]), "s"(kvsrc + c * 32768 + hf * 4096), "s"(lds0 + slot + wc * 8192 + hf * 4096) : "memory");
    }
    TOUCH(c + 1);
    SOFTMAX(PREF_V());
    { const int dk = (c == 0) ? SLOT_A - SLOT_E : ((c == 1) ? SLOT_B - SLOT_A : SLOT_C - SLOT_B); kb[0] += dk; kb[1] += dk; asm volatile("" : "+v"(kb[0]), "+v"(kb[1])); }
    BAR(); SCHED();
    __builtin_amdgcn_s_setprio(1);
    PV_SEG(true, );
    QK_SEG(LA, WAIT_V(1); if (c == 2) WAIT_V(0));
    { const int dv = (c == 1) ? SLOT_E - SLOT_D : 32768;
#pragma unroll
      for (int f = 0; f < 4; ++f) vb[f] += dv; }
  }
  RELANE();
  if (wid == 4 && lane == 0) __builtin_amdgcn_raw_buffer_store_b32(tag, wrs, 0, WS_HFLAG + (rb * 32 + hd) * 128, 16);
  SOFTMAX(PREF_V());
  {
    asm volatile("s_waitcnt vmcnt(0)" :: "v"(pft) : "memory");
    SCHED();
    if (wr == 0) BAR();
    SCHED();
    __builtin_amdgcn_s_setprio(1);
    const unsigned oo = (unsigned)(WS_O + 2 * hd * 262144 + (rb * 16 + (wr ^ sb) * 8 + wc * 2) * 2048 + lane * 16);
    float inv[2] = {0.f, 0.f};
    u32x4 ow;
#define O_PIECE(j_) do { const int dp = (j_) >> 3, n_ = ((j_) >> 2) & 1, q_ = (j_) & 3; \
      ow[q_] = pk2(o[2 * dp + (q_ >> 1)][n_][2 * (q_ & 1)] * inv[n_], o[2 * dp + (q_ >> 1)][n_][2 * (q_ & 1) + 1] * inv[n_]); asm volatile("" : "+v"(ow)); \
      if (q_ == 3) ST128(ow, oo + n_ * 2048 + (dp & 1) * 1024, (dp >> 1) * 262144); } while (0)
    PV_SEG(false, if (i == 4) { inv[0] = __builtin_amdgcn_rcpf(lacc[0][0]); inv[1] = __builtin_amdgcn_rcpf(lacc[1][0]); } if (i >= 16 && (i & 1)) O_PIECE(i >= 16 ? (i - 16) >> 1 : 0));
#pragma unroll
    for (int j = 24; j < 32; ++j) O_PIECE(j);
    __builtin_amdgcn_s_setprio(0);
  }
  RELANE();
  const int isG = (idx >> 1) & 1, kh = xcd & 1, tm = 4 * (xcd >> 1) + ((idx >> 1) & 2) + (idx & 1), tn = 2 * (idx >> 3) + kh;
  const int kd = 1, sd2 = kh ? -kd : kd;
  const int npw = (isG ? 48 : 56) + 2 * sd2;
  const int k2c = ((npw + 15) % 6 < 4) ? K2_C : 0;
#define O_FLAG() do { if (wid == 0 && lane == 0) __builtin_amdgcn_raw_buffer_store_b32(tag, wrs, 0, WS_OFLAG + (rb * 32 + hd) * OFS, 16); } while (0)
#define POLL32(off_, slp) do { for (unsigned spins = 0; spins < 2000000u; ++spins) { \
      if (__builtin_amdgcn_ballot_w64(__builtin_amdgcn_raw_buffer_load_b32(wrs, (off_), 0, 16) != tag) == 0ull) break; \
      __builtin_amdgcn_s_sleep(slp); asm volatile("" ::: "memory"); } \
    asm volatile("" ::: "memory"); } while (0)
  const int c8 = lane & 15, r0 = wc * 4 + (lane >> 4);
  const unsigned goff = (unsigned)((tm * 128 + r0) * 2048 + tn * 256 + c8 * 16);
  const lptr cb = lds + k2c + r0 * 512;
#define G_FIN() do { f32x4 a0[4], a1[4]; \
      _Pragma("unroll") for (int i = 0; i < 4; ++i) { a0[i] = *(const LAS f32x4*)(cb + (wr * 4 + i) * 8192 + (((2 * c8) ^ (r0 & 7)) << 4)); a1[i] = *(const LAS f32x4*)(cb + (wr * 4 + i) * 8192 + (((2 * c8 + 1) ^ (r0 & 7)) << 4)); } \
      _Pragma("unroll") for (int i = 0; i < 4; ++i) \
        ST128((u32x4){pk2(silu(a0[i][0]), silu(a0[i][1])), pk2(silu(a0[i][2]), silu(a0[i][3])), pk2(silu(a1[i][0]), silu(a1[i][1])), pk2(silu(a1[i][2]), silu(a1[i][3]))}, goff + i * 32768, WS_G + wr * 131072); \
      WAIT_V(0); BAR(); } while (0)
  const unsigned f1b = isG ? (unsigned)(WS_HFLAG + ((tm >> 1) * 32 + 16 + (lane & 15)) * 128) : (unsigned)(WS_OFLAG + ((tm >> 1) * 32 + 16 + (lane & 15)) * OFS);
  const unsigned f1 = isG ? f1b : (unsigned)(WS_EFLAG + ((tm >> 1) * 16 + (tm & 1) * 8 + kh * 4 + ((lane >> 2) & 3)) * 64 + (lane & 3) * 4);
  if (wr == 0) {
    const char* ab = ws + (isG ? WS_H : WS_O) + tm * 16384 + wc * 4096;
    const char* wbp = (isG ? w2 : wo) + (tn >> 1) * 512 - 3072 + wc * 16384;
    const unsigned voffA0 = lane * 16, voffA1 = lane * 16 + 1024;
    unsigned voffW[4];
    {
      const int r = lane >> 3, c = lane & 7;
      const unsigned lp = (unsigned)(r * 2048 + ((c ^ (((r >> 1) & 3) << 1)) << 4) + 3072);
      voffW[0] = lp + kh * 256; voffW[1] = voffW[0] + 128 - 1024; voffW[2] = lp + (kh ^ 1) * 256 - 2048; voffW[3] = voffW[2] + 128 - 1024;
    }
    const char *ap, *wp;
    unsigned dst = lds0;
#define K2_RUN(k_) do { ap = ab + (size_t)(k_) * 262144; wp = wbp + (size_t)(k_) * 131072; } while (0)
#define K2_ISSUE(WIDE, ks) do { \
      asm volatile("s_mov_b32 m0, %3\n\ts_nop 0\n\tglobal_load_lds_dwordx4 %0, %2 sc1\n\tglobal_load_lds_dwordx4 %1, %2 offset:1024 sc1" \
                   ::"v"(voffA0), "v"(voffA1), "s"(ap + (ks) * 1024), "s"(dst + wc * 2048) : "memory"); \
      if (WIDE) GLDS4(voffW[0], voffW[1], voffW[2], voffW[3], wp + (ks) * 65536, dst + 8192 + wc * 4096); \
      else GLDS2(voffW[0], voffW[1], wp + (ks) * 65536, dst + 8192 + wc * 4096); \
      dst = (dst == lds0 + 5 * K2_STG) ? lds0 : dst + K2_STG; \
      if (ks) { ap += 262144; wp += 131072; } } while (0)
#define K2_STEP(WIDE, ks, EVENT) do { WAIT_V((WIDE) ? 18 : 12); BAR(); EVENT; K2_ISSUE(WIDE, ks); } while (0)
#define K2_TILES(WIDE, n_) do { _Pragma("unroll 1") for (int i_ = 0; i_ < (n_); ++i_) { K2_STEP(WIDE, 0, ); K2_STEP(WIDE, 1, ); } } while (0)
#define LOOK(f_) asm volatile("buffer_load_dword %0, %1, %2, 0 offen sc1" : "+v"(pol) : "v"(f_), "s"(wrs) : "memory")
    const unsigned f2 = f1b - 16 * (isG ? 128 : OFS);
    const unsigned f3 = (unsigned)(WS_PFLAG + (bid ^ 1) * 128);
    u32x4 xh[8];
#define XH_LD(i) asm volatile("global_load_dwordx4 %0, %1, %2" : "=v"(xh[i]) : "v"(goff), "s"(xn + (i) * 32768) : "memory")
    WAIT_V(0);
    if (isG && lane == 0) __builtin_amdgcn_raw_buffer_store_b32(tag, wrs, 0, WS_EFLAG + (rb * 16 + hd - 16) * 64 + wc * 4, 16);
    POLL32(f1, 4);
    K2_RUN(isG ? 32 + 12 * kh : 32 + 16 * (tm & 1) + 8 * kh);
    K2_ISSUE(1, 0); K2_ISSUE(1, 1);
    WAIT_V(6); BAR();
    K2_ISSUE(1, 0); K2_ISSUE(1, 1); K2_ISSUE(1, 0);
    unsigned pol = 0;
#define CHECK(f_) do { asm volatile("" : "+v"(pol)); if (__builtin_amdgcn_ballot_w64(pol != tag) != 0ull) POLL32(f_, 4); } while (0)
    K2_STEP(1, 1, );
    K2_TILES(1, 2);
    O_FLAG();
    K2_TILES(1, 1);
    LOOK(f1b);
    K2_TILES(1, 2);
    if (!isG) K2_RUN(48 - 16 * (tm & 1) + 6 * kh);
    K2_STEP(1, 0, CHECK(f1b)); K2_STEP(1, 1, );
    K2_TILES(1, isG ? 1 : 3);
    LOOK(f2);
    K2_TILES(1, 2);
    K2_RUN(((isG ? 12 : 14) + kd) * kh);
    K2_STEP(1, 0, CHECK(f2));
    K2_STEP(1, 1, );
#pragma unroll
    for (int i = 0; i < 8; ++i) { K2_STEP(1, 0, XH_LD(i)); K2_STEP(1, 1, ); }
    K2_TILES(1, (isG ? 3 : 5) + sd2);
    asm volatile("" : "+v"(xh[0]), "+v"(xh[1]), "+v"(xh[2]), "+v"(xh[3]), "+v"(xh[4]), "+v"(xh[5]), "+v"(xh[6]), "+v"(xh[7]));
    K2_RUN(isG ? 24 : 60 - 16 * (tm & 1));
    K2_TILES(0, 4);
    K2_RUN(28);
    K2_STEP(0, 0, );
    K2_STEP(0, 1, if (wid == 0 && lane == 0) __builtin_amdgcn_raw_buffer_store_b32(tag, wrs, 0, WS_PFLAG + bid * 128, 16));
    K2_STEP(0, 0, ); K2_STEP(0, 1, );
    const unsigned f4 = (lane & 1) ? (unsigned)(WS_GFLAG + (tm * 8 + tn) * 128) : f3;
    K2_STEP(0, 0, LOOK(f4)); K2_STEP(0, 1, ); K2_STEP(0, 0, );
    WAIT_V(12);
    asm volatile("" : "+v"(pol));
    const unsigned long long miss = __builtin_amdgcn_ballot_w64(pol != tag);
    if (wid == 0 && (miss & 1ull)) POLL32(f3, 2);
    BAR();
    K2_ISSUE(0, 1);
    WAIT_V(12); BAR();
    WAIT_V(8); BAR();
    if (isG) {
      WAIT_V(4); BAR();
      WAIT_V(0); BAR();
      BAR();
      BAR();
    } else {
      u32x4 gv[8];
      const __amdgpu_buffer_rsrc_t ors = __builtin_amdgcn_make_buffer_rsrc(out, 0, 2048 * 2048, 0x00020000);
#define LD8(dst_, rs_, so_, pol_) asm volatile("buffer_load_dwordx4 %0, %8, %10, %11 offen" pol_ "\n\tbuffer_load_dwordx4 %1, %9, %10, %11 offen" pol_ "\n\t" \
        "buffer_load_dwordx4 %2, %8, %10, %12 offen" pol_ "\n\tbuffer_load_dwordx4 %3, %9, %10, %12 offen" pol_ "\n\t" \
        "buffer_load_dwordx4 %4, %8, %10, %13 offen" pol_ "\n\tbuffer_load_dwordx4 %5, %9, %10, %13 offen" pol_ "\n\t" \
        "buffer_load_dwordx4 %6, %8, %10, %14 offen" pol_ "\n\tbuffer_load_dwordx4 %7, %9, %10, %14 offen" pol_ \
        : "=&v"(dst_[0]), "=&v"(dst_[1]), "=&v"(dst_[2]), "=&v"(dst_[3]), "=&v"(dst_[4]), "=&v"(dst_[5]), "=&v"(dst_[6]), "=&v"(dst_[7]) \
        : "v"(goff), "v"(goff + 32768), "s"(rs_), "s"((so_)), "s"((so_) + 65536), "s"((so_) + 131072), "s"((so_) + 196608) : "memory")
      if (miss & 2ull) {
        WAIT_V(4); BAR(); WAIT_V(0); BAR(); BAR();
        POLL32((unsigned)(WS_GFLAG + (tm * 8 + tn) * 128), 1);
      }
      LD8(gv, wrs, WS_G, " sc1");
      if (!(miss & 2ull)) { WAIT_V(12); BAR(); WAIT_V(8); BAR(); BAR(); }
      float xf[8][8];
#pragma unroll
      for (int i = 0; i < 8; ++i) {
#pragma unroll
        for (int e = 0; e < 4; ++e) { xf[i][2 * e] = bf2f(xh[i][e] & 0xffffu); xf[i][2 * e + 1] = U2F(xh[i][e] & 0xffff0000u); }
        asm volatile("" : "+v"(xf[i][0]), "+v"(xf[i][1]), "+v"(xf[i][2]), "+v"(xf[i][3]), "+v"(xf[i][4]), "+v"(xf[i][5]), "+v"(xf[i][6]), "+v"(xf[i][7]));
      }
      asm volatile("s_waitcnt vmcnt(0)" : "+v"(gv[0]), "+v"(gv[1]), "+v"(gv[2]), "+v"(gv[3]), "+v"(gv[4]), "+v"(gv[5]), "+v"(gv[6]), "+v"(gv[7]) :: "memory");
      float gf2[8][8];
#pragma unroll
      for (int i = 0; i < 8; ++i) {
#pragma unroll
        for (int e = 0; e < 4; ++e) { gf2[i][2 * e] = bf2f(gv[i][e] & 0xffffu); gf2[i][2 * e + 1] = U2F(gv[i][e] & 0xffff0000u); }
        asm volatile("" : "+v"(gf2[i][0]), "+v"(gf2[i][1]), "+v"(gf2[i][2]), "+v"(gf2[i][3]), "+v"(gf2[i][4]), "+v"(gf2[i][5]), "+v"(gf2[i][6]), "+v"(gf2[i][7]));
      }
      BAR();
      f32x4 a0[8], a1[8];
#pragma unroll
      for (int i = 0; i < 8; ++i) { a0[i] = *(const LAS f32x4*)(cb + i * 8192 + (((2 * c8) ^ (r0 & 7)) << 4)); a1[i] = *(const LAS f32x4*)(cb + i * 8192 + (((2 * c8 + 1) ^ (r0 & 7)) << 4)); }
      SCHED();
#pragma unroll
      for (int i = 0; i < 8; ++i) {
        const u32x4 w = {pk2(xf[i][0] + a0[i][0] * gf2[i][0], xf[i][1] + a0[i][1] * gf2[i][1]), pk2(xf[i][2] + a0[i][2] * gf2[i][2], xf[i][3] + a0[i][3] * gf2[i][3]),
                         pk2(xf[i][4] + a1[i][0] * gf2[i][4], xf[i][5] + a1[i][1] * gf2[i][5]), pk2(xf[i][6] + a1[i][2] * gf2[i][6], xf[i][7] + a1[i][3] * gf2[i][7])};
        __builtin_amdgcn_raw_buffer_store_b128(w, ors, goff + i * 32768, 0, 16);
      }
    }
  } else {
    const int wm = wc >> 1, wn = wc & 1;
    lptr w2b[4];
#pragma unroll
    for (int ft = 0; ft < 4; ++ft) w2b[ft] = lds + 8192 + wn * 1024 + (g >> 1) * 4096 + (4 * (g & 1) + q4) * 128 + 32 * (ft ^ (2 * (g & 1) + (q4 >> 1))) + 8 * p4;
    lptr ar = lds + wm * 4096 + lane * 16;
    f32x4 ac2[8][4];
#pragma unroll
    for (int m = 0; m < 8; ++m)
#pragma unroll
      for (int n = 0; n < 4; ++n) ac2[m][n] = (f32x4){0.f, 0.f, 0.f, 0.f};
    bf16x8 Af[2][4];
    bf16x4 Wr[4][2];
    int slot = 0, dlt = 0;
#define K2_RW(f, hf) Wr[(f) & 3][hf] = __builtin_amdgcn_ds_read_tr16_b64_v4bf16((LAS bf16x4*)(w2b[(f) & 3] + (((f) >> 2) & 1) * 2048 + (hf) * 8192))
#define K2_W(m) __builtin_shufflevector(Wr[(m) & 3][0], Wr[(m) & 3][1], 0, 1, 2, 3, 4, 5, 6, 7)
#define K2_NEXT() do { dlt = (slot == 5) ? -5 * K2_STG : K2_STG; slot = (slot == 5) ? 0 : slot + 1; } while (0)
#define K2_ADV(x) do { x += dlt; asm volatile("" : "+v"(x)); } while (0)
#define K2_WIDE(st) do { _Pragma("unroll") for (int i = 0; i < 32; ++i) { const int m = i >> 2, q = i & 3; \
        const int n_ = (m & 1) ? 3 - q : q; ac2[m][n_] = MFMA16(K2_W(m), Af[st][n_], ac2[m][n_]); SCHED(); \
        if (q < 2) K2_RW((m + 3) & 7, q); \
        if (q == 2 && m >= 3 && m < 7) Af[(st) ^ 1][m - 3] = ldb128(ar + (m - 3) * 1024); \
        if (i == 2) K2_ADV(ar); \
        if (i == 6) K2_ADV(w2b[0]); \
        if (i == 10) K2_ADV(w2b[1]); \
        if (i == 11) BAR(); \
        if (i == 19) K2_ADV(w2b[2]); \
        if (i == 23) K2_ADV(w2b[3]); \
        if (i == 30) K2_NEXT(); \
        SCHED(); } } while (0)
#define K2_NARROW(st) do { _Pragma("unroll") for (int i = 0; i < 16; ++i) { const int m = i >> 2, q = i & 3; \
        const int n_ = (m & 1) ? 3 - q : q; ac2[m][n_] = MFMA16(K2_W(m), Af[st][n_], ac2[m][n_]); SCHED(); \
        if (q < 2) K2_RW((m + 3) & 3, q); \
        if (q >= 2 && (m == 1 || m == 2)) Af[(st) ^ 1][2 * m + q - 4] = ldb128(ar + (2 * m + q - 4) * 1024); \
        if (i == 2) { K2_ADV(ar); K2_ADV(w2b[0]); } \
        if (i == 3) BAR(); \
        if (i == 5) K2_ADV(w2b[1]); \
        if (i == 9) K2_ADV(w2b[2]); \
        if (i == 14) K2_ADV(w2b[3]); \
        if (i == 15) K2_NEXT(); \
        SCHED(); } } while (0)
    K2_NEXT();
    BAR();
#pragma unroll
    for (int i = 0; i < 4; ++i) { if (i < 3) { K2_RW(i, 0); K2_RW(i, 1); } Af[0][i] = ldb128(ar + i * 1024); SCHED(); }
#pragma unroll 1
    for (int p = 0; p < 4; p += 2) { K2_WIDE(0); K2_WIDE(1); }
    WAIT_V(0);
#pragma unroll 1
    for (int p = 4; p < npw; p += 2) { K2_WIDE(0); K2_WIDE(1); }
    const unsigned po = (unsigned)(WS_P + wc * 8192 + lane * 16);
#pragma unroll
    for (int i = 0; i < 8; ++i) {
      const f32x4 a = ac2[4 + (i >> 1)][2 * (i & 1)], b = ac2[4 + (i >> 1)][2 * (i & 1) + 1];
      ST128((u32x4){pk2(a[0], a[1]), pk2(a[2], a[3]), pk2(b[0], b[1]), pk2(b[2], b[3])}, po + i * 1024, bid * 32768);
    }
    SCHED();
#pragma unroll 1
    for (int p = 0; p < 4; p += 2) { K2_NARROW(0); K2_NARROW(1); }
    WAIT_V(0);
#pragma unroll 1
    for (int p = 4; p < 10; p += 2) { K2_NARROW(0); K2_NARROW(1); }
    K2_NARROW(0);
    u32x4 pv[8];
#pragma unroll
    for (int i = 0; i < 8; ++i) pv[i] = __builtin_amdgcn_raw_buffer_load_b128(wrs, po + i * 1024 + (bid ^ 1) * 32768, 0, 16);
    SCHED();
    K2_NARROW(1);
#pragma unroll 1
    for (int p = 12; p < 16; p += 2) { K2_NARROW(0); K2_NARROW(1); }
    const lptr cw = lds + k2c + (wm * 64 + t) * 512;
#pragma unroll
    for (int m = 0; m < 4; ++m)
#pragma unroll
      for (int n = 0; n < 4; ++n) {
        const unsigned lo = pv[2 * m + (n >> 1)][2 * (n & 1)], hi = pv[2 * m + (n >> 1)][2 * (n & 1) + 1];
        *(LAS f32x4*)(cw + n * 8192 + (((wn * 16 + m * 4 + g) ^ (t & 7)) << 4)) = ac2[m][n] + (f32x4){bf2f(lo & 0xffffu), U2F(lo & 0xffff0000u), bf2f(hi & 0xffffu), U2F(hi & 0xffff0000u)};
      }
    WAIT_L(0); BAR();
    if (isG) G_FIN();
    __builtin_amdgcn_endpgm();
  }
  if (isG) {
      G_FIN();
      if (wid == 0 && lane == 0) __builtin_amdgcn_raw_buffer_store_b32(tag, wrs, 0, WS_GFLAG + (tm * 8 + tn) * 128, 16);
      if (bid == 16 && wid == 0) {
        for (unsigned spins = 0; spins < 2000000u; ++spins) {
          bool ok = true;
#pragma unroll
          for (int i = 0; i < 4; ++i) ok = ok && __builtin_amdgcn_raw_buffer_load_b32(wrs, (unsigned)((lane + i * 64) * 128), WS_HFLAG, 16) == tag;
          if (__builtin_amdgcn_ballot_w64(!ok) == 0ull) break;
          __builtin_amdgcn_s_sleep(4);
          asm volatile("" ::: "memory");
        }
        asm volatile("" ::: "memory");
        if (lane == 0) __builtin_amdgcn_raw_buffer_store_b32(tag, wrs, 0, 0, 16);
      }
    }
}

extern "C" void kernel_launch(void* const* d_in, const int* in_sizes, int n_in, void* d_out, int out_size, void* d_ws, size_t ws_size, hipStream_t stream) {
  const char* x = (const char*)d_in[0]; const char* kc = (const char*)d_in[1]; const char* vc = (const char*)d_in[2];
  const char* wq = (const char*)d_in[3]; const char* wo = (const char*)d_in[4]; const char* w1 = (const char*)d_in[6]; const char* w2 = (const char*)d_in[7];
  (void)hipFuncSetAttribute((const void*)mega_kernel, hipFuncAttributeMaxDynamicSharedMemorySize, K1_LDS);
  mega_kernel<<<256, 512, K1_LDS, stream>>>(x, kc, vc, wq, w1, wo, w2, (char*)d_ws, (char*)d_out);
}
```

```cpp
#include <hip/hip_runtime.h>
#include <stdint.h>

typedef __bf16 bf16x2 __attribute__((ext_vector_type(2)));
typedef __bf16 bf16x4 __attribute__((ext_vector_type(4)));
typedef __bf16 bf16x8 __attribute__((ext_vector_type(8)));
typedef float f32x2 __attribute__((ext_vector_type(2)));
typedef float f32x4 __attribute__((ext_vector_type(4)));
typedef unsigned u32x2 __attribute__((ext_vector_type(2)));
typedef unsigned u32x4 __attribute__((ext_vector_type(4)));
#define LAS __attribute__((address_space(3)))
typedef LAS char* lptr;
#if defined(__HIP_DEVICE_COMPILE__)
#define NOPK __attribute__((target("no-packed-fp32-ops")))
#else
#define NOPK
#endif
#define DI NOPK __device__ __forceinline__
#define F2U(x) __builtin_bit_cast(unsigned, (float)(x))
#define U2F(x) __builtin_bit_cast(float, (unsigned)(x))
#define TID ((int)__builtin_amdgcn_workitem_id_x())
#define BID ((int)__builtin_amdgcn_workgroup_id_x())
#define SCHED() __builtin_amdgcn_sched_barrier(0)
#define BAR() asm volatile("s_barrier" ::: "memory")
#define WAIT_V(n) asm volatile("s_waitcnt vmcnt(%0)" :: "n"(n) : "memory")
#define WAIT_L(n) do { asm volatile("" ::: "memory"); __builtin_amdgcn_s_waitcnt(0xC07F | ((n) << 8)); asm volatile("" ::: "memory"); } while (0)
#define MFMA16(a, b, c) __builtin_amdgcn_mfma_f32_16x16x32_bf16(a, b, c, 0, 0, 0)

DI void glds(unsigned voff, const char* sbase, unsigned ldsaddr) {
  asm volatile("s_mov_b32 m0, %2\n\ts_nop 0\n\tglobal_load_lds_dwordx4 %0, %1" ::"v"(voff), "s"(sbase), "s"(ldsaddr) : "memory");
}
DI unsigned pk2(float lo, float hi) { f32x2 v = {lo, hi}; return __builtin_bit_cast(unsigned, __builtin_convertvector(v, bf16x2)); }
DI float bf2f(unsigned b) { return U2F(b << 16); }
DI bf16x8 ldb128(lptr p) { return *(const LAS bf16x8*)p; }
DI bf16x8 ldtr2(lptr p, int d) {
  bf16x4 a = __builtin_amdgcn_ds_read_tr16_b64_v4bf16((LAS bf16x4*)p);
  bf16x4 b = __builtin_amdgcn_ds_read_tr16_b64_v4bf16((LAS bf16x4*)(p + d));
  return __builtin_shufflevector(a, b, 0, 1, 2, 3, 4, 5, 6, 7);
}
DI void ST128_(__amdgpu_buffer_rsrc_t r, u32x4 v, unsigned voff, int soff) { __builtin_amdgcn_raw_buffer_store_b128(v, r, voff + (unsigned)soff, 0, 16); }
DI float silu(float v) { return v * __builtin_amdgcn_rcpf(1.0f + __builtin_amdgcn_exp2f(v * -1.4426950408889634f)); }

template <int CTRL> DI float dpp_add(float v) { return v + U2F(__builtin_amdgcn_update_dpp(0u, F2U(v), CTRL, 0xf, 0xf, true)); }
DI float half_sum(float v) {
  v = dpp_add<0xB1>(v); v = dpp_add<0x4E>(v); v = dpp_add<0x141>(v); v = dpp_add<0x140>(v);
  auto rr = __builtin_amdgcn_permlane16_swap(F2U(v), F2U(v), false, false);
  return U2F(rr[0]) + U2F(rr[1]);
}
DI float col_max(float v) {
  auto a = __builtin_amdgcn_permlane16_swap(F2U(v), F2U(v), false, false);
  v = __builtin_fmaxf(U2F(a[0]), U2F(a[1]));
  auto b = __builtin_amdgcn_permlane32_swap(F2U(v), F2U(v), false, false);
  return __builtin_fmaxf(U2F(b[0]), U2F(b[1]));
}
typedef __attribute__((address_space(1))) unsigned long long gu64;

#define SX(b, h) (98304 + (b) * 32768 + (h) * 16384)
constexpr int SLOT_B = 0, SLOT_E = 32768, SLOT_A = 65536, SLOT_C = 98304, SLOT_D = 131072, K1_LDS = 163840;

constexpr int WS_HFLAG = 4096;
constexpr int WS_GFLAG = 65536;
constexpr int WS_OFLAG = 131072;
constexpr int OFS = 16;
constexpr int WS_EFLAG = 163840;
constexpr int WS_STATS = 1 << 20;
constexpr int WS_PFLAG = 196608;
constexpr int WS_G = 5 << 20, WS_O = 9 << 20, WS_H = 25 << 20, WS_P = 39 << 20, WS_END = 47 << 20;
constexpr int K2_STG = 24576, K2_C = 98304;

NOPK __global__ __launch_bounds__(512, 2) void mega_kernel(const char* __restrict__ xn, const char* __restrict__ kc, const char* __restrict__ vc,
                                                           const char* __restrict__ wq, const char* __restrict__ w1, const char* __restrict__ wo, const char* __restrict__ w2,
                                                           char* ws, char* __restrict__ out) {
  extern __shared__ __attribute__((aligned(16))) char shm[];
  const lptr lds = (lptr)shm;
  const unsigned lds0 = (unsigned)(uintptr_t)lds;
  const int wid = __builtin_amdgcn_readfirstlane(TID >> 6), wr = wid >> 2, wc = wid & 3;
  const int bid = BID, xcd = bid & 7, idx = bid >> 3, hd = (idx & 2) * 8 + 2 * xcd + (idx & 1), rb = idx >> 2;
  int lane, g, t, q4, p4;
#define RELANE() do { lane = __builtin_amdgcn_mbcnt_hi(~0u, __builtin_amdgcn_mbcnt_lo(~0u, 0u)); asm volatile("" : "+v"(lane)); g = lane >> 4; t = lane & 15; q4 = (lane >> 2) & 3; p4 = lane & 3; } while (0)
  RELANE();

  const bool light = (idx & 2) != 0;
  unsigned voffX[4], voffQ[2], voffH[2];
  {
    const int r = lane >> 3, c = lane & 7;
#pragma unroll
    for (int i = 0; i < 4; ++i) voffX[i] = (unsigned)((8 * i + r) * 2048 + ((c ^ (4 * (i & 1) + (r >> 1))) << 4) - i * 1024);
    const int k = 32 * (wid >> 2) + 16 * (wid & 1) + 4 * ((wid >> 1) & 1) + 8 * (r >> 2) + (r & 3);
    const int cw = c ^ (((r >> 1) & 3) << 1);
    voffQ[0] = (unsigned)(k * 8192 + cw * 16 + 1024); voffQ[1] = voffQ[0] + 128 - 1024;
    voffH[0] = (unsigned)(k * 7168 + 128 + (light ? (cw & 3) : cw) * 16 + 1024);
    voffH[1] = (unsigned)(k * 7168 + cw * 16);
  }
  const char* xb = xn + (size_t)rb * 256 * 2048 + wid * 65536;
  const char* qb = wq + hd * 256 - 1024;
  const char* hb = w1 + (light ? 4096 + 192 * (hd - 16) : 256 * hd) - 1024;
  const bool full = wr == 1 || !light;
  const int sb = hd >= 24;
#define GLDS2(v0_, v1_, sb_, ld_) asm volatile("s_mov_b32 m0, %3\n\ts_nop 0\n\tglobal_load_lds_dwordx4 %0, %2\n\tglobal_load_lds_dwordx4 %1, %2 offset:1024" \
    ::"v"(v0_), "v"(v1_), "s"(sb_), "s"(ld_) : "memory")
#define GLDS4(v0_, v1_, v2_, v3_, sb_, ld_) asm volatile("s_mov_b32 m0, %5\n\ts_nop 0\n\tglobal_load_lds_dwordx4 %0, %4\n\tglobal_load_lds_dwordx4 %1, %4 offset:1024\n\t" \
    "global_load_lds_dwordx4 %2, %4 offset:2048\n\tglobal_load_lds_dwordx4 %3, %4 offset:3072" ::"v"(v0_), "v"(v1_), "v"(v2_), "v"(v3_), "s"(sb_), "s"(ld_) : "memory")
#define ST_X(b, kt) GLDS4(voffX[0], voffX[1], voffX[2], voffX[3], xb + (kt) * 128, lds0 + SX(b, 0) + wid * 4096)
#define ST_W0(wo_, kt) GLDS2(voffQ[0], voffQ[1], qb + (size_t)(kt) * 524288, lds0 + (wo_) + wid * 2048)
#define ST_W1(wo_, kt) GLDS2(voffH[0], voffH[1], hb + (size_t)(kt) * 458752, lds0 + (wo_) + 16384 + wid * 2048)

  lptr wb[4], xr[2];
#pragma unroll
  for (int m = 0; m < 4; ++m) wb[m] = lds + wr * 1024 + (g >> 1) * 2048 + (4 * (g & 1) + q4) * 128 + 32 * ((m ^ wc) ^ (2 * (g & 1) + (q4 >> 1))) + 8 * p4;
#pragma unroll
  for (int ks = 0; ks < 2; ++ks) xr[ks] = lds + 98304 + (wc * 32 + t) * 128 + 16 * ((4 * ks + g) ^ (t >> 1));
#define LDW(dst, h) do { _Pragma("unroll") for (int m = 0; m < 4; ++m) _Pragma("unroll") for (int k = 0; k < 2; ++k) dst[m][k] = ldtr2(wb[m] + (h) * 16384 + k * 8192, 4096); } while (0)
#define LDX(dst, b, h) do { _Pragma("unroll") for (int n = 0; n < 2; ++n) _Pragma("unroll") for (int k = 0; k < 2; ++k) dst[n][k] = ldb128(xr[k] + (SX(b, h) - 98304) + n * 2048); } while (0)

  f32x4 acc[2][2][4][2];
#pragma unroll
  for (int a = 0; a < 2; ++a)
#pragma unroll
    for (int b = 0; b < 2; ++b)
#pragma unroll
      for (int m = 0; m < 4; ++m)
#pragma unroll
        for (int n = 0; n < 2; ++n) acc[a][b][m][n] = (f32x4){0.f, 0.f, 0.f, 0.f};
  bf16x8 At[4][2], B0[2][2], B1[2][2];

#define MM1(ai, m, k, j) do { if ((j) < 2) acc[ai][0][m][(j) & 1] = MFMA16(At[m][k], B0[(j) & 1][k], acc[ai][0][m][(j) & 1]); else acc[ai][1][m][(j) & 1] = MFMA16(At[m][k], B1[(j) & 1][k], acc[ai][1][m][(j) & 1]); SCHED(); } while (0)
#define MM8(ai, m) do { _Pragma("unroll") for (int q = 0; q < 4; ++q) { MM1(ai, m, q & 1, ((m) & 1) ? 3 - q : q); MM1(ai, m, (q & 1) ^ 1, ((m) & 1) ? 3 - q : q); } } while (0)
#define MMA2(ai, CLOSE) do { __builtin_amdgcn_s_setprio(1); \
    cs[ai] = MFMA16(At[0][1], ones, cs[ai]); SCHED(); cs[ai] = MFMA16(At[0][0], ones, cs[ai]); SCHED(); MM8(ai, 0); asm volatile("" : "+v"(cs[ai])); MM8(ai, 1); \
    if ((ai) == 0 || full) { MM8(ai, 2); MM8(ai, 3); } \
    SCHED(); CLOSE; __builtin_amdgcn_s_setprio(0); SCHED(); } while (0)
  char* const stats = ws + WS_STATS;
  const __amdgpu_buffer_rsrc_t wrs = __builtin_amdgcn_make_buffer_rsrc(ws, 0, WS_END, 0x00020000);
  f32x4 cs[2] = {{0.f, 0.f, 0.f, 0.f}, {0.f, 0.f, 0.f, 0.f}};
  bf16x8 ones = __builtin_bit_cast(bf16x8, (u32x4){0x3f803f80u, 0x3f803f80u, 0x3f803f80u, 0x3f803f80u});
#define PIN_SETUP() do { asm volatile("" : "+v"(ones), "+v"(cs[0]), "+v"(cs[1]), "+v"(wb[0]), "+v"(wb[1]), "+v"(wb[2]), "+v"(wb[3]), "+v"(xr[0]), "+v"(xr[1])); \
    _Pragma("unroll") for (int a = 0; a < 2; ++a) _Pragma("unroll") for (int b = 0; b < 2; ++b) \
      asm volatile("" : "+v"(acc[a][b][0][0]), "+v"(acc[a][b][0][1]), "+v"(acc[a][b][1][0]), "+v"(acc[a][b][1][1]), "+v"(acc[a][b][2][0]), "+v"(acc[a][b][2][1]), "+v"(acc[a][b][3][0]), "+v"(acc[a][b][3][1])); } while (0)
  unsigned tag;
  {
    if (wr == 0) {
      unsigned ep;
      ST_X(0, 0); ST_W0(0, 0);
      GLDS4(voffX[0], voffX[1], voffX[2], voffX[3], xb + 262144, lds0 + SX(0, 1) + wid * 4096);
      GLDS2(voffQ[0], voffQ[1], qb + 262144, lds0 + 8192 + wid * 2048);
      asm volatile("global_load_dword %0, %1, %2 sc1" : "=&v"(ep) : "v"(0), "s"(ws) : "memory");
      PIN_SETUP();
      asm volatile("s_waitcnt vmcnt(0)" : "+v"(ep) :: "memory");
      tag = __builtin_amdgcn_readfirstlane(ep) + 1u;
      BAR();
    } else {
      const int row = rb * 256 + hd * 8 + wc * 2 + (lane >> 5);
      u32x4 a[4];
      unsigned ep;
      asm volatile("global_load_dword %0, %1, %2 sc1" : "=&v"(ep) : "v"(0), "s"(ws) : "memory");
      asm volatile("global_load_dwordx4 %0, %4, %5\n\tglobal_load_dwordx4 %1, %4, %5 offset:512\n\tglobal_load_dwordx4 %2, %4, %5 offset:1024\n\tglobal_load_dwordx4 %3, %4, %5 offset:1536"
                   : "=&v"(a[0]), "=&v"(a[1]), "=&v"(a[2]), "=&v"(a[3]) : "v"(row * 2048 + (lane & 31) * 16), "s"(xn) : "memory");
      ST_W1(0, 0); GLDS2(voffH[0], voffH[1], hb - 229376, lds0 + 16384 + wc * 2048);
      PIN_SETUP();
      BAR();
      ST_W0(32768, 1); GLDS2(voffQ[0], voffQ[1], qb + 524288 - 262144, lds0 + 32768 + wc * 2048);
      ST_X(1, 1); GLDS4(voffX[0], voffX[1], voffX[2], voffX[3], xb - 262144 + 128, lds0 + SX(1, 0) + wc * 4096);
      asm volatile("s_waitcnt vmcnt(16)" : "+v"(ep), "+v"(a[0]), "+v"(a[1]), "+v"(a[2]), "+v"(a[3]) :: "memory");
      tag = __builtin_amdgcn_readfirstlane(ep) + 1u;
      float v[32];
#pragma unroll
      for (int i = 0; i < 16; ++i) { v[2 * i] = bf2f(a[i >> 2][i & 3] & 0xffffu); v[2 * i + 1] = U2F(a[i >> 2][i & 3] & 0xffff0000u); }
      float s0 = 0.f, s1 = 0.f, s2 = 0.f, s3 = 0.f;
#pragma unroll
      for (int i = 0; i < 8; ++i) { s0 += v[i]; s1 += v[8 + i]; s2 += v[16 + i]; s3 += v[24 + i]; }
      const float mean = half_sum((s0 + s1) + (s2 + s3)) * (1.0f / 1024.0f);
      s0 = s1 = s2 = s3 = 0.f;
#pragma unroll
      for (int i = 0; i < 8; ++i) { const float d0 = v[i] - mean, d1 = v[8 + i] - mean, d2 = v[16 + i] - mean, d3 = v[24 + i] - mean; s0 += d0 * d0; s1 += d1 * d1; s2 += d2 * d2; s3 += d3 * d3; }
      const float sd = __builtin_amdgcn_sqrtf(half_sum((s0 + s1) + (s2 + s3)) * (1.0f / 1024.0f) + 1e-5f);
      if ((lane & 31) < 2) __hip_atomic_store((gu64*)(stats + row * 16 + (lane & 1) * 8), ((unsigned long long)tag << 32) | F2U((lane & 1) ? sd : mean), __ATOMIC_RELAXED, __HIP_MEMORY_SCOPE_AGENT);
    }
  }
  if (wr == 1) BAR();
  SCHED();
  const int kvg = hd >> 3;
  const char* kb_g = kc + ((size_t)kvg * 2048 + 1536) * 256;
  const char* vb_g = vc + ((size_t)kvg * 2048 + 1536) * 256;
#define VOFFK ((unsigned)((lane >> 3) * 256 + (((lane & 7) ^ (4 * (wid & 1) + (lane >> 4))) << 4)))
#define VOFFV ((unsigned)((lane >> 3) * 256 + (((lane & 7) ^ ((lane >> 4) << 1)) << 4)))
#define ST_KV8(voff, gsrc, slot) do { glds(voff, (gsrc) + wid * 2048, lds0 + (slot) + wid * 2048); glds(voff, (gsrc) + wid * 2048 + 128, lds0 + (slot) + wid * 2048 + 1024); } while (0)
  int wprv = 65536, wnxt = 32768;
#define W_ADV() do { const int d_ = (wnxt == 0) ? -65536 : 32768, cur_ = wnxt - d_; wnxt = wprv; wprv = cur_; \
    _Pragma("unroll") for (int f = 0; f < 4; ++f) wb[f] += d_; \
    asm volatile("" : "+v"(wb[0]), "+v"(wb[1]), "+v"(wb[2]), "+v"(wb[3])); } while (0)
#define PH_A(b, STAGE) do { LDX(B0, b, 0); LDX(B1, b, 1); SCHED(); LDW(At, 0); STAGE; WAIT_V(10); WAIT_L(12); BAR(); SCHED(); MMA2(0, WAIT_L(0); BAR()); } while (0)
#define PH_B(STAGE) do { LDW(At, 1); STAGE; W_ADV(); WAIT_V(8); BAR(); SCHED(); MMA2(1, WAIT_L(0); BAR()); } while (0)
#pragma unroll 1
  for (int kt = 0; kt < 14; kt += 2) {
    PH_A(0, ST_W1(wnxt, kt + 1); ST_W0(wprv, kt + 2));
    PH_B(ST_X(0, kt + 2));
    PH_A(1, ST_W1(wnxt, kt + 2); ST_W0(wprv, kt + 3));
    PH_B(ST_X(1, kt + 3));
  }
  RELANE();
  const unsigned voffK = VOFFK, voffV = VOFFV;
  PH_A(0, ST_W1(0, 15); ST_KV8(voffK, kb_g, SLOT_E));
  PH_B(ST_KV8(voffV, vb_g, SLOT_C); ST_KV8(voffV, vb_g + 16384, SLOT_C + 16384));
  const unsigned soff = (unsigned)((rb * 256 + (g >> 1) * 128 + wc * 32 + (g & 1) * 16 + t) * 16);
  u32x4 sg;
  LDX(B0, 1, 0); LDX(B1, 1, 1); SCHED(); LDW(At, 0);
  asm volatile("global_load_dwordx4 %0, %1, %2 sc1" : "=&v"(sg) : "v"(soff), "s"(stats) : "memory");
  ST_KV8(voffK, kb_g + 16384, SLOT_E + 16384); ST_KV8(voffK, kb_g + 32768, SLOT_A);
  WAIT_V(11); BAR(); SCHED(); MMA2(0, WAIT_L(0); BAR());
  LDW(At, 1);
  ST_KV8(voffK, kb_g + 49152, SLOT_A + 16384);
  WAIT_L(0); BAR(); SCHED();
  const lptr csw = lds + SLOT_D + (t & 3) * 8192 + wr * 4096 + wc * 64 + g * 16;
  MMA2(1, *(LAS f32x4*)csw = cs[0]; *(LAS f32x4*)(csw + 256) = cs[1]; asm volatile("s_waitcnt vmcnt(4)" : "+v"(sg) :: "memory"); WAIT_L(0); BAR());
  f32x4 cv[2][4];
#pragma unroll
  for (int a = 0; a < 2; ++a)
#pragma unroll
    for (int m = 0; m < 4; ++m) cv[a][m] = *(const LAS f32x4*)(lds + SLOT_D + wc * 8192 + wr * 4096 + a * 256 + (m ^ wc) * 64 + g * 16);
  if (__builtin_expect(__builtin_amdgcn_ballot_w64(sg[1] != tag || sg[3] != tag) != 0ull, 0)) {
    for (unsigned spins = 0; spins < 4000000u; ++spins) {
      __builtin_amdgcn_s_sleep(2);
      asm volatile("" ::: "memory");
      sg = __builtin_amdgcn_raw_buffer_load_b128(wrs, soff, WS_STATS, 16);
      if (__builtin_amdgcn_ballot_w64(sg[1] != tag || sg[3] != tag) == 0ull) break;
    }
  }
  float mu[2][2], rs[2][2], sd[2][2];
  {
    auto m16 = __builtin_amdgcn_permlane16_swap(sg[0], sg[0], false, false);
    auto s16 = __builtin_amdgcn_permlane16_swap(sg[2], sg[2], false, false);
#pragma unroll
    for (int n = 0; n < 2; ++n) {
      auto m32 = __builtin_amdgcn_permlane32_swap(m16[n], m16[n], false, false);
      auto s32 = __builtin_amdgcn_permlane32_swap(s16[n], s16[n], false, false);
#pragma unroll
      for (int bj = 0; bj < 2; ++bj) { mu[bj][n] = -U2F(m32[bj]); sd[bj][n] = U2F(s32[bj]); rs[bj][n] = __builtin_amdgcn_rcpf(sd[bj][n]) * 1.4426950408889634f; }
    }
  }
  WAIT_L(0);
  {
    const lptr qw = lds + wc * 8192 + wr * 4096 + ((g >> 1) * 16 + t) * 16 + (g & 1) * 8;
#pragma unroll
    for (int bj = 0; bj < 2; ++bj) {
      lptr qwb = qw + ((bj ^ sb) ? SLOT_D : SLOT_B);
      asm volatile("" : "+v"(qwb));
#pragma unroll
      for (int m = 0; m < 4; ++m)
#pragma unroll
        for (int n = 0; n < 2; ++n) {
          const f32x4 v = (acc[0][bj][m][n] + cv[0][m] * mu[bj][n]) * (rs[bj][n] * 0.08838834764831845f);
          u32x2 w = {pk2(v[0], v[1]), pk2(v[2], v[3])};
          *(LAS u32x2*)(qwb + ((m ^ wc) >> 1) * 2048 + ((m ^ wc) & 1) * 512 + n * 1024) = w;
        }
    }
  }
  WAIT_L(0); BAR(); SCHED();
  unsigned pft = 0;
  const int sl = ((idx >> 2) * 16 + (idx & 1) * 8 + xcd) * 8 + wid;
  const int tstr = light ? 896 : 1024;
  const char* tbase = (light ? w2 : wo) + (sl < tstr ? sl : tstr - 1) * 2048;
#define TOUCH(q_) asm volatile("global_load_dword %0, %1, %2" : "+v"(pft) : "v"((unsigned)((__builtin_amdgcn_mbcnt_lo(~0u, 0u) & 15) * 128)), "s"(tbase + (((q_) + 2) & 3) * tstr * 2048) : "memory")
  TOUCH(0);
  bf16x8 qf[8];
  const lptr qr = lds + (wr ? SLOT_D : SLOT_B) + wc * 8192 + lane * 16;
  if (wr == 1) {
#pragma unroll
    for (int s = 0; s < 8; ++s) qf[s] = ldb128(qr + s * 1024);
  }
#define ST128(...) ST128_(wrs, __VA_ARGS__)
  u32x4 hq[3];
  unsigned hqo;
  {
    u32x2 hw[2][2][2];
#define SILU16(m, ACT) do { \
      const f32x4 a0 = acc[1][0][m][0] + cv[1][m] * mu[0][0], a1 = acc[1][0][m][1] + cv[1][m] * mu[0][1], a2 = acc[1][1][m][0] + cv[1][m] * mu[1][0], a3 = acc[1][1][m][1] + cv[1][m] * mu[1][1]; \
      f32x4 e0 = a0 * -rs[0][0], e1 = a1 * -rs[0][1], e2 = a2 * -rs[1][0], e3 = a3 * -rs[1][1]; SCHED(); \
      if (ACT) { \
      _Pragma("unroll") for (int i = 0; i < 4; ++i) { e0[i] = __builtin_amdgcn_exp2f(e0[i]); e1[i] = __builtin_amdgcn_exp2f(e1[i]); e2[i] = __builtin_amdgcn_exp2f(e2[i]); e3[i] = __builtin_amdgcn_exp2f(e3[i]); } SCHED(); \
      e0 = e0 * sd[0][0] + sd[0][0]; e1 = e1 * sd[0][1] + sd[0][1]; e2 = e2 * sd[1][0] + sd[1][0]; e3 = e3 * sd[1][1] + sd[1][1]; SCHED(); \
      _Pragma("unroll") for (int i = 0; i < 4; ++i) { e0[i] = __builtin_amdgcn_rcpf(e0[i]); e1[i] = __builtin_amdgcn_rcpf(e1[i]); e2[i] = __builtin_amdgcn_rcpf(e2[i]); e3[i] = __builtin_amdgcn_rcpf(e3[i]); } SCHED(); \
      e0 *= a0; e1 *= a1; e2 *= a2; e3 *= a3; SCHED(); } \
      else { _Pragma("unroll") for (int i = 0; i < 4; ++i) e3[i] = a3[i] * __builtin_amdgcn_rcpf(__builtin_amdgcn_exp2f(e3[i]) * sd[1][1] + sd[1][1]); } \
      hw[0][0][(m) & 1] = (u32x2){pk2(e0[0], e0[1]), pk2(e0[2], e0[3])}; hw[0][1][(m) & 1] = (u32x2){pk2(e1[0], e1[1]), pk2(e1[2], e1[3])}; \
      hw[1][0][(m) & 1] = (u32x2){pk2(e2[0], e2[1]), pk2(e2[2], e2[3])}; hw[1][1][(m) & 1] = (u32x2){pk2(e3[0], e3[1]), pk2(e3[2], e3[3])}; SCHED(); } while (0)
#define HFRAG(bj, n) ((wc & 1) ? (u32x4){hw[bj][n][1][0], hw[bj][n][1][1], hw[bj][n][0][0], hw[bj][n][0][1]} : (u32x4){hw[bj][n][0][0], hw[bj][n][0][1], hw[bj][n][1][0], hw[bj][n][1][1]})
    const unsigned hoff = (unsigned)(WS_H + (rb * 16 + wc * 2) * 2048 + lane * 16);
    const int pb = (light ? 16 + 3 * hd : 4 * hd) + (wr ? 0 : 2), sw = full ? (wc >> 1) : 0, p0 = pb + sw, p1 = pb + (sw ^ 1);
    SILU16(0, false); SILU16(1, false);
    hqo = hoff + (p0 >> 1) * 262144 + (p0 & 1) * 1024;
    hq[0] = HFRAG(0, 0); hq[1] = HFRAG(0, 1); hq[2] = HFRAG(1, 0);
    ST128(HFRAG(1, 1), hqo + 2048, 16384);
    asm volatile("" : "+v"(hq[0]), "+v"(hq[1]), "+v"(hq[2]));
    if (wr == 0) {
      BAR(); SCHED();
#pragma unroll
      for (int s = 0; s < 8; ++s) qf[s] = ldb128(qr + s * 1024);
    }
    if (full) {
      SILU16(2, true); SILU16(3, true);
#pragma unroll
      for (int bj = 0; bj < 2; ++bj)
#pragma unroll
        for (int n = 0; n < 2; ++n) ST128(HFRAG(bj, n), hoff + n * 2048, (p1 >> 1) * 262144 + (p1 & 1) * 1024 + bj * 16384);
    }
  }
  if (wr == 1) { WAIT_L(0); BAR(); }
  __builtin_amdgcn_s_setprio(1);
  SCHED();
  RELANE();
  lptr kb[2], vb[4];
#pragma unroll
  for (int e = 0; e < 2; ++e) kb[e] = lds + SLOT_E + (t >> 3) * 2048 + (t & 7) * 128 + (((4 * e + g) ^ (t >> 1)) << 4);
#pragma unroll
  for (int f = 0; f < 4; ++f) vb[f] = lds + SLOT_C + (g >> 1) * 2048 + (4 * (g & 1) + q4) * 128 + 32 * (f ^ (2 * (g & 1) + (q4 >> 1))) + 8 * p4;
  f32x4 o[8][2], s[8][2], lacc[2], negm[2];
#pragma unroll
  for (int n = 0; n < 2; ++n) {
    lacc[n] = (f32x4){0.f, 0.f, 0.f, 0.f};
#pragma unroll
    for (int d = 0; d < 8; ++d) o[d][n] = (f32x4){0.f, 0.f, 0.f, 0.f};
  }
  bf16x8 pf[8], Kf[32];
  bf16x4 Vh[32][2];
#define SM_LIM 1.152921504606847e18f
  constexpr int LA = 6;
#define MX3(a, b, c) __builtin_fmaxf(__builtin_fmaxf((a), (b)), (c))
#define RD_V(f, hf) Vh[f][hf] = __builtin_amdgcn_ds_read_tr16_b64_v4bf16((LAS bf16x4*)(vb[((f) >> 2) & 3] + ((f) >> 4) * 1024 + ((f) & 3) * 8192 + (hf) * 4096))
#define RD_K(f) Kf[f] = ldb128(kb[(f) & 1] + ((f) >> 2) * 4096 + (((f) >> 1) & 1) * 1024)
#define VF(f) __builtin_shufflevector(Vh[f][0], Vh[f][1], 0, 1, 2, 3, 4, 5, 6, 7)
#define PREF_V() do { _Pragma("unroll") for (int f = 0; f < LA; ++f) { RD_V(f, 0); RD_V(f, 1); } SCHED(); } while (0)
#define PV_SEG(NEXTK, FILL) do { \
    _Pragma("unroll") for (int i = 0; i < 64; ++i) { const int d = i >> 3, n = (i >> 2) & 1, k = n ? 3 - (i & 3) : (i & 3), f = (i >> 1) + LA; \
      o[d][n] = MFMA16(VF(4 * d + k), pf[2 * k + n], o[d][n]); SCHED(); \
      if (f < 32) RD_V(f < 32 ? f : 0, i & 1); else if ((NEXTK) && (i & 1)) RD_K(f >= 32 ? f - 32 : 0); \
      FILL; SCHED(); } } while (0)
#define QK_SEG(HAVE, CLOSE) do { \
    _Pragma("unroll") for (int i = 0; i < 64; ++i) { const int r = i >> 3, n = (i >> 2) & 1, k = n ? 3 - (i & 3) : (i & 3), f = (i >> 1) + LA; \
      s[r][n] = MFMA16(Kf[4 * r + k], qf[2 * k + n], (i & 3) == 0 ? negm[n] : s[r][n]); SCHED(); \
      if ((i & 1) && f < 32 && f >= (HAVE)) RD_K(f < 32 ? f : 0); \
      SCHED(); \
      if (i == 59) { __builtin_amdgcn_s_setprio(2); CLOSE; WAIT_L(0); BAR(); SCHED(); } } \
    __builtin_amdgcn_s_setprio(0); } while (0)
#define MX4(m_, v_) MX3(MX3(m_, v_[0], v_[1]), v_[2], v_[3])
#define ROWMAX(n) MX4(MX4(MX4(MX4(MX4(MX4(MX4(MX4(s[0][n][0], s[0][n]), s[1][n]), s[2][n]), s[3][n]), s[4][n]), s[5][n]), s[6][n]), s[7][n])
#define EXP_PACK() do { \
      _Pragma("unroll") for (int k = 0; k < 4; ++k) { \
        _Pragma("unroll") for (int n = 0; n < 2; ++n) { \
          _Pragma("unroll") for (int i = 0; i < 4; ++i) { s[2 * k][n][i] = __builtin_amdgcn_exp2f(s[2 * k][n][i]); s[2 * k + 1][n][i] = __builtin_amdgcn_exp2f(s[2 * k + 1][n][i]); } \
          u32x4 w = {pk2(s[2 * k][n][0], s[2 * k][n][1]), pk2(s[2 * k][n][2], s[2 * k][n][3]), pk2(s[2 * k + 1][n][0], s[2 * k + 1][n][1]), pk2(s[2 * k + 1][n][2], s[2 * k + 1][n][3])}; \
          pf[2 * k + n] = __builtin_bit_cast(bf16x8, w); } \
        asm volatile("" : "+v"(pf[2 * k]), "+v"(pf[2 * k + 1])); \
        SCHED(); } \
      __builtin_amdgcn_s_setprio(2); \
      _Pragma("unroll") for (int i = 0; i < 8; ++i) { ln[i >> 2] = MFMA16(ones, pf[2 * (i & 3) + (i >> 2)], (i & 3) == 0 ? lacc[i >> 2] : ln[i >> 2]); SCHED(); } \
      __builtin_amdgcn_s_setprio(0); } while (0)
#define SOFTMAX(PRE) do { f32x4 ln[2]; \
      EXP_PACK(); \
      PRE; \
      if (__builtin_expect(__builtin_amdgcn_ballot_w64(!(ln[0][0] <= SM_LIM && ln[1][0] <= SM_LIM)) != 0ull, 0)) { \
        _Pragma("unroll") for (int r = 0; r < 8; ++r) _Pragma("unroll") for (int k = 0; k < 4; ++k) { \
          const bf16x8 kf_ = ldb128(kb[k & 1] + r * 4096 + (k >> 1) * 1024); \
          s[r][0] = MFMA16(kf_, qf[2 * k], k == 0 ? negm[0] : s[r][0]); s[r][1] = MFMA16(kf_, qf[2 * k + 1], k == 0 ? negm[1] : s[r][1]); if (k == 3) SCHED(); } \
        const float m0 = ROWMAX(0), m1 = ROWMAX(1); \
        _Pragma("unroll") for (int n = 0; n < 2; ++n) { \
          const float dl = __builtin_fmaxf(col_max(n ? m1 : m0), 0.f), alpha = __builtin_amdgcn_exp2f(-dl); \
          negm[n] -= dl; lacc[n] *= alpha; \
          _Pragma("unroll") for (int r = 0; r < 8; ++r) { s[r][n] -= dl; o[r][n] *= alpha; } } \
        EXP_PACK(); \
        PRE; \
      } \
      lacc[0] = ln[0]; lacc[1] = ln[1]; SCHED(); } while (0)
  {
#pragma unroll
    for (int f = 0; f < 8; ++f) RD_K(f);
#pragma unroll
    for (int d = 0; d < 8; d += 2) asm volatile("" : "+v"(o[d][0]), "+v"(o[d][1]), "+v"(o[d + 1][0]), "+v"(o[d + 1][1]));
    f32x4 u[2][2];
    const f32x4 z4 = {0.f, 0.f, 0.f, 0.f};
#pragma unroll
    for (int i = 0; i < 16; ++i) { const int r = i >> 3, n = (i >> 2) & 1, k = i & 3; u[r][n] = MFMA16(Kf[4 * r + k], qf[2 * k + n], k == 0 ? z4 : u[r][n]); }
#pragma unroll
    for (int n = 0; n < 2; ++n) { const float m = -col_max(MX4(MX3(u[0][n][0], u[0][n][1], __builtin_fmaxf(u[0][n][2], u[0][n][3])), u[1][n])); negm[n] = (f32x4){m, m, m, m}; }
    asm volatile("" : "+v"(negm[0]), "+v"(negm[1]));
    SCHED();
    QK_SEG(8, WAIT_V(1));
  }
  const char* kvsrc = (wr ? kb_g + 65536 : vb_g + 32768) + wc * 8192 - 1024;
  unsigned kvo[2][2];
#pragma unroll
  for (int par = 0; par < 2; ++par) {
    kvo[par][0] = (unsigned)((lane >> 3) * 256 + (((lane & 7) ^ (wr ? 4 * par + (lane >> 4) : (lane >> 4) << 1)) << 4) + 1024);
    kvo[par][1] = kvo[par][0] + 128 - 1024;
  }
#pragma unroll 1
  for (int c = 0; c < 3; ++c) {
    {
      float tv[8], ev[8];
#pragma unroll
      for (int e = 0; e < 4; ++e) { tv[2 * e] = bf2f(hq[0][e] & 0xffffu); tv[2 * e + 1] = U2F(hq[0][e] & 0xffff0000u); }
      SCHED();
#pragma unroll
      for (int e = 0; e < 8; ++e) ev[e] = __builtin_amdgcn_exp2f(tv[e]);
      SCHED();
#pragma unroll
      for (int e = 0; e < 8; ++e) ev[e] = ev[e] * -1.4426950408889634f - 1.4426950408889634f;
      SCHED();
#pragma unroll
      for (int e = 0; e < 8; ++e) ev[e] = __builtin_amdgcn_rcpf(ev[e]);
      SCHED();
      ST128((u32x4){pk2(ev[0] * tv[0], ev[1] * tv[1]), pk2(ev[2] * tv[2], ev[3] * tv[3]), pk2(ev[4] * tv[4], ev[5] * tv[5]), pk2(ev[6] * tv[6], ev[7] * tv[7])}, hqo, c == 2 ? 16384 : c * 2048);
    }
    hq[0] = hq[1]; hq[1] = hq[2];
    SCHED();
    if (__builtin_expect(c + wr < 3, 1)) {
      const int slot = wr ? (c == 0 ? SLOT_B : SLOT_C) : (c == 0 ? SLOT_D : (c == 1 ? SLOT_E : SLOT_A));
#pragma unroll
      for (int hf = 0; hf < 2; ++hf)
        asm volatile("s_mov_b32 m0, %5\n\ts_nop 0\n\t"
                     "global_load_lds_dwordx4 %0, %4\n\tglobal_load_lds_dwordx4 %1, %4 offset:1024\n\t"
                     "global_load_lds_dwordx4 %2, %4 offset:2048\n\tglobal_load_lds_dwordx4 %3, %4 offset:3072"
                     ::"v"(kvo[0][0]), "v"(kvo[0][1]), "v"(kvo[1][0]), "v"(kvo[1][1]), "s"(kvsrc + c * 32768 + hf * 4096), "s"(lds0 + slot + wc * 8192 + hf * 4096) : "memory");
    }
    TOUCH(c + 1);
    SOFTMAX(PREF_V());
    { const int dk = (c == 0) ? SLOT_A - SLOT_E : ((c == 1) ? SLOT_B - SLOT_A : SLOT_C - SLOT_B); kb[0] += dk; kb[1] += dk; asm volatile("" : "+v"(kb[0]), "+v"(kb[1])); }
    BAR(); SCHED();
    __builtin_amdgcn_s_setprio(1);
    PV_SEG(true, );
    QK_SEG(LA, WAIT_V(1); if (c == 2) WAIT_V(0));
    { const int dv = (c == 1) ? SLOT_E - SLOT_D : 32768;
#pragma unroll
      for (int f = 0; f < 4; ++f) vb[f] += dv; }
  }
  RELANE();
  if (wid == 4 && lane == 0) __builtin_amdgcn_raw_buffer_store_b32(tag, wrs, 0, WS_HFLAG + (rb * 32 + hd) * 128, 16);
  SOFTMAX(PREF_V());
  {
    asm volatile("s_waitcnt vmcnt(0)" :: "v"(pft) : "memory");
    SCHED();
    if (wr == 0) BAR();
    SCHED();
    __builtin_amdgcn_s_setprio(1);
    const unsigned oo = (unsigned)(WS_O + 2 * hd * 262144 + (rb * 16 + (wr ^ sb) * 8 + wc * 2) * 2048 + lane * 16);
    float inv[2] = {0.f, 0.f};
    u32x4 ow;
#define O_PIECE(j_) do { const int dp = (j_) >> 3, n_ = ((j_) >> 2) & 1, q_ = (j_) & 3; \
      ow[q_] = pk2(o[2 * dp + (q_ >> 1)][n_][2 * (q_ & 1)] * inv[n_], o[2 * dp + (q_ >> 1)][n_][2 * (q_ & 1) + 1] * inv[n_]); asm volatile("" : "+v"(ow)); \
      if (q_ == 3) ST128(ow, oo + n_ * 2048 + (dp & 1) * 1024, (dp >> 1) * 262144); } while (0)
    PV_SEG(false, if (i == 4) { inv[0] = __builtin_amdgcn_rcpf(lacc[0][0]); inv[1] = __builtin_amdgcn_rcpf(lacc[1][0]); } if (i >= 16 && (i & 1)) O_PIECE(i >= 16 ? (i - 16) >> 1 : 0));
#pragma unroll
    for (int j = 24; j < 32; ++j) O_PIECE(j);
    __builtin_amdgcn_s_setprio(0);
  }
  RELANE();
  const int isG = (idx >> 1) & 1, kh = xcd & 1, tm = 4 * (xcd >> 1) + ((idx >> 1) & 2) + (idx & 1), tn = 2 * (idx >> 3) + kh;
  const int npw = (isG ? 48 : 56) - 4 * kh;
  const int k2c = ((npw + 19) % 6 < 4) ? K2_C : 0;
#define O_FLAG() do { if (wid == 0 && lane == 0) __builtin_amdgcn_raw_buffer_store_b32(tag, wrs, 0, WS_OFLAG + (rb * 32 + hd) * OFS, 16); } while (0)
#define POLL32(off_, slp) do { for (unsigned spins = 0; spins < 2000000u; ++spins) { \
      if (__builtin_amdgcn_ballot_w64(__builtin_amdgcn_raw_buffer_load_b32(wrs, (off_), 0, 16) != tag) == 0ull) break; \
      __builtin_amdgcn_s_sleep(slp); asm volatile("" ::: "memory"); } \
    asm volatile("" ::: "memory"); } while (0)
  const int c8 = lane & 15, r0 = wc * 4 + (lane >> 4);
  const unsigned goff = (unsigned)((tm * 128 + r0) * 2048 + tn * 256 + c8 * 16);
  const lptr cb = lds + k2c + r0 * 512;
#define G_FIN() do { f32x4 a0[4], a1[4]; \
      _Pragma("unroll") for (int i = 0; i < 4; ++i) { a0[i] = *(const LAS f32x4*)(cb + (wr * 4 + i) * 8192 + (((2 * c8) ^ (r0 & 7)) << 4)); a1[i] = *(const LAS f32x4*)(cb + (wr * 4 + i) * 8192 + (((2 * c8 + 1) ^ (r0 & 7)) << 4)); } \
      _Pragma("unroll") for (int i = 0; i < 4; ++i) \
        ST128((u32x4){pk2(silu(a0[i][0]), silu(a0[i][1])), pk2(silu(a0[i][2]), silu(a0[i][3])), pk2(silu(a1[i][0]), silu(a1[i][1])), pk2(silu(a1[i][2]), silu(a1[i][3]))}, goff + i * 32768, WS_G + wr * 131072); \
      WAIT_V(0); BAR(); } while (0)
  const unsigned f1b = isG ? (unsigned)(WS_HFLAG + ((tm >> 1) * 32 + 16 + (lane & 15)) * 128) : (unsigned)(WS_OFLAG + ((tm >> 1) * 32 + 16 + (lane & 15)) * OFS);
  const unsigned f1 = isG ? f1b : (unsigned)(WS_EFLAG + ((tm >> 1) * 16 + (tm & 1) * 8 + kh * 4 + ((lane >> 2) & 3)) * 64 + (lane & 3) * 4);
  if (wr == 0) {
    const char* ab = ws + (isG ? WS_H : WS_O) + tm * 16384 + wc * 4096;
    const char* wbp = (isG ? w2 : wo) + tn * 256 - 1024;
    const int wsd = (kh ? -256 : 256) + wc * 32768;
    const unsigned voffA0 = lane * 16, voffA1 = lane * 16 + 1024;
    unsigned voffW[4];
    {
      const int r = lane >> 3, c = lane & 7;
      voffW[0] = (unsigned)(r * 2048 + ((c ^ (((r >> 1) & 3) << 1)) << 4) + 1024); voffW[1] = voffW[0] + 128 - 1024;
      voffW[2] = voffW[0] + 16384 - 2048; voffW[3] = voffW[2] + 128 - 1024;
    }
    const char *ap, *wp, *wo_ = wbp;
    unsigned dst = lds0;
#define K2_RUN(k_) do { ap = ab + (size_t)(k_) * 262144; wp = wbp + (size_t)(k_) * 131072; } while (0)
#define K2_A(ks, d_) asm volatile("s_mov_b32 m0, %3\n\ts_nop 0\n\tglobal_load_lds_dwordx4 %0, %2 sc1\n\tglobal_load_lds_dwordx4 %1, %2 offset:1024 sc1" \
                   ::"v"(voffA0), "v"(voffA1), "s"(ap + (ks) * 1024), "s"((d_) + wc * 2048) : "memory")
#define K2_ISSUE(WIDE, ks) do { K2_A(ks, dst); \
      if (WIDE) GLDS4(voffW[0], voffW[1], voffW[2], voffW[3], (ks) ? wp + wc * 32768 : wo_ + wsd, dst + 8192 + wc * 4096); \
      else GLDS2(voffW[0], voffW[1], ((ks) ? wp : wo_ + 65536) + wc * 16384, dst + 8192 + wc * 2048); \
      dst = (dst == lds0 + 5 * K2_STG) ? lds0 : dst + K2_STG; \
      if (ks) { wo_ = wp; ap += 262144; wp += 131072; } } while (0)
#define K2_STEP(WIDE, ks, EVENT) do { WAIT_V((WIDE) ? 18 : 12); BAR(); EVENT; K2_ISSUE(WIDE, ks); } while (0)
#define K2_TILES(WIDE, n_) do { _Pragma("unroll 1") for (int i_ = 0; i_ < (n_); ++i_) { K2_STEP(WIDE, 0, ); K2_STEP(WIDE, 1, ); } } while (0)
#define LOOK(f_) asm volatile("buffer_load_dword %0, %1, %2, 0 offen sc1" : "+v"(pol) : "v"(f_), "s"(wrs) : "memory")
    const unsigned f2 = f1b - 16 * (isG ? 128 : OFS);
    const unsigned f3 = (unsigned)(WS_PFLAG + (bid ^ 1) * 128);
    u32x4 xh[8];
#define XH_LD(i) asm volatile("global_load_dwordx4 %0, %1, %2" : "=v"(xh[i]) : "v"(goff), "s"(xn + (i) * 32768) : "memory")
    WAIT_V(0);
    if (isG && lane == 0) __builtin_amdgcn_raw_buffer_store_b32(tag, wrs, 0, WS_EFLAG + (rb * 16 + hd - 16) * 64 + wc * 4, 16);
    POLL32(f1, 4);
    K2_RUN(isG ? 32 + 12 * kh : 32 + 16 * (tm & 1) + 8 * kh);
    K2_A(0, lds0 + 6 * K2_STG); K2_ISSUE(1, 1); K2_ISSUE(1, 0);
    WAIT_V(6); BAR();
    K2_ISSUE(1, 1); K2_ISSUE(1, 0); K2_ISSUE(1, 1);
    unsigned pol = 0;
#define CHECK(f_) do { asm volatile("" : "+v"(pol)); if (__builtin_amdgcn_ballot_w64(pol != tag) != 0ull) POLL32(f_, 4); } while (0)
    K2_TILES(1, 3);
    O_FLAG();
    LOOK(f1b);
    K2_TILES(1, 2);
    if (!isG) K2_RUN(48 - 16 * (tm & 1) + 6 * kh);
    K2_STEP(1, 0, CHECK(f1b)); K2_STEP(1, 1, );
    K2_TILES(1, isG ? 1 : 3);
    LOOK(f2);
    K2_TILES(1, 2);
    K2_RUN((isG ? 12 : 14) * kh);
    K2_STEP(1, 0, CHECK(f2));
    K2_STEP(1, 1, );
#pragma unroll
    for (int i = 0; i < 8; ++i) { K2_STEP(1, 0, XH_LD(i)); K2_STEP(1, 1, ); }
    K2_TILES(1, (isG ? 3 : 5) - 2 * kh);
    asm volatile("" : "+v"(xh[0]), "+v"(xh[1]), "+v"(xh[2]), "+v"(xh[3]), "+v"(xh[4]), "+v"(xh[5]), "+v"(xh[6]), "+v"(xh[7]));
    K2_RUN(isG ? 22 : 60 - 16 * (tm & 1));
    K2_STEP(1, 0, ); K2_STEP(0, 1, );
    K2_TILES(0, 3);
    K2_RUN(26);
    K2_STEP(0, 0, ); K2_STEP(0, 1, );
    K2_STEP(0, 0, if (wid == 0 && lane == 0) __builtin_amdgcn_raw_buffer_store_b32(tag, wrs, 0, WS_PFLAG + bid * 128, 16));
    K2_STEP(0, 1, );
    K2_TILES(0, 2);
    K2_STEP(0, 0, );
    const unsigned f4 = (lane & 1) ? (unsigned)(WS_GFLAG + (tm * 8 + tn) * 128) : f3;
    K2_STEP(0, 1, LOOK(f4)); K2_STEP(0, 0, ); K2_STEP(0, 1, );
    WAIT_V(12);
    asm volatile("" : "+v"(pol));
    const unsigned long long miss = __builtin_amdgcn_ballot_w64(pol != tag);
    if (wid == 0 && (miss & 1ull)) POLL32(f3, 2);
    BAR();
    K2_ISSUE(0, 0);
    WAIT_V(12); BAR();
    WAIT_V(8); BAR();
    if (isG) {
      WAIT_V(4); BAR();
      WAIT_V(0); BAR();
      BAR();
      BAR();
    } else {
      u32x4 gv[8];
      const __amdgpu_buffer_rsrc_t ors = __builtin_amdgcn_make_buffer_rsrc(out, 0, 2048 * 2048, 0x00020000);
#define LD8(dst_, rs_, so_, pol_) asm volatile("buffer_load_dwordx4 %0, %8, %10, %11 offen" pol_ "\n\tbuffer_load_dwordx4 %1, %9, %10, %11 offen" pol_ "\n\t" \
        "buffer_load_dwordx4 %2, %8, %10, %12 offen" pol_ "\n\tbuffer_load_dwordx4 %3, %9, %10, %12 offen" pol_ "\n\t" \
        "buffer_load_dwordx4 %4, %8, %10, %13 offen" pol_ "\n\tbuffer_load_dwordx4 %5, %9, %10, %13 offen" pol_ "\n\t" \
        "buffer_load_dwordx4 %6, %8, %10, %14 offen" pol_ "\n\tbuffer_load_dwordx4 %7, %9, %10, %14 offen" pol_ \
        : "=&v"(dst_[0]), "=&v"(dst_[1]), "=&v"(dst_[2]), "=&v"(dst_[3]), "=&v"(dst_[4]), "=&v"(dst_[5]), "=&v"(dst_[6]), "=&v"(dst_[7]) \
        : "v"(goff), "v"(goff + 32768), "s"(rs_), "s"((so_)), "s"((so_) + 65536), "s"((so_) + 131072), "s"((so_) + 196608) : "memory")
      if (miss & 2ull) {
        WAIT_V(4); BAR(); WAIT_V(0); BAR(); BAR();
        POLL32((unsigned)(WS_GFLAG + (tm * 8 + tn) * 128), 1);
      }
      LD8(gv, wrs, WS_G, " sc1");
      if (!(miss & 2ull)) { WAIT_V(12); BAR(); WAIT_V(8); BAR(); BAR(); }
      float xf[8][8];
#pragma unroll
      for (int i = 0; i < 8; ++i) {
#pragma unroll
        for (int e = 0; e < 4; ++e) { xf[i][2 * e] = bf2f(xh[i][e] & 0xffffu); xf[i][2 * e + 1] = U2F(xh[i][e] & 0xffff0000u); }
        asm volatile("" : "+v"(xf[i][0]), "+v"(xf[i][1]), "+v"(xf[i][2]), "+v"(xf[i][3]), "+v"(xf[i][4]), "+v"(xf[i][5]), "+v"(xf[i][6]), "+v"(xf[i][7]));
      }
      asm volatile("s_waitcnt vmcnt(0)" : "+v"(gv[0]), "+v"(gv[1]), "+v"(gv[2]), "+v"(gv[3]), "+v"(gv[4]), "+v"(gv[5]), "+v"(gv[6]), "+v"(gv[7]) :: "memory");
      float gf2[8][8];
#pragma unroll
      for (int i = 0; i < 8; ++i) {
#pragma unroll
        for (int e = 0; e < 4; ++e) { gf2[i][2 * e] = bf2f(gv[i][e] & 0xffffu); gf2[i][2 * e + 1] = U2F(gv[i][e] & 0xffff0000u); }
        asm volatile("" : "+v"(gf2[i][0]), "+v"(gf2[i][1]), "+v"(gf2[i][2]), "+v"(gf2[i][3]), "+v"(gf2[i][4]), "+v"(gf2[i][5]), "+v"(gf2[i][6]), "+v"(gf2[i][7]));
      }
      BAR();
      f32x4 a0[8], a1[8];
#pragma unroll
      for (int i = 0; i < 8; ++i) { a0[i] = *(const LAS f32x4*)(cb + i * 8192 + (((2 * c8) ^ (r0 & 7)) << 4)); a1[i] = *(const LAS f32x4*)(cb + i * 8192 + (((2 * c8 + 1) ^ (r0 & 7)) << 4)); }
      SCHED();
#pragma unroll
      for (int i = 0; i < 8; ++i) {
        const u32x4 w = {pk2(xf[i][0] + a0[i][0] * gf2[i][0], xf[i][1] + a0[i][1] * gf2[i][1]), pk2(xf[i][2] + a0[i][2] * gf2[i][2], xf[i][3] + a0[i][3] * gf2[i][3]),
                         pk2(xf[i][4] + a1[i][0] * gf2[i][4], xf[i][5] + a1[i][1] * gf2[i][5]), pk2(xf[i][6] + a1[i][2] * gf2[i][6], xf[i][7] + a1[i][3] * gf2[i][7])};
        __builtin_amdgcn_raw_buffer_store_b128(w, ors, goff + i * 32768, 0, 16);
      }
    }
  } else {
    const int wm = wc >> 1, wn = wc & 1;
    lptr w2b[4];
#pragma unroll
    for (int ft = 0; ft < 4; ++ft) w2b[ft] = lds + 8192 + wn * 1024 + (g >> 1) * 2048 + (4 * (g & 1) + q4) * 128 + 32 * (ft ^ (2 * (g & 1) + (q4 >> 1))) + 8 * p4;
    lptr ar = lds + wm * 4096 + lane * 16;
    f32x4 ac2[8][4];
#pragma unroll
    for (int m = 0; m < 8; ++m)
#pragma unroll
      for (int n = 0; n < 4; ++n) ac2[m][n] = (f32x4){0.f, 0.f, 0.f, 0.f};
    bf16x8 Af[2][2][4];
    bf16x4 Wr[4][2][2];
    int slot = 0, dlt = 0;
#define K2_RW(m, ks, hf) Wr[m][ks][hf] = __builtin_amdgcn_ds_read_tr16_b64_v4bf16((LAS bf16x4*)(w2b[m] + (ks) * 8192 + (hf) * 4096))
#define K2_W(m, ks) __builtin_shufflevector(Wr[m][ks][0], Wr[m][ks][1], 0, 1, 2, 3, 4, 5, 6, 7)
#define K2_NEXT() do { dlt = (slot == 5) ? -5 * K2_STG : K2_STG; slot = (slot == 5) ? 0 : slot + 1; } while (0)
#define K2_ADV(x) do { x += dlt; asm volatile("" : "+v"(x)); } while (0)
#define K2_WIDE(st, h) do { _Pragma("unroll") for (int i = 0; i < 32; ++i) { const int m = i >> 3, j = i & 7, q = j >> 1; \
        const int n_ = (m & 1) ? 3 - q : q, k_ = (j ^ q) & 1; asm volatile("v_mfma_f32_16x16x32_bf16 %0, %1, %2, %0" : "+v"(ac2[4 * (h) + m][n_]) : "v"(K2_W(m, k_)), "v"(Af[st][k_][n_])); SCHED(); \
        if (j >= 1 && j < 5) K2_RW((m + 2) & 3, ((j - 1) >> 1) & 1, (j - 1) & 1); \
        if (m >= 2 && (j == 5 || j == 6)) Af[(st) ^ 1][h][(2 * m + j - 9) & 3] = ldb128(ar + ((2 * m + j - 9) & 3) * 1024); \
        if (i == 5) K2_ADV(ar); \
        if (i == 6) K2_ADV(w2b[0]); \
        if (i == 7) K2_ADV(w2b[2]); \
        if (i == 13) K2_ADV(w2b[1]); \
        if (i == 14) BAR(); \
        if (i == 15) K2_ADV(w2b[3]); \
        if (i == 23) K2_NEXT(); \
        SCHED(); } } while (0)
#define K2_NARROW(st, h) do { _Pragma("unroll") for (int i = 0; i < 16; ++i) { const int m = i >> 2, q = i & 3; \
        const int n_ = (m & 1) ? 3 - q : q; ac2[m][n_] = MFMA16(K2_W(m, 0), Af[st][h][n_], ac2[m][n_]); SCHED(); \
        if (q < 2) K2_RW((m + 3) & 3, 0, q); \
        if (q >= 2 && (m == 1 || m == 2)) Af[(st) ^ 1][h][2 * m + q - 4] = ldb128(ar + (2 * m + q - 4) * 1024); \
        if (i == 2) { K2_ADV(ar); K2_ADV(w2b[0]); } \
        if (i == 3) BAR(); \
        if (i == 5) K2_ADV(w2b[1]); \
        if (i == 9) K2_ADV(w2b[2]); \
        if (i == 14) K2_ADV(w2b[3]); \
        if (i == 15) K2_NEXT(); \
        SCHED(); } } while (0)
#define K2_WIDE4() do { K2_WIDE(0, 0); K2_WIDE(0, 1); K2_WIDE(1, 0); K2_WIDE(1, 1); } while (0)
#define K2_NARROW4() do { K2_NARROW(0, 0); K2_NARROW(0, 1); K2_NARROW(1, 0); K2_NARROW(1, 1); } while (0)
    K2_NEXT();
#pragma unroll
    for (int m = 0; m < 8; m += 2)
      asm volatile("" : "+v"(ac2[m][0]), "+v"(ac2[m][1]), "+v"(ac2[m][2]), "+v"(ac2[m][3]), "+v"(ac2[m + 1][0]), "+v"(ac2[m + 1][1]), "+v"(ac2[m + 1][2]), "+v"(ac2[m + 1][3]));
    asm volatile("" : "+v"(ar), "+v"(w2b[0]), "+v"(w2b[1]), "+v"(w2b[2]), "+v"(w2b[3]));
    BAR();
#pragma unroll
    for (int i = 0; i < 4; ++i) { K2_RW(i >> 1, i & 1, 0); K2_RW(i >> 1, i & 1, 1); Af[0][0][i] = ldb128(ar + 6 * K2_STG + i * 1024); Af[0][1][i] = ldb128(ar + i * 1024); SCHED(); }
#pragma unroll 1
    for (int p = 0; p < npw; p += 4) {
      K2_WIDE4();
      WAIT_V(0);
    }
    asm volatile("s_nop 7\n\ts_nop 7" ::: "memory"); SCHED();
    K2_RW(2, 0, 0); K2_RW(2, 0, 1);
    const unsigned po = (unsigned)(WS_P + wc * 8192 + lane * 16);
#pragma unroll
    for (int i = 0; i < 8; ++i) {
      const f32x4 a = ac2[4 + (i >> 1)][2 * (i & 1)], b = ac2[4 + (i >> 1)][2 * (i & 1) + 1];
      ST128((u32x4){pk2(a[0], a[1]), pk2(a[2], a[3]), pk2(b[0], b[1]), pk2(b[2], b[3])}, po + i * 1024, bid * 32768);
    }
    SCHED();
#pragma unroll 1
    for (int p = 0; p < 12; p += 4) {
      K2_NARROW4();
      WAIT_V(0);
    }
    K2_NARROW(0, 0); K2_NARROW(0, 1); K2_NARROW(1, 0);
    u32x4 pv[8];
#pragma unroll
    for (int i = 0; i < 8; ++i) pv[i] = __builtin_amdgcn_raw_buffer_load_b128(wrs, po + i * 1024 + (bid ^ 1) * 32768, 0, 16);
    SCHED();
    K2_NARROW(1, 1);
    K2_NARROW4();
    const lptr cw = lds + k2c + (wm * 64 + t) * 512;
#pragma unroll
    for (int m = 0; m < 4; ++m)
#pragma unroll
      for (int n = 0; n < 4; ++n) {
        const unsigned lo = pv[2 * m + (n >> 1)][2 * (n & 1)], hi = pv[2 * m + (n >> 1)][2 * (n & 1) + 1];
        *(LAS f32x4*)(cw + n * 8192 + (((wn * 16 + m * 4 + g) ^ (t & 7)) << 4)) = ac2[m][n] + (f32x4){bf2f(lo & 0xffffu), U2F(lo & 0xffff0000u), bf2f(hi & 0xffffu), U2F(hi & 0xffff0000u)};
      }
    WAIT_L(0); BAR();
    if (isG) G_FIN();
    __builtin_amdgcn_endpgm();
  }
  if (isG) {
      G_FIN();
      if (wid == 0 && lane == 0) __builtin_amdgcn_raw_buffer_store_b32(tag, wrs, 0, WS_GFLAG + (tm * 8 + tn) * 128, 16);
      if (bid == 16 && wid == 0) {
        for (unsigned spins = 0; spins < 2000000u; ++spins) {
          bool ok = true;
#pragma unroll
          for (int i = 0; i < 4; ++i) ok = ok && __builtin_amdgcn_raw_buffer_load_b32(wrs, (unsigned)((lane + i * 64) * 128), WS_HFLAG, 16) == tag;
          if (__builtin_amdgcn_ballot_w64(!ok) == 0ull) break;
          __builtin_amdgcn_s_sleep(4);
          asm volatile("" ::: "memory");
        }
        asm volatile("" ::: "memory");
        if (lane == 0) __builtin_amdgcn_raw_buffer_store_b32(tag, wrs, 0, 0, 16);
      }
    }
}

extern "C" void kernel_launch(void* const* d_in, const int* in_sizes, int n_in, void* d_out, int out_size, void* d_ws, size_t ws_size, hipStream_t stream) {
  const char* x = (const char*)d_in[0]; const char* kc = (const char*)d_in[1]; const char* vc = (const char*)d_in[2];
  const char* wq = (const char*)d_in[3]; const char* wo = (const char*)d_in[4]; const char* w1 = (const char*)d_in[6]; const char* w2 = (const char*)d_in[7];
  (void)hipFuncSetAttribute((const void*)mega_kernel, hipFuncAttributeMaxDynamicSharedMemorySize, K1_LDS);
  mega_kernel<<<256, 512, K1_LDS, stream>>>(x, kc, vc, wq, w1, wo, w2, (char*)d_ws, (char*)d_out);
}
```

```cpp
#define PROBE_A 22
#define PROBE_B 23
#define PROBE_S 3
#define PROBE_I "s_memrealtime"
#define PROBE_W 7
#include <hip/hip_runtime.h>
#include <stdint.h>

typedef __bf16 bf16x2 __attribute__((ext_vector_type(2)));
typedef __bf16 bf16x4 __attribute__((ext_vector_type(4)));
typedef __bf16 bf16x8 __attribute__((ext_vector_type(8)));
typedef float f32x2 __attribute__((ext_vector_type(2)));
typedef float f32x4 __attribute__((ext_vector_type(4)));
typedef unsigned u32x2 __attribute__((ext_vector_type(2)));
typedef unsigned u32x4 __attribute__((ext_vector_type(4)));
#define LAS __attribute__((address_space(3)))
typedef LAS char* lptr;
#if defined(__HIP_DEVICE_COMPILE__)
#define NOPK __attribute__((target("no-packed-fp32-ops")))
#else
#define NOPK
#endif
#define DI NOPK __device__ __forceinline__
#define F2U(x) __builtin_bit_cast(unsigned, (float)(x))
#define U2F(x) __builtin_bit_cast(float, (unsigned)(x))
#define TID ((int)__builtin_amdgcn_workitem_id_x())
#define BID ((int)__builtin_amdgcn_workgroup_id_x())
#define SCHED() __builtin_amdgcn_sched_barrier(0)
#define BAR() asm volatile("s_barrier" ::: "memory")
#define WAIT_V(n) asm volatile("s_waitcnt vmcnt(%0)" :: "n"(n) : "memory")
#define WAIT_L(n) do { asm volatile("" ::: "memory"); __builtin_amdgcn_s_waitcnt(0xC07F | ((n) << 8)); asm volatile("" ::: "memory"); } while (0)
#define MFMA16(a, b, c) __builtin_amdgcn_mfma_f32_16x16x32_bf16(a, b, c, 0, 0, 0)

DI void glds(unsigned voff, const char* sbase, unsigned ldsaddr) {
  asm volatile("s_mov_b32 m0, %2\n\ts_nop 0\n\tglobal_load_lds_dwordx4 %0, %1" ::"v"(voff), "s"(sbase), "s"(ldsaddr) : "memory");
}
DI unsigned pk2(float lo, float hi) { f32x2 v = {lo, hi}; return __builtin_bit_cast(unsigned, __builtin_convertvector(v, bf16x2)); }
DI float bf2f(unsigned b) { return U2F(b << 16); }
DI bf16x8 ldb128(lptr p) { return *(const LAS bf16x8*)p; }
DI bf16x8 ldtr2(lptr p, int d) {
  bf16x4 a = __builtin_amdgcn_ds_read_tr16_b64_v4bf16((LAS bf16x4*)p);
  bf16x4 b = __builtin_amdgcn_ds_read_tr16_b64_v4bf16((LAS bf16x4*)(p + d));
  return __builtin_shufflevector(a, b, 0, 1, 2, 3, 4, 5, 6, 7);
}
DI void ST128_(__amdgpu_buffer_rsrc_t r, u32x4 v, unsigned voff, int soff) { __builtin_amdgcn_raw_buffer_store_b128(v, r, voff + (unsigned)soff, 0, 16); }
DI float silu(float v) { return v * __builtin_amdgcn_rcpf(1.0f + __builtin_amdgcn_exp2f(v * -1.4426950408889634f)); }

#define STAMP_DECL unsigned long long pa_ = 0, pb_ = 0; const unsigned long long p0_ = __builtin_amdgcn_s_memrealtime();
#define LSTAMP(i)
#define STAMP(i) do { if ((i) == PROBE_A) asm volatile(PROBE_I " %0\n\ts_waitcnt lgkmcnt(0)" : "=s"(pa_) :: "memory"); if ((i) == PROBE_B) asm volatile(PROBE_I " %0\n\ts_waitcnt lgkmcnt(0)" : "=s"(pb_) :: "memory"); } while (0)
#define STAMP_OUT(buf, n) do { if (bid == PROBE_W && wid == 4) { const unsigned long long d_ = 10000 + (((pb_ - pa_) * 16) >> PROBE_S); \
    while (__builtin_amdgcn_s_memrealtime() - p0_ < d_ && d_ < 100000ull) __builtin_amdgcn_s_sleep(1); } } while (0)
template <int CTRL> DI float dpp_add(float v) { return v + U2F(__builtin_amdgcn_update_dpp(0u, F2U(v), CTRL, 0xf, 0xf, true)); }
DI float half_sum(float v) {
  v = dpp_add<0xB1>(v); v = dpp_add<0x4E>(v); v = dpp_add<0x141>(v); v = dpp_add<0x140>(v);
  auto rr = __builtin_amdgcn_permlane16_swap(F2U(v), F2U(v), false, false);
  return U2F(rr[0]) + U2F(rr[1]);
}
DI float col_max(float v) {
  auto a = __builtin_amdgcn_permlane16_swap(F2U(v), F2U(v), false, false);
  v = __builtin_fmaxf(U2F(a[0]), U2F(a[1]));
  auto b = __builtin_amdgcn_permlane32_swap(F2U(v), F2U(v), false, false);
  return __builtin_fmaxf(U2F(b[0]), U2F(b[1]));
}
typedef __attribute__((address_space(1))) unsigned long long gu64;

#define SX(b, h) (98304 + (b) * 32768 + (h) * 16384)
constexpr int SLOT_B = 0, SLOT_E = 32768, SLOT_A = 65536, SLOT_C = 98304, SLOT_D = 131072, K1_LDS = 163840;

constexpr int WS_HFLAG = 4096;
constexpr int WS_GFLAG = 65536;
constexpr int WS_OFLAG = 131072;
constexpr int OFS = 16;
constexpr int WS_EFLAG = 163840;
constexpr int WS_STATS = 1 << 20;
constexpr int WS_PFLAG = 196608;
constexpr int WS_G = 5 << 20, WS_O = 9 << 20, WS_H = 25 << 20, WS_P = 39 << 20, WS_END = 47 << 20;
constexpr int K2_STG = 24576, K2_C = 98304;

NOPK __global__ __launch_bounds__(512, 2) void mega_kernel(const char* __restrict__ xn, const char* __restrict__ kc, const char* __restrict__ vc,
                                                           const char* __restrict__ wq, const char* __restrict__ w1, const char* __restrict__ wo, const char* __restrict__ w2,
                                                           char* ws, char* __restrict__ out) {
  STAMP_DECL
  STAMP(0);
  extern __shared__ __attribute__((aligned(16))) char shm[];
  const lptr lds = (lptr)shm;
  const unsigned lds0 = (unsigned)(uintptr_t)lds;
  const int wid = __builtin_amdgcn_readfirstlane(TID >> 6), wr = wid >> 2, wc = wid & 3;
  const int bid = BID, xcd = bid & 7, idx = bid >> 3, hd = (idx & 2) * 8 + 2 * xcd + (idx & 1), rb = idx >> 2;
  int lane, g, t, q4, p4;
#define RELANE() do { lane = __builtin_amdgcn_mbcnt_hi(~0u, __builtin_amdgcn_mbcnt_lo(~0u, 0u)); asm volatile("" : "+v"(lane)); g = lane >> 4; t = lane & 15; q4 = (lane >> 2) & 3; p4 = lane & 3; } while (0)
  RELANE();

  const bool light = (idx & 2) != 0;
  unsigned voffX[4], voffQ[2], voffH[2];
  {
    const int r = lane >> 3, c = lane & 7;
#pragma unroll
    for (int i = 0; i < 4; ++i) voffX[i] = (unsigned)((8 * i + r) * 2048 + ((c ^ (4 * (i & 1) + (r >> 1))) << 4) - i * 1024);
    const int k = 32 * (wid >> 2) + 16 * (wid & 1) + 4 * ((wid >> 1) & 1) + 8 * (r >> 2) + (r & 3);
    const int cw = c ^ (((r >> 1) & 3) << 1);
    voffQ[0] = (unsigned)(k * 8192 + cw * 16 + 1024); voffQ[1] = voffQ[0] + 128 - 1024;
    voffH[0] = (unsigned)(k * 7168 + 128 + (light ? (cw & 3) : cw) * 16 + 1024);
    voffH[1] = (unsigned)(k * 7168 + cw * 16);
  }
  const char* xb = xn + (size_t)rb * 256 * 2048 + wid * 65536;
  const char* qb = wq + hd * 256 - 1024;
  const char* hb = w1 + (light ? 4096 + 192 * (hd - 16) : 256 * hd) - 1024;
  const bool full = wr == 1 || !light;
  const int sb = hd >= 24;
#define GLDS2(v0_, v1_, sb_, ld_) asm volatile("s_mov_b32 m0, %3\n\ts_nop 0\n\tglobal_load_lds_dwordx4 %0, %2\n\tglobal_load_lds_dwordx4 %1, %2 offset:1024" \
    ::"v"(v0_), "v"(v1_), "s"(sb_), "s"(ld_) : "memory")
#define GLDS4(v0_, v1_, v2_, v3_, sb_, ld_) asm volatile("s_mov_b32 m0, %5\n\ts_nop 0\n\tglobal_load_lds_dwordx4 %0, %4\n\tglobal_load_lds_dwordx4 %1, %4 offset:1024\n\t" \
    "global_load_lds_dwordx4 %2, %4 offset:2048\n\tglobal_load_lds_dwordx4 %3, %4 offset:3072" ::"v"(v0_), "v"(v1_), "v"(v2_), "v"(v3_), "s"(sb_), "s"(ld_) : "memory")
#define ST_X(b, kt) GLDS4(voffX[0], voffX[1], voffX[2], voffX[3], xb + (kt) * 128, lds0 + SX(b, 0) + wid * 4096)
#define ST_W0(wo_, kt) GLDS2(voffQ[0], voffQ[1], qb + (size_t)(kt) * 524288, lds0 + (wo_) + wid * 2048)
#define ST_W1(wo_, kt) GLDS2(voffH[0], voffH[1], hb + (size_t)(kt) * 458752, lds0 + (wo_) + 16384 + wid * 2048)

  lptr wb[4], xr[2];
#pragma unroll
  for (int m = 0; m < 4; ++m) wb[m] = lds + wr * 1024 + (g >> 1) * 2048 + (4 * (g & 1) + q4) * 128 + 32 * ((m ^ wc) ^ (2 * (g & 1) + (q4 >> 1))) + 8 * p4;
#pragma unroll
  for (int ks = 0; ks < 2; ++ks) xr[ks] = lds + 98304 + (wc * 32 + t) * 128 + 16 * ((4 * ks + g) ^ (t >> 1));
#define LDW(dst, h) do { _Pragma("unroll") for (int m = 0; m < 4; ++m) _Pragma("unroll") for (int k = 0; k < 2; ++k) dst[m][k] = ldtr2(wb[m] + (h) * 16384 + k * 8192, 4096); } while (0)
#define LDX(dst, b, h) do { _Pragma("unroll") for (int n = 0; n < 2; ++n) _Pragma("unroll") for (int k = 0; k < 2; ++k) dst[n][k] = ldb128(xr[k] + (SX(b, h) - 98304) + n * 2048); } while (0)

  f32x4 acc[2][2][4][2];
#pragma unroll
  for (int a = 0; a < 2; ++a)
#pragma unroll
    for (int b = 0; b < 2; ++b)
#pragma unroll
      for (int m = 0; m < 4; ++m)
#pragma unroll
        for (int n = 0; n < 2; ++n) acc[a][b][m][n] = (f32x4){0.f, 0.f, 0.f, 0.f};
  bf16x8 At[4][2], B0[2][2], B1[2][2];

#define MM1(ai, m, k, j) do { if ((j) < 2) acc[ai][0][m][(j) & 1] = MFMA16(At[m][k], B0[(j) & 1][k], acc[ai][0][m][(j) & 1]); else acc[ai][1][m][(j) & 1] = MFMA16(At[m][k], B1[(j) & 1][k], acc[ai][1][m][(j) & 1]); SCHED(); } while (0)
#define MM8(ai, m) do { _Pragma("unroll") for (int q = 0; q < 4; ++q) { MM1(ai, m, q & 1, ((m) & 1) ? 3 - q : q); MM1(ai, m, (q & 1) ^ 1, ((m) & 1) ? 3 - q : q); } } while (0)
#define MMA2(ai, CLOSE) do { __builtin_amdgcn_s_setprio(1); \
    cs[ai] = MFMA16(At[0][1], ones, cs[ai]); SCHED(); cs[ai] = MFMA16(At[0][0], ones, cs[ai]); SCHED(); MM8(ai, 0); asm volatile("" : "+v"(cs[ai])); MM8(ai, 1); \
    if ((ai) == 0 || full) { MM8(ai, 2); MM8(ai, 3); } \
    SCHED(); CLOSE; __builtin_amdgcn_s_setprio(0); SCHED(); } while (0)
  char* const stats = ws + WS_STATS;
  const __amdgpu_buffer_rsrc_t wrs = __builtin_amdgcn_make_buffer_rsrc(ws, 0, WS_END, 0x00020000);
  f32x4 cs[2] = {{0.f, 0.f, 0.f, 0.f}, {0.f, 0.f, 0.f, 0.f}};
  bf16x8 ones = __builtin_bit_cast(bf16x8, (u32x4){0x3f803f80u, 0x3f803f80u, 0x3f803f80u, 0x3f803f80u});
#define PIN_SETUP() do { asm volatile("" : "+v"(ones), "+v"(cs[0]), "+v"(cs[1]), "+v"(wb[0]), "+v"(wb[1]), "+v"(wb[2]), "+v"(wb[3]), "+v"(xr[0]), "+v"(xr[1])); \
    _Pragma("unroll") for (int a = 0; a < 2; ++a) _Pragma("unroll") for (int b = 0; b < 2; ++b) \
      asm volatile("" : "+v"(acc[a][b][0][0]), "+v"(acc[a][b][0][1]), "+v"(acc[a][b][1][0]), "+v"(acc[a][b][1][1]), "+v"(acc[a][b][2][0]), "+v"(acc[a][b][2][1]), "+v"(acc[a][b][3][0]), "+v"(acc[a][b][3][1])); } while (0)
  unsigned tag;
  {
    if (wr == 0) {
      unsigned ep;
      ST_X(0, 0); ST_W0(0, 0);
      GLDS4(voffX[0], voffX[1], voffX[2], voffX[3], xb + 262144, lds0 + SX(0, 1) + wid * 4096);
      GLDS2(voffQ[0], voffQ[1], qb + 262144, lds0 + 8192 + wid * 2048);
      asm volatile("global_load_dword %0, %1, %2 sc1" : "=&v"(ep) : "v"(0), "s"(ws) : "memory");
      PIN_SETUP();
      asm volatile("s_waitcnt vmcnt(0)" : "+v"(ep) :: "memory");
      tag = __builtin_amdgcn_readfirstlane(ep) + 1u;
      BAR();
    } else {
      const int row = rb * 256 + hd * 8 + wc * 2 + (lane >> 5);
      u32x4 a[4];
      unsigned ep;
      asm volatile("global_load_dword %0, %1, %2 sc1" : "=&v"(ep) : "v"(0), "s"(ws) : "memory");
      asm volatile("global_load_dwordx4 %0, %4, %5\n\tglobal_load_dwordx4 %1, %4, %5 offset:512\n\tglobal_load_dwordx4 %2, %4, %5 offset:1024\n\tglobal_load_dwordx4 %3, %4, %5 offset:1536"
                   : "=&v"(a[0]), "=&v"(a[1]), "=&v"(a[2]), "=&v"(a[3]) : "v"(row * 2048 + (lane & 31) * 16), "s"(xn) : "memory");
      ST_W1(0, 0); GLDS2(voffH[0], voffH[1], hb - 229376, lds0 + 16384 + wc * 2048);
      PIN_SETUP();
      BAR();
      ST_W0(32768, 1); GLDS2(voffQ[0], voffQ[1], qb + 524288 - 262144, lds0 + 32768 + wc * 2048);
      ST_X(1, 1); GLDS4(voffX[0], voffX[1], voffX[2], voffX[3], xb - 262144 + 128, lds0 + SX(1, 0) + wc * 4096);
      asm volatile("s_waitcnt vmcnt(16)" : "+v"(ep), "+v"(a[0]), "+v"(a[1]), "+v"(a[2]), "+v"(a[3]) :: "memory");
      tag = __builtin_amdgcn_readfirstlane(ep) + 1u;
      float v[32];
#pragma unroll
      for (int i = 0; i < 16; ++i) { v[2 * i] = bf2f(a[i >> 2][i & 3] & 0xffffu); v[2 * i + 1] = U2F(a[i >> 2][i & 3] & 0xffff0000u); }
      float s0 = 0.f, s1 = 0.f, s2 = 0.f, s3 = 0.f;
#pragma unroll
      for (int i = 0; i < 8; ++i) { s0 += v[i]; s1 += v[8 + i]; s2 += v[16 + i]; s3 += v[24 + i]; }
      const float mean = half_sum((s0 + s1) + (s2 + s3)) * (1.0f / 1024.0f);
      s0 = s1 = s2 = s3 = 0.f;
#pragma unroll
      for (int i = 0; i < 8; ++i) { const float d0 = v[i] - mean, d1 = v[8 + i] - mean, d2 = v[16 + i] - mean, d3 = v[24 + i] - mean; s0 += d0 * d0; s1 += d1 * d1; s2 += d2 * d2; s3 += d3 * d3; }
      const float sd = __builtin_amdgcn_sqrtf(half_sum((s0 + s1) + (s2 + s3)) * (1.0f / 1024.0f) + 1e-5f);
      if ((lane & 31) < 2) __hip_atomic_store((gu64*)(stats + row * 16 + (lane & 1) * 8), ((unsigned long long)tag << 32) | F2U((lane & 1) ? sd : mean), __ATOMIC_RELAXED, __HIP_MEMORY_SCOPE_AGENT);
    }
  }
  if (wr == 1) BAR();
  SCHED();
  STAMP(1);
  const int kvg = hd >> 3;
  const char* kb_g = kc + ((size_t)kvg * 2048 + 1536) * 256;
  const char* vb_g = vc + ((size_t)kvg * 2048 + 1536) * 256;
#define VOFFK ((unsigned)((lane >> 3) * 256 + (((lane & 7) ^ (4 * (wid & 1) + (lane >> 4))) << 4)))
#define VOFFV ((unsigned)((lane >> 3) * 256 + (((lane & 7) ^ ((lane >> 4) << 1)) << 4)))
#define ST_KV8(voff, gsrc, slot) do { glds(voff, (gsrc) + wid * 2048, lds0 + (slot) + wid * 2048); glds(voff, (gsrc) + wid * 2048 + 128, lds0 + (slot) + wid * 2048 + 1024); } while (0)
  int wprv = 65536, wnxt = 32768;
#define W_ADV() do { const int d_ = (wnxt == 0) ? -65536 : 32768, cur_ = wnxt - d_; wnxt = wprv; wprv = cur_; \
    _Pragma("unroll") for (int f = 0; f < 4; ++f) wb[f] += d_; \
    asm volatile("" : "+v"(wb[0]), "+v"(wb[1]), "+v"(wb[2]), "+v"(wb[3])); } while (0)
#define PH_A(b, STAGE) do { LDX(B0, b, 0); LDX(B1, b, 1); SCHED(); LDW(At, 0); STAGE; WAIT_V(10); WAIT_L(12); BAR(); SCHED(); MMA2(0, WAIT_L(0); BAR()); } while (0)
#define PH_B(STAGE) do { LDW(At, 1); STAGE; W_ADV(); WAIT_V(8); BAR(); SCHED(); MMA2(1, WAIT_L(0); BAR()); } while (0)
#pragma unroll 1
  for (int kt = 0; kt < 14; kt += 2) {
    PH_A(0, ST_W1(wnxt, kt + 1); ST_W0(wprv, kt + 2));
    PH_B(ST_X(0, kt + 2));
    PH_A(1, ST_W1(wnxt, kt + 2); ST_W0(wprv, kt + 3));
    PH_B(ST_X(1, kt + 3));
  }
  STAMP(2);
  RELANE();
  const unsigned voffK = VOFFK, voffV = VOFFV;
  PH_A(0, ST_W1(0, 15); ST_KV8(voffK, kb_g, SLOT_E));
  PH_B(ST_KV8(voffV, vb_g, SLOT_C); ST_KV8(voffV, vb_g + 16384, SLOT_C + 16384));
  const unsigned soff = (unsigned)((rb * 256 + (g >> 1) * 128 + wc * 32 + (g & 1) * 16 + t) * 16);
  u32x4 sg;
  LDX(B0, 1, 0); LDX(B1, 1, 1); SCHED(); LDW(At, 0);
  asm volatile("global_load_dwordx4 %0, %1, %2 sc1" : "=&v"(sg) : "v"(soff), "s"(stats) : "memory");
  ST_KV8(voffK, kb_g + 16384, SLOT_E + 16384); ST_KV8(voffK, kb_g + 32768, SLOT_A);
  WAIT_V(11); BAR(); SCHED(); MMA2(0, WAIT_L(0); BAR());
  LDW(At, 1);
  ST_KV8(voffK, kb_g + 49152, SLOT_A + 16384);
  WAIT_L(0); BAR(); SCHED();
  const lptr csw = lds + SLOT_D + (t & 3) * 8192 + wr * 4096 + wc * 64 + g * 16;
  MMA2(1, *(LAS f32x4*)csw = cs[0]; *(LAS f32x4*)(csw + 256) = cs[1]; asm volatile("s_waitcnt vmcnt(4)" : "+v"(sg) :: "memory"); WAIT_L(0); BAR());
  f32x4 cv[2][4];
#pragma unroll
  for (int a = 0; a < 2; ++a)
#pragma unroll
    for (int m = 0; m < 4; ++m) cv[a][m] = *(const LAS f32x4*)(lds + SLOT_D + wc * 8192 + wr * 4096 + a * 256 + (m ^ wc) * 64 + g * 16);
  if (__builtin_expect(__builtin_amdgcn_ballot_w64(sg[1] != tag || sg[3] != tag) != 0ull, 0)) {
    for (unsigned spins = 0; spins < 4000000u; ++spins) {
      __builtin_amdgcn_s_sleep(2);
      asm volatile("" ::: "memory");
      sg = __builtin_amdgcn_raw_buffer_load_b128(wrs, soff, WS_STATS, 16);
      if (__builtin_amdgcn_ballot_w64(sg[1] != tag || sg[3] != tag) == 0ull) break;
    }
  }
  float mu[2][2], rs[2][2], sd[2][2];
  {
    auto m16 = __builtin_amdgcn_permlane16_swap(sg[0], sg[0], false, false);
    auto s16 = __builtin_amdgcn_permlane16_swap(sg[2], sg[2], false, false);
#pragma unroll
    for (int n = 0; n < 2; ++n) {
      auto m32 = __builtin_amdgcn_permlane32_swap(m16[n], m16[n], false, false);
      auto s32 = __builtin_amdgcn_permlane32_swap(s16[n], s16[n], false, false);
#pragma unroll
      for (int bj = 0; bj < 2; ++bj) { mu[bj][n] = -U2F(m32[bj]); sd[bj][n] = U2F(s32[bj]); rs[bj][n] = __builtin_amdgcn_rcpf(sd[bj][n]) * 1.4426950408889634f; }
    }
  }
  WAIT_L(0);
  STAMP(3);
  {
    const lptr qw = lds + wc * 8192 + wr * 4096 + ((g >> 1) * 16 + t) * 16 + (g & 1) * 8;
#pragma unroll
    for (int bj = 0; bj < 2; ++bj) {
      lptr qwb = qw + ((bj ^ sb) ? SLOT_D : SLOT_B);
      asm volatile("" : "+v"(qwb));
#pragma unroll
      for (int m = 0; m < 4; ++m)
#pragma unroll
        for (int n = 0; n < 2; ++n) {
          const f32x4 v = (acc[0][bj][m][n] + cv[0][m] * mu[bj][n]) * (rs[bj][n] * 0.08838834764831845f);
          u32x2 w = {pk2(v[0], v[1]), pk2(v[2], v[3])};
          *(LAS u32x2*)(qwb + ((m ^ wc) >> 1) * 2048 + ((m ^ wc) & 1) * 512 + n * 1024) = w;
        }
    }
  }
  WAIT_L(0); BAR(); SCHED();
  STAMP(4);
  unsigned pft = 0;
  const int sl = ((idx >> 2) * 16 + (idx & 1) * 8 + xcd) * 8 + wid;
  const int tstr = light ? 896 : 1024;
  const char* tbase = (light ? w2 : wo) + (sl < tstr ? sl : tstr - 1) * 2048;
#define TOUCH(q_) asm volatile("global_load_dword %0, %1, %2" : "+v"(pft) : "v"((unsigned)((__builtin_amdgcn_mbcnt_lo(~0u, 0u) & 15) * 128)), "s"(tbase + (((q_) + 2) & 3) * tstr * 2048) : "memory")
  TOUCH(0);
  bf16x8 qf[8];
  const lptr qr = lds + (wr ? SLOT_D : SLOT_B) + wc * 8192 + lane * 16;
  if (wr == 1) {
#pragma unroll
    for (int s = 0; s < 8; ++s) qf[s] = ldb128(qr + s * 1024);
  }
#define ST128(...) ST128_(wrs, __VA_ARGS__)
  u32x4 hq[3];
  unsigned hqo;
  {
    u32x2 hw[2][2][2];
#define SILU16(m, ACT) do { \
      const f32x4 a0 = acc[1][0][m][0] + cv[1][m] * mu[0][0], a1 = acc[1][0][m][1] + cv[1][m] * mu[0][1], a2 = acc[1][1][m][0] + cv[1][m] * mu[1][0], a3 = acc[1][1][m][1] + cv[1][m] * mu[1][1]; \
      f32x4 e0 = a0 * -rs[0][0], e1 = a1 * -rs[0][1], e2 = a2 * -rs[1][0], e3 = a3 * -rs[1][1]; SCHED(); \
      if (ACT) { \
      _Pragma("unroll") for (int i = 0; i < 4; ++i) { e0[i] = __builtin_amdgcn_exp2f(e0[i]); e1[i] = __builtin_amdgcn_exp2f(e1[i]); e2[i] = __builtin_amdgcn_exp2f(e2[i]); e3[i] = __builtin_amdgcn_exp2f(e3[i]); } SCHED(); \
      e0 = e0 * sd[0][0] + sd[0][0]; e1 = e1 * sd[0][1] + sd[0][1]; e2 = e2 * sd[1][0] + sd[1][0]; e3 = e3 * sd[1][1] + sd[1][1]; SCHED(); \
      _Pragma("unroll") for (int i = 0; i < 4; ++i) { e0[i] = __builtin_amdgcn_rcpf(e0[i]); e1[i] = __builtin_amdgcn_rcpf(e1[i]); e2[i] = __builtin_amdgcn_rcpf(e2[i]); e3[i] = __builtin_amdgcn_rcpf(e3[i]); } SCHED(); \
      e0 *= a0; e1 *= a1; e2 *= a2; e3 *= a3; SCHED(); } \
      else { _Pragma("unroll") for (int i = 0; i < 4; ++i) e3[i] = a3[i] * __builtin_amdgcn_rcpf(__builtin_amdgcn_exp2f(e3[i]) * sd[1][1] + sd[1][1]); } \
      hw[0][0][(m) & 1] = (u32x2){pk2(e0[0], e0[1]), pk2(e0[2], e0[3])}; hw[0][1][(m) & 1] = (u32x2){pk2(e1[0], e1[1]), pk2(e1[2], e1[3])}; \
      hw[1][0][(m) & 1] = (u32x2){pk2(e2[0], e2[1]), pk2(e2[2], e2[3])}; hw[1][1][(m) & 1] = (u32x2){pk2(e3[0], e3[1]), pk2(e3[2], e3[3])}; SCHED(); } while (0)
#define HFRAG(bj, n) ((wc & 1) ? (u32x4){hw[bj][n][1][0], hw[bj][n][1][1], hw[bj][n][0][0], hw[bj][n][0][1]} : (u32x4){hw[bj][n][0][0], hw[bj][n][0][1], hw[bj][n][1][0], hw[bj][n][1][1]})
    const unsigned hoff = (unsigned)(WS_H + (rb * 16 + wc * 2) * 2048 + lane * 16);
    const int pb = (light ? 16 + 3 * hd : 4 * hd) + (wr ? 0 : 2), sw = full ? (wc >> 1) : 0, p0 = pb + sw, p1 = pb + (sw ^ 1);
    SILU16(0, false); SILU16(1, false);
    hqo = hoff + (p0 >> 1) * 262144 + (p0 & 1) * 1024;
    hq[0] = HFRAG(0, 0); hq[1] = HFRAG(0, 1); hq[2] = HFRAG(1, 0);
    ST128(HFRAG(1, 1), hqo + 2048, 16384);
    asm volatile("" : "+v"(hq[0]), "+v"(hq[1]), "+v"(hq[2]));
    if (wr == 0) {
      BAR(); SCHED();
#pragma unroll
      for (int s = 0; s < 8; ++s) qf[s] = ldb128(qr + s * 1024);
    }
    if (full) {
      SILU16(2, true); SILU16(3, true);
#pragma unroll
      for (int bj = 0; bj < 2; ++bj)
#pragma unroll
        for (int n = 0; n < 2; ++n) ST128(HFRAG(bj, n), hoff + n * 2048, (p1 >> 1) * 262144 + (p1 & 1) * 1024 + bj * 16384);
    }
  }
  STAMP(5);
  if (wr == 1) { WAIT_L(0); BAR(); }
  __builtin_amdgcn_s_setprio(1);
  SCHED();
  STAMP(6);
  RELANE();
  lptr kb[2], vb[4];
#pragma unroll
  for (int e = 0; e < 2; ++e) kb[e] = lds + SLOT_E + (t >> 3) * 2048 + (t & 7) * 128 + (((4 * e + g) ^ (t >> 1)) << 4);
#pragma unroll
  for (int f = 0; f < 4; ++f) vb[f] = lds + SLOT_C + (g >> 1) * 2048 + (4 * (g & 1) + q4) * 128 + 32 * (f ^ (2 * (g & 1) + (q4 >> 1))) + 8 * p4;
  f32x4 o[8][2], s[8][2], lacc[2], negm[2];
#pragma unroll
  for (int n = 0; n < 2; ++n) {
    lacc[n] = (f32x4){0.f, 0.f, 0.f, 0.f};
#pragma unroll
    for (int d = 0; d < 8; ++d) o[d][n] = (f32x4){0.f, 0.f, 0.f, 0.f};
  }
  bf16x8 pf[8], Kf[32];
  bf16x4 Vh[32][2];
#define SM_LIM 1.152921504606847e18f
  constexpr int LA = 6;
#define MX3(a, b, c) __builtin_fmaxf(__builtin_fmaxf((a), (b)), (c))
#define RD_V(f, hf) Vh[f][hf] = __builtin_amdgcn_ds_read_tr16_b64_v4bf16((LAS bf16x4*)(vb[((f) >> 2) & 3] + ((f) >> 4) * 1024 + ((f) & 3) * 8192 + (hf) * 4096))
#define RD_K(f) Kf[f] = ldb128(kb[(f) & 1] + ((f) >> 2) * 4096 + (((f) >> 1) & 1) * 1024)
#define VF(f) __builtin_shufflevector(Vh[f][0], Vh[f][1], 0, 1, 2, 3, 4, 5, 6, 7)
#define PREF_V() do { _Pragma("unroll") for (int f = 0; f < LA; ++f) { RD_V(f, 0); RD_V(f, 1); } SCHED(); } while (0)
#define PV_SEG(NEXTK, FILL) do { \
    _Pragma("unroll") for (int i = 0; i < 64; ++i) { const int d = i >> 3, n = (i >> 2) & 1, k = n ? 3 - (i & 3) : (i & 3), f = (i >> 1) + LA; \
      o[d][n] = MFMA16(VF(4 * d + k), pf[2 * k + n], o[d][n]); SCHED(); \
      if (f < 32) RD_V(f < 32 ? f : 0, i & 1); else if ((NEXTK) && (i & 1)) RD_K(f >= 32 ? f - 32 : 0); \
      FILL; SCHED(); } } while (0)
#define QK_SEG(HAVE, CLOSE) do { \
    _Pragma("unroll") for (int i = 0; i < 64; ++i) { const int r = i >> 3, n = (i >> 2) & 1, k = n ? 3 - (i & 3) : (i & 3), f = (i >> 1) + LA; \
      s[r][n] = MFMA16(Kf[4 * r + k], qf[2 * k + n], (i & 3) == 0 ? negm[n] : s[r][n]); SCHED(); \
      if ((i & 1) && f < 32 && f >= (HAVE)) RD_K(f < 32 ? f : 0); \
      SCHED(); \
      if (i == 59) { __builtin_amdgcn_s_setprio(2); CLOSE; WAIT_L(0); BAR(); SCHED(); } } \
    __builtin_amdgcn_s_setprio(0); } while (0)
#define MX4(m_, v_) MX3(MX3(m_, v_[0], v_[1]), v_[2], v_[3])
#define ROWMAX(n) MX4(MX4(MX4(MX4(MX4(MX4(MX4(MX4(s[0][n][0], s[0][n]), s[1][n]), s[2][n]), s[3][n]), s[4][n]), s[5][n]), s[6][n]), s[7][n])
#define EXP_PACK() do { \
      _Pragma("unroll") for (int k = 0; k < 4; ++k) { \
        _Pragma("unroll") for (int n = 0; n < 2; ++n) { \
          _Pragma("unroll") for (int i = 0; i < 4; ++i) { s[2 * k][n][i] = __builtin_amdgcn_exp2f(s[2 * k][n][i]); s[2 * k + 1][n][i] = __builtin_amdgcn_exp2f(s[2 * k + 1][n][i]); } \
          u32x4 w = {pk2(s[2 * k][n][0], s[2 * k][n][1]), pk2(s[2 * k][n][2], s[2 * k][n][3]), pk2(s[2 * k + 1][n][0], s[2 * k + 1][n][1]), pk2(s[2 * k + 1][n][2], s[2 * k + 1][n][3])}; \
          pf[2 * k + n] = __builtin_bit_cast(bf16x8, w); } \
        asm volatile("" : "+v"(pf[2 * k]), "+v"(pf[2 * k + 1])); \
        SCHED(); } \
      __builtin_amdgcn_s_setprio(2); \
      _Pragma("unroll") for (int i = 0; i < 8; ++i) { ln[i >> 2] = MFMA16(ones, pf[2 * (i & 3) + (i >> 2)], (i & 3) == 0 ? lacc[i >> 2] : ln[i >> 2]); SCHED(); } \
      __builtin_amdgcn_s_setprio(0); } while (0)
#define SOFTMAX(PRE) do { f32x4 ln[2]; \
      EXP_PACK(); \
      PRE; \
      if (__builtin_expect(__builtin_amdgcn_ballot_w64(!(ln[0][0] <= SM_LIM && ln[1][0] <= SM_LIM)) != 0ull, 0)) { \
        _Pragma("unroll") for (int r = 0; r < 8; ++r) _Pragma("unroll") for (int k = 0; k < 4; ++k) { \
          const bf16x8 kf_ = ldb128(kb[k & 1] + r * 4096 + (k >> 1) * 1024); \
          s[r][0] = MFMA16(kf_, qf[2 * k], k == 0 ? negm[0] : s[r][0]); s[r][1] = MFMA16(kf_, qf[2 * k + 1], k == 0 ? negm[1] : s[r][1]); if (k == 3) SCHED(); } \
        const float m0 = ROWMAX(0), m1 = ROWMAX(1); \
        _Pragma("unroll") for (int n = 0; n < 2; ++n) { \
          const float dl = __builtin_fmaxf(col_max(n ? m1 : m0), 0.f), alpha = __builtin_amdgcn_exp2f(-dl); \
          negm[n] -= dl; lacc[n] *= alpha; \
          _Pragma("unroll") for (int r = 0; r < 8; ++r) { s[r][n] -= dl; o[r][n] *= alpha; } } \
        EXP_PACK(); \
        PRE; \
      } \
      lacc[0] = ln[0]; lacc[1] = ln[1]; SCHED(); } while (0)
  {
#pragma unroll
    for (int f = 0; f < 8; ++f) RD_K(f);
#pragma unroll
    for (int d = 0; d < 8; d += 2) asm volatile("" : "+v"(o[d][0]), "+v"(o[d][1]), "+v"(o[d + 1][0]), "+v"(o[d + 1][1]));
    f32x4 u[2][2];
    const f32x4 z4 = {0.f, 0.f, 0.f, 0.f};
#pragma unroll
    for (int i = 0; i < 16; ++i) { const int r = i >> 3, n = (i >> 2) & 1, k = i & 3; u[r][n] = MFMA16(Kf[4 * r + k], qf[2 * k + n], k == 0 ? z4 : u[r][n]); }
#pragma unroll
    for (int n = 0; n < 2; ++n) { const float m = -col_max(MX4(MX3(u[0][n][0], u[0][n][1], __builtin_fmaxf(u[0][n][2], u[0][n][3])), u[1][n])); negm[n] = (f32x4){m, m, m, m}; }
    asm volatile("" : "+v"(negm[0]), "+v"(negm[1]));
    SCHED();
    QK_SEG(8, WAIT_V(1));
  }
  STAMP(7);
  const char* kvsrc = (wr ? kb_g + 65536 : vb_g + 32768) + wc * 8192 - 1024;
  unsigned kvo[2][2];
#pragma unroll
  for (int par = 0; par < 2; ++par) {
    kvo[par][0] = (unsigned)((lane >> 3) * 256 + (((lane & 7) ^ (wr ? 4 * par + (lane >> 4) : (lane >> 4) << 1)) << 4) + 1024);
    kvo[par][1] = kvo[par][0] + 128 - 1024;
  }
#pragma unroll 1
  for (int c = 0; c < 3; ++c) {
    if (c == 0) STAMP(8); else if (c == 1) STAMP(12);
    {
      float tv[8], ev[8];
#pragma unroll
      for (int e = 0; e < 4; ++e) { tv[2 * e] = bf2f(hq[0][e] & 0xffffu); tv[2 * e + 1] = U2F(hq[0][e] & 0xffff0000u); }
      SCHED();
#pragma unroll
      for (int e = 0; e < 8; ++e) ev[e] = __builtin_amdgcn_exp2f(tv[e]);
      SCHED();
#pragma unroll
      for (int e = 0; e < 8; ++e) ev[e] = ev[e] * -1.4426950408889634f - 1.4426950408889634f;
      SCHED();
#pragma unroll
      for (int e = 0; e < 8; ++e) ev[e] = __builtin_amdgcn_rcpf(ev[e]);
      SCHED();
      ST128((u32x4){pk2(ev[0] * tv[0], ev[1] * tv[1]), pk2(ev[2] * tv[2], ev[3] * tv[3]), pk2(ev[4] * tv[4], ev[5] * tv[5]), pk2(ev[6] * tv[6], ev[7] * tv[7])}, hqo, c == 2 ? 16384 : c * 2048);
    }
    hq[0] = hq[1]; hq[1] = hq[2];
    SCHED();
    if (__builtin_expect(c + wr < 3, 1)) {
      const int slot = wr ? (c == 0 ? SLOT_B : SLOT_C) : (c == 0 ? SLOT_D : (c == 1 ? SLOT_E : SLOT_A));
#pragma unroll
      for (int hf = 0; hf < 2; ++hf)
        asm volatile("s_mov_b32 m0, %5\n\ts_nop 0\n\t"
                     "global_load_lds_dwordx4 %0, %4\n\tglobal_load_lds_dwordx4 %1, %4 offset:1024\n\t"
                     "global_load_lds_dwordx4 %2, %4 offset:2048\n\tglobal_load_lds_dwordx4 %3, %4 offset:3072"
                     ::"v"(kvo[0][0]), "v"(kvo[0][1]), "v"(kvo[1][0]), "v"(kvo[1][1]), "s"(kvsrc + c * 32768 + hf * 4096), "s"(lds0 + slot + wc * 8192 + hf * 4096) : "memory");
    }
    TOUCH(c + 1);
    SOFTMAX(PREF_V());
    { const int dk = (c == 0) ? SLOT_A - SLOT_E : ((c == 1) ? SLOT_B - SLOT_A : SLOT_C - SLOT_B); kb[0] += dk; kb[1] += dk; asm volatile("" : "+v"(kb[0]), "+v"(kb[1])); }
    if (c == 0) STAMP(9); else if (c == 1) STAMP(13);
    BAR(); SCHED();
    if (c == 0) STAMP(10); else if (c == 1) STAMP(14);
    __builtin_amdgcn_s_setprio(1);
    PV_SEG(true, );
    QK_SEG(LA, WAIT_V(1); if (c == 2) WAIT_V(0));
    { const int dv = (c == 1) ? SLOT_E - SLOT_D : 32768;
#pragma unroll
      for (int f = 0; f < 4; ++f) vb[f] += dv; }
    if (c == 0) STAMP(11); else if (c == 2) STAMP(15);
  }
  RELANE();
  if (wid == 4 && lane == 0) __builtin_amdgcn_raw_buffer_store_b32(tag, wrs, 0, WS_HFLAG + (rb * 32 + hd) * 128, 16);
  STAMP(16);
  SOFTMAX(PREF_V());
  {
    asm volatile("s_waitcnt vmcnt(0)" :: "v"(pft) : "memory");
    SCHED();
    STAMP(17);
    if (wr == 0) BAR();
    SCHED();
    STAMP(18);
    __builtin_amdgcn_s_setprio(1);
    const unsigned oo = (unsigned)(WS_O + 2 * hd * 262144 + (rb * 16 + (wr ^ sb) * 8 + wc * 2) * 2048 + lane * 16);
    float inv[2] = {0.f, 0.f};
    u32x4 ow;
#define O_PIECE(j_) do { const int dp = (j_) >> 3, n_ = ((j_) >> 2) & 1, q_ = (j_) & 3; \
      ow[q_] = pk2(o[2 * dp + (q_ >> 1)][n_][2 * (q_ & 1)] * inv[n_], o[2 * dp + (q_ >> 1)][n_][2 * (q_ & 1) + 1] * inv[n_]); asm volatile("" : "+v"(ow)); \
      if (q_ == 3) ST128(ow, oo + n_ * 2048 + (dp & 1) * 1024, (dp >> 1) * 262144); } while (0)
    PV_SEG(false, if (i == 4) { inv[0] = __builtin_amdgcn_rcpf(lacc[0][0]); inv[1] = __builtin_amdgcn_rcpf(lacc[1][0]); } if (i >= 16 && (i & 1)) O_PIECE(i >= 16 ? (i - 16) >> 1 : 0));
#pragma unroll
    for (int j = 24; j < 32; ++j) O_PIECE(j);
    __builtin_amdgcn_s_setprio(0);
  }
  STAMP(19);
  RELANE();
  const int isG = (idx >> 1) & 1, kh = xcd & 1, tm = 4 * (xcd >> 1) + ((idx >> 1) & 2) + (idx & 1), tn = 2 * (idx >> 3) + kh;
  const int npw = (isG ? 48 : 56) - 4 * kh;
  const int k2c = ((npw + 19) % 6 < 4) ? K2_C : 0;
#define O_FLAG() do { if (wid == 0 && lane == 0) __builtin_amdgcn_raw_buffer_store_b32(tag, wrs, 0, WS_OFLAG + (rb * 32 + hd) * OFS, 16); } while (0)
#define POLL32(off_, slp) do { for (unsigned spins = 0; spins < 2000000u; ++spins) { \
      if (__builtin_amdgcn_ballot_w64(__builtin_amdgcn_raw_buffer_load_b32(wrs, (off_), 0, 16) != tag) == 0ull) break; \
      __builtin_amdgcn_s_sleep(slp); asm volatile("" ::: "memory"); } \
    asm volatile("" ::: "memory"); } while (0)
  const int c8 = lane & 15, r0 = wc * 4 + (lane >> 4);
  const unsigned goff = (unsigned)((tm * 128 + r0) * 2048 + tn * 256 + c8 * 16);
  const lptr cb = lds + k2c + r0 * 512;
#define G_FIN() do { f32x4 a0[4], a1[4]; \
      _Pragma("unroll") for (int i = 0; i < 4; ++i) { a0[i] = *(const LAS f32x4*)(cb + (wr * 4 + i) * 8192 + (((2 * c8) ^ (r0 & 7)) << 4)); a1[i] = *(const LAS f32x4*)(cb + (wr * 4 + i) * 8192 + (((2 * c8 + 1) ^ (r0 & 7)) << 4)); } \
      _Pragma("unroll") for (int i = 0; i < 4; ++i) \
        ST128((u32x4){pk2(silu(a0[i][0]), silu(a0[i][1])), pk2(silu(a0[i][2]), silu(a0[i][3])), pk2(silu(a1[i][0]), silu(a1[i][1])), pk2(silu(a1[i][2]), silu(a1[i][3]))}, goff + i * 32768, WS_G + wr * 131072); \
      WAIT_V(0); BAR(); } while (0)
  const unsigned f1b = isG ? (unsigned)(WS_HFLAG + ((tm >> 1) * 32 + 16 + (lane & 15)) * 128) : (unsigned)(WS_OFLAG + ((tm >> 1) * 32 + 16 + (lane & 15)) * OFS);
  const unsigned f1 = isG ? f1b : (unsigned)(WS_EFLAG + ((tm >> 1) * 16 + (tm & 1) * 8 + kh * 4 + ((lane >> 2) & 3)) * 64 + (lane & 3) * 4);
  STAMP(20);
  if (wr == 0) {
    const char* ab = ws + (isG ? WS_H : WS_O) + tm * 16384 + wc * 4096;
    const char* wbp = (isG ? w2 : wo) + tn * 256 - 1024;
    const int wsd = (kh ? -256 : 256) + wc * 32768;
    const unsigned voffA0 = lane * 16, voffA1 = lane * 16 + 1024;
    unsigned voffW[4];
    {
      const int r = lane >> 3, c = lane & 7;
      voffW[0] = (unsigned)(r * 2048 + ((c ^ (((r >> 1) & 3) << 1)) << 4) + 1024); voffW[1] = voffW[0] + 128 - 1024;
      voffW[2] = voffW[0] + 16384 - 2048; voffW[3] = voffW[2] + 128 - 1024;
    }
    const char *ap, *wp, *wo_ = wbp;
    unsigned dst = lds0;
#define K2_RUN(k_) do { ap = ab + (size_t)(k_) * 262144; wp = wbp + (size_t)(k_) * 131072; } while (0)
#define K2_A(ks, d_) asm volatile("s_mov_b32 m0, %3\n\ts_nop 0\n\tglobal_load_lds_dwordx4 %0, %2 sc1\n\tglobal_load_lds_dwordx4 %1, %2 offset:1024 sc1" \
                   ::"v"(voffA0), "v"(voffA1), "s"(ap + (ks) * 1024), "s"((d_) + wc * 2048) : "memory")
#define K2_ISSUE(WIDE, ks) do { K2_A(ks, dst); \
      if (WIDE) GLDS4(voffW[0], voffW[1], voffW[2], voffW[3], (ks) ? wp + wc * 32768 : wo_ + wsd, dst + 8192 + wc * 4096); \
      else GLDS2(voffW[0], voffW[1], ((ks) ? wp : wo_ + 65536) + wc * 16384, dst + 8192 + wc * 2048); \
      dst = (dst == lds0 + 5 * K2_STG) ? lds0 : dst + K2_STG; \
      if (ks) { wo_ = wp; ap += 262144; wp += 131072; } } while (0)
#define K2_STEP(WIDE, ks, EVENT) do { WAIT_V((WIDE) ? 18 : 12); BAR(); EVENT; K2_ISSUE(WIDE, ks); } while (0)
#define K2_TILES(WIDE, n_) do { _Pragma("unroll 1") for (int i_ = 0; i_ < (n_); ++i_) { K2_STEP(WIDE, 0, ); K2_STEP(WIDE, 1, ); } } while (0)
#define LOOK(f_) asm volatile("buffer_load_dword %0, %1, %2, 0 offen sc1" : "+v"(pol) : "v"(f_), "s"(wrs) : "memory")
    const unsigned f2 = f1b - 16 * (isG ? 128 : OFS);
    const unsigned f3 = (unsigned)(WS_PFLAG + (bid ^ 1) * 128);
    u32x4 xh[8];
#define XH_LD(i) asm volatile("global_load_dwordx4 %0, %1, %2" : "=v"(xh[i]) : "v"(goff), "s"(xn + (i) * 32768) : "memory")
    WAIT_V(0);
    if (isG && lane == 0) __builtin_amdgcn_raw_buffer_store_b32(tag, wrs, 0, WS_EFLAG + (rb * 16 + hd - 16) * 64 + wc * 4, 16);
    POLL32(f1, 4);
    K2_RUN(isG ? 32 + 12 * kh : 32 + 16 * (tm & 1) + 8 * kh);
    K2_A(0, lds0 + 6 * K2_STG); K2_ISSUE(1, 1); K2_ISSUE(1, 0);
    STAMP(21);
    WAIT_V(6); BAR();
    K2_ISSUE(1, 1); K2_ISSUE(1, 0); K2_ISSUE(1, 1);
    STAMP(22);
    unsigned pol = 0;
#define CHECK(f_) do { asm volatile("" : "+v"(pol)); if (__builtin_amdgcn_ballot_w64(pol != tag) != 0ull) POLL32(f_, 4); } while (0)
    K2_TILES(1, 3);
    O_FLAG();
    LOOK(f1b);
    K2_TILES(1, 2);
    if (!isG) K2_RUN(48 - 16 * (tm & 1) + 6 * kh);
    K2_STEP(1, 0, CHECK(f1b)); K2_STEP(1, 1, );
    K2_TILES(1, isG ? 1 : 3);
    LOOK(f2);
    K2_TILES(1, 2);
    K2_RUN((isG ? 12 : 14) * kh);
    K2_STEP(1, 0, CHECK(f2));
    K2_STEP(1, 1, );
#pragma unroll
    for (int i = 0; i < 8; ++i) { K2_STEP(1, 0, XH_LD(i)); K2_STEP(1, 1, ); }
    K2_TILES(1, (isG ? 3 : 5) - 2 * kh);
    asm volatile("" : "+v"(xh[0]), "+v"(xh[1]), "+v"(xh[2]), "+v"(xh[3]), "+v"(xh[4]), "+v"(xh[5]), "+v"(xh[6]), "+v"(xh[7]));
    K2_RUN(isG ? 22 : 60 - 16 * (tm & 1));
    K2_STEP(1, 0, ); K2_STEP(0, 1, );
    K2_TILES(0, 3);
    K2_RUN(26);
    K2_STEP(0, 0, ); K2_STEP(0, 1, );
    K2_STEP(0, 0, if (wid == 0 && lane == 0) __builtin_amdgcn_raw_buffer_store_b32(tag, wrs, 0, WS_PFLAG + bid * 128, 16));
    K2_STEP(0, 1, );
    K2_TILES(0, 2);
    K2_STEP(0, 0, );
    const unsigned f4 = (lane & 1) ? (unsigned)(WS_GFLAG + (tm * 8 + tn) * 128) : f3;
    K2_STEP(0, 1, LOOK(f4)); K2_STEP(0, 0, ); K2_STEP(0, 1, );
    WAIT_V(12);
    asm volatile("" : "+v"(pol));
    const unsigned long long miss = __builtin_amdgcn_ballot_w64(pol != tag);
    if (wid == 0 && (miss & 1ull)) POLL32(f3, 2);
    BAR();
    K2_ISSUE(0, 0);
    WAIT_V(12); BAR();
    WAIT_V(8); BAR();
    if (isG) {
      WAIT_V(4); BAR();
      WAIT_V(0); BAR();
      BAR();
      STAMP(23);
      BAR();
    } else {
      u32x4 gv[8];
      const __amdgpu_buffer_rsrc_t ors = __builtin_amdgcn_make_buffer_rsrc(out, 0, 2048 * 2048, 0x00020000);
#define LD8(dst_, rs_, so_, pol_) asm volatile("buffer_load_dwordx4 %0, %8, %10, %11 offen" pol_ "\n\tbuffer_load_dwordx4 %1, %9, %10, %11 offen" pol_ "\n\t" \
        "buffer_load_dwordx4 %2, %8, %10, %12 offen" pol_ "\n\tbuffer_load_dwordx4 %3, %9, %10, %12 offen" pol_ "\n\t" \
        "buffer_load_dwordx4 %4, %8, %10, %13 offen" pol_ "\n\tbuffer_load_dwordx4 %5, %9, %10, %13 offen" pol_ "\n\t" \
        "buffer_load_dwordx4 %6, %8, %10, %14 offen" pol_ "\n\tbuffer_load_dwordx4 %7, %9, %10, %14 offen" pol_ \
        : "=&v"(dst_[0]), "=&v"(dst_[1]), "=&v"(dst_[2]), "=&v"(dst_[3]), "=&v"(dst_[4]), "=&v"(dst_[5]), "=&v"(dst_[6]), "=&v"(dst_[7]) \
        : "v"(goff), "v"(goff + 32768), "s"(rs_), "s"((so_)), "s"((so_) + 65536), "s"((so_) + 131072), "s"((so_) + 196608) : "memory")
      if (miss & 2ull) {
        WAIT_V(4); BAR(); WAIT_V(0); BAR(); BAR();
        POLL32((unsigned)(WS_GFLAG + (tm * 8 + tn) * 128), 1);
      }
      LD8(gv, wrs, WS_G, " sc1");
      if (!(miss & 2ull)) { WAIT_V(12); BAR(); WAIT_V(8); BAR(); BAR(); }
      float xf[8][8];
#pragma unroll
      for (int i = 0; i < 8; ++i) {
#pragma unroll
        for (int e = 0; e < 4; ++e) { xf[i][2 * e] = bf2f(xh[i][e] & 0xffffu); xf[i][2 * e + 1] = U2F(xh[i][e] & 0xffff0000u); }
        asm volatile("" : "+v"(xf[i][0]), "+v"(xf[i][1]), "+v"(xf[i][2]), "+v"(xf[i][3]), "+v"(xf[i][4]), "+v"(xf[i][5]), "+v"(xf[i][6]), "+v"(xf[i][7]));
      }
      asm volatile("s_waitcnt vmcnt(0)" : "+v"(gv[0]), "+v"(gv[1]), "+v"(gv[2]), "+v"(gv[3]), "+v"(gv[4]), "+v"(gv[5]), "+v"(gv[6]), "+v"(gv[7]) :: "memory");
      float gf2[8][8];
#pragma unroll
      for (int i = 0; i < 8; ++i) {
#pragma unroll
        for (int e = 0; e < 4; ++e) { gf2[i][2 * e] = bf2f(gv[i][e] & 0xffffu); gf2[i][2 * e + 1] = U2F(gv[i][e] & 0xffff0000u); }
        asm volatile("" : "+v"(gf2[i][0]), "+v"(gf2[i][1]), "+v"(gf2[i][2]), "+v"(gf2[i][3]), "+v"(gf2[i][4]), "+v"(gf2[i][5]), "+v"(gf2[i][6]), "+v"(gf2[i][7]));
      }
      STAMP(23);
      BAR();
      f32x4 a0[8], a1[8];
#pragma unroll
      for (int i = 0; i < 8; ++i) { a0[i] = *(const LAS f32x4*)(cb + i * 8192 + (((2 * c8) ^ (r0 & 7)) << 4)); a1[i] = *(const LAS f32x4*)(cb + i * 8192 + (((2 * c8 + 1) ^ (r0 & 7)) << 4)); }
      SCHED();
#pragma unroll
      for (int i = 0; i < 8; ++i) {
        const u32x4 w = {pk2(xf[i][0] + a0[i][0] * gf2[i][0], xf[i][1] + a0[i][1] * gf2[i][1]), pk2(xf[i][2] + a0[i][2] * gf2[i][2], xf[i][3] + a0[i][3] * gf2[i][3]),
                         pk2(xf[i][4] + a1[i][0] * gf2[i][4], xf[i][5] + a1[i][1] * gf2[i][5]), pk2(xf[i][6] + a1[i][2] * gf2[i][6], xf[i][7] + a1[i][3] * gf2[i][7])};
        __builtin_amdgcn_raw_buffer_store_b128(w, ors, goff + i * 32768, 0, 16);
      }
    }
  } else {
    STAMP(21);
    const int wm = wc >> 1, wn = wc & 1;
    lptr w2b[4];
#pragma unroll
    for (int ft = 0; ft < 4; ++ft) w2b[ft] = lds + 8192 + wn * 1024 + (g >> 1) * 2048 + (4 * (g & 1) + q4) * 128 + 32 * (ft ^ (2 * (g & 1) + (q4 >> 1))) + 8 * p4;
    lptr ar = lds + wm * 4096 + lane * 16;
    f32x4 ac2[8][4];
#pragma unroll
    for (int m = 0; m < 8; ++m)
#pragma unroll
      for (int n = 0; n < 4; ++n) ac2[m][n] = (f32x4){0.f, 0.f, 0.f, 0.f};
    bf16x8 Af[2][2][4];
    bf16x4 Wr[4][2][2];
    int slot = 0, dlt = 0;
#define K2_RW(m, ks, hf) Wr[m][ks][hf] = __builtin_amdgcn_ds_read_tr16_b64_v4bf16((LAS bf16x4*)(w2b[m] + (ks) * 8192 + (hf) * 4096))
#define K2_W(m, ks) __builtin_shufflevector(Wr[m][ks][0], Wr[m][ks][1], 0, 1, 2, 3, 4, 5, 6, 7)
#define K2_NEXT() do { dlt = (slot == 5) ? -5 * K2_STG : K2_STG; slot = (slot == 5) ? 0 : slot + 1; } while (0)
#define K2_ADV(x) do { x += dlt; asm volatile("" : "+v"(x)); } while (0)
#define K2_WIDE(st, h) do { _Pragma("unroll") for (int i = 0; i < 32; ++i) { const int m = i >> 3, j = i & 7, q = j >> 1; \
        const int n_ = (m & 1) ? 3 - q : q, k_ = (j ^ q) & 1; asm volatile("v_mfma_f32_16x16x32_bf16 %0, %1, %2, %0" : "+v"(ac2[4 * (h) + m][n_]) : "v"(K2_W(m, k_)), "v"(Af[st][k_][n_])); SCHED(); \
        if (j >= 1 && j < 5) K2_RW((m + 2) & 3, ((j - 1) >> 1) & 1, (j - 1) & 1); \
        if (m >= 2 && (j == 5 || j == 6)) Af[(st) ^ 1][h][(2 * m + j - 9) & 3] = ldb128(ar + ((2 * m + j - 9) & 3) * 1024); \
        if (i == 5) K2_ADV(ar); \
        if (i == 6) K2_ADV(w2b[0]); \
        if (i == 7) K2_ADV(w2b[2]); \
        if (i == 13) K2_ADV(w2b[1]); \
        if (i == 14) BAR(); \
        if (i == 15) K2_ADV(w2b[3]); \
        if (i == 23) K2_NEXT(); \
        SCHED(); } } while (0)
#define K2_NARROW(st, h) do { _Pragma("unroll") for (int i = 0; i < 16; ++i) { const int m = i >> 2, q = i & 3; \
        const int n_ = (m & 1) ? 3 - q : q; ac2[m][n_] = MFMA16(K2_W(m, 0), Af[st][h][n_], ac2[m][n_]); SCHED(); \
        if (q < 2) K2_RW((m + 3) & 3, 0, q); \
        if (q >= 2 && (m == 1 || m == 2)) Af[(st) ^ 1][h][2 * m + q - 4] = ldb128(ar + (2 * m + q - 4) * 1024); \
        if (i == 2) { K2_ADV(ar); K2_ADV(w2b[0]); } \
        if (i == 3) BAR(); \
        if (i == 5) K2_ADV(w2b[1]); \
        if (i == 9) K2_ADV(w2b[2]); \
        if (i == 14) K2_ADV(w2b[3]); \
        if (i == 15) K2_NEXT(); \
        SCHED(); } } while (0)
#define K2_WIDE4() do { K2_WIDE(0, 0); K2_WIDE(0, 1); K2_WIDE(1, 0); K2_WIDE(1, 1); } while (0)
#define K2_NARROW4() do { K2_NARROW(0, 0); K2_NARROW(0, 1); K2_NARROW(1, 0); K2_NARROW(1, 1); } while (0)
    K2_NEXT();
#pragma unroll
    for (int m = 0; m < 8; m += 2)
      asm volatile("" : "+v"(ac2[m][0]), "+v"(ac2[m][1]), "+v"(ac2[m][2]), "+v"(ac2[m][3]), "+v"(ac2[m + 1][0]), "+v"(ac2[m + 1][1]), "+v"(ac2[m + 1][2]), "+v"(ac2[m + 1][3]));
    asm volatile("" : "+v"(ar), "+v"(w2b[0]), "+v"(w2b[1]), "+v"(w2b[2]), "+v"(w2b[3]));
    BAR();
    STAMP(22);
#pragma unroll
    for (int i = 0; i < 4; ++i) { K2_RW(i >> 1, i & 1, 0); K2_RW(i >> 1, i & 1, 1); Af[0][0][i] = ldb128(ar + 6 * K2_STG + i * 1024); Af[0][1][i] = ldb128(ar + i * 1024); SCHED(); }
#pragma unroll 1
    for (int p = 0; p < npw; p += 4) {
      K2_WIDE4();
      WAIT_V(0);
    }
    asm volatile("s_nop 7\n\ts_nop 7" ::: "memory"); SCHED();
    K2_RW(2, 0, 0); K2_RW(2, 0, 1);
    const unsigned po = (unsigned)(WS_P + wc * 8192 + lane * 16);
#pragma unroll
    for (int i = 0; i < 8; ++i) {
      const f32x4 a = ac2[4 + (i >> 1)][2 * (i & 1)], b = ac2[4 + (i >> 1)][2 * (i & 1) + 1];
      ST128((u32x4){pk2(a[0], a[1]), pk2(a[2], a[3]), pk2(b[0], b[1]), pk2(b[2], b[3])}, po + i * 1024, bid * 32768);
    }
    SCHED();
#pragma unroll 1
    for (int p = 0; p < 12; p += 4) {
      K2_NARROW4();
      WAIT_V(0);
    }
    K2_NARROW(0, 0); K2_NARROW(0, 1); K2_NARROW(1, 0);
    u32x4 pv[8];
#pragma unroll
    for (int i = 0; i < 8; ++i) pv[i] = __builtin_amdgcn_raw_buffer_load_b128(wrs, po + i * 1024 + (bid ^ 1) * 32768, 0, 16);
    SCHED();
    K2_NARROW(1, 1);
    K2_NARROW4();
    STAMP(23);
    const lptr cw = lds + k2c + (wm * 64 + t) * 512;
#pragma unroll
    for (int m = 0; m < 4; ++m)
#pragma unroll
      for (int n = 0; n < 4; ++n) {
        const unsigned lo = pv[2 * m + (n >> 1)][2 * (n & 1)], hi = pv[2 * m + (n >> 1)][2 * (n & 1) + 1];
        *(LAS f32x4*)(cw + n * 8192 + (((wn * 16 + m * 4 + g) ^ (t & 7)) << 4)) = ac2[m][n] + (f32x4){bf2f(lo & 0xffffu), U2F(lo & 0xffff0000u), bf2f(hi & 0xffffu), U2F(hi & 0xffff0000u)};
      }
    WAIT_L(0); BAR();
    if (isG) G_FIN();
  STAMP(24);
  STAMP_OUT(ws + (57 << 20), 30);
    __builtin_amdgcn_endpgm();
  }
  if (isG) {
      G_FIN();
      if (wid == 0 && lane == 0) __builtin_amdgcn_raw_buffer_store_b32(tag, wrs, 0, WS_GFLAG + (tm * 8 + tn) * 128, 16);
      if (bid == 16 && wid == 0) {
        for (unsigned spins = 0; spins < 2000000u; ++spins) {
          bool ok = true;
#pragma unroll
          for (int i = 0; i < 4; ++i) ok = ok && __builtin_amdgcn_raw_buffer_load_b32(wrs, (unsigned)((lane + i * 64) * 128), WS_HFLAG, 16) == tag;
          if (__builtin_amdgcn_ballot_w64(!ok) == 0ull) break;
          __builtin_amdgcn_s_sleep(4);
          asm volatile("" ::: "memory");
        }
        asm volatile("" ::: "memory");
        if (lane == 0) __builtin_amdgcn_raw_buffer_store_b32(tag, wrs, 0, 0, 16);
      }
    }
  STAMP(24);
  STAMP_OUT(ws + (57 << 20), 30);
}

extern "C" void kernel_launch(void* const* d_in, const int* in_sizes, int n_in, void* d_out, int out_size, void* d_ws, size_t ws_size, hipStream_t stream) {
  const char* x = (const char*)d_in[0]; const char* kc = (const char*)d_in[1]; const char* vc = (const char*)d_in[2];
  const char* wq = (const char*)d_in[3]; const char* wo = (const char*)d_in[4]; const char* w1 = (const char*)d_in[6]; const char* w2 = (const char*)d_in[7];
  (void)hipFuncSetAttribute((const void*)mega_kernel, hipFuncAttributeMaxDynamicSharedMemorySize, K1_LDS);
  mega_kernel<<<256, 512, K1_LDS, stream>>>(x, kc, vc, wq, w1, wo, w2, (char*)d_ws, (char*)d_out);
}
```

```cpp
#define PROBE_A 19
#define PROBE_B 22
#define PROBE_S 5
#define PROBE_I "s_memtime"
#define PROBE_W 23
#include <hip/hip_runtime.h>
#include <stdint.h>

typedef __bf16 bf16x2 __attribute__((ext_vector_type(2)));
typedef __bf16 bf16x4 __attribute__((ext_vector_type(4)));
typedef __bf16 bf16x8 __attribute__((ext_vector_type(8)));
typedef float f32x2 __attribute__((ext_vector_type(2)));
typedef float f32x4 __attribute__((ext_vector_type(4)));
typedef unsigned u32x2 __attribute__((ext_vector_type(2)));
typedef unsigned u32x4 __attribute__((ext_vector_type(4)));
#define LAS __attribute__((address_space(3)))
typedef LAS char* lptr;
#if defined(__HIP_DEVICE_COMPILE__)
#define NOPK __attribute__((target("no-packed-fp32-ops")))
#else
#define NOPK
#endif
#define DI NOPK __device__ __forceinline__
#define F2U(x) __builtin_bit_cast(unsigned, (float)(x))
#define U2F(x) __builtin_bit_cast(float, (unsigned)(x))
#define TID ((int)__builtin_amdgcn_workitem_id_x())
#define BID ((int)__builtin_amdgcn_workgroup_id_x())
#define SCHED() __builtin_amdgcn_sched_barrier(0)
#define BAR() asm volatile("s_barrier" ::: "memory")
#define WAIT_V(n) asm volatile("s_waitcnt vmcnt(%0)" :: "n"(n) : "memory")
#define WAIT_L(n) do { asm volatile("" ::: "memory"); __builtin_amdgcn_s_waitcnt(0xC07F | ((n) << 8)); asm volatile("" ::: "memory"); } while (0)
#define MFMA16(a, b, c) __builtin_amdgcn_mfma_f32_16x16x32_bf16(a, b, c, 0, 0, 0)

DI void glds(unsigned voff, const char* sbase, unsigned ldsaddr) {
  asm volatile("s_mov_b32 m0, %2\n\ts_nop 0\n\tglobal_load_lds_dwordx4 %0, %1" ::"v"(voff), "s"(sbase), "s"(ldsaddr) : "memory");
}
DI unsigned pk2(float lo, float hi) { f32x2 v = {lo, hi}; return __builtin_bit_cast(unsigned, __builtin_convertvector(v, bf16x2)); }
DI float bf2f(unsigned b) { return U2F(b << 16); }
DI bf16x8 ldb128(lptr p) { return *(const LAS bf16x8*)p; }
DI bf16x8 ldtr2(lptr p, int d) {
  bf16x4 a = __builtin_amdgcn_ds_read_tr16_b64_v4bf16((LAS bf16x4*)p);
  bf16x4 b = __builtin_amdgcn_ds_read_tr16_b64_v4bf16((LAS bf16x4*)(p + d));
  return __builtin_shufflevector(a, b, 0, 1, 2, 3, 4, 5, 6, 7);
}
DI void ST128_(__amdgpu_buffer_rsrc_t r, u32x4 v, unsigned voff, int soff) { __builtin_amdgcn_raw_buffer_store_b128(v, r, voff + (unsigned)soff, 0, 16); }
DI float silu(float v) { return v * __builtin_amdgcn_rcpf(1.0f + __builtin_amdgcn_exp2f(v * -1.4426950408889634f)); }

#define STAMP_DECL unsigned long long pa_ = 0, pb_ = 0; const unsigned long long p0_ = __builtin_amdgcn_s_memrealtime();
#define LSTAMP(i)
#define STAMP(i) do { if ((i) == PROBE_A) asm volatile(PROBE_I " %0\n\ts_waitcnt lgkmcnt(0)" : "=s"(pa_) :: "memory"); if ((i) == PROBE_B) asm volatile(PROBE_I " %0\n\ts_waitcnt lgkmcnt(0)" : "=s"(pb_) :: "memory"); } while (0)
#define STAMP_OUT(buf, n) do { if (bid == PROBE_W && wid == 4) { const unsigned long long d_ = 10000 + (((pb_ - pa_) * 16) >> PROBE_S); \
    while (__builtin_amdgcn_s_memrealtime() - p0_ < d_ && d_ < 100000ull) __builtin_amdgcn_s_sleep(1); } } while (0)
template <int CTRL> DI float dpp_add(float v) { return v + U2F(__builtin_amdgcn_update_dpp(0u, F2U(v), CTRL, 0xf, 0xf, true)); }
DI float half_sum(float v) {
  v = dpp_add<0xB1>(v); v = dpp_add<0x4E>(v); v = dpp_add<0x141>(v); v = dpp_add<0x140>(v);
  auto rr = __builtin_amdgcn_permlane16_swap(F2U(v), F2U(v), false, false);
  return U2F(rr[0]) + U2F(rr[1]);
}
DI float col_max(float v) {
  auto a = __builtin_amdgcn_permlane16_swap(F2U(v), F2U(v), false, false);
  v = __builtin_fmaxf(U2F(a[0]), U2F(a[1]));
  auto b = __builtin_amdgcn_permlane32_swap(F2U(v), F2U(v), false, false);
  return __builtin_fmaxf(U2F(b[0]), U2F(b[1]));
}
typedef __attribute__((address_space(1))) unsigned long long gu64;

#define SX(b, h) (98304 + (b) * 32768 + (h) * 16384)
constexpr int SLOT_B = 0, SLOT_E = 32768, SLOT_A = 65536, SLOT_C = 98304, SLOT_D = 131072, K1_LDS = 163840;

constexpr int WS_HFLAG = 4096;
constexpr int WS_GFLAG = 65536;
constexpr int WS_OFLAG = 131072;
constexpr int OFS = 16;
constexpr int WS_EFLAG = 163840;
constexpr int WS_STATS = 1 << 20;
constexpr int WS_PFLAG = 196608;
constexpr int WS_G = 5 << 20, WS_O = 9 << 20, WS_H = 25 << 20, WS_P = 39 << 20, WS_END = 47 << 20;
constexpr int K2_STG = 24576, K2_C = 98304;

NOPK __global__ __launch_bounds__(512, 2) void mega_kernel(const char* __restrict__ xn, const char* __restrict__ kc, const char* __restrict__ vc,
                                                           const char* __restrict__ wq, const char* __restrict__ w1, const char* __restrict__ wo, const char* __restrict__ w2,
                                                           char* ws, char* __restrict__ out) {
  STAMP_DECL
  STAMP(0);
  extern __shared__ __attribute__((aligned(16))) char shm[];
  const lptr lds = (lptr)shm;
  const unsigned lds0 = (unsigned)(uintptr_t)lds;
  const int wid = __builtin_amdgcn_readfirstlane(TID >> 6), wr = wid >> 2, wc = wid & 3;
  const int bid = BID, xcd = bid & 7, idx = bid >> 3, hd = (idx & 2) * 8 + 2 * xcd + (idx & 1), rb = idx >> 2;
  int lane, g, t, q4, p4;
#define RELANE() do { lane = __builtin_amdgcn_mbcnt_hi(~0u, __builtin_amdgcn_mbcnt_lo(~0u, 0u)); asm volatile("" : "+v"(lane)); g = lane >> 4; t = lane & 15; q4 = (lane >> 2) & 3; p4 = lane & 3; } while (0)
  RELANE();

  const bool light = (idx & 2) != 0;
  unsigned voffX[4], voffQ[2], voffH[2];
  {
    const int r = lane >> 3, c = lane & 7;
#pragma unroll
    for (int i = 0; i < 4; ++i) voffX[i] = (unsigned)((8 * i + r) * 2048 + ((c ^ (4 * (i & 1) + (r >> 1))) << 4) - i * 1024);
    const int k = 32 * (wid >> 2) + 16 * (wid & 1) + 4 * ((wid >> 1) & 1) + 8 * (r >> 2) + (r & 3);
    const int cw = c ^ (((r >> 1) & 3) << 1);
    voffQ[0] = (unsigned)(k * 8192 + cw * 16 + 1024); voffQ[1] = voffQ[0] + 128 - 1024;
    voffH[0] = (unsigned)(k * 7168 + 128 + (light ? (cw & 3) : cw) * 16 + 1024);
    voffH[1] = (unsigned)(k * 7168 + cw * 16);
  }
  const char* xb = xn + (size_t)rb * 256 * 2048 + wid * 65536;
  const char* qb = wq + hd * 256 - 1024;
  const char* hb = w1 + (light ? 4096 + 192 * (hd - 16) : 256 * hd) - 1024;
  const bool full = wr == 1 || !light;
  const int sb = hd >= 24;
#define GLDS2(v0_, v1_, sb_, ld_) asm volatile("s_mov_b32 m0, %3\n\ts_nop 0\n\tglobal_load_lds_dwordx4 %0, %2\n\tglobal_load_lds_dwordx4 %1, %2 offset:1024" \
    ::"v"(v0_), "v"(v1_), "s"(sb_), "s"(ld_) : "memory")
#define GLDS4(v0_, v1_, v2_, v3_, sb_, ld_) asm volatile("s_mov_b32 m0, %5\n\ts_nop 0\n\tglobal_load_lds_dwordx4 %0, %4\n\tglobal_load_lds_dwordx4 %1, %4 offset:1024\n\t" \
    "global_load_lds_dwordx4 %2, %4 offset:2048\n\tglobal_load_lds_dwordx4 %3, %4 offset:3072" ::"v"(v0_), "v"(v1_), "v"(v2_), "v"(v3_), "s"(sb_), "s"(ld_) : "memory")
#define ST_X(b, kt) GLDS4(voffX[0], voffX[1], voffX[2], voffX[3], xb + (kt) * 128, lds0 + SX(b, 0) + wid * 4096)
#define ST_W0(wo_, kt) GLDS2(voffQ[0], voffQ[1], qb + (size_t)(kt) * 524288, lds0 + (wo_) + wid * 2048)
#define ST_W1(wo_, kt) GLDS2(voffH[0], voffH[1], hb + (size_t)(kt) * 458752, lds0 + (wo_) + 16384 + wid * 2048)

  lptr wb[4], xr[2];
#pragma unroll
  for (int m = 0; m < 4; ++m) wb[m] = lds + wr * 1024 + (g >> 1) * 2048 + (4 * (g & 1) + q4) * 128 + 32 * ((m ^ wc) ^ (2 * (g & 1) + (q4 >> 1))) + 8 * p4;
#pragma unroll
  for (int ks = 0; ks < 2; ++ks) xr[ks] = lds + 98304 + (wc * 32 + t) * 128 + 16 * ((4 * ks + g) ^ (t >> 1));
#define LDW(dst, h) do { _Pragma("unroll") for (int m = 0; m < 4; ++m) _Pragma("unroll") for (int k = 0; k < 2; ++k) dst[m][k] = ldtr2(wb[m] + (h) * 16384 + k * 8192, 4096); } while (0)
#define LDX(dst, b, h) do { _Pragma("unroll") for (int n = 0; n < 2; ++n) _Pragma("unroll") for (int k = 0; k < 2; ++k) dst[n][k] = ldb128(xr[k] + (SX(b, h) - 98304) + n * 2048); } while (0)

  f32x4 acc[2][2][4][2];
#pragma unroll
  for (int a = 0; a < 2; ++a)
#pragma unroll
    for (int b = 0; b < 2; ++b)
#pragma unroll
      for (int m = 0; m < 4; ++m)
#pragma unroll
        for (int n = 0; n < 2; ++n) acc[a][b][m][n] = (f32x4){0.f, 0.f, 0.f, 0.f};
  bf16x8 At[4][2], B0[2][2], B1[2][2];

#define MM1(ai, m, k, j) do { if ((j) < 2) acc[ai][0][m][(j) & 1] = MFMA16(At[m][k], B0[(j) & 1][k], acc[ai][0][m][(j) & 1]); else acc[ai][1][m][(j) & 1] = MFMA16(At[m][k], B1[(j) & 1][k], acc[ai][1][m][(j) & 1]); SCHED(); } while (0)
#define MM8(ai, m) do { _Pragma("unroll") for (int q = 0; q < 4; ++q) { MM1(ai, m, q & 1, ((m) & 1) ? 3 - q : q); MM1(ai, m, (q & 1) ^ 1, ((m) & 1) ? 3 - q : q); } } while (0)
#define MMA2(ai, CLOSE) do { __builtin_amdgcn_s_setprio(1); \
    cs[ai] = MFMA16(At[0][1], ones, cs[ai]); SCHED(); cs[ai] = MFMA16(At[0][0], ones, cs[ai]); SCHED(); MM8(ai, 0); asm volatile("" : "+v"(cs[ai])); MM8(ai, 1); \
    if ((ai) == 0 || full) { MM8(ai, 2); MM8(ai, 3); } \
    SCHED(); CLOSE; __builtin_amdgcn_s_setprio(0); SCHED(); } while (0)
  char* const stats = ws + WS_STATS;
  const __amdgpu_buffer_rsrc_t wrs = __builtin_amdgcn_make_buffer_rsrc(ws, 0, WS_END, 0x00020000);
  f32x4 cs[2] = {{0.f, 0.f, 0.f, 0.f}, {0.f, 0.f, 0.f, 0.f}};
  bf16x8 ones = __builtin_bit_cast(bf16x8, (u32x4){0x3f803f80u, 0x3f803f80u, 0x3f803f80u, 0x3f803f80u});
#define PIN_SETUP() do { asm volatile("" : "+v"(ones), "+v"(cs[0]), "+v"(cs[1]), "+v"(wb[0]), "+v"(wb[1]), "+v"(wb[2]), "+v"(wb[3]), "+v"(xr[0]), "+v"(xr[1])); \
    _Pragma("unroll") for (int a = 0; a < 2; ++a) _Pragma("unroll") for (int b = 0; b < 2; ++b) \
      asm volatile("" : "+v"(acc[a][b][0][0]), "+v"(acc[a][b][0][1]), "+v"(acc[a][b][1][0]), "+v"(acc[a][b][1][1]), "+v"(acc[a][b][2][0]), "+v"(acc[a][b][2][1]), "+v"(acc[a][b][3][0]), "+v"(acc[a][b][3][1])); } while (0)
  unsigned tag;
  {
    if (wr == 0) {
      unsigned ep;
      ST_X(0, 0); ST_W0(0, 0);
      GLDS4(voffX[0], voffX[1], voffX[2], voffX[3], xb + 262144, lds0 + SX(0, 1) + wid * 4096);
      GLDS2(voffQ[0], voffQ[1], qb + 262144, lds0 + 8192 + wid * 2048);
      asm volatile("global_load_dword %0, %1, %2 sc1" : "=&v"(ep) : "v"(0), "s"(ws) : "memory");
      PIN_SETUP();
      asm volatile("s_waitcnt vmcnt(0)" : "+v"(ep) :: "memory");
      tag = __builtin_amdgcn_readfirstlane(ep) + 1u;
      BAR();
    } else {
      const int row = rb * 256 + hd * 8 + wc * 2 + (lane >> 5);
      u32x4 a[4];
      unsigned ep;
      asm volatile("global_load_dword %0, %1, %2 sc1" : "=&v"(ep) : "v"(0), "s"(ws) : "memory");
      asm volatile("global_load_dwordx4 %0, %4, %5\n\tglobal_load_dwordx4 %1, %4, %5 offset:512\n\tglobal_load_dwordx4 %2, %4, %5 offset:1024\n\tglobal_load_dwordx4 %3, %4, %5 offset:1536"
                   : "=&v"(a[0]), "=&v"(a[1]), "=&v"(a[2]), "=&v"(a[3]) : "v"(row * 2048 + (lane & 31) * 16), "s"(xn) : "memory");
      ST_W1(0, 0); GLDS2(voffH[0], voffH[1], hb - 229376, lds0 + 16384 + wc * 2048);
      PIN_SETUP();
      BAR();
      ST_W0(32768, 1); GLDS2(voffQ[0], voffQ[1], qb + 524288 - 262144, lds0 + 32768 + wc * 2048);
      ST_X(1, 1); GLDS4(voffX[0], voffX[1], voffX[2], voffX[3], xb - 262144 + 128, lds0 + SX(1, 0) + wc * 4096);
      asm volatile("s_waitcnt vmcnt(16)" : "+v"(ep), "+v"(a[0]), "+v"(a[1]), "+v"(a[2]), "+v"(a[3]) :: "memory");
      tag = __builtin_amdgcn_readfirstlane(ep) + 1u;
      float v[32];
#pragma unroll
      for (int i = 0; i < 16; ++i) { v[2 * i] = bf2f(a[i >> 2][i & 3] & 0xffffu); v[2 * i + 1] = U2F(a[i >> 2][i & 3] & 0xffff0000u); }
      float s0 = 0.f, s1 = 0.f, s2 = 0.f, s3 = 0.f;
#pragma unroll
      for (int i = 0; i < 8; ++i) { s0 += v[i]; s1 += v[8 + i]; s2 += v[16 + i]; s3 += v[24 + i]; }
      const float mean = half_sum((s0 + s1) + (s2 + s3)) * (1.0f / 1024.0f);
      s0 = s1 = s2 = s3 = 0.f;
#pragma unroll
      for (int i = 0; i < 8; ++i) { const float d0 = v[i] - mean, d1 = v[8 + i] - mean, d2 = v[16 + i] - mean, d3 = v[24 + i] - mean; s0 += d0 * d0; s1 += d1 * d1; s2 += d2 * d2; s3 += d3 * d3; }
      const float sd = __builtin_amdgcn_sqrtf(half_sum((s0 + s1) + (s2 + s3)) * (1.0f / 1024.0f) + 1e-5f);
      if ((lane & 31) < 2) __hip_atomic_store((gu64*)(stats + row * 16 + (lane & 1) * 8), ((unsigned long long)tag << 32) | F2U((lane & 1) ? sd : mean), __ATOMIC_RELAXED, __HIP_MEMORY_SCOPE_AGENT);
    }
  }
  if (wr == 1) BAR();
  SCHED();
  STAMP(1);
  const int kvg = hd >> 3;
  const char* kb_g = kc + ((size_t)kvg * 2048 + 1536) * 256;
  const char* vb_g = vc + ((size_t)kvg * 2048 + 1536) * 256;
#define VOFFK ((unsigned)((lane >> 3) * 256 + (((lane & 7) ^ (4 * (wid & 1) + (lane >> 4))) << 4)))
#define VOFFV ((unsigned)((lane >> 3) * 256 + (((lane & 7) ^ ((lane >> 4) << 1)) << 4)))
#define ST_KV8(voff, gsrc, slot) do { glds(voff, (gsrc) + wid * 2048, lds0 + (slot) + wid * 2048); glds(voff, (gsrc) + wid * 2048 + 128, lds0 + (slot) + wid * 2048 + 1024); } while (0)
  int wprv = 65536, wnxt = 32768;
#define W_ADV() do { const int d_ = (wnxt == 0) ? -65536 : 32768, cur_ = wnxt - d_; wnxt = wprv; wprv = cur_; \
    _Pragma("unroll") for (int f = 0; f < 4; ++f) wb[f] += d_; \
    asm volatile("" : "+v"(wb[0]), "+v"(wb[1]), "+v"(wb[2]), "+v"(wb[3])); } while (0)
#define PH_A(b, STAGE) do { LDX(B0, b, 0); LDX(B1, b, 1); SCHED(); LDW(At, 0); STAGE; WAIT_V(10); WAIT_L(12); BAR(); SCHED(); MMA2(0, WAIT_L(0); BAR()); } while (0)
#define PH_B(STAGE) do { LDW(At, 1); STAGE; W_ADV(); WAIT_V(8); BAR(); SCHED(); MMA2(1, WAIT_L(0); BAR()); } while (0)
#pragma unroll 1
  for (int kt = 0; kt < 14; kt += 2) {
    PH_A(0, ST_W1(wnxt, kt + 1); ST_W0(wprv, kt + 2));
    PH_B(ST_X(0, kt + 2));
    PH_A(1, ST_W1(wnxt, kt + 2); ST_W0(wprv, kt + 3));
    PH_B(ST_X(1, kt + 3));
  }
  STAMP(2);
  RELANE();
  const unsigned voffK = VOFFK, voffV = VOFFV;
  PH_A(0, ST_W1(0, 15); ST_KV8(voffK, kb_g, SLOT_E));
  PH_B(ST_KV8(voffV, vb_g, SLOT_C); ST_KV8(voffV, vb_g + 16384, SLOT_C + 16384));
  const unsigned soff = (unsigned)((rb * 256 + (g >> 1) * 128 + wc * 32 + (g & 1) * 16 + t) * 16);
  u32x4 sg;
  LDX(B0, 1, 0); LDX(B1, 1, 1); SCHED(); LDW(At, 0);
  asm volatile("global_load_dwordx4 %0, %1, %2 sc1" : "=&v"(sg) : "v"(soff), "s"(stats) : "memory");
  ST_KV8(voffK, kb_g + 16384, SLOT_E + 16384); ST_KV8(voffK, kb_g + 32768, SLOT_A);
  WAIT_V(11); BAR(); SCHED(); MMA2(0, WAIT_L(0); BAR());
  LDW(At, 1);
  ST_KV8(voffK, kb_g + 49152, SLOT_A + 16384);
  WAIT_L(0); BAR(); SCHED();
  const lptr csw = lds + SLOT_D + (t & 3) * 8192 + wr * 4096 + wc * 64 + g * 16;
  MMA2(1, *(LAS f32x4*)csw = cs[0]; *(LAS f32x4*)(csw + 256) = cs[1]; asm volatile("s_waitcnt vmcnt(4)" : "+v"(sg) :: "memory"); WAIT_L(0); BAR());
  f32x4 cv[2][4];
#pragma unroll
  for (int a = 0; a < 2; ++a)
#pragma unroll
    for (int m = 0; m < 4; ++m) cv[a][m] = *(const LAS f32x4*)(lds + SLOT_D + wc * 8192 + wr * 4096 + a * 256 + (m ^ wc) * 64 + g * 16);
  if (__builtin_expect(__builtin_amdgcn_ballot_w64(sg[1] != tag || sg[3] != tag) != 0ull, 0)) {
    for (unsigned spins = 0; spins < 4000000u; ++spins) {
      __builtin_amdgcn_s_sleep(2);
      asm volatile("" ::: "memory");
      sg = __builtin_amdgcn_raw_buffer_load_b128(wrs, soff, WS_STATS, 16);
      if (__builtin_amdgcn_ballot_w64(sg[1] != tag || sg[3] != tag) == 0ull) break;
    }
  }
  float mu[2][2], rs[2][2], sd[2][2];
  {
    auto m16 = __builtin_amdgcn_permlane16_swap(sg[0], sg[0], false, false);
    auto s16 = __builtin_amdgcn_permlane16_swap(sg[2], sg[2], false, false);
#pragma unroll
    for (int n = 0; n < 2; ++n) {
      auto m32 = __builtin_amdgcn_permlane32_swap(m16[n], m16[n], false, false);
      auto s32 = __builtin_amdgcn_permlane32_swap(s16[n], s16[n], false, false);
#pragma unroll
      for (int bj = 0; bj < 2; ++bj) { mu[bj][n] = -U2F(m32[bj]); sd[bj][n] = U2F(s32[bj]); rs[bj][n] = __builtin_amdgcn_rcpf(sd[bj][n]) * 1.4426950408889634f; }
    }
  }
  WAIT_L(0);
  STAMP(3);
  {
    const lptr qw = lds + wc * 8192 + wr * 4096 + ((g >> 1) * 16 + t) * 16 + (g & 1) * 8;
#pragma unroll
    for (int bj = 0; bj < 2; ++bj) {
      lptr qwb = qw + ((bj ^ sb) ? SLOT_D : SLOT_B);
      asm volatile("" : "+v"(qwb));
#pragma unroll
      for (int m = 0; m < 4; ++m)
#pragma unroll
        for (int n = 0; n < 2; ++n) {
          const f32x4 v = (acc[0][bj][m][n] + cv[0][m] * mu[bj][n]) * (rs[bj][n] * 0.08838834764831845f);
          u32x2 w = {pk2(v[0], v[1]), pk2(v[2], v[3])};
          *(LAS u32x2*)(qwb + ((m ^ wc) >> 1) * 2048 + ((m ^ wc) & 1) * 512 + n * 1024) = w;
        }
    }
  }
  WAIT_L(0); BAR(); SCHED();
  STAMP(4);
  unsigned pft = 0;
  const int sl = ((idx >> 2) * 16 + (idx & 1) * 8 + xcd) * 8 + wid;
  const int tstr = light ? 896 : 1024;
  const char* tbase = (light ? w2 : wo) + (sl < tstr ? sl : tstr - 1) * 2048;
#define TOUCH(q_) asm volatile("global_load_dword %0, %1, %2" : "+v"(pft) : "v"((unsigned)((__builtin_amdgcn_mbcnt_lo(~0u, 0u) & 15) * 128)), "s"(tbase + (((q_) + 2) & 3) * tstr * 2048) : "memory")
  TOUCH(0);
  bf16x8 qf[8];
  const lptr qr = lds + (wr ? SLOT_D : SLOT_B) + wc * 8192 + lane * 16;
  if (wr == 1) {
#pragma unroll
    for (int s = 0; s < 8; ++s) qf[s] = ldb128(qr + s * 1024);
  }
#define ST128(...) ST128_(wrs, __VA_ARGS__)
  u32x4 hq[3];
  unsigned hqo;
  {
    u32x2 hw[2][2][2];
#define SILU16(m, ACT) do { \
      const f32x4 a0 = acc[1][0][m][0] + cv[1][m] * mu[0][0], a1 = acc[1][0][m][1] + cv[1][m] * mu[0][1], a2 = acc[1][1][m][0] + cv[1][m] * mu[1][0], a3 = acc[1][1][m][1] + cv[1][m] * mu[1][1]; \
      f32x4 e0 = a0 * -rs[0][0], e1 = a1 * -rs[0][1], e2 = a2 * -rs[1][0], e3 = a3 * -rs[1][1]; SCHED(); \
      if (ACT) { \
      _Pragma("unroll") for (int i = 0; i < 4; ++i) { e0[i] = __builtin_amdgcn_exp2f(e0[i]); e1[i] = __builtin_amdgcn_exp2f(e1[i]); e2[i] = __builtin_amdgcn_exp2f(e2[i]); e3[i] = __builtin_amdgcn_exp2f(e3[i]); } SCHED(); \
      e0 = e0 * sd[0][0] + sd[0][0]; e1 = e1 * sd[0][1] + sd[0][1]; e2 = e2 * sd[1][0] + sd[1][0]; e3 = e3 * sd[1][1] + sd[1][1]; SCHED(); \
      _Pragma("unroll") for (int i = 0; i < 4; ++i) { e0[i] = __builtin_amdgcn_rcpf(e0[i]); e1[i] = __builtin_amdgcn_rcpf(e1[i]); e2[i] = __builtin_amdgcn_rcpf(e2[i]); e3[i] = __builtin_amdgcn_rcpf(e3[i]); } SCHED(); \
      e0 *= a0; e1 *= a1; e2 *= a2; e3 *= a3; SCHED(); } \
      else { _Pragma("unroll") for (int i = 0; i < 4; ++i) e3[i] = a3[i] * __builtin_amdgcn_rcpf(__builtin_amdgcn_exp2f(e3[i]) * sd[1][1] + sd[1][1]); } \
      hw[0][0][(m) & 1] = (u32x2){pk2(e0[0], e0[1]), pk2(e0[2], e0[3])}; hw[0][1][(m) & 1] = (u32x2){pk2(e1[0], e1[1]), pk2(e1[2], e1[3])}; \
      hw[1][0][(m) & 1] = (u32x2){pk2(e2[0], e2[1]), pk2(e2[2], e2[3])}; hw[1][1][(m) & 1] = (u32x2){pk2(e3[0], e3[1]), pk2(e3[2], e3[3])}; SCHED(); } while (0)
#define HFRAG(bj, n) ((wc & 1) ? (u32x4){hw[bj][n][1][0], hw[bj][n][1][1], hw[bj][n][0][0], hw[bj][n][0][1]} : (u32x4){hw[bj][n][0][0], hw[bj][n][0][1], hw[bj][n][1][0], hw[bj][n][1][1]})
    const unsigned hoff = (unsigned)(WS_H + (rb * 16 + wc * 2) * 2048 + lane * 16);
    const int pb = (light ? 16 + 3 * hd : 4 * hd) + (wr ? 0 : 2), sw = full ? (wc >> 1) : 0, p0 = pb + sw, p1 = pb + (sw ^ 1);
    SILU16(0, false); SILU16(1, false);
    hqo = hoff + (p0 >> 1) * 262144 + (p0 & 1) * 1024;
    hq[0] = HFRAG(0, 0); hq[1] = HFRAG(0, 1); hq[2] = HFRAG(1, 0);
    ST128(HFRAG(1, 1), hqo + 2048, 16384);
    asm volatile("" : "+v"(hq[0]), "+v"(hq[1]), "+v"(hq[2]));
    if (wr == 0) {
      BAR(); SCHED();
#pragma unroll
      for (int s = 0; s < 8; ++s) qf[s] = ldb128(qr + s * 1024);
    }
    if (full) {
      SILU16(2, true); SILU16(3, true);
#pragma unroll
      for (int bj = 0; bj < 2; ++bj)
#pragma unroll
        for (int n = 0; n < 2; ++n) ST128(HFRAG(bj, n), hoff + n * 2048, (p1 >> 1) * 262144 + (p1 & 1) * 1024 + bj * 16384);
    }
  }
  STAMP(5);
  if (wr == 1) { WAIT_L(0); BAR(); }
  __builtin_amdgcn_s_setprio(1);
  SCHED();
  STAMP(6);
  RELANE();
  lptr kb[2], vb[4];
#pragma unroll
  for (int e = 0; e < 2; ++e) kb[e] = lds + SLOT_E + (t >> 3) * 2048 + (t & 7) * 128 + (((4 * e + g) ^ (t >> 1)) << 4);
#pragma unroll
  for (int f = 0; f < 4; ++f) vb[f] = lds + SLOT_C + (g >> 1) * 2048 + (4 * (g & 1) + q4) * 128 + 32 * (f ^ (2 * (g & 1) + (q4 >> 1))) + 8 * p4;
  f32x4 o[8][2], s[8][2], lacc[2], negm[2];
#pragma unroll
  for (int n = 0; n < 2; ++n) {
    lacc[n] = (f32x4){0.f, 0.f, 0.f, 0.f};
#pragma unroll
    for (int d = 0; d < 8; ++d) o[d][n] = (f32x4){0.f, 0.f, 0.f, 0.f};
  }
  bf16x8 pf[8], Kf[32];
  bf16x4 Vh[32][2];
#define SM_LIM 1.152921504606847e18f
  constexpr int LA = 6;
#define MX3(a, b, c) __builtin_fmaxf(__builtin_fmaxf((a), (b)), (c))
#define RD_V(f, hf) Vh[f][hf] = __builtin_amdgcn_ds_read_tr16_b64_v4bf16((LAS bf16x4*)(vb[((f) >> 2) & 3] + ((f) >> 4) * 1024 + ((f) & 3) * 8192 + (hf) * 4096))
#define RD_K(f) Kf[f] = ldb128(kb[(f) & 1] + ((f) >> 2) * 4096 + (((f) >> 1) & 1) * 1024)
#define VF(f) __builtin_shufflevector(Vh[f][0], Vh[f][1], 0, 1, 2, 3, 4, 5, 6, 7)
#define PREF_V() do { _Pragma("unroll") for (int f = 0; f < LA; ++f) { RD_V(f, 0); RD_V(f, 1); } SCHED(); } while (0)
#define PV_SEG(NEXTK, FILL) do { \
    _Pragma("unroll") for (int i = 0; i < 64; ++i) { const int d = i >> 3, n = (i >> 2) & 1, k = n ? 3 - (i & 3) : (i & 3), f = (i >> 1) + LA; \
      o[d][n] = MFMA16(VF(4 * d + k), pf[2 * k + n], o[d][n]); SCHED(); \
      if (f < 32) RD_V(f < 32 ? f : 0, i & 1); else if ((NEXTK) && (i & 1)) RD_K(f >= 32 ? f - 32 : 0); \
      FILL; SCHED(); } } while (0)
#define QK_SEG(HAVE, CLOSE) do { \
    _Pragma("unroll") for (int i = 0; i < 64; ++i) { const int r = i >> 3, n = (i >> 2) & 1, k = n ? 3 - (i & 3) : (i & 3), f = (i >> 1) + LA; \
      s[r][n] = MFMA16(Kf[4 * r + k], qf[2 * k + n], (i & 3) == 0 ? negm[n] : s[r][n]); SCHED(); \
      if ((i & 1) && f < 32 && f >= (HAVE)) RD_K(f < 32 ? f : 0); \
      SCHED(); \
      if (i == 59) { __builtin_amdgcn_s_setprio(2); CLOSE; WAIT_L(0); BAR(); SCHED(); } } \
    __builtin_amdgcn_s_setprio(0); } while (0)
#define MX4(m_, v_) MX3(MX3(m_, v_[0], v_[1]), v_[2], v_[3])
#define ROWMAX(n) MX4(MX4(MX4(MX4(MX4(MX4(MX4(MX4(s[0][n][0], s[0][n]), s[1][n]), s[2][n]), s[3][n]), s[4][n]), s[5][n]), s[6][n]), s[7][n])
#define EXP_PACK() do { \
      _Pragma("unroll") for (int k = 0; k < 4; ++k) { \
        _Pragma("unroll") for (int n = 0; n < 2; ++n) { \
          _Pragma("unroll") for (int i = 0; i < 4; ++i) { s[2 * k][n][i] = __builtin_amdgcn_exp2f(s[2 * k][n][i]); s[2 * k + 1][n][i] = __builtin_amdgcn_exp2f(s[2 * k + 1][n][i]); } \
          u32x4 w = {pk2(s[2 * k][n][0], s[2 * k][n][1]), pk2(s[2 * k][n][2], s[2 * k][n][3]), pk2(s[2 * k + 1][n][0], s[2 * k + 1][n][1]), pk2(s[2 * k + 1][n][2], s[2 * k + 1][n][3])}; \
          pf[2 * k + n] = __builtin_bit_cast(bf16x8, w); } \
        asm volatile("" : "+v"(pf[2 * k]), "+v"(pf[2 * k + 1])); \
        SCHED(); } \
      __builtin_amdgcn_s_setprio(2); \
      _Pragma("unroll") for (int i = 0; i < 8; ++i) { ln[i >> 2] = MFMA16(ones, pf[2 * (i & 3) + (i >> 2)], (i & 3) == 0 ? lacc[i >> 2] : ln[i >> 2]); SCHED(); } \
      __builtin_amdgcn_s_setprio(0); } while (0)
#define SOFTMAX(PRE) do { f32x4 ln[2]; \
      EXP_PACK(); \
      PRE; \
      if (__builtin_expect(__builtin_amdgcn_ballot_w64(!(ln[0][0] <= SM_LIM && ln[1][0] <= SM_LIM)) != 0ull, 0)) { \
        _Pragma("unroll") for (int r = 0; r < 8; ++r) _Pragma("unroll") for (int k = 0; k < 4; ++k) { \
          const bf16x8 kf_ = ldb128(kb[k & 1] + r * 4096 + (k >> 1) * 1024); \
          s[r][0] = MFMA16(kf_, qf[2 * k], k == 0 ? negm[0] : s[r][0]); s[r][1] = MFMA16(kf_, qf[2 * k + 1], k == 0 ? negm[1] : s[r][1]); if (k == 3) SCHED(); } \
        const float m0 = ROWMAX(0), m1 = ROWMAX(1); \
        _Pragma("unroll") for (int n = 0; n < 2; ++n) { \
          const float dl = __builtin_fmaxf(col_max(n ? m1 : m0), 0.f), alpha = __builtin_amdgcn_exp2f(-dl); \
          negm[n] -= dl; lacc[n] *= alpha; \
          _Pragma("unroll") for (int r = 0; r < 8; ++r) { s[r][n] -= dl; o[r][n] *= alpha; } } \
        EXP_PACK(); \
        PRE; \
      } \
      lacc[0] = ln[0]; lacc[1] = ln[1]; SCHED(); } while (0)
  {
#pragma unroll
    for (int f = 0; f < 8; ++f) RD_K(f);
#pragma unroll
    for (int d = 0; d < 8; d += 2) asm volatile("" : "+v"(o[d][0]), "+v"(o[d][1]), "+v"(o[d + 1][0]), "+v"(o[d + 1][1]));
    f32x4 u[2][2];
    const f32x4 z4 = {0.f, 0.f, 0.f, 0.f};
#pragma unroll
    for (int i = 0; i < 16; ++i) { const int r = i >> 3, n = (i >> 2) & 1, k = i & 3; u[r][n] = MFMA16(Kf[4 * r + k], qf[2 * k + n], k == 0 ? z4 : u[r][n]); }
#pragma unroll
    for (int n = 0; n < 2; ++n) { const float m = -col_max(MX4(MX3(u[0][n][0], u[0][n][1], __builtin_fmaxf(u[0][n][2], u[0][n][3])), u[1][n])); negm[n] = (f32x4){m, m, m, m}; }
    asm volatile("" : "+v"(negm[0]), "+v"(negm[1]));
    SCHED();
    QK_SEG(8, WAIT_V(1));
  }
  STAMP(7);
  const char* kvsrc = (wr ? kb_g + 65536 : vb_g + 32768) + wc * 8192 - 1024;
  unsigned kvo[2][2];
#pragma unroll
  for (int par = 0; par < 2; ++par) {
    kvo[par][0] = (unsigned)((lane >> 3) * 256 + (((lane & 7) ^ (wr ? 4 * par + (lane >> 4) : (lane >> 4) << 1)) << 4) + 1024);
    kvo[par][1] = kvo[par][0] + 128 - 1024;
  }
#pragma unroll 1
  for (int c = 0; c < 3; ++c) {
    if (c == 0) STAMP(8); else if (c == 1) STAMP(12);
    {
      float tv[8], ev[8];
#pragma unroll
      for (int e = 0; e < 4; ++e) { tv[2 * e] = bf2f(hq[0][e] & 0xffffu); tv[2 * e + 1] = U2F(hq[0][e] & 0xffff0000u); }
      SCHED();
#pragma unroll
      for (int e = 0; e < 8; ++e) ev[e] = __builtin_amdgcn_exp2f(tv[e]);
      SCHED();
#pragma unroll
      for (int e = 0; e < 8; ++e) ev[e] = ev[e] * -1.4426950408889634f - 1.4426950408889634f;
      SCHED();
#pragma unroll
      for (int e = 0; e < 8; ++e) ev[e] = __builtin_amdgcn_rcpf(ev[e]);
      SCHED();
      ST128((u32x4){pk2(ev[0] * tv[0], ev[1] * tv[1]), pk2(ev[2] * tv[2], ev[3] * tv[3]), pk2(ev[4] * tv[4], ev[5] * tv[5]), pk2(ev[6] * tv[6], ev[7] * tv[7])}, hqo, c == 2 ? 16384 : c * 2048);
    }
    hq[0] = hq[1]; hq[1] = hq[2];
    SCHED();
    if (__builtin_expect(c + wr < 3, 1)) {
      const int slot = wr ? (c == 0 ? SLOT_B : SLOT_C) : (c == 0 ? SLOT_D : (c == 1 ? SLOT_E : SLOT_A));
#pragma unroll
      for (int hf = 0; hf < 2; ++hf)
        asm volatile("s_mov_b32 m0, %5\n\ts_nop 0\n\t"
                     "global_load_lds_dwordx4 %0, %4\n\tglobal_load_lds_dwordx4 %1, %4 offset:1024\n\t"
                     "global_load_lds_dwordx4 %2, %4 offset:2048\n\tglobal_load_lds_dwordx4 %3, %4 offset:3072"
                     ::"v"(kvo[0][0]), "v"(kvo[0][1]), "v"(kvo[1][0]), "v"(kvo[1][1]), "s"(kvsrc + c * 32768 + hf * 4096), "s"(lds0 + slot + wc * 8192 + hf * 4096) : "memory");
    }
    TOUCH(c + 1);
    SOFTMAX(PREF_V());
    { const int dk = (c == 0) ? SLOT_A - SLOT_E : ((c == 1) ? SLOT_B - SLOT_A : SLOT_C - SLOT_B); kb[0] += dk; kb[1] += dk; asm volatile("" : "+v"(kb[0]), "+v"(kb[1])); }
    if (c == 0) STAMP(9); else if (c == 1) STAMP(13);
    BAR(); SCHED();
    if (c == 0) STAMP(10); else if (c == 1) STAMP(14);
    __builtin_amdgcn_s_setprio(1);
    PV_SEG(true, );
    QK_SEG(LA, WAIT_V(1); if (c == 2) WAIT_V(0));
    { const int dv = (c == 1) ? SLOT_E - SLOT_D : 32768;
#pragma unroll
      for (int f = 0; f < 4; ++f) vb[f] += dv; }
    if (c == 0) STAMP(11); else if (c == 2) STAMP(15);
  }
  RELANE();
  if (wid == 4 && lane == 0) __builtin_amdgcn_raw_buffer_store_b32(tag, wrs, 0, WS_HFLAG + (rb * 32 + hd) * 128, 16);
  STAMP(16);
  SOFTMAX(PREF_V());
  {
    asm volatile("s_waitcnt vmcnt(0)" :: "v"(pft) : "memory");
    SCHED();
    STAMP(17);
    if (wr == 0) BAR();
    SCHED();
    STAMP(18);
    __builtin_amdgcn_s_setprio(1);
    const unsigned oo = (unsigned)(WS_O + 2 * hd * 262144 + (rb * 16 + (wr ^ sb) * 8 + wc * 2) * 2048 + lane * 16);
    float inv[2] = {0.f, 0.f};
    u32x4 ow;
#define O_PIECE(j_) do { const int dp = (j_) >> 3, n_ = ((j_) >> 2) & 1, q_ = (j_) & 3; \
      ow[q_] = pk2(o[2 * dp + (q_ >> 1)][n_][2 * (q_ & 1)] * inv[n_], o[2 * dp + (q_ >> 1)][n_][2 * (q_ & 1) + 1] * inv[n_]); asm volatile("" : "+v"(ow)); \
      if (q_ == 3) ST128(ow, oo + n_ * 2048 + (dp & 1) * 1024, (dp >> 1) * 262144); } while (0)
    PV_SEG(false, if (i == 4) { inv[0] = __builtin_amdgcn_rcpf(lacc[0][0]); inv[1] = __builtin_amdgcn_rcpf(lacc[1][0]); } if (i >= 16 && (i & 1)) O_PIECE(i >= 16 ? (i - 16) >> 1 : 0));
#pragma unroll
    for (int j = 24; j < 32; ++j) O_PIECE(j);
    __builtin_amdgcn_s_setprio(0);
  }
  STAMP(19);
  RELANE();
  const int isG = (idx >> 1) & 1, kh = xcd & 1, tm = 4 * (xcd >> 1) + ((idx >> 1) & 2) + (idx & 1), tn = 2 * (idx >> 3) + kh;
  const int npw = (isG ? 48 : 56) - 4 * kh;
  const int k2c = ((npw + 19) % 6 < 4) ? K2_C : 0;
#define O_FLAG() do { if (wid == 0 && lane == 0) __builtin_amdgcn_raw_buffer_store_b32(tag, wrs, 0, WS_OFLAG + (rb * 32 + hd) * OFS, 16); } while (0)
#define POLL32(off_, slp) do { for (unsigned spins = 0; spins < 2000000u; ++spins) { \
      if (__builtin_amdgcn_ballot_w64(__builtin_amdgcn_raw_buffer_load_b32(wrs, (off_), 0, 16) != tag) == 0ull) break; \
      __builtin_amdgcn_s_sleep(slp); asm volatile("" ::: "memory"); } \
    asm volatile("" ::: "memory"); } while (0)
  const int c8 = lane & 15, r0 = wc * 4 + (lane >> 4);
  const unsigned goff = (unsigned)((tm * 128 + r0) * 2048 + tn * 256 + c8 * 16);
  const lptr cb = lds + k2c + r0 * 512;
#define G_FIN() do { f32x4 a0[4], a1[4]; \
      _Pragma("unroll") for (int i = 0; i < 4; ++i) { a0[i] = *(const LAS f32x4*)(cb + (wr * 4 + i) * 8192 + (((2 * c8) ^ (r0 & 7)) << 4)); a1[i] = *(const LAS f32x4*)(cb + (wr * 4 + i) * 8192 + (((2 * c8 + 1) ^ (r0 & 7)) << 4)); } \
      _Pragma("unroll") for (int i = 0; i < 4; ++i) \
        ST128((u32x4){pk2(silu(a0[i][0]), silu(a0[i][1])), pk2(silu(a0[i][2]), silu(a0[i][3])), pk2(silu(a1[i][0]), silu(a1[i][1])), pk2(silu(a1[i][2]), silu(a1[i][3]))}, goff + i * 32768, WS_G + wr * 131072); \
      WAIT_V(0); BAR(); } while (0)
  const unsigned f1b = isG ? (unsigned)(WS_HFLAG + ((tm >> 1) * 32 + 16 + (lane & 15)) * 128) : (unsigned)(WS_OFLAG + ((tm >> 1) * 32 + 16 + (lane & 15)) * OFS);
  const unsigned f1 = isG ? f1b : (unsigned)(WS_EFLAG + ((tm >> 1) * 16 + (tm & 1) * 8 + kh * 4 + ((lane >> 2) & 3)) * 64 + (lane & 3) * 4);
  STAMP(20);
  if (wr == 0) {
    const char* ab = ws + (isG ? WS_H : WS_O) + tm * 16384 + wc * 4096;
    const char* wbp = (isG ? w2 : wo) + tn * 256 - 1024;
    const int wsd = (kh ? -256 : 256) + wc * 32768;
    const unsigned voffA0 = lane * 16, voffA1 = lane * 16 + 1024;
    unsigned voffW[4];
    {
      const int r = lane >> 3, c = lane & 7;
      voffW[0] = (unsigned)(r * 2048 + ((c ^ (((r >> 1) & 3) << 1)) << 4) + 1024); voffW[1] = voffW[0] + 128 - 1024;
      voffW[2] = voffW[0] + 16384 - 2048; voffW[3] = voffW[2] + 128 - 1024;
    }
    const char *ap, *wp, *wo_ = wbp;
    unsigned dst = lds0;
#define K2_RUN(k_) do { ap = ab + (size_t)(k_) * 262144; wp = wbp + (size_t)(k_) * 131072; } while (0)
#define K2_A(ks, d_) asm volatile("s_mov_b32 m0, %3\n\ts_nop 0\n\tglobal_load_lds_dwordx4 %0, %2 sc1\n\tglobal_load_lds_dwordx4 %1, %2 offset:1024 sc1" \
                   ::"v"(voffA0), "v"(voffA1), "s"(ap + (ks) * 1024), "s"((d_) + wc * 2048) : "memory")
#define K2_ISSUE(WIDE, ks) do { K2_A(ks, dst); \
      if (WIDE) GLDS4(voffW[0], voffW[1], voffW[2], voffW[3], (ks) ? wp + wc * 32768 : wo_ + wsd, dst + 8192 + wc * 4096); \
      else GLDS2(voffW[0], voffW[1], ((ks) ? wp : wo_ + 65536) + wc * 16384, dst + 8192 + wc * 2048); \
      dst = (dst == lds0 + 5 * K2_STG) ? lds0 : dst + K2_STG; \
      if (ks) { wo_ = wp; ap += 262144; wp += 131072; } } while (0)
#define K2_STEP(WIDE, ks, EVENT) do { WAIT_V((WIDE) ? 18 : 12); BAR(); EVENT; K2_ISSUE(WIDE, ks); } while (0)
#define K2_TILES(WIDE, n_) do { _Pragma("unroll 1") for (int i_ = 0; i_ < (n_); ++i_) { K2_STEP(WIDE, 0, ); K2_STEP(WIDE, 1, ); } } while (0)
#define LOOK(f_) asm volatile("buffer_load_dword %0, %1, %2, 0 offen sc1" : "+v"(pol) : "v"(f_), "s"(wrs) : "memory")
    const unsigned f2 = f1b - 16 * (isG ? 128 : OFS);
    const unsigned f3 = (unsigned)(WS_PFLAG + (bid ^ 1) * 128);
    u32x4 xh[8];
#define XH_LD(i) asm volatile("global_load_dwordx4 %0, %1, %2" : "+v"(xh[i]) : "v"(goff), "s"(xn + (i) * 32768) : "memory")
    WAIT_V(0);
    if (isG && lane == 0) __builtin_amdgcn_raw_buffer_store_b32(tag, wrs, 0, WS_EFLAG + (rb * 16 + hd - 16) * 64 + wc * 4, 16);
    POLL32(f1, 4);
    K2_RUN(isG ? 32 + 12 * kh : 32 + 16 * (tm & 1) + 8 * kh);
    K2_A(0, lds0 + 6 * K2_STG); K2_ISSUE(1, 1); K2_ISSUE(1, 0);
    STAMP(21);
    WAIT_V(6); BAR();
    K2_ISSUE(1, 1); K2_ISSUE(1, 0); K2_ISSUE(1, 1);
    STAMP(22);
    unsigned pol = 0;
#define CHECK(f_) do { asm volatile("" : "+v"(pol)); if (__builtin_amdgcn_ballot_w64(pol != tag) != 0ull) POLL32(f_, 4); } while (0)
    K2_TILES(1, 3);
    O_FLAG();
    LOOK(f1b);
    K2_TILES(1, 2);
    if (!isG) K2_RUN(48 - 16 * (tm & 1) + 6 * kh);
    K2_STEP(1, 0, CHECK(f1b)); K2_STEP(1, 1, );
    K2_TILES(1, isG ? 1 : 3);
    LOOK(f2);
    K2_TILES(1, 2);
    K2_RUN((isG ? 12 : 14) * kh);
    K2_STEP(1, 0, CHECK(f2));
    K2_STEP(1, 1, );
    asm volatile("" : "=v"(xh[0]), "=v"(xh[1]), "=v"(xh[2]), "=v"(xh[3]), "=v"(xh[4]), "=v"(xh[5]), "=v"(xh[6]), "=v"(xh[7]));
    if (!isG) {
#pragma unroll
      for (int i = 0; i < 8; ++i) { K2_STEP(1, 0, XH_LD(i)); K2_STEP(1, 1, ); }
    }
    K2_TILES(1, (isG ? 11 : 5) - 2 * kh);
    asm volatile("" : "+v"(xh[0]), "+v"(xh[1]), "+v"(xh[2]), "+v"(xh[3]), "+v"(xh[4]), "+v"(xh[5]), "+v"(xh[6]), "+v"(xh[7]));
    K2_RUN(isG ? 22 : 60 - 16 * (tm & 1));
    K2_STEP(1, 0, ); K2_STEP(0, 1, );
    K2_TILES(0, 3);
    K2_RUN(26);
    K2_STEP(0, 0, ); K2_STEP(0, 1, );
    K2_STEP(0, 0, if (wid == 0 && lane == 0) __builtin_amdgcn_raw_buffer_store_b32(tag, wrs, 0, WS_PFLAG + bid * 128, 16));
    K2_STEP(0, 1, );
    K2_TILES(0, 2);
    K2_STEP(0, 0, );
    const unsigned f4 = (lane & 1) ? (unsigned)(WS_GFLAG + (tm * 8 + tn) * 128) : f3;
    K2_STEP(0, 1, LOOK(f4)); K2_STEP(0, 0, ); K2_STEP(0, 1, );
    WAIT_V(12);
    asm volatile("" : "+v"(pol));
    const unsigned long long miss = __builtin_amdgcn_ballot_w64(pol != tag);
    if (wid == 0 && (miss & 1ull)) POLL32(f3, 2);
    BAR();
    K2_ISSUE(0, 0);
    WAIT_V(12); BAR();
    WAIT_V(8); BAR();
    if (isG) {
      WAIT_V(4); BAR();
      WAIT_V(0); BAR();
      BAR();
      STAMP(23);
      BAR();
    } else {
      u32x4 gv[8];
      const __amdgpu_buffer_rsrc_t ors = __builtin_amdgcn_make_buffer_rsrc(out, 0, 2048 * 2048, 0x00020000);
#define LD8(dst_, rs_, so_, pol_) asm volatile("buffer_load_dwordx4 %0, %8, %10, %11 offen" pol_ "\n\tbuffer_load_dwordx4 %1, %9, %10, %11 offen" pol_ "\n\t" \
        "buffer_load_dwordx4 %2, %8, %10, %12 offen" pol_ "\n\tbuffer_load_dwordx4 %3, %9, %10, %12 offen" pol_ "\n\t" \
        "buffer_load_dwordx4 %4, %8, %10, %13 offen" pol_ "\n\tbuffer_load_dwordx4 %5, %9, %10, %13 offen" pol_ "\n\t" \
        "buffer_load_dwordx4 %6, %8, %10, %14 offen" pol_ "\n\tbuffer_load_dwordx4 %7, %9, %10, %14 offen" pol_ \
        : "=&v"(dst_[0]), "=&v"(dst_[1]), "=&v"(dst_[2]), "=&v"(dst_[3]), "=&v"(dst_[4]), "=&v"(dst_[5]), "=&v"(dst_[6]), "=&v"(dst_[7]) \
        : "v"(goff), "v"(goff + 32768), "s"(rs_), "s"((so_)), "s"((so_) + 65536), "s"((so_) + 131072), "s"((so_) + 196608) : "memory")
      if (miss & 2ull) {
        WAIT_V(4); BAR(); WAIT_V(0); BAR(); BAR();
        POLL32((unsigned)(WS_GFLAG + (tm * 8 + tn) * 128), 1);
      }
      LD8(gv, wrs, WS_G, " sc1");
      if (!(miss & 2ull)) { WAIT_V(12); BAR(); WAIT_V(8); BAR(); BAR(); }
      float xf[8][8];
#pragma unroll
      for (int i = 0; i < 8; ++i) {
#pragma unroll
        for (int e = 0; e < 4; ++e) { xf[i][2 * e] = bf2f(xh[i][e] & 0xffffu); xf[i][2 * e + 1] = U2F(xh[i][e] & 0xffff0000u); }
        asm volatile("" : "+v"(xf[i][0]), "+v"(xf[i][1]), "+v"(xf[i][2]), "+v"(xf[i][3]), "+v"(xf[i][4]), "+v"(xf[i][5]), "+v"(xf[i][6]), "+v"(xf[i][7]));
      }
      asm volatile("s_waitcnt vmcnt(0)" : "+v"(gv[0]), "+v"(gv[1]), "+v"(gv[2]), "+v"(gv[3]), "+v"(gv[4]), "+v"(gv[5]), "+v"(gv[6]), "+v"(gv[7]) :: "memory");
      float gf2[8][8];
#pragma unroll
      for (int i = 0; i < 8; ++i) {
#pragma unroll
        for (int e = 0; e < 4; ++e) { gf2[i][2 * e] = bf2f(gv[i][e] & 0xffffu); gf2[i][2 * e + 1] = U2F(gv[i][e] & 0xffff0000u); }
        asm volatile("" : "+v"(gf2[i][0]), "+v"(gf2[i][1]), "+v"(gf2[i][2]), "+v"(gf2[i][3]), "+v"(gf2[i][4]), "+v"(gf2[i][5]), "+v"(gf2[i][6]), "+v"(gf2[i][7]));
      }
      STAMP(23);
      BAR();
      f32x4 a0[8], a1[8];
#pragma unroll
      for (int i = 0; i < 8; ++i) { a0[i] = *(const LAS f32x4*)(cb + i * 8192 + (((2 * c8) ^ (r0 & 7)) << 4)); a1[i] = *(const LAS f32x4*)(cb + i * 8192 + (((2 * c8 + 1) ^ (r0 & 7)) << 4)); }
      SCHED();
#pragma unroll
      for (int i = 0; i < 8; ++i) {
        const u32x4 w = {pk2(xf[i][0] + a0[i][0] * gf2[i][0], xf[i][1] + a0[i][1] * gf2[i][1]), pk2(xf[i][2] + a0[i][2] * gf2[i][2], xf[i][3] + a0[i][3] * gf2[i][3]),
                         pk2(xf[i][4] + a1[i][0] * gf2[i][4], xf[i][5] + a1[i][1] * gf2[i][5]), pk2(xf[i][6] + a1[i][2] * gf2[i][6], xf[i][7] + a1[i][3] * gf2[i][7])};
        __builtin_amdgcn_raw_buffer_store_b128(w, ors, goff + i * 32768, 0, 16);
      }
    }
  } else {
    STAMP(21);
    const int wm = wc >> 1, wn = wc & 1;
    lptr w2b[4];
#pragma unroll
    for (int ft = 0; ft < 4; ++ft) w2b[ft] = lds + 8192 + wn * 1024 + (g >> 1) * 2048 + (4 * (g & 1) + q4) * 128 + 32 * (ft ^ (2 * (g & 1) + (q4 >> 1))) + 8 * p4;
    lptr ar = lds + wm * 4096 + lane * 16;
    f32x4 ac2[8][4];
#pragma unroll
    for (int m = 0; m < 8; ++m)
#pragma unroll
      for (int n = 0; n < 4; ++n) ac2[m][n] = (f32x4){0.f, 0.f, 0.f, 0.f};
    bf16x8 Af[2][2][4];
    bf16x4 Wr[4][2][2];
    int slot = 0, dlt = 0;
#define K2_RW(m, ks, hf) Wr[m][ks][hf] = __builtin_amdgcn_ds_read_tr16_b64_v4bf16((LAS bf16x4*)(w2b[m] + (ks) * 8192 + (hf) * 4096))
#define K2_W(m, ks) __builtin_shufflevector(Wr[m][ks][0], Wr[m][ks][1], 0, 1, 2, 3, 4, 5, 6, 7)
#define K2_NEXT() do { dlt = (slot == 5) ? -5 * K2_STG : K2_STG; slot = (slot == 5) ? 0 : slot + 1; } while (0)
#define K2_ADV(x) do { x += dlt; asm volatile("" : "+v"(x)); } while (0)
#define K2_WIDE(st, h) do { _Pragma("unroll") for (int i = 0; i < 32; ++i) { const int m = i >> 3, j = i & 7, q = j >> 1; \
        const int n_ = (m & 1) ? 3 - q : q, k_ = (j ^ q) & 1; asm volatile("v_mfma_f32_16x16x32_bf16 %0, %1, %2, %0" : "+v"(ac2[4 * (h) + m][n_]) : "v"(K2_W(m, k_)), "v"(Af[st][k_][n_])); SCHED(); \
        if (j >= 1 && j < 5) K2_RW((m + 2) & 3, ((j - 1) >> 1) & 1, (j - 1) & 1); \
        if (m >= 2 && (j == 5 || j == 6)) Af[(st) ^ 1][h][(2 * m + j - 9) & 3] = ldb128(ar + ((2 * m + j - 9) & 3) * 1024); \
        if (i == 5) K2_ADV(ar); \
        if (i == 6) K2_ADV(w2b[0]); \
        if (i == 7) K2_ADV(w2b[2]); \
        if (i == 13) K2_ADV(w2b[1]); \
        if (i == 14) BAR(); \
        if (i == 15) K2_ADV(w2b[3]); \
        if (i == 23) K2_NEXT(); \
        SCHED(); } } while (0)
#define K2_NARROW(st, h) do { _Pragma("unroll") for (int i = 0; i < 16; ++i) { const int m = i >> 2, q = i & 3; \
        const int n_ = (m & 1) ? 3 - q : q; ac2[m][n_] = MFMA16(K2_W(m, 0), Af[st][h][n_], ac2[m][n_]); SCHED(); \
        if (q < 2) K2_RW((m + 3) & 3, 0, q); \
        if (q >= 2 && (m == 1 || m == 2)) Af[(st) ^ 1][h][2 * m + q - 4] = ldb128(ar + (2 * m + q - 4) * 1024); \
        if (i == 2) { K2_ADV(ar); K2_ADV(w2b[0]); } \
        if (i == 3) BAR(); \
        if (i == 5) K2_ADV(w2b[1]); \
        if (i == 9) K2_ADV(w2b[2]); \
        if (i == 14) K2_ADV(w2b[3]); \
        if (i == 15) K2_NEXT(); \
        SCHED(); } } while (0)
#define K2_WIDE4() do { K2_WIDE(0, 0); K2_WIDE(0, 1); K2_WIDE(1, 0); K2_WIDE(1, 1); } while (0)
#define K2_NARROW4() do { K2_NARROW(0, 0); K2_NARROW(0, 1); K2_NARROW(1, 0); K2_NARROW(1, 1); } while (0)
    K2_NEXT();
#pragma unroll
    for (int m = 0; m < 8; m += 2)
      asm volatile("" : "+v"(ac2[m][0]), "+v"(ac2[m][1]), "+v"(ac2[m][2]), "+v"(ac2[m][3]), "+v"(ac2[m + 1][0]), "+v"(ac2[m + 1][1]), "+v"(ac2[m + 1][2]), "+v"(ac2[m + 1][3]));
    asm volatile("" : "+v"(ar), "+v"(w2b[0]), "+v"(w2b[1]), "+v"(w2b[2]), "+v"(w2b[3]));
    BAR();
    STAMP(22);
#pragma unroll
    for (int i = 0; i < 4; ++i) { K2_RW(i >> 1, i & 1, 0); K2_RW(i >> 1, i & 1, 1); Af[0][0][i] = ldb128(ar + 6 * K2_STG + i * 1024); Af[0][1][i] = ldb128(ar + i * 1024); SCHED(); }
#pragma unroll 1
    for (int p = 0; p < npw; p += 4) {
      K2_WIDE4();
      WAIT_V(0);
    }
    asm volatile("s_nop 7\n\ts_nop 7" ::: "memory"); SCHED();
    STAMP(40);
    K2_RW(2, 0, 0); K2_RW(2, 0, 1);
    const unsigned po = (unsigned)(WS_P + wc * 8192 + lane * 16);
#pragma unroll
    for (int i = 0; i < 8; ++i) {
      const f32x4 a = ac2[4 + (i >> 1)][2 * (i & 1)], b = ac2[4 + (i >> 1)][2 * (i & 1) + 1];
      ST128((u32x4){pk2(a[0], a[1]), pk2(a[2], a[3]), pk2(b[0], b[1]), pk2(b[2], b[3])}, po + i * 1024, bid * 32768);
    }
    SCHED();
#pragma unroll 1
    for (int p = 0; p < 12; p += 4) {
      K2_NARROW4();
      WAIT_V(0);
    }
    K2_NARROW(0, 0); K2_NARROW(0, 1); K2_NARROW(1, 0);
    u32x4 pv[8];
#pragma unroll
    for (int i = 0; i < 8; ++i) pv[i] = __builtin_amdgcn_raw_buffer_load_b128(wrs, po + i * 1024 + (bid ^ 1) * 32768, 0, 16);
    SCHED();
    K2_NARROW(1, 1);
    K2_NARROW4();
    STAMP(23);
    const lptr cw = lds + k2c + (wm * 64 + t) * 512;
#pragma unroll
    for (int m = 0; m < 4; ++m)
#pragma unroll
      for (int n = 0; n < 4; ++n) {
        const unsigned lo = pv[2 * m + (n >> 1)][2 * (n & 1)], hi = pv[2 * m + (n >> 1)][2 * (n & 1) + 1];
        *(LAS f32x4*)(cw + n * 8192 + (((wn * 16 + m * 4 + g) ^ (t & 7)) << 4)) = ac2[m][n] + (f32x4){bf2f(lo & 0xffffu), U2F(lo & 0xffff0000u), bf2f(hi & 0xffffu), U2F(hi & 0xffff0000u)};
      }
    WAIT_L(0); BAR();
    if (isG) G_FIN();
  STAMP(24);
  STAMP_OUT(ws + (57 << 20), 30);
    __builtin_amdgcn_endpgm();
  }
  if (isG) {
    G_FIN();
    if (wid == 0 && lane == 0) __builtin_amdgcn_raw_buffer_store_b32(tag, wrs, 0, WS_GFLAG + (tm * 8 + tn) * 128, 16);
    if (bid == 16 && wid == 0) {
      for (unsigned spins = 0; spins < 2000000u; ++spins) {
        bool ok = true;
#pragma unroll
        for (int i = 0; i < 4; ++i) ok = ok && __builtin_amdgcn_raw_buffer_load_b32(wrs, (unsigned)((lane + i * 64) * 128), WS_HFLAG, 16) == tag;
        if (__builtin_amdgcn_ballot_w64(!ok) == 0ull) break;
        __builtin_amdgcn_s_sleep(4);
        asm volatile("" ::: "memory");
      }
      asm volatile("" ::: "memory");
      if (lane == 0) __builtin_amdgcn_raw_buffer_store_b32(tag, wrs, 0, 0, 16);
    }
  }
  STAMP(24);
  STAMP_OUT(ws + (57 << 20), 30);
}

extern "C" void kernel_launch(void* const* d_in, const int* in_sizes, int n_in, void* d_out, int out_size, void* d_ws, size_t ws_size, hipStream_t stream) {
  const char* x = (const char*)d_in[0]; const char* kc = (const char*)d_in[1]; const char* vc = (const char*)d_in[2];
  const char* wq = (const char*)d_in[3]; const char* wo = (const char*)d_in[4]; const char* w1 = (const char*)d_in[6]; const char* w2 = (const char*)d_in[7];
  (void)hipFuncSetAttribute((const void*)mega_kernel, hipFuncAttributeMaxDynamicSharedMemorySize, K1_LDS);
  mega_kernel<<<256, 512, K1_LDS, stream>>>(x, kc, vc, wq, w1, wo, w2, (char*)d_ws, (char*)d_out);
}
```

```cpp
#include <hip/hip_runtime.h>
#include <stdint.h>

typedef __bf16 bf16x2 __attribute__((ext_vector_type(2)));
typedef __bf16 bf16x4 __attribute__((ext_vector_type(4)));
typedef __bf16 bf16x8 __attribute__((ext_vector_type(8)));
typedef float f32x2 __attribute__((ext_vector_type(2)));
typedef float f32x4 __attribute__((ext_vector_type(4)));
typedef unsigned u32x2 __attribute__((ext_vector_type(2)));
typedef unsigned u32x4 __attribute__((ext_vector_type(4)));
#define LAS __attribute__((address_space(3)))
typedef LAS char* lptr;
#if defined(__HIP_DEVICE_COMPILE__)
#define NOPK __attribute__((target("no-packed-fp32-ops")))
#else
#define NOPK
#endif
#define DI NOPK __device__ __forceinline__
#define F2U(x) __builtin_bit_cast(unsigned, (float)(x))
#define U2F(x) __builtin_bit_cast(float, (unsigned)(x))
#define TID ((int)__builtin_amdgcn_workitem_id_x())
#define BID ((int)__builtin_amdgcn_workgroup_id_x())
#define SCHED() __builtin_amdgcn_sched_barrier(0)
#define BAR() asm volatile("s_barrier" ::: "memory")
#define WAIT_V(n) asm volatile("s_waitcnt vmcnt(%0)" :: "n"(n) : "memory")
#define WAIT_L(n) do { asm volatile("" ::: "memory"); __builtin_amdgcn_s_waitcnt(0xC07F | ((n) << 8)); asm volatile("" ::: "memory"); } while (0)
#define MFMA16(a, b, c) __builtin_amdgcn_mfma_f32_16x16x32_bf16(a, b, c, 0, 0, 0)

DI void glds(unsigned voff, const char* sbase, unsigned ldsaddr) {
  asm volatile("s_mov_b32 m0, %2\n\ts_nop 0\n\tglobal_load_lds_dwordx4 %0, %1" ::"v"(voff), "s"(sbase), "s"(ldsaddr) : "memory");
}
DI unsigned pk2(float lo, float hi) { f32x2 v = {lo, hi}; return __builtin_bit_cast(unsigned, __builtin_convertvector(v, bf16x2)); }
DI float bf2f(unsigned b) { return U2F(b << 16); }
DI bf16x8 ldb128(lptr p) { return *(const LAS bf16x8*)p; }
DI bf16x8 ldtr2(lptr p, int d) {
  bf16x4 a = __builtin_amdgcn_ds_read_tr16_b64_v4bf16((LAS bf16x4*)p);
  bf16x4 b = __builtin_amdgcn_ds_read_tr16_b64_v4bf16((LAS bf16x4*)(p + d));
  return __builtin_shufflevector(a, b, 0, 1, 2, 3, 4, 5, 6, 7);
}
DI void ST128_(__amdgpu_buffer_rsrc_t r, u32x4 v, unsigned voff, int soff) { __builtin_amdgcn_raw_buffer_store_b128(v, r, voff + (unsigned)soff, 0, 16); }
DI float silu(float v) { return v * __builtin_amdgcn_rcpf(1.0f + __builtin_amdgcn_exp2f(v * -1.4426950408889634f)); }

template <int CTRL> DI float dpp_add(float v) { return v + U2F(__builtin_amdgcn_update_dpp(0u, F2U(v), CTRL, 0xf, 0xf, true)); }
DI float half_sum(float v) {
  v = dpp_add<0xB1>(v); v = dpp_add<0x4E>(v); v = dpp_add<0x141>(v); v = dpp_add<0x140>(v);
  auto rr = __builtin_amdgcn_permlane16_swap(F2U(v), F2U(v), false, false);
  return U2F(rr[0]) + U2F(rr[1]);
}
DI float col_max(float v) {
  auto a = __builtin_amdgcn_permlane16_swap(F2U(v), F2U(v), false, false);
  v = __builtin_fmaxf(U2F(a[0]), U2F(a[1]));
  auto b = __builtin_amdgcn_permlane32_swap(F2U(v), F2U(v), false, false);
  return __builtin_fmaxf(U2F(b[0]), U2F(b[1]));
}
typedef __attribute__((address_space(1))) unsigned long long gu64;

#define SX(b, h) (98304 + (b) * 32768 + (h) * 16384)
constexpr int SLOT_B = 0, SLOT_E = 32768, SLOT_A = 65536, SLOT_C = 98304, SLOT_D = 131072, K1_LDS = 163840;

constexpr int WS_HFLAG = 4096;
constexpr int WS_GFLAG = 65536;
constexpr int WS_OFLAG = 131072;
constexpr int OFS = 16;
constexpr int WS_EFLAG = 163840;
constexpr int WS_STATS = 1 << 20;
constexpr int WS_PFLAG = 196608;
constexpr int WS_G = 5 << 20, WS_O = 9 << 20, WS_H = 25 << 20, WS_P = 39 << 20, WS_END = 47 << 20;
constexpr int K2_STG = 24576, K2_C = 98304;

NOPK __global__ __launch_bounds__(512, 2) void mega_kernel(const char* __restrict__ xn, const char* __restrict__ kc, const char* __restrict__ vc,
                                                           const char* __restrict__ wq, const char* __restrict__ w1, const char* __restrict__ wo, const char* __restrict__ w2,
                                                           char* ws, char* __restrict__ out) {
  extern __shared__ __attribute__((aligned(16))) char shm[];
  const lptr lds = (lptr)shm;
  const unsigned lds0 = (unsigned)(uintptr_t)lds;
  const int wid = __builtin_amdgcn_readfirstlane(TID >> 6), wr = wid >> 2, wc = wid & 3;
  const int bid = BID, xcd = bid & 7, idx = bid >> 3, hd = (idx & 2) * 8 + 2 * xcd + (idx & 1), rb = idx >> 2;
  int lane, g, t, q4, p4;
#define RELANE() do { lane = __builtin_amdgcn_mbcnt_hi(~0u, __builtin_amdgcn_mbcnt_lo(~0u, 0u)); asm volatile("" : "+v"(lane)); g = lane >> 4; t = lane & 15; q4 = (lane >> 2) & 3; p4 = lane & 3; } while (0)
  RELANE();

  const bool light = (idx & 2) != 0;
  unsigned voffX[4], voffQ[2], voffH[2];
  {
    const int r = lane >> 3, c = lane & 7;
#pragma unroll
    for (int i = 0; i < 4; ++i) voffX[i] = (unsigned)((8 * i + r) * 2048 + ((c ^ (4 * (i & 1) + (r >> 1))) << 4) - i * 1024);
    const int k = 32 * (wid >> 2) + 16 * (wid & 1) + 4 * ((wid >> 1) & 1) + 8 * (r >> 2) + (r & 3);
    const int cw = c ^ (((r >> 1) & 3) << 1);
    voffQ[0] = (unsigned)(k * 8192 + cw * 16 + 1024); voffQ[1] = voffQ[0] + 128 - 1024;
    voffH[0] = (unsigned)(k * 7168 + 128 + (light ? (cw & 3) : cw) * 16 + 1024);
    voffH[1] = (unsigned)(k * 7168 + cw * 16);
  }
  const char* xb = xn + (size_t)rb * 256 * 2048 + wid * 65536;
  const char* qb = wq + hd * 256 - 1024;
  const char* hb = w1 + (light ? 4096 + 192 * (hd - 16) : 256 * hd) - 1024;
  const bool full = wr == 1 || !light;
  const int sb = hd >= 24;
#define GLDS2(v0_, v1_, sb_, ld_) asm volatile("s_mov_b32 m0, %3\n\ts_nop 0\n\tglobal_load_lds_dwordx4 %0, %2\n\tglobal_load_lds_dwordx4 %1, %2 offset:1024" \
    ::"v"(v0_), "v"(v1_), "s"(sb_), "s"(ld_) : "memory")
#define GLDS4(v0_, v1_, v2_, v3_, sb_, ld_) asm volatile("s_mov_b32 m0, %5\n\ts_nop 0\n\tglobal_load_lds_dwordx4 %0, %4\n\tglobal_load_lds_dwordx4 %1, %4 offset:1024\n\t" \
    "global_load_lds_dwordx4 %2, %4 offset:2048\n\tglobal_load_lds_dwordx4 %3, %4 offset:3072" ::"v"(v0_), "v"(v1_), "v"(v2_), "v"(v3_), "s"(sb_), "s"(ld_) : "memory")
#define ST_X(b, kt) GLDS4(voffX[0], voffX[1], voffX[2], voffX[3], xb + (kt) * 128, lds0 + SX(b, 0) + wid * 4096)
#define ST_W0(wo_, kt) GLDS2(voffQ[0], voffQ[1], qb + (size_t)(kt) * 524288, lds0 + (wo_) + wid * 2048)
#define ST_W1(wo_, kt) GLDS2(voffH[0], voffH[1], hb + (size_t)(kt) * 458752, lds0 + (wo_) + 16384 + wid * 2048)

  lptr wb[4], xr[2];
#pragma unroll
  for (int m = 0; m < 4; ++m) wb[m] = lds + wr * 1024 + (g >> 1) * 2048 + (4 * (g & 1) + q4) * 128 + 32 * ((m ^ wc) ^ (2 * (g & 1) + (q4 >> 1))) + 8 * p4;
#pragma unroll
  for (int ks = 0; ks < 2; ++ks) xr[ks] = lds + 98304 + (wc * 32 + t) * 128 + 16 * ((4 * ks + g) ^ (t >> 1));
#define LDW(dst, h) do { _Pragma("unroll") for (int m = 0; m < 4; ++m) _Pragma("unroll") for (int k = 0; k < 2; ++k) dst[m][k] = ldtr2(wb[m] + (h) * 16384 + k * 8192, 4096); } while (0)
#define LDX(dst, b, h) do { _Pragma("unroll") for (int n = 0; n < 2; ++n) _Pragma("unroll") for (int k = 0; k < 2; ++k) dst[n][k] = ldb128(xr[k] + (SX(b, h) - 98304) + n * 2048); } while (0)

  f32x4 acc[2][2][4][2];
#pragma unroll
  for (int a = 0; a < 2; ++a)
#pragma unroll
    for (int b = 0; b < 2; ++b)
#pragma unroll
      for (int m = 0; m < 4; ++m)
#pragma unroll
        for (int n = 0; n < 2; ++n) acc[a][b][m][n] = (f32x4){0.f, 0.f, 0.f, 0.f};
  bf16x8 At[4][2], B0[2][2], B1[2][2];

#define MM1(ai, m, k, j) do { if ((j) < 2) acc[ai][0][m][(j) & 1] = MFMA16(At[m][k], B0[(j) & 1][k], acc[ai][0][m][(j) & 1]); else acc[ai][1][m][(j) & 1] = MFMA16(At[m][k], B1[(j) & 1][k], acc[ai][1][m][(j) & 1]); SCHED(); } while (0)
#define MM8(ai, m) do { _Pragma("unroll") for (int q = 0; q < 4; ++q) { MM1(ai, m, q & 1, ((m) & 1) ? 3 - q : q); MM1(ai, m, (q & 1) ^ 1, ((m) & 1) ? 3 - q : q); } } while (0)
#define MMA2(ai, CLOSE) do { __builtin_amdgcn_s_setprio(1); \
    cs[ai] = MFMA16(At[0][1], ones, cs[ai]); SCHED(); cs[ai] = MFMA16(At[0][0], ones, cs[ai]); SCHED(); MM8(ai, 0); asm volatile("" : "+v"(cs[ai])); MM8(ai, 1); \
    if ((ai) == 0 || full) { MM8(ai, 2); MM8(ai, 3); } \
    SCHED(); CLOSE; __builtin_amdgcn_s_setprio(0); SCHED(); } while (0)
  char* const stats = ws + WS_STATS;
  const __amdgpu_buffer_rsrc_t wrs = __builtin_amdgcn_make_buffer_rsrc(ws, 0, WS_END, 0x00020000);
  f32x4 cs[2] = {{0.f, 0.f, 0.f, 0.f}, {0.f, 0.f, 0.f, 0.f}};
  bf16x8 ones = __builtin_bit_cast(bf16x8, (u32x4){0x3f803f80u, 0x3f803f80u, 0x3f803f80u, 0x3f803f80u});
#define PIN_SETUP() do { asm volatile("" : "+v"(ones), "+v"(cs[0]), "+v"(cs[1]), "+v"(wb[0]), "+v"(wb[1]), "+v"(wb[2]), "+v"(wb[3]), "+v"(xr[0]), "+v"(xr[1])); \
    _Pragma("unroll") for (int a = 0; a < 2; ++a) _Pragma("unroll") for (int b = 0; b < 2; ++b) \
      asm volatile("" : "+v"(acc[a][b][0][0]), "+v"(acc[a][b][0][1]), "+v"(acc[a][b][1][0]), "+v"(acc[a][b][1][1]), "+v"(acc[a][b][2][0]), "+v"(acc[a][b][2][1]), "+v"(acc[a][b][3][0]), "+v"(acc[a][b][3][1])); } while (0)
  unsigned tag;
  {
    if (wr == 0) {
      unsigned ep;
      ST_X(0, 0); ST_W0(0, 0);
      GLDS4(voffX[0], voffX[1], voffX[2], voffX[3], xb + 262144, lds0 + SX(0, 1) + wid * 4096);
      GLDS2(voffQ[0], voffQ[1], qb + 262144, lds0 + 8192 + wid * 2048);
      asm volatile("global_load_dword %0, %1, %2 sc1" : "=&v"(ep) : "v"(0), "s"(ws) : "memory");
      PIN_SETUP();
      asm volatile("s_waitcnt vmcnt(0)" : "+v"(ep) :: "memory");
      tag = __builtin_amdgcn_readfirstlane(ep) + 1u;
      BAR();
    } else {
      const int row = rb * 256 + hd * 8 + wc * 2 + (lane >> 5);
      u32x4 a[4];
      unsigned ep;
      asm volatile("global_load_dword %0, %1, %2 sc1" : "=&v"(ep) : "v"(0), "s"(ws) : "memory");
      asm volatile("global_load_dwordx4 %0, %4, %5\n\tglobal_load_dwordx4 %1, %4, %5 offset:512\n\tglobal_load_dwordx4 %2, %4, %5 offset:1024\n\tglobal_load_dwordx4 %3, %4, %5 offset:1536"
                   : "=&v"(a[0]), "=&v"(a[1]), "=&v"(a[2]), "=&v"(a[3]) : "v"(row * 2048 + (lane & 31) * 16), "s"(xn) : "memory");
      ST_W1(0, 0); GLDS2(voffH[0], voffH[1], hb - 229376, lds0 + 16384 + wc * 2048);
      PIN_SETUP();
      BAR();
      ST_W0(32768, 1); GLDS2(voffQ[0], voffQ[1], qb + 524288 - 262144, lds0 + 32768 + wc * 2048);
      ST_X(1, 1); GLDS4(voffX[0], voffX[1], voffX[2], voffX[3], xb - 262144 + 128, lds0 + SX(1, 0) + wc * 4096);
      asm volatile("s_waitcnt vmcnt(16)" : "+v"(ep), "+v"(a[0]), "+v"(a[1]), "+v"(a[2]), "+v"(a[3]) :: "memory");
      tag = __builtin_amdgcn_readfirstlane(ep) + 1u;
      float v[32];
#pragma unroll
      for (int i = 0; i < 16; ++i) { v[2 * i] = bf2f(a[i >> 2][i & 3] & 0xffffu); v[2 * i + 1] = U2F(a[i >> 2][i & 3] & 0xffff0000u); }
      float s0 = 0.f, s1 = 0.f, s2 = 0.f, s3 = 0.f;
#pragma unroll
      for (int i = 0; i < 8; ++i) { s0 += v[i]; s1 += v[8 + i]; s2 += v[16 + i]; s3 += v[24 + i]; }
      const float mean = half_sum((s0 + s1) + (s2 + s3)) * (1.0f / 1024.0f);
      s0 = s1 = s2 = s3 = 0.f;
#pragma unroll
      for (int i = 0; i < 8; ++i) { const float d0 = v[i] - mean, d1 = v[8 + i] - mean, d2 = v[16 + i] - mean, d3 = v[24 + i] - mean; s0 += d0 * d0; s1 += d1 * d1; s2 += d2 * d2; s3 += d3 * d3; }
      const float sd = __builtin_amdgcn_sqrtf(half_sum((s0 + s1) + (s2 + s3)) * (1.0f / 1024.0f) + 1e-5f);
      if ((lane & 31) < 2) __hip_atomic_store((gu64*)(stats + row * 16 + (lane & 1) * 8), ((unsigned long long)tag << 32) | F2U((lane & 1) ? sd : mean), __ATOMIC_RELAXED, __HIP_MEMORY_SCOPE_AGENT);
    }
  }
  if (wr == 1) BAR();
  SCHED();
  const int kvg = hd >> 3;
  const char* kb_g = kc + ((size_t)kvg * 2048 + 1536) * 256;
  const char* vb_g = vc + ((size_t)kvg * 2048 + 1536) * 256;
#define VOFFK ((unsigned)((lane >> 3) * 256 + (((lane & 7) ^ (4 * (wid & 1) + (lane >> 4))) << 4)))
#define VOFFV ((unsigned)((lane >> 3) * 256 + (((lane & 7) ^ ((lane >> 4) << 1)) << 4)))
#define ST_KV8(voff, gsrc, slot) do { glds(voff, (gsrc) + wid * 2048, lds0 + (slot) + wid * 2048); glds(voff, (gsrc) + wid * 2048 + 128, lds0 + (slot) + wid * 2048 + 1024); } while (0)
  int wprv = 65536, wnxt = 32768;
#define W_ADV() do { const int d_ = (wnxt == 0) ? -65536 : 32768, cur_ = wnxt - d_; wnxt = wprv; wprv = cur_; \
    _Pragma("unroll") for (int f = 0; f < 4; ++f) wb[f] += d_; \
    asm volatile("" : "+v"(wb[0]), "+v"(wb[1]), "+v"(wb[2]), "+v"(wb[3])); } while (0)
#define PH_A(b, STAGE) do { LDX(B0, b, 0); LDX(B1, b, 1); SCHED(); LDW(At, 0); STAGE; WAIT_V(10); WAIT_L(12); BAR(); SCHED(); MMA2(0, WAIT_L(0); BAR()); } while (0)
#define PH_B(STAGE) do { LDW(At, 1); STAGE; W_ADV(); WAIT_V(8); BAR(); SCHED(); MMA2(1, WAIT_L(0); BAR()); } while (0)
#pragma unroll 1
  for (int kt = 0; kt < 14; kt += 2) {
    PH_A(0, ST_W1(wnxt, kt + 1); ST_W0(wprv, kt + 2));
    PH_B(ST_X(0, kt + 2));
    PH_A(1, ST_W1(wnxt, kt + 2); ST_W0(wprv, kt + 3));
    PH_B(ST_X(1, kt + 3));
  }
  RELANE();
  const unsigned voffK = VOFFK, voffV = VOFFV;
  PH_A(0, ST_W1(0, 15); ST_KV8(voffK, kb_g, SLOT_E));
  PH_B(ST_KV8(voffV, vb_g, SLOT_C); ST_KV8(voffV, vb_g + 16384, SLOT_C + 16384));
  const unsigned soff = (unsigned)((rb * 256 + (g >> 1) * 128 + wc * 32 + (g & 1) * 16 + t) * 16);
  u32x4 sg;
  LDX(B0, 1, 0); LDX(B1, 1, 1); SCHED(); LDW(At, 0);
  asm volatile("global_load_dwordx4 %0, %1, %2 sc1" : "=&v"(sg) : "v"(soff), "s"(stats) : "memory");
  ST_KV8(voffK, kb_g + 16384, SLOT_E + 16384); ST_KV8(voffK, kb_g + 32768, SLOT_A);
  WAIT_V(11); BAR(); SCHED(); MMA2(0, WAIT_L(0); BAR());
  LDW(At, 1);
  ST_KV8(voffK, kb_g + 49152, SLOT_A + 16384);
  WAIT_L(0); BAR(); SCHED();
  const lptr csw = lds + SLOT_D + (t & 3) * 8192 + wr * 4096 + wc * 64 + g * 16;
  MMA2(1, *(LAS f32x4*)csw = cs[0]; *(LAS f32x4*)(csw + 256) = cs[1]; asm volatile("s_waitcnt vmcnt(4)" : "+v"(sg) :: "memory"); WAIT_L(0); BAR());
  f32x4 cv[2][4];
#pragma unroll
  for (int a = 0; a < 2; ++a)
#pragma unroll
    for (int m = 0; m < 4; ++m) cv[a][m] = *(const LAS f32x4*)(lds + SLOT_D + wc * 8192 + wr * 4096 + a * 256 + (m ^ wc) * 64 + g * 16);
  if (__builtin_expect(__builtin_amdgcn_ballot_w64(sg[1] != tag || sg[3] != tag) != 0ull, 0)) {
    for (unsigned spins = 0; spins < 4000000u; ++spins) {
      __builtin_amdgcn_s_sleep(2);
      asm volatile("" ::: "memory");
      sg = __builtin_amdgcn_raw_buffer_load_b128(wrs, soff, WS_STATS, 16);
      if (__builtin_amdgcn_ballot_w64(sg[1] != tag || sg[3] != tag) == 0ull) break;
    }
  }
  float mu[2][2], rs[2][2], sd[2][2];
  {
    auto m16 = __builtin_amdgcn_permlane16_swap(sg[0], sg[0], false, false);
    auto s16 = __builtin_amdgcn_permlane16_swap(sg[2], sg[2], false, false);
#pragma unroll
    for (int n = 0; n < 2; ++n) {
      auto m32 = __builtin_amdgcn_permlane32_swap(m16[n], m16[n], false, false);
      auto s32 = __builtin_amdgcn_permlane32_swap(s16[n], s16[n], false, false);
#pragma unroll
      for (int bj = 0; bj < 2; ++bj) { mu[bj][n] = -U2F(m32[bj]); sd[bj][n] = U2F(s32[bj]); rs[bj][n] = __builtin_amdgcn_rcpf(sd[bj][n]) * 1.4426950408889634f; }
    }
  }
  WAIT_L(0);
  {
    const lptr qw = lds + wc * 8192 + wr * 4096 + ((g >> 1) * 16 + t) * 16 + (g & 1) * 8;
#pragma unroll
    for (int bj = 0; bj < 2; ++bj) {
      lptr qwb = qw + ((bj ^ sb) ? SLOT_D : SLOT_B);
      asm volatile("" : "+v"(qwb));
#pragma unroll
      for (int m = 0; m < 4; ++m)
#pragma unroll
        for (int n = 0; n < 2; ++n) {
          const f32x4 v = (acc[0][bj][m][n] + cv[0][m] * mu[bj][n]) * (rs[bj][n] * 0.08838834764831845f);
          u32x2 w = {pk2(v[0], v[1]), pk2(v[2], v[3])};
          *(LAS u32x2*)(qwb + ((m ^ wc) >> 1) * 2048 + ((m ^ wc) & 1) * 512 + n * 1024) = w;
        }
    }
  }
  WAIT_L(0); BAR(); SCHED();
  unsigned pft = 0;
  const int sl = ((idx >> 2) * 16 + (idx & 1) * 8 + xcd) * 8 + wid;
  const int tstr = light ? 896 : 1024;
  const char* tbase = (light ? w2 : wo) + (sl < tstr ? sl : tstr - 1) * 2048;
#define TOUCH(q_) asm volatile("global_load_dword %0, %1, %2" : "+v"(pft) : "v"((unsigned)((__builtin_amdgcn_mbcnt_lo(~0u, 0u) & 15) * 128)), "s"(tbase + (((q_) + 2) & 3) * tstr * 2048) : "memory")
  TOUCH(0);
  bf16x8 qf[8];
  const lptr qr = lds + (wr ? SLOT_D : SLOT_B) + wc * 8192 + lane * 16;
  if (wr == 1) {
#pragma unroll
    for (int s = 0; s < 8; ++s) qf[s] = ldb128(qr + s * 1024);
  }
#define ST128(...) ST128_(wrs, __VA_ARGS__)
  u32x4 hq[3];
  unsigned hqo;
  {
    u32x2 hw[2][2][2];
#define SILU16(m, ACT) do { \
      const f32x4 a0 = acc[1][0][m][0] + cv[1][m] * mu[0][0], a1 = acc[1][0][m][1] + cv[1][m] * mu[0][1], a2 = acc[1][1][m][0] + cv[1][m] * mu[1][0], a3 = acc[1][1][m][1] + cv[1][m] * mu[1][1]; \
      f32x4 e0 = a0 * -rs[0][0], e1 = a1 * -rs[0][1], e2 = a2 * -rs[1][0], e3 = a3 * -rs[1][1]; SCHED(); \
      if (ACT) { \
      _Pragma("unroll") for (int i = 0; i < 4; ++i) { e0[i] = __builtin_amdgcn_exp2f(e0[i]); e1[i] = __builtin_amdgcn_exp2f(e1[i]); e2[i] = __builtin_amdgcn_exp2f(e2[i]); e3[i] = __builtin_amdgcn_exp2f(e3[i]); } SCHED(); \
      e0 = e0 * sd[0][0] + sd[0][0]; e1 = e1 * sd[0][1] + sd[0][1]; e2 = e2 * sd[1][0] + sd[1][0]; e3 = e3 * sd[1][1] + sd[1][1]; SCHED(); \
      _Pragma("unroll") for (int i = 0; i < 4; ++i) { e0[i] = __builtin_amdgcn_rcpf(e0[i]); e1[i] = __builtin_amdgcn_rcpf(e1[i]); e2[i] = __builtin_amdgcn_rcpf(e2[i]); e3[i] = __builtin_amdgcn_rcpf(e3[i]); } SCHED(); \
      e0 *= a0; e1 *= a1; e2 *= a2; e3 *= a3; SCHED(); } \
      else { _Pragma("unroll") for (int i = 0; i < 4; ++i) e3[i] = a3[i] * __builtin_amdgcn_rcpf(__builtin_amdgcn_exp2f(e3[i]) * sd[1][1] + sd[1][1]); } \
      hw[0][0][(m) & 1] = (u32x2){pk2(e0[0], e0[1]), pk2(e0[2], e0[3])}; hw[0][1][(m) & 1] = (u32x2){pk2(e1[0], e1[1]), pk2(e1[2], e1[3])}; \
      hw[1][0][(m) & 1] = (u32x2){pk2(e2[0], e2[1]), pk2(e2[2], e2[3])}; hw[1][1][(m) & 1] = (u32x2){pk2(e3[0], e3[1]), pk2(e3[2], e3[3])}; SCHED(); } while (0)
#define HFRAG(bj, n) ((wc & 1) ? (u32x4){hw[bj][n][1][0], hw[bj][n][1][1], hw[bj][n][0][0], hw[bj][n][0][1]} : (u32x4){hw[bj][n][0][0], hw[bj][n][0][1], hw[bj][n][1][0], hw[bj][n][1][1]})
    const unsigned hoff = (unsigned)(WS_H + (rb * 16 + wc * 2) * 2048 + lane * 16);
    const int pb = (light ? 16 + 3 * hd : 4 * hd) + (wr ? 0 : 2), sw = full ? (wc >> 1) : 0, p0 = pb + sw, p1 = pb + (sw ^ 1);
    SILU16(0, false); SILU16(1, false);
    hqo = hoff + (p0 >> 1) * 262144 + (p0 & 1) * 1024;
    hq[0] = HFRAG(0, 0); hq[1] = HFRAG(0, 1); hq[2] = HFRAG(1, 0);
    ST128(HFRAG(1, 1), hqo + 2048, 16384);
    asm volatile("" : "+v"(hq[0]), "+v"(hq[1]), "+v"(hq[2]));
    if (wr == 0) {
      BAR(); SCHED();
#pragma unroll
      for (int s = 0; s < 8; ++s) qf[s] = ldb128(qr + s * 1024);
    }
    if (full) {
      SILU16(2, true); SILU16(3, true);
#pragma unroll
      for (int bj = 0; bj < 2; ++bj)
#pragma unroll
        for (int n = 0; n < 2; ++n) ST128(HFRAG(bj, n), hoff + n * 2048, (p1 >> 1) * 262144 + (p1 & 1) * 1024 + bj * 16384);
    }
  }
  if (wr == 1) { WAIT_L(0); BAR(); }
  __builtin_amdgcn_s_setprio(1);
  SCHED();
  RELANE();
  lptr kb[2], vb[4];
#pragma unroll
  for (int e = 0; e < 2; ++e) kb[e] = lds + SLOT_E + (t >> 3) * 2048 + (t & 7) * 128 + (((4 * e + g) ^ (t >> 1)) << 4);
#pragma unroll
  for (int f = 0; f < 4; ++f) vb[f] = lds + SLOT_C + (g >> 1) * 2048 + (4 * (g & 1) + q4) * 128 + 32 * (f ^ (2 * (g & 1) + (q4 >> 1))) + 8 * p4;
  f32x4 o[8][2], s[8][2], lacc[2], negm[2];
#pragma unroll
  for (int n = 0; n < 2; ++n) {
    lacc[n] = (f32x4){0.f, 0.f, 0.f, 0.f};
#pragma unroll
    for (int d = 0; d < 8; ++d) o[d][n] = (f32x4){0.f, 0.f, 0.f, 0.f};
  }
  bf16x8 pf[8], Kf[32];
  bf16x4 Vh[32][2];
#define SM_LIM 1.152921504606847e18f
  constexpr int LA = 6;
#define MX3(a, b, c) __builtin_fmaxf(__builtin_fmaxf((a), (b)), (c))
#define RD_V(f, hf) Vh[f][hf] = __builtin_amdgcn_ds_read_tr16_b64_v4bf16((LAS bf16x4*)(vb[((f) >> 2) & 3] + ((f) >> 4) * 1024 + ((f) & 3) * 8192 + (hf) * 4096))
#define RD_K(f) Kf[f] = ldb128(kb[(f) & 1] + ((f) >> 2) * 4096 + (((f) >> 1) & 1) * 1024)
#define VF(f) __builtin_shufflevector(Vh[f][0], Vh[f][1], 0, 1, 2, 3, 4, 5, 6, 7)
#define PREF_V() do { _Pragma("unroll") for (int f = 0; f < LA; ++f) { RD_V(f, 0); RD_V(f, 1); } SCHED(); } while (0)
#define PV_SEG(NEXTK, FILL) do { \
    _Pragma("unroll") for (int i = 0; i < 64; ++i) { const int d = i >> 3, n = (i >> 2) & 1, k = n ? 3 - (i & 3) : (i & 3), f = (i >> 1) + LA; \
      o[d][n] = MFMA16(VF(4 * d + k), pf[2 * k + n], o[d][n]); SCHED(); \
      if (f < 32) RD_V(f < 32 ? f : 0, i & 1); else if ((NEXTK) && (i & 1)) RD_K(f >= 32 ? f - 32 : 0); \
      FILL; SCHED(); } } while (0)
#define QK_SEG(HAVE, CLOSE) do { \
    _Pragma("unroll") for (int i = 0; i < 64; ++i) { const int r = i >> 3, n = (i >> 2) & 1, k = n ? 3 - (i & 3) : (i & 3), f = (i >> 1) + LA; \
      s[r][n] = MFMA16(Kf[4 * r + k], qf[2 * k + n], (i & 3) == 0 ? negm[n] : s[r][n]); SCHED(); \
      if ((i & 1) && f < 32 && f >= (HAVE)) RD_K(f < 32 ? f : 0); \
      SCHED(); \
      if (i == 59) { __builtin_amdgcn_s_setprio(2); CLOSE; WAIT_L(0); BAR(); SCHED(); } } \
    __builtin_amdgcn_s_setprio(0); } while (0)
#define MX4(m_, v_) MX3(MX3(m_, v_[0], v_[1]), v_[2], v_[3])
#define ROWMAX(n) MX4(MX4(MX4(MX4(MX4(MX4(MX4(MX4(s[0][n][0], s[0][n]), s[1][n]), s[2][n]), s[3][n]), s[4][n]), s[5][n]), s[6][n]), s[7][n])
#define EXP_PACK() do { \
      _Pragma("unroll") for (int k = 0; k < 4; ++k) { \
        _Pragma("unroll") for (int n = 0; n < 2; ++n) { \
          _Pragma("unroll") for (int i = 0; i < 4; ++i) { s[2 * k][n][i] = __builtin_amdgcn_exp2f(s[2 * k][n][i]); s[2 * k + 1][n][i] = __builtin_amdgcn_exp2f(s[2 * k + 1][n][i]); } \
          u32x4 w = {pk2(s[2 * k][n][0], s[2 * k][n][1]), pk2(s[2 * k][n][2], s[2 * k][n][3]), pk2(s[2 * k + 1][n][0], s[2 * k + 1][n][1]), pk2(s[2 * k + 1][n][2], s[2 * k + 1][n][3])}; \
          pf[2 * k + n] = __builtin_bit_cast(bf16x8, w); } \
        asm volatile("" : "+v"(pf[2 * k]), "+v"(pf[2 * k + 1])); \
        SCHED(); } \
      __builtin_amdgcn_s_setprio(2); \
      _Pragma("unroll") for (int i = 0; i < 8; ++i) { ln[i >> 2] = MFMA16(ones, pf[2 * (i & 3) + (i >> 2)], (i & 3) == 0 ? lacc[i >> 2] : ln[i >> 2]); SCHED(); } \
      __builtin_amdgcn_s_setprio(0); } while (0)
#define SOFTMAX(PRE) do { f32x4 ln[2]; \
      EXP_PACK(); \
      PRE; \
      if (__builtin_expect(__builtin_amdgcn_ballot_w64(!(ln[0][0] <= SM_LIM && ln[1][0] <= SM_LIM)) != 0ull, 0)) { \
        _Pragma("unroll") for (int r = 0; r < 8; ++r) _Pragma("unroll") for (int k = 0; k < 4; ++k) { \
          const bf16x8 kf_ = ldb128(kb[k & 1] + r * 4096 + (k >> 1) * 1024); \
          s[r][0] = MFMA16(kf_, qf[2 * k], k == 0 ? negm[0] : s[r][0]); s[r][1] = MFMA16(kf_, qf[2 * k + 1], k == 0 ? negm[1] : s[r][1]); if (k == 3) SCHED(); } \
        const float m0 = ROWMAX(0), m1 = ROWMAX(1); \
        _Pragma("unroll") for (int n = 0; n < 2; ++n) { \
          const float dl = __builtin_fmaxf(col_max(n ? m1 : m0), 0.f), alpha = __builtin_amdgcn_exp2f(-dl); \
          negm[n] -= dl; lacc[n] *= alpha; \
          _Pragma("unroll") for (int r = 0; r < 8; ++r) { s[r][n] -= dl; o[r][n] *= alpha; } } \
        EXP_PACK(); \
        PRE; \
      } \
      lacc[0] = ln[0]; lacc[1] = ln[1]; SCHED(); } while (0)
  {
#pragma unroll
    for (int f = 0; f < 8; ++f) RD_K(f);
#pragma unroll
    for (int d = 0; d < 8; d += 2) asm volatile("" : "+v"(o[d][0]), "+v"(o[d][1]), "+v"(o[d + 1][0]), "+v"(o[d + 1][1]));
    f32x4 u[2][2];
    const f32x4 z4 = {0.f, 0.f, 0.f, 0.f};
#pragma unroll
    for (int i = 0; i < 16; ++i) { const int r = i >> 3, n = (i >> 2) & 1, k = i & 3; u[r][n] = MFMA16(Kf[4 * r + k], qf[2 * k + n], k == 0 ? z4 : u[r][n]); }
#pragma unroll
    for (int n = 0; n < 2; ++n) { const float m = -col_max(MX4(MX3(u[0][n][0], u[0][n][1], __builtin_fmaxf(u[0][n][2], u[0][n][3])), u[1][n])); negm[n] = (f32x4){m, m, m, m}; }
    asm volatile("" : "+v"(negm[0]), "+v"(negm[1]));
    SCHED();
    QK_SEG(8, WAIT_V(1));
  }
  const char* kvsrc = (wr ? kb_g + 65536 : vb_g + 32768) + wc * 8192 - 1024;
  unsigned kvo[2][2];
#pragma unroll
  for (int par = 0; par < 2; ++par) {
    kvo[par][0] = (unsigned)((lane >> 3) * 256 + (((lane & 7) ^ (wr ? 4 * par + (lane >> 4) : (lane >> 4) << 1)) << 4) + 1024);
    kvo[par][1] = kvo[par][0] + 128 - 1024;
  }
#pragma unroll 1
  for (int c = 0; c < 3; ++c) {
    {
      float tv[8], ev[8];
#pragma unroll
      for (int e = 0; e < 4; ++e) { tv[2 * e] = bf2f(hq[0][e] & 0xffffu); tv[2 * e + 1] = U2F(hq[0][e] & 0xffff0000u); }
      SCHED();
#pragma unroll
      for (int e = 0; e < 8; ++e) ev[e] = __builtin_amdgcn_exp2f(tv[e]);
      SCHED();
#pragma unroll
      for (int e = 0; e < 8; ++e) ev[e] = ev[e] * -1.4426950408889634f - 1.4426950408889634f;
      SCHED();
#pragma unroll
      for (int e = 0; e < 8; ++e) ev[e] = __builtin_amdgcn_rcpf(ev[e]);
      SCHED();
      ST128((u32x4){pk2(ev[0] * tv[0], ev[1] * tv[1]), pk2(ev[2] * tv[2], ev[3] * tv[3]), pk2(ev[4] * tv[4], ev[5] * tv[5]), pk2(ev[6] * tv[6], ev[7] * tv[7])}, hqo, c == 2 ? 16384 : c * 2048);
    }
    hq[0] = hq[1]; hq[1] = hq[2];
    SCHED();
    if (__builtin_expect(c + wr < 3, 1)) {
      const int slot = wr ? (c == 0 ? SLOT_B : SLOT_C) : (c == 0 ? SLOT_D : (c == 1 ? SLOT_E : SLOT_A));
#pragma unroll
      for (int hf = 0; hf < 2; ++hf)
        asm volatile("s_mov_b32 m0, %5\n\ts_nop 0\n\t"
                     "global_load_lds_dwordx4 %0, %4\n\tglobal_load_lds_dwordx4 %1, %4 offset:1024\n\t"
                     "global_load_lds_dwordx4 %2, %4 offset:2048\n\tglobal_load_lds_dwordx4 %3, %4 offset:3072"
                     ::"v"(kvo[0][0]), "v"(kvo[0][1]), "v"(kvo[1][0]), "v"(kvo[1][1]), "s"(kvsrc + c * 32768 + hf * 4096), "s"(lds0 + slot + wc * 8192 + hf * 4096) : "memory");
    }
    TOUCH(c + 1);
    SOFTMAX(PREF_V());
    { const int dk = (c == 0) ? SLOT_A - SLOT_E : ((c == 1) ? SLOT_B - SLOT_A : SLOT_C - SLOT_B); kb[0] += dk; kb[1] += dk; asm volatile("" : "+v"(kb[0]), "+v"(kb[1])); }
    BAR(); SCHED();
    __builtin_amdgcn_s_setprio(1);
    PV_SEG(true, );
    QK_SEG(LA, WAIT_V(1); if (c == 2) WAIT_V(0));
    { const int dv = (c == 1) ? SLOT_E - SLOT_D : 32768;
#pragma unroll
      for (int f = 0; f < 4; ++f) vb[f] += dv; }
  }
  RELANE();
  if (wid == 4 && lane == 0) __builtin_amdgcn_raw_buffer_store_b32(tag, wrs, 0, WS_HFLAG + (rb * 32 + hd) * 128, 16);
  SOFTMAX(PREF_V());
  {
    asm volatile("s_waitcnt vmcnt(0)" :: "v"(pft) : "memory");
    SCHED();
    if (wr == 0) BAR();
    SCHED();
    __builtin_amdgcn_s_setprio(1);
    const unsigned oo = (unsigned)(WS_O + 2 * hd * 262144 + (rb * 16 + (wr ^ sb) * 8 + wc * 2) * 2048 + lane * 16);
    float inv[2] = {0.f, 0.f};
    u32x4 ow;
#define O_PIECE(j_) do { const int dp = (j_) >> 3, n_ = ((j_) >> 2) & 1, q_ = (j_) & 3; \
      ow[q_] = pk2(o[2 * dp + (q_ >> 1)][n_][2 * (q_ & 1)] * inv[n_], o[2 * dp + (q_ >> 1)][n_][2 * (q_ & 1) + 1] * inv[n_]); asm volatile("" : "+v"(ow)); \
      if (q_ == 3) ST128(ow, oo + n_ * 2048 + (dp & 1) * 1024, (dp >> 1) * 262144); } while (0)
    PV_SEG(false, if (i == 4) { inv[0] = __builtin_amdgcn_rcpf(lacc[0][0]); inv[1] = __builtin_amdgcn_rcpf(lacc[1][0]); } if (i >= 16 && (i & 1)) O_PIECE(i >= 16 ? (i - 16) >> 1 : 0));
#pragma unroll
    for (int j = 24; j < 32; ++j) O_PIECE(j);
    __builtin_amdgcn_s_setprio(0);
  }
  RELANE();
  const int isG = (idx >> 1) & 1, kh = xcd & 1, tm = 4 * (xcd >> 1) + ((idx >> 1) & 2) + (idx & 1), tn = 2 * (idx >> 3) + kh;
  const int npw = (isG ? 48 : 56) - 4 * kh;
  const int k2c = ((npw + 19) % 6 < 4) ? K2_C : 0;
#define O_FLAG() do { if (wid == 0 && lane == 0) __builtin_amdgcn_raw_buffer_store_b32(tag, wrs, 0, WS_OFLAG + (rb * 32 + hd) * OFS, 16); } while (0)
#define POLL32(off_, slp) do { for (unsigned spins = 0; spins < 2000000u; ++spins) { \
      if (__builtin_amdgcn_ballot_w64(__builtin_amdgcn_raw_buffer_load_b32(wrs, (off_), 0, 16) != tag) == 0ull) break; \
      __builtin_amdgcn_s_sleep(slp); asm volatile("" ::: "memory"); } \
    asm volatile("" ::: "memory"); } while (0)
  const int c8 = lane & 15, r0 = wc * 4 + (lane >> 4);
  const unsigned goff = (unsigned)((tm * 128 + r0) * 2048 + tn * 256 + c8 * 16);
  const lptr cb = lds + k2c + r0 * 512;
#define G_FIN() do { f32x4 a0[4], a1[4]; \
      _Pragma("unroll") for (int i = 0; i < 4; ++i) { a0[i] = *(const LAS f32x4*)(cb + (wr * 4 + i) * 8192 + (((2 * c8) ^ (r0 & 7)) << 4)); a1[i] = *(const LAS f32x4*)(cb + (wr * 4 + i) * 8192 + (((2 * c8 + 1) ^ (r0 & 7)) << 4)); } \
      _Pragma("unroll") for (int i = 0; i < 4; ++i) \
        ST128((u32x4){pk2(silu(a0[i][0]), silu(a0[i][1])), pk2(silu(a0[i][2]), silu(a0[i][3])), pk2(silu(a1[i][0]), silu(a1[i][1])), pk2(silu(a1[i][2]), silu(a1[i][3]))}, goff + i * 32768, WS_G + wr * 131072); \
      WAIT_V(0); BAR(); } while (0)
  const unsigned f1b = isG ? (unsigned)(WS_HFLAG + ((tm >> 1) * 32 + 16 + (lane & 15)) * 128) : (unsigned)(WS_OFLAG + ((tm >> 1) * 32 + 16 + (lane & 15)) * OFS);
  const unsigned f1 = isG ? f1b : (unsigned)(WS_EFLAG + ((tm >> 1) * 16 + (tm & 1) * 8 + kh * 4 + ((lane >> 2) & 3)) * 64 + (lane & 3) * 4);
  if (wr == 0) {
    const char* ab = ws + (isG ? WS_H : WS_O) + tm * 16384 + wc * 4096;
    const char* wbp = (isG ? w2 : wo) + tn * 256 - 1024;
    const int wsd = (kh ? -256 : 256) + wc * 32768;
    const unsigned voffA0 = lane * 16, voffA1 = lane * 16 + 1024;
    unsigned voffW[4];
    {
      const int r = lane >> 3, c = lane & 7;
      voffW[0] = (unsigned)(r * 2048 + ((c ^ (((r >> 1) & 3) << 1)) << 4) + 1024); voffW[1] = voffW[0] + 128 - 1024;
      voffW[2] = voffW[0] + 16384 - 2048; voffW[3] = voffW[2] + 128 - 1024;
    }
    const char *ap, *wp, *wo_ = wbp;
    unsigned dst = lds0;
#define K2_RUN(k_) do { ap = ab + (size_t)(k_) * 262144; wp = wbp + (size_t)(k_) * 131072; } while (0)
#define K2_A(ks, d_) asm volatile("s_mov_b32 m0, %3\n\ts_nop 0\n\tglobal_load_lds_dwordx4 %0, %2 sc1\n\tglobal_load_lds_dwordx4 %1, %2 offset:1024 sc1" \
                   ::"v"(voffA0), "v"(voffA1), "s"(ap + (ks) * 1024), "s"((d_) + wc * 2048) : "memory")
#define K2_ISSUE(WIDE, ks) do { K2_A(ks, dst); \
      if (WIDE) GLDS4(voffW[0], voffW[1], voffW[2], voffW[3], (ks) ? wp + wc * 32768 : wo_ + wsd, dst + 8192 + wc * 4096); \
      else GLDS2(voffW[0], voffW[1], ((ks) ? wp : wo_ + 65536) + wc * 16384, dst + 8192 + wc * 2048); \
      dst = (dst == lds0 + 5 * K2_STG) ? lds0 : dst + K2_STG; \
      if (ks) { wo_ = wp; ap += 262144; wp += 131072; } } while (0)
#define K2_STEP(WIDE, ks, EVENT) do { WAIT_V((WIDE) ? 18 : 12); BAR(); EVENT; K2_ISSUE(WIDE, ks); } while (0)
#define K2_TILES(WIDE, n_) do { _Pragma("unroll 1") for (int i_ = 0; i_ < (n_); ++i_) { K2_STEP(WIDE, 0, ); K2_STEP(WIDE, 1, ); } } while (0)
#define LOOK(f_) asm volatile("buffer_load_dword %0, %1, %2, 0 offen sc1" : "+v"(pol) : "v"(f_), "s"(wrs) : "memory")
    const unsigned f2 = f1b - 16 * (isG ? 128 : OFS);
    const unsigned f3 = (unsigned)(WS_PFLAG + (bid ^ 1) * 128);
    u32x4 xh[8];
#define XH_LD(i) asm volatile("global_load_dwordx4 %0, %1, %2" : "+v"(xh[i]) : "v"(goff), "s"(xn + (i) * 32768) : "memory")
    WAIT_V(0);
    if (isG && lane == 0) __builtin_amdgcn_raw_buffer_store_b32(tag, wrs, 0, WS_EFLAG + (rb * 16 + hd - 16) * 64 + wc * 4, 16);
    POLL32(f1, 4);
    K2_RUN(isG ? 32 + 12 * kh : 32 + 16 * (tm & 1) + 8 * kh);
    K2_A(0, lds0 + 6 * K2_STG); K2_ISSUE(1, 1); K2_ISSUE(1, 0);
    WAIT_V(6); BAR();
    K2_ISSUE(1, 1); K2_ISSUE(1, 0); K2_ISSUE(1, 1);
    unsigned pol = 0;
#define CHECK(f_) do { asm volatile("" : "+v"(pol)); if (__builtin_amdgcn_ballot_w64(pol != tag) != 0ull) POLL32(f_, 4); } while (0)
    K2_TILES(1, 3);
    O_FLAG();
    LOOK(f1b);
    K2_TILES(1, 2);
    if (!isG) K2_RUN(48 - 16 * (tm & 1) + 6 * kh);
    K2_STEP(1, 0, CHECK(f1b)); K2_STEP(1, 1, );
    K2_TILES(1, isG ? 1 : 3);
    LOOK(f2);
    K2_TILES(1, 2);
    K2_RUN((isG ? 12 : 14) * kh);
    K2_STEP(1, 0, CHECK(f2));
    K2_STEP(1, 1, );
    asm volatile("" : "=v"(xh[0]), "=v"(xh[1]), "=v"(xh[2]), "=v"(xh[3]), "=v"(xh[4]), "=v"(xh[5]), "=v"(xh[6]), "=v"(xh[7]));
    if (!isG) {
#pragma unroll
      for (int i = 0; i < 8; ++i) { K2_STEP(1, 0, XH_LD(i)); K2_STEP(1, 1, ); }
    }
    K2_TILES(1, (isG ? 11 : 5) - 2 * kh);
    asm volatile("" : "+v"(xh[0]), "+v"(xh[1]), "+v"(xh[2]), "+v"(xh[3]), "+v"(xh[4]), "+v"(xh[5]), "+v"(xh[6]), "+v"(xh[7]));
    K2_RUN(isG ? 22 : 60 - 16 * (tm & 1));
    K2_STEP(1, 0, ); K2_STEP(0, 1, );
    K2_TILES(0, 3);
    K2_RUN(26);
    K2_STEP(0, 0, ); K2_STEP(0, 1, );
    K2_STEP(0, 0, if (wid == 0 && lane == 0) __builtin_amdgcn_raw_buffer_store_b32(tag, wrs, 0, WS_PFLAG + bid * 128, 16));
    K2_STEP(0, 1, );
    K2_TILES(0, 2);
    K2_STEP(0, 0, );
    const unsigned f4 = (lane & 1) ? (unsigned)(WS_GFLAG + (tm * 8 + tn) * 128) : f3;
    K2_STEP(0, 1, LOOK(f4)); K2_STEP(0, 0, ); K2_STEP(0, 1, );
    WAIT_V(12);
    asm volatile("" : "+v"(pol));
    const unsigned long long miss = __builtin_amdgcn_ballot_w64(pol != tag);
    if (wid == 0 && (miss & 1ull)) POLL32(f3, 2);
    BAR();
    K2_ISSUE(0, 0);
    WAIT_V(12); BAR();
    WAIT_V(8); BAR();
    if (isG) {
      WAIT_V(4); BAR();
      WAIT_V(0); BAR();
      BAR();
      BAR();
    } else {
      u32x4 gv[8];
      const __amdgpu_buffer_rsrc_t ors = __builtin_amdgcn_make_buffer_rsrc(out, 0, 2048 * 2048, 0x00020000);
#define LD8(dst_, rs_, so_, pol_) asm volatile("buffer_load_dwordx4 %0, %8, %10, %11 offen" pol_ "\n\tbuffer_load_dwordx4 %1, %9, %10, %11 offen" pol_ "\n\t" \
        "buffer_load_dwordx4 %2, %8, %10, %12 offen" pol_ "\n\tbuffer_load_dwordx4 %3, %9, %10, %12 offen" pol_ "\n\t" \
        "buffer_load_dwordx4 %4, %8, %10, %13 offen" pol_ "\n\tbuffer_load_dwordx4 %5, %9, %10, %13 offen" pol_ "\n\t" \
        "buffer_load_dwordx4 %6, %8, %10, %14 offen" pol_ "\n\tbuffer_load_dwordx4 %7, %9, %10, %14 offen" pol_ \
        : "=&v"(dst_[0]), "=&v"(dst_[1]), "=&v"(dst_[2]), "=&v"(dst_[3]), "=&v"(dst_[4]), "=&v"(dst_[5]), "=&v"(dst_[6]), "=&v"(dst_[7]) \
        : "v"(goff), "v"(goff + 32768), "s"(rs_), "s"((so_)), "s"((so_) + 65536), "s"((so_) + 131072), "s"((so_) + 196608) : "memory")
      if (miss & 2ull) {
        WAIT_V(4); BAR(); WAIT_V(0); BAR(); BAR();
        POLL32((unsigned)(WS_GFLAG + (tm * 8 + tn) * 128), 1);
      }
      LD8(gv, wrs, WS_G, " sc1");
      if (!(miss & 2ull)) { WAIT_V(12); BAR(); WAIT_V(8); BAR(); BAR(); }
      float xf[8][8];
#pragma unroll
      for (int i = 0; i < 8; ++i) {
#pragma unroll
        for (int e = 0; e < 4; ++e) { xf[i][2 * e] = bf2f(xh[i][e] & 0xffffu); xf[i][2 * e + 1] = U2F(xh[i][e] & 0xffff0000u); }
        asm volatile("" : "+v"(xf[i][0]), "+v"(xf[i][1]), "+v"(xf[i][2]), "+v"(xf[i][3]), "+v"(xf[i][4]), "+v"(xf[i][5]), "+v"(xf[i][6]), "+v"(xf[i][7]));
      }
      asm volatile("s_waitcnt vmcnt(0)" : "+v"(gv[0]), "+v"(gv[1]), "+v"(gv[2]), "+v"(gv[3]), "+v"(gv[4]), "+v"(gv[5]), "+v"(gv[6]), "+v"(gv[7]) :: "memory");
      float gf2[8][8];
#pragma unroll
      for (int i = 0; i < 8; ++i) {
#pragma unroll
        for (int e = 0; e < 4; ++e) { gf2[i][2 * e] = bf2f(gv[i][e] & 0xffffu); gf2[i][2 * e + 1] = U2F(gv[i][e] & 0xffff0000u); }
        asm volatile("" : "+v"(gf2[i][0]), "+v"(gf2[i][1]), "+v"(gf2[i][2]), "+v"(gf2[i][3]), "+v"(gf2[i][4]), "+v"(gf2[i][5]), "+v"(gf2[i][6]), "+v"(gf2[i][7]));
      }
      BAR();
      f32x4 a0[8], a1[8];
#pragma unroll
      for (int i = 0; i < 8; ++i) { a0[i] = *(const LAS f32x4*)(cb + i * 8192 + (((2 * c8) ^ (r0 & 7)) << 4)); a1[i] = *(const LAS f32x4*)(cb + i * 8192 + (((2 * c8 + 1) ^ (r0 & 7)) << 4)); }
      SCHED();
#pragma unroll
      for (int i = 0; i < 8; ++i) {
        const u32x4 w = {pk2(xf[i][0] + a0[i][0] * gf2[i][0], xf[i][1] + a0[i][1] * gf2[i][1]), pk2(xf[i][2] + a0[i][2] * gf2[i][2], xf[i][3] + a0[i][3] * gf2[i][3]),
                         pk2(xf[i][4] + a1[i][0] * gf2[i][4], xf[i][5] + a1[i][1] * gf2[i][5]), pk2(xf[i][6] + a1[i][2] * gf2[i][6], xf[i][7] + a1[i][3] * gf2[i][7])};
        __builtin_amdgcn_raw_buffer_store_b128(w, ors, goff + i * 32768, 0, 16);
      }
    }
  } else {
    const int wm = wc >> 1, wn = wc & 1;
    lptr w2b[4];
#pragma unroll
    for (int ft = 0; ft < 4; ++ft) w2b[ft] = lds + 8192 + wn * 1024 + (g >> 1) * 2048 + (4 * (g & 1) + q4) * 128 + 32 * (ft ^ (2 * (g & 1) + (q4 >> 1))) + 8 * p4;
    lptr ar = lds + wm * 4096 + lane * 16;
    f32x4 ac2[8][4];
#pragma unroll
    for (int m = 0; m < 8; ++m)
#pragma unroll
      for (int n = 0; n < 4; ++n) ac2[m][n] = (f32x4){0.f, 0.f, 0.f, 0.f};
    bf16x8 Af[2][2][4];
    bf16x4 Wr[4][2][2];
    int slot = 0, dlt = 0;
#define K2_RW(m, ks, hf) Wr[m][ks][hf] = __builtin_amdgcn_ds_read_tr16_b64_v4bf16((LAS bf16x4*)(w2b[m] + (ks) * 8192 + (hf) * 4096))
#define K2_W(m, ks) __builtin_shufflevector(Wr[m][ks][0], Wr[m][ks][1], 0, 1, 2, 3, 4, 5, 6, 7)
#define K2_NEXT() do { dlt = (slot == 5) ? -5 * K2_STG : K2_STG; slot = (slot == 5) ? 0 : slot + 1; } while (0)
#define K2_ADV(x) do { x += dlt; asm volatile("" : "+v"(x)); } while (0)
#define K2_WIDE(st, h) do { _Pragma("unroll") for (int i = 0; i < 32; ++i) { const int m = i >> 3, j = i & 7, q = j >> 1; \
        const int n_ = (m & 1) ? 3 - q : q, k_ = (j ^ q) & 1; asm volatile("v_mfma_f32_16x16x32_bf16 %0, %1, %2, %0" : "+v"(ac2[4 * (h) + m][n_]) : "v"(K2_W(m, k_)), "v"(Af[st][k_][n_])); SCHED(); \
        if (j >= 1 && j < 5) K2_RW((m + 2) & 3, ((j - 1) >> 1) & 1, (j - 1) & 1); \
        if (m >= 2 && (j == 5 || j == 6)) Af[(st) ^ 1][h][(2 * m + j - 9) & 3] = ldb128(ar + ((2 * m + j - 9) & 3) * 1024); \
        if (i == 5) K2_ADV(ar); \
        if (i == 6) K2_ADV(w2b[0]); \
        if (i == 7) K2_ADV(w2b[2]); \
        if (i == 13) K2_ADV(w2b[1]); \
        if (i == 14) BAR(); \
        if (i == 15) K2_ADV(w2b[3]); \
        if (i == 23) K2_NEXT(); \
        SCHED(); } } while (0)
#define K2_NARROW(st, h) do { _Pragma("unroll") for (int i = 0; i < 16; ++i) { const int m = i >> 2, q = i & 3; \
        const int n_ = (m & 1) ? 3 - q : q; ac2[m][n_] = MFMA16(K2_W(m, 0), Af[st][h][n_], ac2[m][n_]); SCHED(); \
        if (q < 2) K2_RW((m + 3) & 3, 0, q); \
        if (q >= 2 && (m == 1 || m == 2)) Af[(st) ^ 1][h][2 * m + q - 4] = ldb128(ar + (2 * m + q - 4) * 1024); \
        if (i == 2) { K2_ADV(ar); K2_ADV(w2b[0]); } \
        if (i == 3) BAR(); \
        if (i == 5) K2_ADV(w2b[1]); \
        if (i == 9) K2_ADV(w2b[2]); \
        if (i == 14) K2_ADV(w2b[3]); \
        if (i == 15) K2_NEXT(); \
        SCHED(); } } while (0)
#define K2_WIDE4() do { K2_WIDE(0, 0); K2_WIDE(0, 1); K2_WIDE(1, 0); K2_WIDE(1, 1); } while (0)
#define K2_NARROW4() do { K2_NARROW(0, 0); K2_NARROW(0, 1); K2_NARROW(1, 0); K2_NARROW(1, 1); } while (0)
    K2_NEXT();
#pragma unroll
    for (int m = 0; m < 8; m += 2)
      asm volatile("" : "+v"(ac2[m][0]), "+v"(ac2[m][1]), "+v"(ac2[m][2]), "+v"(ac2[m][3]), "+v"(ac2[m + 1][0]), "+v"(ac2[m + 1][1]), "+v"(ac2[m + 1][2]), "+v"(ac2[m + 1][3]));
    asm volatile("" : "+v"(ar), "+v"(w2b[0]), "+v"(w2b[1]), "+v"(w2b[2]), "+v"(w2b[3]));
    BAR();
#pragma unroll
    for (int i = 0; i < 4; ++i) { K2_RW(i >> 1, i & 1, 0); K2_RW(i >> 1, i & 1, 1); Af[0][0][i] = ldb128(ar + 6 * K2_STG + i * 1024); Af[0][1][i] = ldb128(ar + i * 1024); SCHED(); }
#pragma unroll 1
    for (int p = 0; p < npw; p += 4) {
      K2_WIDE4();
      WAIT_V(0);
    }
    asm volatile("s_nop 7\n\ts_nop 7" ::: "memory"); SCHED();
    K2_RW(2, 0, 0); K2_RW(2, 0, 1);
    const unsigned po = (unsigned)(WS_P + wc * 8192 + lane * 16);
#pragma unroll
    for (int i = 0; i < 8; ++i) {
      const f32x4 a = ac2[4 + (i >> 1)][2 * (i & 1)], b = ac2[4 + (i >> 1)][2 * (i & 1) + 1];
      ST128((u32x4){pk2(a[0], a[1]), pk2(a[2], a[3]), pk2(b[0], b[1]), pk2(b[2], b[3])}, po + i * 1024, bid * 32768);
    }
    SCHED();
#pragma unroll 1
    for (int p = 0; p < 12; p += 4) {
      K2_NARROW4();
      WAIT_V(0);
    }
    K2_NARROW(0, 0); K2_NARROW(0, 1); K2_NARROW(1, 0);
    u32x4 pv[8];
#pragma unroll
    for (int i = 0; i < 8; ++i) pv[i] = __builtin_amdgcn_raw_buffer_load_b128(wrs, po + i * 1024 + (bid ^ 1) * 32768, 0, 16);
    SCHED();
    K2_NARROW(1, 1);
    K2_NARROW4();
    const lptr cw = lds + k2c + (wm * 64 + t) * 512;
#pragma unroll
    for (int m = 0; m < 4; ++m)
#pragma unroll
      for (int n = 0; n < 4; ++n) {
        const unsigned lo = pv[2 * m + (n >> 1)][2 * (n & 1)], hi = pv[2 * m + (n >> 1)][2 * (n & 1) + 1];
        *(LAS f32x4*)(cw + n * 8192 + (((wn * 16 + m * 4 + g) ^ (t & 7)) << 4)) = ac2[m][n] + (f32x4){bf2f(lo & 0xffffu), U2F(lo & 0xffff0000u), bf2f(hi & 0xffffu), U2F(hi & 0xffff0000u)};
      }
    WAIT_L(0); BAR();
    if (isG) G_FIN();
    __builtin_amdgcn_endpgm();
  }
  if (isG) {
    G_FIN();
    if (wid == 0 && lane == 0) __builtin_amdgcn_raw_buffer_store_b32(tag, wrs, 0, WS_GFLAG + (tm * 8 + tn) * 128, 16);
    if (bid == 16 && wid == 0) {
      for (unsigned spins = 0; spins < 2000000u; ++spins) {
        bool ok = true;
#pragma unroll
        for (int i = 0; i < 4; ++i) ok = ok && __builtin_amdgcn_raw_buffer_load_b32(wrs, (unsigned)((lane + i * 64) * 128), WS_HFLAG, 16) == tag;
        if (__builtin_amdgcn_ballot_w64(!ok) == 0ull) break;
        __builtin_amdgcn_s_sleep(4);
        asm volatile("" ::: "memory");
      }
      asm volatile("" ::: "memory");
      if (lane == 0) __builtin_amdgcn_raw_buffer_store_b32(tag, wrs, 0, 0, 16);
    }
  }
}

extern "C" void kernel_launch(void* const* d_in, const int* in_sizes, int n_in, void* d_out, int out_size, void* d_ws, size_t ws_size, hipStream_t stream) {
  const char* x = (const char*)d_in[0]; const char* kc = (const char*)d_in[1]; const char* vc = (const char*)d_in[2];
  const char* wq = (const char*)d_in[3]; const char* wo = (const char*)d_in[4]; const char* w1 = (const char*)d_in[6]; const char* w2 = (const char*)d_in[7];
  (void)hipFuncSetAttribute((const void*)mega_kernel, hipFuncAttributeMaxDynamicSharedMemorySize, K1_LDS);
  mega_kernel<<<256, 512, K1_LDS, stream>>>(x, kc, vc, wq, w1, wo, w2, (char*)d_ws, (char*)d_out);
}
```
